```python
import torch

def _softmax(x, axis=-1):
    return torch.softmax(x, dim=axis)
def _gelu(x):
    return torch.nn.functional.gelu(x, approximate="tanh")
def _silu(x):
    return torch.nn.functional.silu(x)
def _sigmoid(x):
    return torch.sigmoid(x)
def _layernorm(x, axis=-1, eps=1e-5):
    m = x.mean(dim=axis, keepdim=True)
    v = x.var(dim=axis, keepdim=True, unbiased=False)
    return (x - m) / torch.sqrt(v + eps)
def _scatter_add(tgt, idx, src, axis=0):
    return tgt.clone().index_add_(axis, idx, src)
def _segment_sum(src, seg, n):
    out = torch.zeros((n,) + src.shape[1:], dtype=src.dtype)
    return out.index_add_(0, seg, src)
def _pad(x, pw):
    flat = tuple(v for pair in reversed(pw) for v in pair)
    return torch.nn.functional.pad(x, flat)

kernel_name = 'interleave_layers__grouped_kv_mqa__sliding_block__prompt'

def setup_inputs():

    _w = (lambda a: (lambda t: t.to(torch.bfloat16) if t.is_floating_point() else t)(torch.as_tensor(a)))
    def _A_setup():
        B, S, D, H = 1, 2048, 576, 32
        Dh = D // H
        return {
            "x": _w((lambda *s: torch.randn(*s, dtype=torch.bfloat16) * 0.05)(B, S, D)),
            "wq": _w((lambda *s: torch.randn(*s, dtype=torch.bfloat16) * 0.05)(D, H * Dh)),
            "wk": _w((lambda *s: torch.randn(*s, dtype=torch.bfloat16) * 0.05)(D, Dh)),
            "wv": _w((lambda *s: torch.randn(*s, dtype=torch.bfloat16) * 0.05)(D, Dh)),
            "wo": _w((lambda *s: torch.randn(*s, dtype=torch.bfloat16) * 0.05)(H * Dh, D)),
        }
    _A_in = _A_setup()

    def _B_setup():
        B, S, D = 1, 2048, 576
        return {
            "x": _w((lambda *s: torch.randn(*s, dtype=torch.bfloat16) * 0.05)(B, S, D)),
            "wqkv": _w((lambda *s: torch.randn(*s, dtype=torch.bfloat16) * 0.05)(D, 3 * D)),
            "wo": _w((lambda *s: torch.randn(*s, dtype=torch.bfloat16) * 0.05)(D, D)),
        }
    _B_in = _B_setup()

    return {
        "A_x": _A_in["x"],
        "A_wq": _A_in["wq"],
        "A_wk": _A_in["wk"],
        "A_wv": _A_in["wv"],
        "A_wo": _A_in["wo"],
        "B_x": _B_in["x"],
        "B_wqkv": _B_in["wqkv"],
        "B_wo": _B_in["wo"],
    }

def reference(A_x, A_wq, A_wk, A_wv, A_wo, B_x, B_wqkv, B_wo):
    def _A_ref(x, wq, wk, wv, wo):
        B, S, D = x.shape
        H = 32
        Dh = D // H
        q = (lambda x, *d: x.permute(*d))((x @ wq).reshape(B, S, H, Dh), 0, 2, 1, 3)
        k = x @ wk
        v = x @ wv
        scores = torch.einsum("bhqd,bkd->bhqk", q, k) * (Dh ** -0.5)
        p = _softmax(scores, axis=-1)
        o = torch.einsum("bhqk,bkd->bhqd", p, v)
        return (lambda x, *d: x.permute(*d))(o, 0, 2, 1, 3).reshape(B, S, H * Dh) @ wo

    def _B_ref(x, wqkv, wo):
        B, S, D = x.shape
        H = 32; Dh = D // H; W = 256
        qkv = (lambda x, *d: x.permute(*d))((x @ wqkv).reshape(B, S, 3, H, Dh), 2, 0, 3, 1, 4)
        q, k, v = qkv[0], qkv[1], qkv[2]
        scores = torch.einsum("bhqd,bhkd->bhqk", q, k) * (Dh ** -0.5)
        idx = torch.arange(S)
        mask = (idx[:, None] - idx[None, :] >= 0) & (idx[:, None] - idx[None, :] < W)
        scores = torch.where(mask[None, None], scores, -1e9)
        p = _softmax(scores, axis=-1)
        o = torch.einsum("bhqk,bhkd->bhqd", p, v)
        return (lambda x, *d: x.permute(*d))(o, 0, 2, 1, 3).reshape(B, S, D) @ wo

    x = A_x
    x = x + _A_ref(_layernorm(x, axis=-1), A_wq, A_wk, A_wv, A_wo)
    x = x + _B_ref(_layernorm(x, axis=-1), B_wqkv, B_wo)
    x = x + _A_ref(_layernorm(x, axis=-1), A_wq, A_wk, A_wv, A_wo)
    x = x + _B_ref(_layernorm(x, axis=-1), B_wqkv, B_wo)
    return x
```

```cpp
#include <hip/hip_runtime.h>
#include <hip/hip_bf16.h>
typedef unsigned short u16;
__device__ __forceinline__ float b2f(u16 v) { return __uint_as_float(((unsigned)v) << 16); }
__device__ __forceinline__ u16 f2b(float f) { unsigned u = __float_as_uint(f); u += 0x7fffu + ((u >> 16) & 1u); return (u16)(u >> 16); }
constexpr int S = 2048, D = 576, H = 32, DH = 18;

__global__ void k_lnproj(const u16* x, const u16* W, int ldw, int N, u16* out, int ldo, float scale_q, int nq) {
    __shared__ float xs[D]; __shared__ float red[2];
    int r = blockIdx.x;
    for (int k = threadIdx.x; k < D; k += blockDim.x) xs[k] = b2f(x[r * D + k]);
    __syncthreads();
    if (threadIdx.x == 0) { float s = 0, s2 = 0; for (int k = 0; k < D; ++k) s += xs[k]; float m = s / D; for (int k = 0; k < D; ++k) s2 += (xs[k] - m) * (xs[k] - m); red[0] = m; red[1] = rsqrtf(s2 / D + 1e-5f); }
    __syncthreads();
    for (int n = threadIdx.x; n < N; n += blockDim.x) {
        float a = 0, cs = 0; for (int k = 0; k < D; ++k) { float w = b2f(W[k * ldw + n]); a += xs[k] * w; cs += w; }
        float v = (a - red[0] * cs) * red[1]; if (n < nq) v *= scale_q;
        out[r * ldo + n] = f2b(v);
    }
}
__global__ void k_outproj(const u16* o, const u16* W, const u16* xin, u16* xout) {
    __shared__ float xs[D];
    int r = blockIdx.x;
    for (int k = threadIdx.x; k < D; k += blockDim.x) xs[k] = b2f(o[r * D + k]);
    __syncthreads();
    for (int n = threadIdx.x; n < D; n += blockDim.x) {
        float a = 0; for (int k = 0; k < D; ++k) a += xs[k] * b2f(W[k * D + n]);
        xout[r * D + n] = f2b(b2f(xin[r * D + n]) + a);
    }
}
__global__ void k_attn(const u16* q, int ldq, const u16* k, const u16* v, int ldkv, int hstride, int window, u16* o) {
    int idx = blockIdx.x * blockDim.x + threadIdx.x; int r = idx / H, h = idx % H;
    float qq[DH], acc[DH]; for (int d = 0; d < DH; ++d) { qq[d] = b2f(q[r * ldq + h * DH + d]); acc[d] = 0; }
    int k0 = 0, k1 = S - 1; if (window) { k0 = r - window + 1 < 0 ? 0 : r - window + 1; k1 = r; }
    float m = -1e30f, l = 0;
    for (int j = k0; j <= k1; ++j) {
        float s = 0; for (int d = 0; d < DH; ++d) s += qq[d] * b2f(k[j * ldkv + h * hstride + d]);
        float mn = fmaxf(m, s); float al = exp2f(m - mn); float p = exp2f(s - mn); m = mn;
        float pb = b2f(f2b(p));
        l = l * al + pb; for (int d = 0; d < DH; ++d) acc[d] = acc[d] * al + pb * b2f(v[j * ldkv + h * hstride + d]);
    }
    for (int d = 0; d < DH; ++d) o[r * D + h * DH + d] = f2b(acc[d] / l);
}
extern "C" void kernel_launch(void* const* d_in, const int* in_sizes, int n_in, void* d_out, int out_size, void* d_ws, size_t ws_size, hipStream_t stream) {
    const u16 *x = (const u16*)d_in[0], *wq = (const u16*)d_in[1], *wk = (const u16*)d_in[2], *wv = (const u16*)d_in[3], *woA = (const u16*)d_in[4], *wqkv = (const u16*)d_in[6], *woB = (const u16*)d_in[7];
    u16* ws = (u16*)d_ws; u16* q = ws; u16* kk = q + S * 1728; u16* vv = kk + S * D; u16* o = vv + S * D; u16* x1 = o + S * D; u16* x2 = x1 + S * D;
    const float sc = 0.23570226039551584f * 1.4426950408889634f;
    const u16* xin = x;
    for (int rep = 0; rep < 2; ++rep) {
        k_lnproj<<<S, 256, 0, stream>>>(xin, wq, D, D, q, D, sc, D);
        k_lnproj<<<S, 64, 0, stream>>>(xin, wk, DH, DH, kk, DH, 1.f, 0);
        k_lnproj<<<S, 64, 0, stream>>>(xin, wv, DH, DH, vv, DH, 1.f, 0);
        k_attn<<<S * H / 64, 64, 0, stream>>>(q, D, kk, vv, DH, 0, 0, o);
        k_outproj<<<S, 256, 0, stream>>>(o, woA, xin, x1);
        k_lnproj<<<S, 256, 0, stream>>>(x1, wqkv, 1728, 1728, q, 1728, sc, D);
        k_attn<<<S * H / 64, 64, 0, stream>>>(q, 1728, q + D, q + 2 * D, 1728, DH, 256, o);
        u16* xo = rep == 0 ? x2 : (u16*)d_out;
        k_outproj<<<S, 256, 0, stream>>>(o, woB, x1, xo);
        xin = x2;
    }
}
```

```cpp
#include <hip/hip_runtime.h>
#include <hip/hip_bf16.h>

typedef unsigned short u16;
typedef unsigned u32;
typedef unsigned long long u64;
typedef __bf16 bf16x8 __attribute__((ext_vector_type(8)));
typedef __bf16 bf16x2 __attribute__((ext_vector_type(2)));
typedef short s16x4 __attribute__((ext_vector_type(4)));
typedef float f32x2 __attribute__((ext_vector_type(2)));
typedef float f32x4 __attribute__((ext_vector_type(4)));
typedef float f32x16 __attribute__((ext_vector_type(16)));
typedef u32 u32x2 __attribute__((ext_vector_type(2)));
typedef u32 u32x4 __attribute__((ext_vector_type(4)));
#define LAS __attribute__((address_space(3)))
#define GAS __attribute__((address_space(1)))
#define DI __device__ __forceinline__

constexpr int SEQ = 2048, DM = 576;
constexpr u32 TILE_B = 2560;
constexpr u32 KT_V = 1024, KT_VBLK = 336, KT_KREM = 2368;
constexpr u32 XF_BYTES = 32u * 18 * 4 * 1024;
constexpr u32 WS_EPOCH = 0;
constexpr u32 WS_FLAG = 4096;
constexpr u32 WS_XF = WS_FLAG + 11u * 256 * 128 + 4096;
constexpr u32 WS_OF = WS_XF + 3 * XF_BYTES;
constexpr u32 ST_BYTES = 2048u * 16 * 8;
constexpr u32 WS_ST = WS_OF + 4 * XF_BYTES;
constexpr u32 KVA_BYTES = 64 * TILE_B;
constexpr u32 WS_KVA = WS_ST + 3 * ST_BYTES;
constexpr u32 KVB_BYTES = 32 * 64 * TILE_B;
constexpr u32 WS_KVB = WS_KVA + 2 * KVA_BYTES;
constexpr u32 WF_BYTES = 8u * 16 * 18 * 1024;
constexpr u32 WS_WF = WS_KVB + 2 * KVB_BYTES;
constexpr u32 WS_CSP = WS_WF + WF_BYTES;
constexpr u32 WS_STAMP = WS_CSP + 8u * 16 * 18 * 64;
constexpr u32 WS_USED = WS_STAMP + 256u * 64 * 8;
constexpr int L_XCH = 0;
constexpr int L_RING = 0;
constexpr int CHUNK_B = 8 * TILE_B;
constexpr int L_STG = 40960;
constexpr int STG_P = 168;
constexpr int L_R1 = 61440;
constexpr int WP = 160;
constexpr int L_STATP = 153600;
constexpr int L_MEAN = L_STATP + 1024;
constexpr int L_CSA = L_MEAN + 512;
constexpr int L_CSB = L_CSA + 1024;
constexpr int L_QREM = L_CSB + 1024;
constexpr int L_QT1 = L_QREM + 1024;
constexpr int LDS_BYTES = 163840;
static_assert(L_QT1 + 4096 <= LDS_BYTES, "lds");

constexpr float QSCALE = 0.23570226039551584f * 1.4426950408889634f;
constexpr float NEG_BIG = -1e30f;
constexpr u32 SPIN_CAP = 1u << 20;
constexpr int AUX_SC1 = 16;

struct Params { const u16* x; const u16* wq; const u16* wk; const u16* wv; const u16* woA; const u16* wqkv; const u16* woB; u16* out; char* ws; };

DI u32 pk(float a, float b) { f32x2 v = {a, b}; return __builtin_bit_cast(u32, __builtin_convertvector(v, bf16x2)); }
DI float asf(u32 d) { return __builtin_bit_cast(float, d); }
DI u32 asu(float f) { return __builtin_bit_cast(u32, f); }
DI float bflo(u32 d) { return __builtin_bit_cast(float, d << 16); }
DI float bfhi(u32 d) { return __builtin_bit_cast(float, d & 0xffff0000u); }
DI bf16x8 asb(u32x4 v) { return __builtin_bit_cast(bf16x8, v); }
DI f32x4 mfma16(u32x4 a, u32x4 b, f32x4 c) { return __builtin_amdgcn_mfma_f32_16x16x32_bf16(asb(a), asb(b), c, 0, 0, 0); }
DI f32x16 mfma32(u32x4 a, u32x4 b, f32x16 c) { return __builtin_amdgcn_mfma_f32_32x32x16_bf16(asb(a), asb(b), c, 0, 0, 0); }
template <class T> DI T lld(LAS char* p) { return *(LAS const T*)p; }
template <class T> DI void lst(LAS char* p, T v) { *(LAS T*)p = v; }
DI u32x2 trr(LAS char* p) { return __builtin_bit_cast(u32x2, __builtin_amdgcn_ds_read_tr16_b64_v4i16((LAS s16x4*)p)); }
#define BAR() asm volatile("s_waitcnt lgkmcnt(0)\n\ts_barrier" ::: "memory")
#define DRAIN() asm volatile("s_waitcnt vmcnt(0)" ::: "memory")
typedef __amdgpu_buffer_rsrc_t rsrc_t;
DI u32x4 ld128(rsrc_t rs, u32 voff, u32 soff) { return __builtin_amdgcn_raw_buffer_load_b128(rs, voff, soff, AUX_SC1); }
DI u32 ld32(rsrc_t rs, u32 voff, u32 soff) { return __builtin_amdgcn_raw_buffer_load_b32(rs, voff, soff, AUX_SC1); }
DI void st128(rsrc_t rs, u32x4 v, u32 voff, u32 soff) { __builtin_amdgcn_raw_buffer_store_b128(v, rs, voff + soff, 0, AUX_SC1); }
DI void st64(rsrc_t rs, u32x2 v, u32 voff, u32 soff) { __builtin_amdgcn_raw_buffer_store_b64(v, rs, voff, soff, AUX_SC1); }
DI void st32(rsrc_t rs, u32 v, u32 voff, u32 soff) { __builtin_amdgcn_raw_buffer_store_b32(v, rs, voff, soff, AUX_SC1); }
DI void glds_ws(u32x4 srd, u32 voff, u32 soff, u32 ldsdst) {
    u32 keep;
    asm volatile("s_mov_b32 %0, m0\n\ts_mov_b32 m0, %1\n\ts_nop 2\n\tbuffer_load_dwordx4 %2, %3, %4 offen sc1 lds\n\ts_mov_b32 m0, %0"
                 : "=&s"(keep) : "s"(ldsdst), "v"(voff), "s"(srd), "s"(soff) : "memory");
}
DI void glds_in(const void* g, u32 ldsdst) {
    u32 keep;
    asm volatile("s_mov_b32 %0, m0\n\ts_mov_b32 m0, %1\n\ts_nop 2\n\tglobal_load_lds_dwordx4 %2, off\n\ts_mov_b32 m0, %0"
                 : "=&s"(keep) : "s"(ldsdst), "v"(g) : "memory");
}
DI float sum_g(float v) {
    auto a = __builtin_amdgcn_permlane32_swap(__builtin_bit_cast(u32, v), __builtin_bit_cast(u32, v), false, false);
    v = __builtin_bit_cast(float, (u32)a[0]) + __builtin_bit_cast(float, (u32)a[1]);
    auto b = __builtin_amdgcn_permlane16_swap(__builtin_bit_cast(u32, v), __builtin_bit_cast(u32, v), false, false);
    return __builtin_bit_cast(float, (u32)b[0]) + __builtin_bit_cast(float, (u32)b[1]);
}
DI float max_h(float v) {
    auto a = __builtin_amdgcn_permlane32_swap(__builtin_bit_cast(u32, v), __builtin_bit_cast(u32, v), false, false);
    return fmaxf(__builtin_bit_cast(float, (u32)a[0]), __builtin_bit_cast(float, (u32)a[1]));
}
DI int crow(int reg, int h) { return (reg & 3) + 8 * (reg >> 2) + 4 * h; }

#define STAMP(n) do {} while (0)

struct Ctx {
    LAS char* L; u32 Lb;
    char* ws; rsrc_t rs; u32x4 srd;
    int tid, lane, wave, i, j;
    u32 elo, ehi;
};

DI Ctx fresh(const Ctx& c0) {
    Ctx c = c0;
    asm volatile("" : "+v"(c.lane), "+v"(c.tid), "+s"(c.wave), "+s"(c.i), "+s"(c.j));
    return c;
}
DI GAS u64* flag_ptr(const Ctx& c, int phase, int id) { return (GAS u64*)(c.ws + WS_FLAG + (u32)(phase * 256 + id) * 128); }
DI void publish(const Ctx& c, int phase) {
    DRAIN();
    BAR();
    if (c.tid == 0) __hip_atomic_store(flag_ptr(c, phase, c.i * 8 + c.j), ((u64)c.ehi << 32) | c.elo, __ATOMIC_RELAXED, __HIP_MEMORY_SCOPE_AGENT);
}
DI void poll(const Ctx& c, int phase, int id) {
    const u64 want = ((u64)c.ehi << 32) | c.elo;
    GAS u64* f = flag_ptr(c, phase, id < 0 ? 0 : id);
    for (u32 spins = 0; spins < SPIN_CAP; ++spins) {
        const u64 v = __hip_atomic_load(f, __ATOMIC_RELAXED, __HIP_MEMORY_SCOPE_AGENT);
        if (__all(id < 0 || v == want)) break;
        __builtin_amdgcn_s_sleep(1);
    }
    __builtin_amdgcn_fence(__ATOMIC_ACQUIRE, "wavefront");
}
DI void wait_group(const Ctx& c, int phase) {
    if (c.wave == 0) poll(c, phase, c.lane < 8 ? c.i * 8 + c.lane : -1);
    BAR();
}

template <int MODE>
DI void load_wimage(const Ctx& c, const u16* w, const u16* wk, const u16* wv) {
    int id = c.wave * 64 + c.lane, row = id / 10, ch = id - row * 10;
    const char* xsrc = nullptr; int xoff = 0;
    if (MODE == 1) { const int jj = c.j; xsrc = (const char*)((jj >= 3 && jj < 6) ? wv : wk); const int sub = jj < 6 ? jj % 3 : 0; xoff = sub == 0 ? 0 : (sub == 1 ? 16 : 20); }
#pragma unroll
    for (int n = 0; n < 12; ++n) {
        if (n < 11 || c.wave < 2) {
            const char* src = (const char*)w + row * 1152 + c.j * 144 + (ch < 9 ? ch : 8) * 16;
            if (MODE == 1 && ch == 9) src = xsrc + row * 36 + xoff;
            glds_in(src, c.Lb + L_R1 + (c.wave + 8 * n) * 1024);
        }
        row += 51; ch += 2; if (ch >= 10) { ch -= 10; row += 1; }
    }
}

template <bool PROJ>
DI void g1_mma(const Ctx& c, const u32x4 (&xf)[9], f32x4 (&acc)[5]) {
    const int lane = c.lane, kh = c.wave >> 2, q = (lane & 15) >> 2, p = lane & 3, g = lane >> 4;
    const int rowN = PROJ ? 4 * g + q : 18 * (g >> 1) + 4 * (g & 1) + q;
    const int rowR = PROJ ? 72 * g + q : 72 * g + 18 * (q >> 1) + (q & 1);
    LAS char* bN = c.L + L_R1 + (kh * 288 + rowN) * WP + 8 * p;
    LAS char* bR = c.L + L_R1 + (kh * 288 + rowR) * WP + 8 * p;
    const int own = 64 * kh, oth = 64 - 64 * kh;
#pragma unroll
    for (int sl = 0; sl < 9; ++sl) {
        const int r0 = sl < 8 ? (PROJ ? 72 * (sl >> 1) + 32 * (sl & 1) : 36 * sl) : (PROJ ? 64 : 16);
        const int hs = sl < 8 ? (PROJ ? 16 : 8) : (PROJ ? 4 : 36);
        LAS char* b = sl < 8 ? bN : bR;
#pragma unroll
        for (int u = 0; u < 5; ++u) {
            LAS char* bb = u < 2 ? b + own + 32 * u : (u == 2 ? b + 128 : b + oth + 32 * (u - 3));
            const u32x2 lo = trr(bb + r0 * WP), hi = trr(bb + (r0 + hs) * WP);
            const u32x4 w = {lo[0], lo[1], hi[0], hi[1]};
            acc[u] = mfma16(w, xf[sl], acc[u]);
        }
    }
}
DI void g1_exchange(const Ctx& c, f32x4 (&acc)[5]) {
    LAS char* xw = c.L + L_XCH + c.wave * 3072 + c.lane * 16;
    lst(xw, acc[3]); lst(xw + 1024, acc[4]); lst(xw + 2048, acc[2]);
    BAR();
    LAS char* xr = c.L + L_XCH + (c.wave ^ 4) * 3072 + c.lane * 16;
    acc[0] += lld<f32x4>(xr); acc[1] += lld<f32x4>(xr + 1024); acc[2] += lld<f32x4>(xr + 2048);
}
DI void load_xfrags(const Ctx& c, u32 buf, u32x4 (&xf)[9]) {
    const int m = c.wave & 3, kh = c.wave >> 2;
    const u32 so = buf + (u32)(c.i * 18 * 4 + m) * 1024;
#pragma unroll
    for (int sl = 0; sl < 9; ++sl) { const int s = sl < 8 ? 8 * kh + sl : 16 + kh; xf[sl] = ld128(c.rs, c.lane * 16, so + (u32)s * 4096); }
}
DI void load_stats(const Ctx& c, u32 stbuf) {
    const int tok = c.wave * 8 + (c.lane & 7), pp = c.lane >> 3;
    const u32x4 v = ld128(c.rs, (u32)(c.i * 64 + tok) * 128 + pp * 16, stbuf);
    float s = asf(v[0]) + asf(v[2]), s2 = asf(v[1]) + asf(v[3]);
    s += __shfl_xor(s, 8); s2 += __shfl_xor(s2, 8);
    s = sum_g(s); s2 = sum_g(s2);
    if (c.lane < 8) {
        lst(c.L + L_STATP + tok * 8, f32x2{s, s2}); lst(c.L + L_STATP + 512 + tok * 8, f32x2{0.f, 0.f});
        const float mean = s * (1.f / DM), var = s2 * (1.f / DM) - mean * mean;
        lst(c.L + L_MEAN + tok * 4, mean); lst(c.L + L_MEAN + 256 + tok * 4, __builtin_amdgcn_rsqf(fmaxf(var, 0.f) + 1e-5f));
    }
}

struct KV { u32x4 km; u32 kr; u32x4 v0, v1; };
DI int vrow_of(int r) { return r < 18 ? r : ((r >= 20 && r < 28) ? 20 : 18); }
DI KV load_tile_ws(const Ctx& c, u32 tile) {
    const int r = c.lane & 31, h = c.lane >> 5;
    KV t;
    t.km = ld128(c.rs, c.lane * 16, tile);
    t.kr = ld32(c.rs, KT_KREM + r * 4, tile);
    t.v0 = ld128(c.rs, KT_V + h * KT_VBLK + vrow_of(r) * 16, tile);
    t.v1 = ld128(c.rs, KT_V + (2 + h) * KT_VBLK + vrow_of(r) * 16, tile);
    return t;
}
DI f32x16 qk(const KV& t, u32x4 qm, u32x4 qr, f32x16 c0) {
    const u32x4 krf = {t.kr, 0x00003f80u, 0u, 0u};
    f32x16 s = mfma32(t.km, qm, c0);
    return mfma32(krf, qr, s);
}
DI void pexp(const f32x16& s, u32x4& p0, u32x4& p1) {
    float e[16];
#pragma unroll
    for (int r = 0; r < 16; ++r) e[r] = __builtin_amdgcn_exp2f(s[r]);
    p0 = u32x4{pk(e[0], e[1]), pk(e[2], e[3]), pk(e[4], e[5]), pk(e[6], e[7])};
    p1 = u32x4{pk(e[8], e[9]), pk(e[10], e[11]), pk(e[12], e[13]), pk(e[14], e[15])};
}
DI float rowmax(const f32x16& s) {
    float a = fmaxf(fmaxf(s[0], s[1]), s[2]), b = fmaxf(fmaxf(s[3], s[4]), s[5]);
#pragma unroll
    for (int r = 6; r < 16; r += 2) { if (r & 2) a = fmaxf(fmaxf(a, s[r]), s[r + 1]); else b = fmaxf(fmaxf(b, s[r]), s[r + 1]); }
    return max_h(fmaxf(a, b));
}
DI f32x16 mask_c(int lane, bool diag) {
    const int r = lane & 31, h = lane >> 5; f32x16 m;
#pragma unroll
    for (int e = 0; e < 16; ++e) { const bool ok = diag ? (crow(e, h) <= r) : (crow(e, h) > r); m[e] = ok ? 0.f : NEG_BIG; }
    return m;
}
DI f32x16 attn_slow(const Ctx& c, u32 tile0, int n0, int n1, bool masked, u32x4 qm, u32x4 qr) {
    const int h = c.lane >> 5;
    qr[1] = 0u;
    f32x16 o = {}; float m = NEG_BIG;
    for (int n = n0; n <= n1; ++n) {
        const KV t = load_tile_ws(c, tile0 + (u32)n * TILE_B);
        f32x16 z = {};
        f32x16 s = qk(t, qm, qr, z);
        if (masked && (n == n1 || n == n1 - 8)) {
            const int r = c.lane & 31;
#pragma unroll
            for (int e = 0; e < 16; ++e) { const bool ok = (n == n1) ? (crow(e, h) <= r) : (crow(e, h) > r); s[e] = ok ? s[e] : NEG_BIG; }
        }
        const float mn = fmaxf(m, rowmax(s)), al = __builtin_amdgcn_exp2f(m - mn);
        m = mn;
#pragma unroll
        for (int e = 0; e < 16; ++e) { o[e] *= al; s[e] = s[e] <= NEG_BIG ? NEG_BIG : s[e] - mn; }
        u32x4 p0, p1; pexp(s, p0, p1);
        o = mfma32(t.v0, p0, o); o = mfma32(t.v1, p1, o);
    }
    return o;
}
DI bool attn_unsafe(const f32x16& o) {
    float z = 0.f;
#pragma unroll
    for (int e = 0; e < 10; ++e) z = __builtin_fmaf(o[e], 0.f, z);
    const float l = o[10];
    return __any(!(l > 0x1p-40f && l < 0x1p30f && z == 0.f));
}
DI void store_o(const Ctx& c, u32 obuf, const f32x16& o) {
    const int r = c.lane & 31, h = c.lane >> 5, a = c.wave >> 1, t = c.wave & 1;
    const float inv = __builtin_amdgcn_rcpf(o[10]);
    const u32x4 mainp = {pk(o[0] * inv, o[1] * inv), pk(o[2] * inv, o[3] * inv), pk(o[4] * inv, o[5] * inv), pk(o[6] * inv, o[7] * inv)};
    const int m = 2 * t + (r >> 4), tok = r & 15;
    const u32 so = obuf + (u32)(c.i * 18 * 4) * 1024;
    st128(c.rs, mainp, (u32)((2 * c.j + (a >> 1)) * 4 + m) * 1024 + ((2 * (a & 1) + h) * 16 + tok) * 16, so);
    if (h == 0) st32(c.rs, pk(o[8] * inv, o[9] * inv), (u32)((16 + (c.j >> 2)) * 4 + m) * 1024 + ((c.j & 3) * 16 + tok) * 16 + 4 * a, so);
}

DI void a_proj(const Ctx& c0, const Params& p, int rep) {
    const Ctx c = fresh(c0);
    const int lane = c.lane, m = c.wave & 3, kh = c.wave >> 2, tok = lane & 15, g = lane >> 4;
    u32x4 xf[9]; f32x4 acc[5] = {};
    if (rep == 0) {
        const char* xr = (const char*)p.x + (size_t)(c.i * 64 + m * 16 + tok) * (DM * 2);
#pragma unroll
        for (int sl = 0; sl < 8; ++sl) {
            const int c0 = 288 * kh + 72 * (sl >> 1) + 32 * (sl & 1) + 4 * g;
            const u32x2 lo = *(const u32x2*)(xr + c0 * 2), hi = *(const u32x2*)(xr + (c0 + 16) * 2);
            xf[sl] = u32x4{lo[0], lo[1], hi[0], hi[1]};
        }
        xf[8] = *(const u32x4*)(xr + (72 * (4 * kh + g) + 64) * 2);
        DRAIN(); BAR();
        const u32x4 ones = {0x3f803f80u, 0x3f803f80u, 0x3f803f80u, 0x3f803f80u};
        f32x4 s1 = {}, s2 = {};
#pragma unroll
        for (int sl = 0; sl < 9; ++sl) { s1 = mfma16(ones, xf[sl], s1); s2 = mfma16(xf[sl], xf[sl], s2); }
        const int rg = tok & 3;
        float d = rg == 0 ? s2[0] : (rg == 1 ? s2[1] : (rg == 2 ? s2[2] : s2[3]));
        d = sum_g(g == (tok >> 2) ? d : 0.f);
        if (g == 0) lst(c.L + L_STATP + kh * 512 + (m * 16 + tok) * 8, f32x2{s1[0], d});
        {
            const int q = (lane & 15) >> 2, pp = lane & 3;
            LAS char* bN = c.L + L_R1 + (kh * 288 + 4 * g + q) * WP + 8 * pp;
            LAS char* bR = c.L + L_R1 + (kh * 288 + 72 * g + q) * WP + 8 * pp;
            f32x4 ca = {}, cb = {};
#pragma unroll
            for (int sl = 0; sl < 9; ++sl) {
                const int r0 = sl < 8 ? 72 * (sl >> 1) + 32 * (sl & 1) : 64, hs = sl < 8 ? 16 : 4;
                LAS char* b = sl < 8 ? bN : bR;
                { const u32x2 lo = trr(b + 32 * m + r0 * WP), hi = trr(b + 32 * m + (r0 + hs) * WP); ca = mfma16(u32x4{lo[0], lo[1], hi[0], hi[1]}, ones, ca); }
                if (m == 0) { const u32x2 lo = trr(b + 128 + r0 * WP), hi = trr(b + 128 + (r0 + hs) * WP); cb = mfma16(u32x4{lo[0], lo[1], hi[0], hi[1]}, ones, cb); }
            }
            if (tok == 0) { lst(c.L + L_CSA + c.wave * 128 + g * 16, ca); if (m == 0) lst(c.L + L_CSA + c.wave * 128 + 64 + g * 16, cb); }
        }
    } else {
        wait_group(c, 5);
        load_xfrags(c, WS_XF + 1 * XF_BYTES, xf);
        load_stats(c, WS_ST + 1 * ST_BYTES);
        DRAIN(); BAR();
    }
    g1_mma<true>(c, xf, acc);
    g1_exchange(c, acc);
    const f32x2 sa = lld<f32x2>(c.L + L_STATP + (m * 16 + tok) * 8), sb = lld<f32x2>(c.L + L_STATP + 512 + (m * 16 + tok) * 8);
    const float mean = (sa[0] + sb[0]) * (1.f / DM), var = (sa[1] + sb[1]) * (1.f / DM) - mean * mean;
    const float rstd = __builtin_amdgcn_rsqf(fmaxf(var, 0.f) + 1e-5f);
    LAS char* stg = c.L + L_STG + (m * 16 + tok) * STG_P + g * 8;
#pragma unroll
    for (int u = 0; u < 3; ++u) {
        if (u < 2 || kh == 1) {
            const int t = u < 2 ? 2 * kh + u : 4;
            f32x4 cs;
            if (u < 2) cs = lld<f32x4>(c.L + L_CSA + t * 128 + g * 16) + lld<f32x4>(c.L + L_CSA + (4 + t) * 128 + g * 16);
            else cs = lld<f32x4>(c.L + L_CSA + 64 + g * 16) + lld<f32x4>(c.L + L_CSA + 4 * 128 + 64 + g * 16);
            const float sc = (u < 2 || g < 2) ? rstd * QSCALE : rstd;
            f32x4 v;
#pragma unroll
            for (int e = 0; e < 4; ++e) v[e] = (acc[u][e] - mean * cs[e]) * sc;
            lst(stg + t * 32, u32x2{pk(v[0], v[1]), pk(v[2], v[3])});
        }
    }
    BAR();
    if (c.wave == 0) {
        const u32 kva = WS_KVA + rep * KVA_BYTES + (u32)(2 * c.i) * TILE_B;
        const int j = c.j;
        if (j < 2) {
            LAS char* s = c.L + L_STG + lane * STG_P + 144;
            const u32x2 lo = lld<u32x2>(s), hi = lld<u32x2>(s + 8);
            st128(c.rs, u32x4{lo[0], lo[1], hi[0], hi[1]}, (lane >> 5) * TILE_B + j * 512 + (lane & 31) * 16, kva);
        } else if (j == 2) {
            st32(c.rs, lld<u32>(c.L + L_STG + lane * STG_P + 144 + 12), (lane >> 5) * TILE_B + KT_KREM + (lane & 31) * 4, kva);
        } else if (j < 6) {
            const int s4 = lane >> 4, q = (lane & 15) >> 2, pp = lane & 3;
            LAS char* s = c.L + L_STG + (16 * s4 + q + 4 * (pp >> 1)) * STG_P + 144 + 8 * (pp & 1);
            const u32x2 lo = trr(s), hi = trr(s + 8 * STG_P);
            const int dl = lane & 7, hh = (lane >> 3) & 1, drow = j < 5 ? 8 * (j - 3) + dl : 10 + dl;
            if (j < 5 || dl >= 6) st128(c.rs, u32x4{lo[0], lo[1], hi[0], hi[1]}, (s4 >> 1) * TILE_B + KT_V + ((s4 & 1) * 2 + hh) * KT_VBLK + drow * 16, kva);
        } else if (j == 6) {
            if (lane < 16) st128(c.rs, u32x4{0x3f803f80u, 0x3f803f80u, 0x3f803f80u, 0x3f803f80u}, (lane >> 3) * TILE_B + KT_V + ((lane >> 1) & 3) * KT_VBLK + (18 + 2 * (lane & 1)) * 16, kva);
        }
    }
    publish(c, rep * 6);
}

DI void a_attn(const Ctx& c0, const Params& p, int rep) {
    const Ctx c = fresh(c0);
    const int lane = c.lane, r = lane & 31, h = lane >> 5, a = c.wave >> 1, t = c.wave & 1;
    const int ph = rep * 6;
    const u32 kva = WS_KVA + rep * KVA_BYTES;
    u32x4 qm, qr;
    { LAS char* s = c.L + L_STG + (32 * t + r) * STG_P + 36 * a;
      qm = u32x4{lld<u32>(s + 16 * h), lld<u32>(s + 16 * h + 4), lld<u32>(s + 16 * h + 8), lld<u32>(s + 16 * h + 12)};
      const u32 rem = lld<u32>(s + 32);
      qr = u32x4{h == 0 ? rem : 0u, 0u, 0u, 0u}; }
    const int ch0 = c.i >> 2;
    auto chunk_ids = [&](int n) { const int ch = (ch0 + n) & 7; return ch * 32 + (lane & 31); };
    auto issue_chunk = [&](int n) {
        const int ch = (ch0 + n) & 7; const u32 so = kva + (u32)ch * CHUNK_B; const u32 dst = c.Lb + L_RING + (n % 3) * CHUNK_B;
        glds_ws(c.srd, lane * 16, so + c.wave * 1024, dst + c.wave * 1024);
        glds_ws(c.srd, lane * 16, so + (c.wave + 8) * 1024, dst + (c.wave + 8) * 1024);
        if (c.wave < 4) glds_ws(c.srd, lane * 16, so + (c.wave + 16) * 1024, dst + (c.wave + 16) * 1024);
    };
    if (c.wave == 0) poll(c, ph, lane < 32 ? chunk_ids(0) : chunk_ids(1));
    BAR();
    issue_chunk(0); issue_chunk(1);
    load_wimage<0>(c, p.woA, nullptr, nullptr);
    DRAIN(); BAR();
    const int vr = vrow_of(r);
    LAS char* kb = c.L + L_RING + lane * 16;
    LAS char* rb = c.L + L_RING + KT_KREM + r * 4;
    LAS char* vb = c.L + L_RING + KT_V + h * KT_VBLK + vr * 16;
    auto ldk = [&](int off, KV& k) { k.km = lld<u32x4>(kb + off); k.kr = lld<u32>(rb + off); };
    auto ldv = [&](int off, KV& k) { k.v0 = lld<u32x4>(vb + off); k.v1 = lld<u32x4>(vb + off + 2 * KT_VBLK); };
    const f32x16 zero = {};
    KV A, B;
    ldk(0, A);
    { const f32x16 s = qk(A, qm, qr, zero); const float m0 = rowmax(s); if (h == 0) qr[1] = pk(-m0, 0.f); }
    f32x16 sA = qk(A, qm, qr, zero), sB, o = {};
    ldv(0, A); ldk(TILE_B, B);
    int slot = 0;
    const u64 want = ((u64)c.ehi << 32) | c.elo;
    for (int n = 0; n < 8; ++n) {
        const int cur = slot * CHUNK_B; const int nslot = slot == 2 ? 0 : slot + 1, nxt = nslot * CHUNK_B;
        u64 fl = want;
        if (c.wave == 0 && n + 2 < 8) fl = __hip_atomic_load(flag_ptr(c, ph, chunk_ids(n + 2)), __ATOMIC_RELAXED, __HIP_MEMORY_SCOPE_AGENT);
#pragma unroll
        for (int tt = 0; tt < 8; tt += 2) {
            u32x4 p0, p1;
            KV An;
            if (tt == 6) {
                if (c.wave == 0 && !__all(fl == want)) poll(c, ph, chunk_ids(n + 2));
                DRAIN(); BAR();
                if (n + 2 < 8) issue_chunk(n + 2);
            }
            const bool more = tt < 6 || n < 7;
            if (more) ldk(tt < 6 ? cur + (tt + 2) * TILE_B : nxt, An);
            ldv(cur + (tt + 1) * TILE_B, B);
            sB = qk(B, qm, qr, zero);
            pexp(sA, p0, p1);
            o = mfma32(A.v0, p0, o); o = mfma32(A.v1, p1, o);
            KV Bn;
            if (more) { ldk(tt < 6 ? cur + (tt + 3 < 8 ? (tt + 3) * TILE_B : 0) : nxt + TILE_B, Bn); ldv(tt < 6 ? cur + (tt + 2) * TILE_B : nxt, An); sA = qk(An, qm, qr, zero); }
            pexp(sB, p0, p1);
            o = mfma32(B.v0, p0, o); o = mfma32(B.v1, p1, o);
            if (more) { A = An; B.km = Bn.km; B.kr = Bn.kr; }
        }
        slot = nslot;
    }
    if (attn_unsafe(o)) o = attn_slow(c, kva, 0, 63, false, qm, qr);
    store_o(c, WS_OF + (rep * 2) * XF_BYTES, o);
    publish(c, ph + 1);
}

template <bool ISB>
DI void out_proj(const Ctx& c0, const Params& p, int rep, u32 (&xres)[3][2], u32x4 (&w0)[9], u32x4 (&w1)[9]) {
    const Ctx c = fresh(c0);
    const int lane = c.lane, m = c.wave & 3, kh = c.wave >> 2, tok = lane & 15, g = lane >> 4;
    const int layer = rep * 2 + (ISB ? 1 : 0);
    wait_group(c, rep * 6 + (ISB ? 4 : 1));
    u32x4 xf[9]; f32x4 acc[5] = {};
    load_xfrags(c, WS_OF + layer * XF_BYTES, xf);
    if (!ISB) {
        const u32 so = WS_WF + (u32)((c.j * 8 + c.wave) * 2) * 18 * 1024;
#pragma unroll
        for (int s = 0; s < 9; ++s) { w0[s] = ld128(c.rs, lane * 16, so + s * 1024); w1[s] = ld128(c.rs, lane * 16, so + (18 + s) * 1024); }
    }
    g1_mma<false>(c, xf, acc);
    g1_exchange(c, acc);
    float s1 = 0.f, s2 = 0.f;
#pragma unroll
    for (int u = 0; u < 3; ++u) {
        const float v0 = bflo(xres[u][0]) + acc[u][0], v1 = bfhi(xres[u][0]) + acc[u][1], v2 = bflo(xres[u][1]) + acc[u][2], v3 = bfhi(xres[u][1]) + acc[u][3];
        xres[u][0] = pk(v0, v1); xres[u][1] = pk(v2, v3);
        if (u < 2 || (kh == 1 && g < 2)) {
            const float a0 = bflo(xres[u][0]), a1 = bfhi(xres[u][0]), a2 = bflo(xres[u][1]), a3 = bfhi(xres[u][1]);
            s1 += (a0 + a1) + (a2 + a3); s2 += (a0 * a0 + a1 * a1) + (a2 * a2 + a3 * a3);
        }
    }
    if (ISB && rep == 1) {
        char* o = (char*)p.out + (size_t)(c.i * 64 + m * 16 + tok) * (DM * 2) + (c.j * 72 + 4 * g) * 2;
        *(u32x2*)(o + (2 * kh) * 32) = u32x2{xres[0][0], xres[0][1]};
        *(u32x2*)(o + (2 * kh + 1) * 32) = u32x2{xres[1][0], xres[1][1]};
        if (kh == 1 && g < 2) *(u32x2*)(o + 128) = u32x2{xres[2][0], xres[2][1]};
        return;
    }
    const int xi = layer == 0 ? 0 : (layer == 1 ? 1 : 2);
    const u32 so = WS_XF + xi * XF_BYTES + (u32)(c.i * 18 * 4 + m) * 1024;
    st128(c.rs, u32x4{xres[0][0], xres[0][1], xres[1][0], xres[1][1]}, lane * 16, so + (u32)(2 * c.j + kh) * 4096);
    if (kh == 1 && g < 2) st64(c.rs, u32x2{xres[2][0], xres[2][1]}, ((c.j & 3) * 16 + tok) * 16 + 8 * g, so + (u32)(16 + (c.j >> 2)) * 4096);
    s1 = sum_g(s1); s2 = sum_g(s2);
    if (g == 0) st64(c.rs, u32x2{__builtin_bit_cast(u32, s1), __builtin_bit_cast(u32, s2)}, (u32)(c.i * 64 + m * 16 + tok) * 128 + (2 * c.j + kh) * 8, WS_ST + xi * ST_BYTES);
    publish(c, rep * 6 + (ISB ? 5 : 2));
    if (ISB) load_wimage<1>(c, p.wq, p.wk, p.wv);
}

struct T_ { static constexpr bool value = true; };
struct F_ { static constexpr bool value = false; };
DI void b_proj(const Ctx& c0, const Params& p, int rep, u32x4 (&w0)[9], u32x4 (&w1)[9], u32x4& qm, u32x4& qr) {
    const Ctx c = fresh(c0);
    const int lane = c.lane, tok = lane & 15, g = lane >> 4, a = c.wave >> 1, odd = c.wave & 1;
    wait_group(c, rep * 6 + 2);
    const int xi = rep == 0 ? 0 : 2;
    { const u32 so = WS_XF + xi * XF_BYTES + (u32)(c.i * 72) * 1024;
#pragma unroll
      for (int n = 0; n < 9; ++n) glds_ws(c.srd, lane * 16, so + (c.wave + 8 * n) * 1024, c.Lb + L_R1 + (c.wave + 8 * n) * 1024); }
    load_stats(c, WS_ST + xi * ST_BYTES);
    DRAIN(); BAR();
    f32x4 a0[4] = {}, a1[4] = {};
    const u32 wso = WS_WF + (u32)((c.j * 8 + c.wave) * 2) * 18 * 1024;
    LAS char* xb = c.L + L_R1 + lane * 16;
    const bool t0 = !odd, t1 = !odd || a == 0, has1 = !odd || a < 2;
    auto body = [&](auto T0, auto T1, auto H1) {
#pragma unroll
        for (int s = 0; s < 18; ++s) {
            const u32x4 wa = w0[s % 9], wb = w1[s % 9];
#pragma unroll
            for (int m = 0; m < 4; ++m) {
                const u32x4 x = lld<u32x4>(xb + (s * 4 + m) * 1024);
                a0[m] = T0.value ? mfma16(wa, x, a0[m]) : mfma16(x, wa, a0[m]);
                if (H1.value) a1[m] = T1.value ? mfma16(wb, x, a1[m]) : mfma16(x, wb, a1[m]);
            }
            if (s < 9) { w0[s] = ld128(c.rs, lane * 16, wso + (9 + s) * 1024); if (H1.value) w1[s] = ld128(c.rs, lane * 16, wso + (27 + s) * 1024); }
        }
    };
    if (!odd) body(T_{}, T_{}, T_{}); else if (a == 0) body(F_{}, T_{}, T_{}); else if (a == 1) body(F_{}, F_{}, T_{}); else body(F_{}, F_{}, F_{});
    (void)t0; (void)t1; (void)has1;
    const u32 kvb = WS_KVB + rep * KVB_BYTES;
    const u32 hb = kvb + (u32)((4 * c.j + a) * 64 + 2 * c.i) * TILE_B;
    auto fin_t = [&](const f32x4 (&acc)[4], int slot, float sc, u32 (&d)[4][2]) {
        const f32x4 cs = lld<f32x4>(c.L + L_CSB + (c.wave * 2 + slot) * 64 + g * 16);
#pragma unroll
        for (int m = 0; m < 4; ++m) {
            const float mean = lld<float>(c.L + L_MEAN + (m * 16 + tok) * 4), rs = lld<float>(c.L + L_MEAN + 256 + (m * 16 + tok) * 4) * sc;
            d[m][0] = pk((acc[m][0] - mean * cs[0]) * rs, (acc[m][1] - mean * cs[1]) * rs);
            d[m][1] = pk((acc[m][2] - mean * cs[2]) * rs, (acc[m][3] - mean * cs[3]) * rs);
        }
    };
    auto fin_p = [&](const f32x4 (&acc)[4], int slot, u32 (&d)[4][2]) {
        const float cs = lld<float>(c.L + L_CSB + (c.wave * 2 + slot) * 64 + tok * 4);
#pragma unroll
        for (int m = 0; m < 4; ++m) {
            const f32x4 mean = lld<f32x4>(c.L + L_MEAN + (m * 16 + 4 * g) * 4), rs = lld<f32x4>(c.L + L_MEAN + 256 + (m * 16 + 4 * g) * 4);
            d[m][0] = pk((acc[m][0] - mean[0] * cs) * rs[0], (acc[m][1] - mean[1] * cs) * rs[1]);
            d[m][1] = pk((acc[m][2] - mean[2] * cs) * rs[2], (acc[m][3] - mean[3] * cs) * rs[3]);
        }
    };
    auto frag16 = [&](const u32 (&d)[4][2], int t) {
        auto x = __builtin_amdgcn_permlane16_swap(d[2 * t][0], d[2 * t + 1][0], false, false);
        auto y = __builtin_amdgcn_permlane16_swap(d[2 * t][1], d[2 * t + 1][1], false, false);
        return u32x4{(u32)x[0], (u32)y[0], (u32)x[1], (u32)y[1]};
    };
    auto frag32 = [&](const u32 (&d)[4][2], int t) {
        auto x = __builtin_amdgcn_permlane32_swap(d[2 * t][0], d[2 * t + 1][0], false, false);
        auto y = __builtin_amdgcn_permlane32_swap(d[2 * t][1], d[2 * t + 1][1], false, false);
        return u32x4{(u32)x[0], (u32)y[0], (u32)x[1], (u32)y[1]};
    };
    u32 d[4][2];
    if (!odd) {
        fin_t(a1, 1, 1.f, d);
        st128(c.rs, frag16(d, 0), lane * 16, hb); st128(c.rs, frag16(d, 1), lane * 16, hb + TILE_B);
        fin_t(a0, 0, QSCALE, d);
        qm = frag16(d, 0);
        lst(c.L + L_QT1 + a * 1024 + lane * 16, frag16(d, 1));
    } else {
        fin_p(a0, 0, d);
        st128(c.rs, frag32(d, 0), KT_V + g * KT_VBLK + tok * 16, hb); st128(c.rs, frag32(d, 1), KT_V + g * KT_VBLK + tok * 16, hb + TILE_B);
        if (a == 0) {
            fin_t(a1, 1, g < 2 ? QSCALE : 1.f, d);
#pragma unroll
            for (int m = 0; m < 4; ++m) {
                if (g < 2) { lst(c.L + L_QREM + (2 * g) * 256 + (m * 16 + tok) * 4, d[m][0]); lst(c.L + L_QREM + (2 * g + 1) * 256 + (m * 16 + tok) * 4, d[m][1]); }
                else {
                    const u32 hb0 = kvb + (u32)(4 * c.j * 64 + 2 * c.i + (m >> 1)) * TILE_B;
                    st32(c.rs, d[m][0], (u32)(2 * (g - 2)) * 64 * TILE_B + KT_KREM + ((m & 1) * 16 + tok) * 4, hb0);
                    st32(c.rs, d[m][1], (u32)(2 * (g - 2) + 1) * 64 * TILE_B + KT_KREM + ((m & 1) * 16 + tok) * 4, hb0);
                }
            }
        } else if (a == 1) {
            fin_p(a1, 1, d);
            const u32 hb0 = kvb + (u32)(4 * c.j * 64 + 2 * c.i) * TILE_B;
            const u32 vo = (u32)(tok >> 1) * 64 * TILE_B + KT_V + g * KT_VBLK + (16 + (tok & 1)) * 16;
            const u32x4 f0 = frag32(d, 0), f1 = frag32(d, 1);
            if (tok < 8) { st128(c.rs, f0, vo, hb0); st128(c.rs, f1, vo, hb0 + TILE_B); }
        } else if (a == 2) {
            const u32 hb0 = kvb + (u32)(4 * c.j * 64 + 2 * c.i) * TILE_B;
            st128(c.rs, u32x4{0x3f803f80u, 0x3f803f80u, 0x3f803f80u, 0x3f803f80u},
                  (u32)(lane >> 4) * 64 * TILE_B + ((lane >> 3) & 1) * TILE_B + KT_V + ((lane >> 1) & 3) * KT_VBLK + (18 + 2 * (lane & 1)) * 16, hb0);
        }
    }
    publish(c, rep * 6 + 3);
    if (odd) qm = lld<u32x4>(c.L + L_QT1 + a * 1024 + lane * 16);
    const u32 rem = lld<u32>(c.L + L_QREM + a * 256 + (32 * odd + (lane & 31)) * 4);
    qr = u32x4{lane < 32 ? rem : 0u, 0u, 0u, 0u};
}

DI void b_attn(const Ctx& c0, const Params& p, int rep, u32x4 qm, u32x4 qr) {
    const Ctx c = fresh(c0);
    const int lane = c.lane, h = lane >> 5, a = c.wave >> 1, t = c.wave & 1;
    const int qt = 2 * c.i + t;
    const f32x16 cd = mask_c(lane, true), co = mask_c(lane, false), zero = {};
    if (c.wave == 0) poll(c, rep * 6 + 3, (lane < 5 && c.i - lane >= 0) ? (c.i - lane) * 8 + c.j : -1);
    BAR();
    const u32 hb = WS_KVB + rep * KVB_BYTES + (u32)((4 * c.j + a) * 64) * TILE_B;
    KV T[9];
#pragma unroll
    for (int u = 0; u < 9; ++u) { const int kt = qt - u < 0 ? 0 : qt - u; T[u] = load_tile_ws(c, hb + (u32)kt * TILE_B); }
    load_wimage<0>(c, p.woB, nullptr, nullptr);
    { const f32x16 s = qk(T[0], qm, qr, cd); const float m0 = rowmax(s); if (h == 0) qr[1] = pk(-m0, 0.f); }
    f32x16 o = {};
#pragma unroll
    for (int u = 0; u < 9; ++u) {
        if (u <= qt) {
            const f32x16 s = qk(T[u], qm, qr, u == 0 ? cd : (u == 8 ? co : zero));
            u32x4 p0, p1; pexp(s, p0, p1);
            o = mfma32(T[u].v0, p0, o); o = mfma32(T[u].v1, p1, o);
        }
    }
    if (attn_unsafe(o)) o = attn_slow(c, hb, qt - 8 < 0 ? 0 : qt - 8, qt, true, qm, qr);
    store_o(c, WS_OF + (rep * 2 + 1) * XF_BYTES, o);
    publish(c, rep * 6 + 4);
}

__global__ void __launch_bounds__(512, 2) fused_block_kernel(Params p) {
    __shared__ __attribute__((aligned(16))) char lds_[LDS_BYTES];
    Ctx c;
    c.L = (LAS char*)lds_; c.Lb = (u32)(size_t)c.L;
    c.ws = p.ws;
    c.tid = threadIdx.x; c.lane = c.tid & 63; c.wave = __builtin_amdgcn_readfirstlane(c.tid >> 6);
    { const int wg = blockIdx.x, slot = wg >> 3; c.i = (wg & 7) * 4 + (slot >> 3); c.j = slot & 7; }
    c.rs = __builtin_amdgcn_make_buffer_rsrc((void*)p.ws, 0, WS_USED, 0x00020000);
    { const u64 b = (u64)p.ws; c.srd = u32x4{(u32)b, (u32)(b >> 32) & 0xffffu, WS_USED, 0x00020000u}; }
    STAMP(0);
    const u64 e0 = __hip_atomic_load((GAS u64*)(p.ws + WS_EPOCH), __ATOMIC_RELAXED, __HIP_MEMORY_SCOPE_AGENT);
    load_wimage<1>(c, p.wq, p.wk, p.wv);
    const int lane = c.lane;
    { const int fr = blockIdx.x * 8 + c.wave;
      if (fr < 2016) {
        const int jj = fr / 252, rem = fr - jj * 252, u = rem / 18, s = rem - u * 18;
        const int wv = u < 8 ? u : (u - 8 < 5 ? u - 8 : 6), slot = u < 8 ? 0 : 1, n = lane & 15, g = lane >> 4, a = wv >> 1;
        int col;
        if (!(wv & 1)) col = (slot ? 576 : 0) + 18 * (4 * jj + a) + n;
        else if (slot == 0) col = 1152 + 18 * (4 * jj + a) + n;
        else if (a == 0) col = (n < 8 ? 0 : 576) + 18 * (4 * jj + ((n & 7) >> 1)) + 16 + (n & 1);
        else col = 1152 + 18 * (4 * jj + ((n & 7) >> 1)) + 16 + (n & 1);
        u32 w[8]; float cs = 0.f;
#pragma unroll
        for (int e = 0; e < 8; ++e) {
            const int k = s < 16 ? 72 * (s >> 1) + 32 * (s & 1) + 16 * (e >> 2) + 4 * g + (e & 3) : 72 * (4 * (s - 16) + g) + 64 + e;
            w[e] = p.wqkv[k * 1728 + col];
        }
#pragma unroll
        for (int e = 0; e < 8; ++e) cs += bflo(w[e]);
        const u32 fo = (u32)(((jj * 8 + wv) * 2 + slot) * 18 + s);
        st128(c.rs, u32x4{w[0] | (w[1] << 16), w[2] | (w[3] << 16), w[4] | (w[5] << 16), w[6] | (w[7] << 16)}, lane * 16, WS_WF + fo * 1024);
        cs = sum_g(cs);
        if (g == 0) st32(c.rs, __builtin_bit_cast(u32, cs), n * 4, WS_CSP + fo * 64);
      } }
    u32 xres[3][2];
    { const int m = c.wave & 3, kh = c.wave >> 2, tok = lane & 15, g = lane >> 4;
      const char* xr = (const char*)p.x + (size_t)(c.i * 64 + m * 16 + tok) * (DM * 2) + (c.j * 72 + 4 * g) * 2;
      const u32x2 r0 = *(const u32x2*)(xr + (2 * kh) * 32), r1 = *(const u32x2*)(xr + (2 * kh + 1) * 32);
      u32x2 r2 = {0u, 0u}; if (g < 2) r2 = *(const u32x2*)(xr + 128);
      xres[0][0] = r0[0]; xres[0][1] = r0[1]; xres[1][0] = r1[0]; xres[1][1] = r1[1]; xres[2][0] = r2[0]; xres[2][1] = r2[1]; }
    { const u64 e = e0 + 1; c.elo = __builtin_amdgcn_readfirstlane((u32)e); c.ehi = __builtin_amdgcn_readfirstlane((u32)(e >> 32)); }
    u32x4 w0[9], w1[9];
#pragma unroll 1
    for (int rep = 0; rep < 2; ++rep) {
        STAMP(1 + rep * 8);
        a_proj(c, p, rep);
        STAMP(2 + rep * 8);
        a_attn(c, p, rep);
        STAMP(3 + rep * 8);
        if (rep == 0) {
            if (blockIdx.x == 0 && c.tid == 0) __hip_atomic_store((GAS u64*)(p.ws + WS_EPOCH), ((u64)c.ehi << 32) | c.elo, __ATOMIC_RELAXED, __HIP_MEMORY_SCOPE_AGENT);
#pragma unroll
            for (int slot = 0; slot < 2; ++slot) {
                float cs = 0.f; const int n = lane & 15, g = lane >> 4;
                const u32 so = WS_CSP + (u32)(((c.j * 8 + c.wave) * 2 + slot) * 18) * 64;
#pragma unroll
                for (int u = 0; u < 5; ++u) { const int s = g + 4 * u; if (s < 18) cs += __builtin_bit_cast(float, ld32(c.rs, s * 64 + n * 4, so)); }
                cs = sum_g(cs);
                if (g == 0) lst(c.L + L_CSB + (c.wave * 2 + slot) * 64 + n * 4, cs);
            }
        }
        out_proj<false>(c, p, rep, xres, w0, w1);
        STAMP(4 + rep * 8);
        u32x4 qm, qr;
        b_proj(c, p, rep, w0, w1, qm, qr);
        STAMP(5 + rep * 8);
        b_attn(c, p, rep, qm, qr);
        STAMP(6 + rep * 8);
        out_proj<true>(c, p, rep, xres, w0, w1);
        STAMP(7 + rep * 8);
    }
}

extern "C" void kernel_launch(void* const* d_in, const int* in_sizes, int n_in, void* d_out, int out_size, void* d_ws, size_t ws_size, hipStream_t stream) {
    Params p;
    p.x = (const u16*)d_in[0]; p.wq = (const u16*)d_in[1]; p.wk = (const u16*)d_in[2]; p.wv = (const u16*)d_in[3]; p.woA = (const u16*)d_in[4];
    p.wqkv = (const u16*)d_in[6]; p.woB = (const u16*)d_in[7]; p.out = (u16*)d_out; p.ws = (char*)d_ws;
    fused_block_kernel<<<256, 512, 0, stream>>>(p);
}
```

```cpp
#include <hip/hip_runtime.h>
#include <hip/hip_bf16.h>

typedef unsigned short u16;
typedef unsigned u32;
typedef unsigned long long u64;
typedef __bf16 bf16x8 __attribute__((ext_vector_type(8)));
typedef __bf16 bf16x2 __attribute__((ext_vector_type(2)));
typedef short s16x4 __attribute__((ext_vector_type(4)));
typedef float f32x2 __attribute__((ext_vector_type(2)));
typedef float f32x4 __attribute__((ext_vector_type(4)));
typedef float f32x16 __attribute__((ext_vector_type(16)));
typedef u32 u32x2 __attribute__((ext_vector_type(2)));
typedef u32 u32x4 __attribute__((ext_vector_type(4)));
#define LAS __attribute__((address_space(3)))
#define GAS __attribute__((address_space(1)))
#define DI __device__ __forceinline__

constexpr int SEQ = 2048, DM = 576;
constexpr u32 TILE_B = 2560;
constexpr u32 KT_V = 1024, KT_VBLK = 304, KT_KREM = 2240;
constexpr u32 XF_BYTES = 32u * 18 * 4 * 1024;
constexpr u32 WS_EPOCH = 0;
constexpr u32 WS_FLAG = 4096;
constexpr int PH_FMT = 11;
constexpr u32 WS_XF = WS_FLAG + 12u * 256 * 128 + 4096;
constexpr u32 WS_OF = WS_XF + 3 * XF_BYTES;
constexpr u32 ST_BYTES = 2048u * 16 * 8;
constexpr u32 WS_ST = WS_OF + 4 * XF_BYTES;
constexpr u32 KVA_BYTES = 64 * TILE_B;
constexpr u32 WS_KVA = WS_ST + 3 * ST_BYTES;
constexpr u32 KVB_BYTES = 32 * 64 * TILE_B;
constexpr u32 WS_KVB = WS_KVA + 2 * KVA_BYTES;
constexpr u32 WF_BYTES = 8u * 16 * 18 * 1024;
constexpr u32 WS_WF = WS_KVB + 2 * KVB_BYTES;
constexpr u32 WS_CSP = WS_WF + WF_BYTES;
constexpr u32 WIMG_BYTES = 90 * 1024;
constexpr u32 WS_WIMG = WS_CSP + 8u * 16 * 18 * 64;
constexpr u32 WS_WIMGA = WS_WIMG + 16 * WIMG_BYTES;
constexpr u32 WS_STAMP = WS_WIMGA + 8 * WIMG_BYTES;
constexpr u32 WS_USED = WS_STAMP + 256u * 64 * 8 + 256u * 8 * 8 * 8;
constexpr int L_XCH = 0;
constexpr int L_RING = 0;
constexpr int CHUNK_B = 8 * TILE_B;
constexpr int L_STG = 40960;
constexpr int STG_P = 168;
constexpr int L_XIN = 0;
constexpr int L_XIN2 = 153600;
constexpr int L_FMT = 24576;
constexpr int L_KVB = 0;
constexpr int L_MSK = 102400;
constexpr int L_R1 = 61440;
constexpr int WP = 160;
constexpr int L_STATP = 153600;
constexpr int L_MEAN = L_STATP + 1024;
constexpr int L_CSA = L_MEAN + 512;
constexpr int L_CSB = L_CSA + 1024;
constexpr int L_QREM = L_CSB + 1024;
constexpr int L_QT1 = L_QREM + 1024;
constexpr int LDS_BYTES = 163840;
static_assert(L_QT1 + 4096 <= LDS_BYTES, "lds");

constexpr float QSCALE = 0.23570226039551584f * 1.4426950408889634f;
constexpr float NEG_BIG = -1e30f;
constexpr u32 SPIN_CAP = 1u << 20;
constexpr int AUX_SC1 = 16;

struct Params { const u16* x; const u16* wq; const u16* wk; const u16* wv; const u16* woA; const u16* wqkv; const u16* woB; u16* out; char* ws; };

DI u32 pk(float a, float b) { f32x2 v = {a, b}; return __builtin_bit_cast(u32, __builtin_convertvector(v, bf16x2)); }
DI float asf(u32 d) { return __builtin_bit_cast(float, d); }
DI u32 asu(float f) { return __builtin_bit_cast(u32, f); }
DI float bflo(u32 d) { return __builtin_bit_cast(float, d << 16); }
DI float bfhi(u32 d) { return __builtin_bit_cast(float, d & 0xffff0000u); }
DI bf16x8 asb(u32x4 v) { return __builtin_bit_cast(bf16x8, v); }
DI f32x4 mfma16(u32x4 a, u32x4 b, f32x4 c) { return __builtin_amdgcn_mfma_f32_16x16x32_bf16(asb(a), asb(b), c, 0, 0, 0); }
DI f32x16 mfma32(u32x4 a, u32x4 b, f32x16 c) { return __builtin_amdgcn_mfma_f32_32x32x16_bf16(asb(a), asb(b), c, 0, 0, 0); }
DI void MF16(f32x4& acc, u32x4 a, u32x4 b) { asm volatile("v_mfma_f32_16x16x32_bf16 %0, %1, %2, %0" : "+v"(acc) : "v"(a), "v"(b)); }
#define SETTLE4(x) asm volatile("s_nop 7" : "+v"(x))
template <class T> DI T lld(LAS char* p) { return *(LAS const T*)p; }
template <class T> DI void lst(LAS char* p, T v) { *(LAS T*)p = v; }
DI u32x2 trr(LAS char* p) { return __builtin_bit_cast(u32x2, __builtin_amdgcn_ds_read_tr16_b64_v4i16((LAS s16x4*)p)); }
#define BAR() asm volatile("s_waitcnt lgkmcnt(0)\n\ts_barrier" ::: "memory")
#define DRAIN() asm volatile("s_waitcnt vmcnt(0)" ::: "memory")
typedef __amdgpu_buffer_rsrc_t rsrc_t;
DI u32x4 ld128(rsrc_t rs, u32 voff, u32 soff) { return __builtin_amdgcn_raw_buffer_load_b128(rs, voff, soff, AUX_SC1); }
DI u32x2 ld64(rsrc_t rs, u32 voff, u32 soff) { return __builtin_amdgcn_raw_buffer_load_b64(rs, voff, soff, AUX_SC1); }
DI u32 ld32(rsrc_t rs, u32 voff, u32 soff) { return __builtin_amdgcn_raw_buffer_load_b32(rs, voff, soff, AUX_SC1); }
DI void st128(rsrc_t rs, u32x4 v, u32 voff, u32 soff) { __builtin_amdgcn_raw_buffer_store_b128(v, rs, voff + soff, 0, AUX_SC1); }
DI void st64(rsrc_t rs, u32x2 v, u32 voff, u32 soff) { __builtin_amdgcn_raw_buffer_store_b64(v, rs, voff, soff, AUX_SC1); }
DI void st32(rsrc_t rs, u32 v, u32 voff, u32 soff) { __builtin_amdgcn_raw_buffer_store_b32(v, rs, voff, soff, AUX_SC1); }
DI void glds_ws(u32x4 srd, u32 voff, u32 soff, u32 ldsdst) {
    soff = __builtin_amdgcn_readfirstlane(soff); ldsdst = __builtin_amdgcn_readfirstlane(ldsdst);
    asm volatile("s_mov_b32 m0, %0\n\ts_nop 3\n\tbuffer_load_dwordx4 %1, %2, %3 offen sc1 lds" :: "s"(ldsdst), "v"(voff), "s"(srd), "s"(soff) : "memory");
}
DI void glds_in(const void* g, u32 ldsdst) {
    ldsdst = __builtin_amdgcn_readfirstlane(ldsdst);
    asm volatile("s_mov_b32 m0, %0\n\ts_nop 0\n\tglobal_load_lds_dwordx4 %1, off" :: "s"(ldsdst), "v"(g) : "memory");
}
DI void flag_load(u64& fl, u64 nomatch, u32 voff, u32x4 srd, u32 soff) {
    soff = __builtin_amdgcn_readfirstlane(soff);
    fl = nomatch;
    asm volatile("s_nop 4\n\tbuffer_load_dwordx2 %0, %1, %2, %3 offen sc1" : "+v"(fl) : "v"(voff), "s"(srd), "s"(soff) : "memory");
}
DI float sum_g(float v) {
    auto a = __builtin_amdgcn_permlane32_swap(__builtin_bit_cast(u32, v), __builtin_bit_cast(u32, v), false, false);
    v = __builtin_bit_cast(float, (u32)a[0]) + __builtin_bit_cast(float, (u32)a[1]);
    auto b = __builtin_amdgcn_permlane16_swap(__builtin_bit_cast(u32, v), __builtin_bit_cast(u32, v), false, false);
    return __builtin_bit_cast(float, (u32)b[0]) + __builtin_bit_cast(float, (u32)b[1]);
}
DI float max_h(float v) {
    auto a = __builtin_amdgcn_permlane32_swap(__builtin_bit_cast(u32, v), __builtin_bit_cast(u32, v), false, false);
    return fmaxf(__builtin_bit_cast(float, (u32)a[0]), __builtin_bit_cast(float, (u32)a[1]));
}
DI u32x4 ones4() { u32 o = 0x3f803f80u; asm volatile("" : "+v"(o)); return u32x4{o, o, o, o}; }
DI int crow(int reg, int h) { return (reg & 3) + 8 * (reg >> 2) + 4 * h; }

#define STAMP(n) do {} while (0)
#define CSTAMP(n) do {} while (0)

struct Ctx {
    LAS char* L; u32 Lb;
    char* ws; rsrc_t rs; u32x4 srd;
    int tid, lane, wave, i, j;
    u32 elo, ehi;
};

DI Ctx fresh(const Ctx& c0) {
    Ctx c = c0;
    asm volatile("" : "+v"(c.lane)); asm volatile("" : "+v"(c.tid));
    asm volatile("" : "+s"(c.wave)); asm volatile("" : "+s"(c.i)); asm volatile("" : "+s"(c.j));
    return c;
}
DI GAS u64* flag_ptr(const Ctx& c, int phase, int id) { return (GAS u64*)(c.ws + WS_FLAG + (u32)(phase * 256 + id) * 128); }
DI void publish(const Ctx& c, int phase) {
    DRAIN();
    BAR();
    if (c.tid == 0) __hip_atomic_store(flag_ptr(c, phase, c.i * 8 + c.j), ((u64)c.ehi << 32) | c.elo, __ATOMIC_RELAXED, __HIP_MEMORY_SCOPE_AGENT);
}
DI void poll(const Ctx& c, int phase, int id) {
    const u64 want = ((u64)c.ehi << 32) | c.elo;
    GAS u64* f = flag_ptr(c, phase, id < 0 ? 0 : id);
    for (u32 spins = 0; spins < SPIN_CAP; ++spins) {
        const u64 v = __hip_atomic_load(f, __ATOMIC_RELAXED, __HIP_MEMORY_SCOPE_AGENT);
        if (__all(id < 0 || v == want)) break;
        __builtin_amdgcn_s_sleep(1);
    }
    __builtin_amdgcn_fence(__ATOMIC_ACQUIRE, "wavefront");
}
DI void wait_group(const Ctx& c, int phase) {
    if (c.wave == 0) poll(c, phase, c.lane < 8 ? c.i * 8 + c.lane : -1);
    BAR();
}

DI void load_wimage_a(const Ctx& c, const Params& p, int w0) {
    if (c.wave < w0) return;
    const int jj = c.j, nw = 8 - w0, g0 = c.wave - w0;
    const u16 *pk_ = p.wk, *pv_ = p.wv;
    asm volatile("" : "+s"(pk_), "+s"(pv_));
    const char* xsrc = (const char*)((jj >= 3 && jj < 6) ? pv_ : pk_); const int sub = jj < 6 ? jj % 3 : 0;
    xsrc += sub == 0 ? 0 : (sub == 1 ? 16 : 20);
    const char* msrc = (const char*)p.wq + jj * 144;
    const char* a[5]; u32 st[5];
#pragma unroll
    for (int k = 0; k < 5; ++k) {
        const u32 id = 64 * k + c.lane, row = (id * 6554u) >> 16, ch = id - row * 10;
        const bool ex = ch == 9;
        const u32 s1 = ex ? 32 * 36 : 32 * 1152;
        st[k] = s1 * nw;
        a[k] = (ex ? xsrc : msrc) + (ex ? row * 36 : row * 1152 + ch * 16) + (u32)g0 * s1;
    }
#pragma unroll
    for (int n = 0; n < 3; ++n) {
        const int G = g0 + nw * n;
        if (G < 18) {
#pragma unroll
            for (int k = 0; k < 5; ++k) { glds_in(a[k], c.Lb + L_R1 + (G * 5 + k) * 1024); a[k] += st[k]; }
        }
    }
}
DI void load_wimage_o(const Ctx& c, u32 base) {
    const u32 so = base + (u32)c.j * WIMG_BYTES;
    if (c.wave >= 1) {
#pragma unroll
        for (int n = 0; n < 13; ++n)
            if (n < 12 || c.wave < 7) glds_ws(c.srd, c.lane * 16, so + (c.wave - 1 + 7 * n) * 1024, c.Lb + L_R1 + (c.wave - 1 + 7 * n) * 1024);
    }
}
template <int P0, int P1>
DI void load_wimage_part(const Ctx& c, u32 base, int w0) {
    const u32 so = base + (u32)c.j * WIMG_BYTES; const int nw = 8 - w0;
    if (c.wave >= w0) {
#pragma unroll
        for (int n = 0; n < (P1 - P0 + 6) / 7; ++n) {
            const int pc = P0 + (c.wave - w0) + nw * n;
            if (pc < P1) glds_ws(c.srd, c.lane * 16, so + pc * 1024, c.Lb + L_R1 + pc * 1024);
        }
    }
}
DI void load_wimage_o4(const Ctx& c, int which, int part) {
    const u32 so = WS_WIMG + (u32)(which * 8 + c.j) * WIMG_BYTES + (u32)part * 30 * 1024; const u32 dst = c.Lb + L_R1 + part * 30 * 1024;
    if (c.wave < 4) {
#pragma unroll
        for (int n = 0; n < 8; ++n)
            if (n < 7 || c.wave < 2) glds_ws(c.srd, c.lane * 16, so + (c.wave + 4 * n) * 1024, dst + (c.wave + 4 * n) * 1024);
    }
}
struct Fmt { u32x4 v[2]; u32 g[8]; u32 dst[2], cdst[2]; int kind[2]; };
DI void fmt_issue(const Ctx& c, const Params& p, Fmt& m) {
    const u16 *pw = p.wqkv, *pa = p.woA, *pb = p.woB;
    asm volatile("" : "+s"(pw), "+s"(pa), "+s"(pb));
    const int lane = c.lane, rho = lane >> 1, g = rho >> 3, e = rho & 7;
#pragma unroll
    for (int r = 0; r < 2; ++r) {
        const int fr = blockIdx.x * 8 + c.wave + 2048 * r;
        int col, A, P, Q, pitch; const GAS char* src;
        if (fr < 2016) {
            const int jj = fr / 252, rem = fr - jj * 252, u = rem / 18, s = rem - u * 18;
            const int wv = u < 8 ? u : (u - 8 < 5 ? u - 8 : 6), slot = u < 8 ? 0 : 1, a = wv >> 1;
            const bool isrem = (wv & 1) && slot == 1;
            const u32 fo = (u32)(((jj * 8 + wv) * 2 + slot) * 18 + s);
            m.dst[r] = WS_WF + fo * 1024; m.cdst[r] = WS_CSP + fo * 64; m.kind[r] = isrem ? 3 : 1;
            P = s < 16 ? 16 : 4; Q = 2; pitch = 1728 * 2; src = (const GAS char*)pw;
            if (!isrem) {
                col = ((wv & 1) ? 1152 : (slot ? 576 : 0)) + 18 * (4 * jj + a) + 8 * (lane & 1);
                A = s < 16 ? 72 * (s >> 1) + 32 * (s & 1) + 4 * g : 72 * (4 * (s - 16) + g) + 64;
            } else {
                const int n = lane & 15, g2 = lane >> 4;
                col = (a == 0 ? (n < 8 ? 0 : 576) : 1152) + 18 * (4 * jj + ((n & 7) >> 1)) + 16 + (n & 1);
                A = s < 16 ? 72 * (s >> 1) + 32 * (s & 1) + 4 * g2 : 72 * (4 * (s - 16) + g2) + 64;
#pragma unroll
                for (int q = 0; q < 8; ++q) m.g[q] = *(const GAS u16*)(src + (A + P * (q >> 2) + (q & 3)) * pitch + col * 2);
            }
        } else {
            const bool on = fr < 3456;
            const int q = on ? fr - 2016 : 0, im = q / 720, rem = q - im * 720, jj = rem / 90, st = rem - jj * 90, s = st / 5, t = st - s * 5;
            m.dst[r] = WS_WIMG + (u32)(im * 8 + jj) * WIMG_BYTES + (u32)st * 1024; m.cdst[r] = 0; m.kind[r] = on ? 2 : 0;
            col = 72 * jj + 16 * t + (t == 4 ? 0 : 8 * (lane & 1));
            if (s < 16) { A = 36 * s + 18 * (g >> 1) + 4 * (g & 1); P = 8; Q = 2; } else { A = 72 * (4 * (s - 16) + g) + 16; P = 36; Q = 18; }
            src = (const GAS char*)(im ? pb : pa); pitch = 576 * 2;
        }
        if (m.kind[r] == 1 || m.kind[r] == 2) m.v[r] = *(const GAS u32x4*)(src + (A + P * (e >> 2) + Q * ((e >> 1) & 1) + (e & 1)) * pitch + col * 2);
    }
}
DI void fmt_finish(const Ctx& c, Fmt& m) {
    asm volatile("" : "+v"(m.v[0]), "+v"(m.v[1]), "+v"(m.g[0]), "+v"(m.g[1]), "+v"(m.g[2]), "+v"(m.g[3]), "+v"(m.g[4]), "+v"(m.g[5]), "+v"(m.g[6]), "+v"(m.g[7]));
    const int lane = c.lane, i16 = lane & 15, g = lane >> 4, q = i16 >> 2, p = i16 & 3;
#pragma unroll
    for (int r = 0; r < 2; ++r) {
        if (m.kind[r] != 0) {
            u32x4 f;
            if (m.kind[r] == 3) { const u32* w = m.g; f = u32x4{w[0] | (w[1] << 16), w[2] | (w[3] << 16), w[4] | (w[5] << 16), w[6] | (w[7] << 16)}; }
            else {
                LAS char* t = c.L + L_FMT + c.wave * 2048 + r * 1024;
                lst(t + lane * 16, m.v[r]);
                asm volatile("s_waitcnt lgkmcnt(0)" ::: "memory");
                const u32x2 lo = trr(t + (8 * g + q) * 32 + 8 * p), hi = trr(t + (8 * g + 4 + q) * 32 + 8 * p);
                f = u32x4{lo[0], lo[1], hi[0], hi[1]};
            }
            st128(c.rs, f, lane * 16, m.dst[r]);
            if (m.kind[r] != 2) {
                float cs = ((bflo(f[0]) + bfhi(f[0])) + (bflo(f[1]) + bfhi(f[1]))) + ((bflo(f[2]) + bfhi(f[2])) + (bflo(f[3]) + bfhi(f[3])));
                cs = sum_g(cs);
                if (lane < 16) st32(c.rs, asu(cs), lane * 4, m.cdst[r]);
            }
        }
    }
}

template <bool PROJ, bool NAT = false, class HK>
DI void g1_mma(const Ctx& c, const u32x4 (&xf)[9], f32x4 (&acc)[5], HK hook) {
    const int lane = c.lane, kh = c.wave >> 2, q = (lane & 15) >> 2, p = lane & 3, g = lane >> 4;
    LAS char *bN, *bR, *bNo, *bNt, *bRo, *bRt;
    if (PROJ) {
        if (NAT) { bN = c.L + L_R1 + (kh * 256 + 4 * g + q) * WP + 8 * p; bR = c.L + L_R1 + (512 + 32 * kh + 8 * g + q) * WP + 8 * p; }
        else { bN = c.L + L_R1 + (kh * 288 + 4 * g + q) * WP + 8 * p; bR = c.L + L_R1 + (kh * 288 + 72 * g + q) * WP + 8 * p; }
        bNo = bN + 64 * kh; bNt = bN + 64 - 64 * kh; bRo = bR + 64 * kh; bRt = bR + 64 - 64 * kh;
    } else {
        bN = c.L + L_R1 + kh * 8 * 5120 + lane * 16; bR = c.L + L_R1 + (16 + kh) * 5120 + lane * 16;
        bNo = bN + 2048 * kh; bNt = bN + 2048 - 2048 * kh; bRo = bR + 2048 * kh; bRt = bR + 2048 - 2048 * kh;
    }
    u32x4 w[2][5];
    auto rd = [&](int sl, u32x4 (&d)[5]) {
#pragma unroll
        for (int u = 0; u < 5; ++u) {
            if (PROJ) {
                const int r0 = NAT ? (sl < 8 ? 32 * sl : 0) : (sl < 8 ? 72 * (sl >> 1) + 32 * (sl & 1) : 64), hs = sl < 8 ? 16 : 4;
                LAS char* bb = u < 2 ? (sl < 8 ? bNo : bRo) + 32 * u : (u == 2 ? (sl < 8 ? bN : bR) + 128 : (sl < 8 ? bNt : bRt) + 32 * (u - 3));
                const u32x2 lo = trr(bb + r0 * WP), hi = trr(bb + (r0 + hs) * WP);
                d[u] = u32x4{lo[0], lo[1], hi[0], hi[1]};
            } else {
                LAS char* bb = u < 2 ? (sl < 8 ? bNo : bRo) + 1024 * u : (u == 2 ? (sl < 8 ? bN : bR) + 4096 : (sl < 8 ? bNt : bRt) + 1024 * (u - 3));
                d[u] = lld<u32x4>(bb + (sl < 8 ? sl * 5120 : 0));
            }
        }
    };
    rd(0, w[0]);
#pragma unroll
    for (int u = 0; u < 5; ++u) asm volatile("" : "+v"(acc[u]));
#pragma unroll
    for (int sl = 0; sl < 9; ++sl) {
        if (sl + 1 < 9) rd(sl + 1, w[(sl + 1) & 1]);
#pragma unroll
        for (int u = 0; u < 5; ++u) MF16(acc[u], w[sl & 1][u], xf[sl]);
        hook(sl, w[sl & 1]);
    }
#pragma unroll
    for (int u = 0; u < 5; ++u) SETTLE4(acc[u]);
}
DI void g1_exchange(const Ctx& c, f32x4 (&acc)[5]) {
    LAS char* xw = c.L + L_XCH + c.wave * 3072 + c.lane * 16;
    lst(xw, acc[3]); lst(xw + 1024, acc[4]); lst(xw + 2048, acc[2]);
    BAR();
    LAS char* xr = c.L + L_XCH + (c.wave ^ 4) * 3072 + c.lane * 16;
    acc[0] += lld<f32x4>(xr); acc[1] += lld<f32x4>(xr + 1024); acc[2] += lld<f32x4>(xr + 2048);
}
DI void load_xfrags(const Ctx& c, u32 buf, u32x4 (&xf)[9]) {
    const int m = c.wave & 3, kh = c.wave >> 2;
    const u32 so = buf + (u32)(c.i * 18 * 4 + m) * 1024;
#pragma unroll
    for (int sl = 0; sl < 9; ++sl) { const int s = sl < 8 ? 8 * kh + sl : 16 + kh; xf[sl] = ld128(c.rs, c.lane * 16, so + (u32)s * 4096); }
}
DI u32x4 stats_issue(const Ctx& c, u32 stbuf) { return ld128(c.rs, (u32)(c.i * 64 + c.wave * 8 + (c.lane & 7)) * 128 + (c.lane >> 3) * 16, stbuf); }
DI void stats_finish(const Ctx& c, u32x4 v) {
    const int tok = c.wave * 8 + (c.lane & 7);
    float s = asf(v[0]) + asf(v[2]), s2 = asf(v[1]) + asf(v[3]);
    s += __shfl_xor(s, 8); s2 += __shfl_xor(s2, 8);
    s = sum_g(s); s2 = sum_g(s2);
    if (c.lane < 8) {
        lst(c.L + L_STATP + tok * 8, f32x2{s, s2}); lst(c.L + L_STATP + 512 + tok * 8, f32x2{0.f, 0.f});
        const float mean = s * (1.f / DM), var = s2 * (1.f / DM) - mean * mean;
        lst(c.L + L_MEAN + tok * 4, mean); lst(c.L + L_MEAN + 256 + tok * 4, __builtin_amdgcn_rsqf(fmaxf(var, 0.f) + 1e-5f));
    }
}
DI void load_stats(const Ctx& c, u32 stbuf) { stats_finish(c, stats_issue(c, stbuf)); }

struct KV { u32x4 km; u32x2 kr; u32x4 v0, v1; };
DI int vrow_of(int r) { return r < 18 ? r : 18; }
DI KV load_tile_ws(const Ctx& c, u32 tile) {
    const int r = c.lane & 31, h = c.lane >> 5;
    KV t;
    t.km = ld128(c.rs, c.lane * 16, tile);
    t.kr = ld64(c.rs, KT_KREM + r * 8, tile);
    t.v0 = ld128(c.rs, KT_V + h * KT_VBLK + vrow_of(r) * 16, tile);
    t.v1 = ld128(c.rs, KT_V + (2 + h) * KT_VBLK + vrow_of(r) * 16, tile);
    return t;
}
DI f32x16 mfma32k8(u32x2 a, u32x2 b, f32x16 c) { return __builtin_amdgcn_mfma_f32_32x32x8bf16_1k(__builtin_bit_cast(s16x4, a), __builtin_bit_cast(s16x4, b), c, 0, 0, 0); }
DI f32x16 qk(const KV& t, u32x4 qm, u32x2 qr, f32x16 c0) {
    return mfma32k8(t.kr, qr, mfma32(t.km, qm, c0));
}
DI void QK1(f32x16& s, u32x4 km, u32x4 qm) { asm volatile("v_mfma_f32_32x32x16_bf16 %0, %1, %2, 0" : "=&v"(s) : "v"(km), "v"(qm)); }
DI void QK1C(f32x16& s, u32x4 km, u32x4 qm, const f32x16& c0) { asm volatile("v_mfma_f32_32x32x16_bf16 %0, %1, %2, %3" : "=&v"(s) : "v"(km), "v"(qm), "v"(c0)); }
DI void QK2(f32x16& s, u32x2 kr, u32x2 qr) { asm volatile("v_mfma_f32_32x32x8_bf16 %0, %1, %2, %0" : "+v"(s) : "v"(kr), "v"(qr)); }
DI void PV(f32x16& o, u32x4 v, u32x4 p) { asm volatile("v_mfma_f32_32x32x16_bf16 %0, %1, %2, %0" : "+v"(o) : "v"(v), "v"(p)); }
DI float EX(float x) { float r; asm volatile("v_exp_f32 %0, %1" : "=v"(r) : "v"(x)); return r; }
DI u32 CV(float a, float b) { u32 r; asm volatile("v_cvt_pk_bf16_f32 %0, %1, %2" : "=v"(r) : "v"(a), "v"(b)); return r; }
struct Pipe { float e[4]; u32x4 p1; };
template <bool NEXT = true, class F0, class F1, class F2, class F3, class F4>
DI void attn_step(const f32x16& sc, f32x16& sn, f32x16& o, Pipe& pp, const u32x4& km, const u32x2& kr, const u32x4& v0, const u32x4& v1, u32x4 qm, u32x2 qr, F0 first, F1 LK, F2 LR, F3 LV1, F4 LV0) {
    u32x4 p0; float a0, a1, a2, a3, b0, b1, b2, b3;
    if (NEXT) first(sn, km, qm);
    LK();
    pp.p1[2] = CV(pp.e[0], pp.e[1]); pp.p1[3] = CV(pp.e[2], pp.e[3]);
    a0 = EX(sc[0]); a1 = EX(sc[1]); a2 = EX(sc[2]); a3 = EX(sc[3]);
    if (NEXT) QK2(sn, kr, qr); else asm volatile("s_nop 0");
    LR();
    p0[0] = CV(a0, a1); p0[1] = CV(a2, a3);
    b0 = EX(sc[4]); b1 = EX(sc[5]); b2 = EX(sc[6]); b3 = EX(sc[7]);
    PV(o, v1, pp.p1); LV1();
    p0[2] = CV(b0, b1); p0[3] = CV(b2, b3);
    a0 = EX(sc[8]); a1 = EX(sc[9]); a2 = EX(sc[10]); a3 = EX(sc[11]);
    PV(o, v0, p0); LV0();
    pp.p1[0] = CV(a0, a1); pp.p1[1] = CV(a2, a3);
    pp.e[0] = EX(sc[12]); pp.e[1] = EX(sc[13]); pp.e[2] = EX(sc[14]); pp.e[3] = EX(sc[15]);
}
template <class F1, class F2, class F3, class F4>
DI void attn_step2(const f32x16& scx, f32x16& snx, f32x16& ox, Pipe& px, const f32x16& scy, f32x16& sny, f32x16& oy, Pipe& py,
                   const u32x4& km, const u32x2& kr, const u32x4& v0, const u32x4& v1, u32x4 qmx, u32x2 qrx, u32x4 qmy, u32x2 qry, F1 LK, F2 LR, F3 LV1, F4 LV0) {
    u32x4 p0x, p0y; float a0, a1, a2, a3, b0, b1, b2, b3, c0, c1, c2, c3, d0, d1, d2, d3;
#define GRP(MF, PD0, PD1, E0, E1, E2, E3, D0, D1, D2, D3, SRC, I) MF; PD0 = CV(E0, E1); PD1 = CV(E2, E3); D0 = EX(SRC[I]); D1 = EX(SRC[I + 1]); D2 = EX(SRC[I + 2]); D3 = EX(SRC[I + 3])
    GRP(QK1(snx, km, qmx), px.p1[2], px.p1[3], px.e[0], px.e[1], px.e[2], px.e[3], a0, a1, a2, a3, scx, 0);
    GRP(QK1(sny, km, qmy); LK(), py.p1[2], py.p1[3], py.e[0], py.e[1], py.e[2], py.e[3], b0, b1, b2, b3, scy, 0);
    GRP(QK2(snx, kr, qrx), p0x[0], p0x[1], a0, a1, a2, a3, c0, c1, c2, c3, scx, 4);
    GRP(QK2(sny, kr, qry); LR(), p0y[0], p0y[1], b0, b1, b2, b3, d0, d1, d2, d3, scy, 4);
    GRP(PV(ox, v1, px.p1), p0x[2], p0x[3], c0, c1, c2, c3, a0, a1, a2, a3, scx, 8);
    GRP(PV(oy, v1, py.p1); LV1(), p0y[2], p0y[3], d0, d1, d2, d3, b0, b1, b2, b3, scy, 8);
    GRP(PV(ox, v0, p0x), px.p1[0], px.p1[1], a0, a1, a2, a3, px.e[0], px.e[1], px.e[2], px.e[3], scx, 12);
    GRP(PV(oy, v0, p0y); LV0(), py.p1[0], py.p1[1], b0, b1, b2, b3, py.e[0], py.e[1], py.e[2], py.e[3], scy, 12);
#undef GRP
}
DI void attn_finish(f32x16& o, Pipe& pp, u32x4 v1) {
    asm volatile("s_nop 0");
    pp.p1[2] = CV(pp.e[0], pp.e[1]); pp.p1[3] = CV(pp.e[2], pp.e[3]);
    asm volatile("s_nop 1");
    PV(o, v1, pp.p1);
    asm volatile("s_nop 7\n\ts_nop 7" : "+v"(o));
}
DI void pexp(const f32x16& s, u32x4& p0, u32x4& p1) {
    float e[16];
#pragma unroll
    for (int r = 0; r < 16; ++r) e[r] = __builtin_amdgcn_exp2f(s[r]);
    p0 = u32x4{pk(e[0], e[1]), pk(e[2], e[3]), pk(e[4], e[5]), pk(e[6], e[7])};
    p1 = u32x4{pk(e[8], e[9]), pk(e[10], e[11]), pk(e[12], e[13]), pk(e[14], e[15])};
}
DI float rowmax(const f32x16& s) {
    float a = fmaxf(fmaxf(s[0], s[1]), s[2]), b = fmaxf(fmaxf(s[3], s[4]), s[5]);
#pragma unroll
    for (int r = 6; r < 16; r += 2) { if (r & 2) a = fmaxf(fmaxf(a, s[r]), s[r + 1]); else b = fmaxf(fmaxf(b, s[r]), s[r + 1]); }
    return max_h(fmaxf(a, b));
}
DI f32x16 mask_c(int lane, bool diag) {
    const int r = lane & 31, h = lane >> 5; f32x16 m;
#pragma unroll
    for (int e = 0; e < 16; ++e) { const bool ok = diag ? (crow(e, h) <= r) : (crow(e, h) > r); m[e] = ok ? 0.f : NEG_BIG; }
    return m;
}
DI f32x16 attn_slow(const Ctx& c, u32 tile0, int n0, int n1, bool masked, u32x4 qm, u32x2 qr) {
    const int h = c.lane >> 5;
    qr[1] = 0u;
    f32x16 o = {}; float m = NEG_BIG;
    for (int n = n0; n <= n1; ++n) {
        const KV t = load_tile_ws(c, tile0 + (u32)n * TILE_B);
        f32x16 z = {};
        f32x16 s = qk(t, qm, qr, z);
        if (masked && (n == n1 || n == n1 - 8)) {
            const int r = c.lane & 31;
#pragma unroll
            for (int e = 0; e < 16; ++e) { const bool ok = (n == n1) ? (crow(e, h) <= r) : (crow(e, h) > r); s[e] = ok ? s[e] : NEG_BIG; }
        }
        const float mn = fmaxf(m, rowmax(s)), al = __builtin_amdgcn_exp2f(m - mn);
        m = mn;
#pragma unroll
        for (int e = 0; e < 16; ++e) { o[e] *= al; s[e] = s[e] <= NEG_BIG ? NEG_BIG : s[e] - mn; }
        u32x4 p0, p1; pexp(s, p0, p1);
        o = mfma32(t.v0, p0, o); o = mfma32(t.v1, p1, o);
    }
    return o;
}
DI bool attn_unsafe(const f32x16& o) {
    float z = 0.f;
#pragma unroll
    for (int e = 0; e < 10; ++e) z = __builtin_fmaf(o[e], 0.f, z);
    const float l = o[10];
    return __any(!(l > 0x1p-40f && l < 0x1p30f && z == 0.f));
}
DI void store_o(const Ctx& c, u32 obuf, const f32x16& o, int a, int t) {
    const int r = c.lane & 31, h = c.lane >> 5;
    const float inv = __builtin_amdgcn_rcpf(o[10]);
    const u32x4 mainp = {pk(o[0] * inv, o[1] * inv), pk(o[2] * inv, o[3] * inv), pk(o[4] * inv, o[5] * inv), pk(o[6] * inv, o[7] * inv)};
    const int m = 2 * t + (r >> 4), tok = r & 15;
    const u32 so = obuf + (u32)(c.i * 18 * 4) * 1024;
    st128(c.rs, mainp, (u32)((2 * c.j + (a >> 1)) * 4 + m) * 1024 + ((2 * (a & 1) + h) * 16 + tok) * 16, so);
    if (h == 0) st32(c.rs, pk(o[8] * inv, o[9] * inv), (u32)((16 + (c.j >> 2)) * 4 + m) * 1024 + ((c.j & 3) * 16 + tok) * 16 + 4 * a, so);
}

DI void a_proj(Ctx& c0, const Params& p, int rep) {
    Ctx c = fresh(c0);
    const int lane = c.lane, m = c.wave & 3, kh = c.wave >> 2, tok = lane & 15, g = lane >> 4;
    u32x4 xf[9]; f32x4 acc[5] = {};
    f32x4 s1 = {}, s2 = {}, ca = {}, cb = {};
    Fmt fm; u64 e0 = 0;
    if (rep == 0) {
        e0 = __hip_atomic_load((GAS u64*)(p.ws + WS_EPOCH), __ATOMIC_RELAXED, __HIP_MEMORY_SCOPE_AGENT);
        const char* xt = (const char*)p.x + (size_t)(c.i * 64) * (DM * 2);
#pragma unroll
        for (int n = 0; n < 8; ++n) {
            const int row = c.wave * 8 + n;
            glds_in(xt + row * (DM * 2) + (lane ^ (row & 15)) * 16, c.Lb + (row < 60 ? L_XIN + row * 1024 : L_XIN2 + (row - 60) * 1024));
        }
        xf[8] = *(const u32x4*)(xt + (m * 16 + tok) * (DM * 2) + (512 + 32 * kh + 8 * g) * 2);
        load_wimage_a(c, p, 0);
        STAMP(13);
        DRAIN(); BAR();
        STAMP(18);
        asm volatile("" : "+v"(xf[8]), "+v"(e0));
        fmt_issue(c, p, fm);
        { const int row = m * 16 + tok, t2 = tok ^ (g >> 1);
          LAS char* xb = c.L + (row < 60 ? L_XIN + row * 1024 : L_XIN2 + (row - 60) * 1024) + kh * 512 + 8 * (g & 1);
#pragma unroll
          for (int sl = 0; sl < 8; ++sl) {
              const int K = 4 * (sl & 3);
              const u32x2 lo = lld<u32x2>(xb + (sl >> 2) * 256 + ((K ^ t2) * 16)), hi = lld<u32x2>(xb + (sl >> 2) * 256 + (((K + 2) ^ t2) * 16));
              xf[sl] = u32x4{lo[0], lo[1], hi[0], hi[1]};
          } }
        STAMP(21);
        if (c.tid < 180) st128(c.rs, lld<u32x4>(c.L + L_R1 + c.i * 2880 + c.tid * 16), c.tid * 16, WS_WIMGA + (u32)c.j * WIMG_BYTES + (u32)c.i * 2880);
        BAR();
        STAMP(2);
    } else {
        wait_group(c, 5);
        load_xfrags(c, WS_XF + 1 * XF_BYTES, xf);
        load_stats(c, WS_ST + 1 * ST_BYTES);
        DRAIN(); BAR();
        STAMP(32);
    }
    if (rep != 0) g1_mma<true>(c, xf, acc, [](int, const u32x4 (&)[5]) {});
    else {
        u32x4 ones = ones4();
        const int tlc = m < 2 ? 2 * kh + m : 2 * (1 - kh) + (m - 2), q = (lane & 15) >> 2, pp = lane & 3;
        LAS char* bN = c.L + L_R1 + (kh * 256 + 4 * g + q) * WP + 8 * pp + 32 * tlc;
        LAS char* bR = c.L + L_R1 + (512 + 32 * kh + 8 * g + q) * WP + 8 * pp + 32 * tlc;
        asm volatile("" : "+v"(s1), "+v"(s2), "+v"(ca), "+v"(cb), "+v"(ones));
        u32x4 wx[2];
        auto rdx = [&](int sl) {
            const int r0 = sl < 8 ? 32 * sl : 0, hs = sl < 8 ? 16 : 4; LAS char* b = sl < 8 ? bN : bR;
            const u32x2 lo = trr(b + r0 * WP), hi = trr(b + (r0 + hs) * WP);
            wx[sl & 1] = u32x4{lo[0], lo[1], hi[0], hi[1]}; };
        rdx(0);
        g1_mma<true, true>(c, xf, acc, [&](int sl, const u32x4 (&w)[5]) {
            if (sl + 1 < 9) rdx(sl + 1);
            MF16(s1, ones, xf[sl]); MF16(s2, xf[sl], xf[sl]); MF16(cb, w[2], ones); MF16(ca, wx[sl & 1], ones);
        });
        SETTLE4(s1); SETTLE4(s2); SETTLE4(ca); SETTLE4(cb);
        const int rg = tok & 3;
        float d = rg == 0 ? s2[0] : (rg == 1 ? s2[1] : (rg == 2 ? s2[2] : s2[3]));
        d = sum_g(g == (tok >> 2) ? d : 0.f);
        if (g == 0) lst(c.L + L_STATP + kh * 512 + (m * 16 + tok) * 8, f32x2{s1[0], d});
        const int tl = m < 2 ? 2 * kh + m : 2 * (1 - kh) + (m - 2);
        if (tok == 0) { lst(c.L + L_CSA + (kh * 5 + tl) * 64 + g * 16, ca); if (m == 0) lst(c.L + L_CSA + (kh * 5 + 4) * 64 + g * 16, cb); }
    }
    STAMP(3 + rep * 30);
    g1_exchange(c, acc);
    const f32x2 sa = lld<f32x2>(c.L + L_STATP + (m * 16 + tok) * 8), sb = lld<f32x2>(c.L + L_STATP + 512 + (m * 16 + tok) * 8);
    const float mean = (sa[0] + sb[0]) * (1.f / DM), var = (sa[1] + sb[1]) * (1.f / DM) - mean * mean;
    const float rstd = __builtin_amdgcn_rsqf(fmaxf(var, 0.f) + 1e-5f);
    LAS char* stg = c.L + L_STG + (m * 16 + tok) * STG_P + g * 8;
#pragma unroll
    for (int u = 0; u < 3; ++u) {
        if (u < 2 || kh == 1) {
            const int t = u < 2 ? 2 * kh + u : 4;
            f32x4 cs;
            cs = lld<f32x4>(c.L + L_CSA + t * 64 + g * 16) + lld<f32x4>(c.L + L_CSA + (5 + t) * 64 + g * 16);
            const float sc = (u < 2 || g < 2) ? rstd * QSCALE : rstd;
            f32x4 v;
#pragma unroll
            for (int e = 0; e < 4; ++e) v[e] = (acc[u][e] - mean * cs[e]) * sc;
            lst(stg + t * 32, u32x2{pk(v[0], v[1]), pk(v[2], v[3])});
        }
    }
    BAR();
    STAMP(4 + rep * 30);
    if (rep == 0) {
        fmt_finish(c, fm);
        const u64 e = e0 + 1; c0.elo = c.elo = __builtin_amdgcn_readfirstlane((u32)e); c0.ehi = c.ehi = __builtin_amdgcn_readfirstlane((u32)(e >> 32));
    }
    if (c.wave == 0) {
        const u32 kva = WS_KVA + rep * KVA_BYTES + (u32)(2 * c.i) * TILE_B;
        const int j = c.j;
        if (j < 2) {
            LAS char* s = c.L + L_STG + lane * STG_P + 144;
            const u32x2 lo = lld<u32x2>(s), hi = lld<u32x2>(s + 8);
            st128(c.rs, u32x4{lo[0], lo[1], hi[0], hi[1]}, (lane >> 5) * TILE_B + j * 512 + (lane & 31) * 16, kva);
        } else if (j == 2) {
            st64(c.rs, u32x2{lld<u32>(c.L + L_STG + lane * STG_P + 144 + 12), 0x00003f80u}, (lane >> 5) * TILE_B + KT_KREM + (lane & 31) * 8, kva);
        } else if (j < 6) {
            const int s4 = lane >> 4, q = (lane & 15) >> 2, pp = lane & 3;
            LAS char* s = c.L + L_STG + (16 * s4 + q + 4 * (pp >> 1)) * STG_P + 144 + 8 * (pp & 1);
            const u32x2 lo = trr(s), hi = trr(s + 8 * STG_P);
            const int dl = lane & 7, hh = (lane >> 3) & 1, drow = j < 5 ? 8 * (j - 3) + dl : 10 + dl;
            if (j < 5 || dl >= 6) st128(c.rs, u32x4{lo[0], lo[1], hi[0], hi[1]}, (s4 >> 1) * TILE_B + KT_V + ((s4 & 1) * 2 + hh) * KT_VBLK + drow * 16, kva);
        } else if (j == 6) {
            if (lane < 8) st128(c.rs, ones4(), (lane >> 2) * TILE_B + KT_V + (lane & 3) * KT_VBLK + 18 * 16, kva);
        }
    }
    publish(c, rep * 6);
}

DI void a_attn(const Ctx& c0, const Params& p, int rep, u32 (&xres)[3][2]) {
    const Ctx c = fresh(c0);
    const int lane = c.lane, r = lane & 31, h = lane >> 5, a = c.wave & 3, kh = c.wave >> 2;
    const int ph = rep * 6;
    const u32 kva = WS_KVA + rep * KVA_BYTES;
    u32x4 qmx, qmy; u32x2 qrx, qry;
    { LAS char* s = c.L + L_STG + r * STG_P + 36 * a;
      qmx = u32x4{lld<u32>(s + 16 * h), lld<u32>(s + 16 * h + 4), lld<u32>(s + 16 * h + 8), lld<u32>(s + 16 * h + 12)};
      const u32 rx = lld<u32>(s + 32);
      s += 32 * STG_P;
      qmy = u32x4{lld<u32>(s + 16 * h), lld<u32>(s + 16 * h + 4), lld<u32>(s + 16 * h + 8), lld<u32>(s + 16 * h + 12)};
      const u32 ry = lld<u32>(s + 32);
      qrx = u32x2{h == 0 ? rx : 0u, 0u}; qry = u32x2{h == 0 ? ry : 0u, 0u}; }
    const int ch0 = c.i >> 2;
    auto chunk_ids = [&](int n) { const int ch = (ch0 + n) & 7; return ch * 32 + (lane & 31); };
    auto issue_piece = [&](int n, int k) {
        const int ch = (ch0 + n) & 7; const u32 so = kva + (u32)ch * CHUNK_B; const u32 dst = c.Lb + L_RING + (n % 3) * CHUNK_B;
        glds_ws(c.srd, lane * 16, so + (c.wave + 4 * k) * 1024, dst + (c.wave + 4 * k) * 1024);
    };
    auto issue_chunk8 = [&](int n) {
        const int ch = (ch0 + n) & 7; const u32 so = kva + (u32)ch * CHUNK_B; const u32 dst = c.Lb + L_RING + (n % 3) * CHUNK_B;
        glds_ws(c.srd, lane * 16, so + c.wave * 1024, dst + c.wave * 1024);
        glds_ws(c.srd, lane * 16, so + (c.wave + 8) * 1024, dst + (c.wave + 8) * 1024);
        if (c.wave < 4) glds_ws(c.srd, lane * 16, so + (c.wave + 16) * 1024, dst + (c.wave + 16) * 1024);
    };
    if (c.wave == 0) poll(c, ph, lane < 32 ? chunk_ids(0) : chunk_ids(1));
    BAR();
    STAMP(6 + rep * 30);
    issue_chunk8(0); issue_chunk8(1);
    if (c.wave < 4) asm volatile("s_waitcnt vmcnt(3)" ::: "memory"); else asm volatile("s_waitcnt vmcnt(2)" ::: "memory");
    BAR();
    STAMP(7 + rep * 30); if (rep == 0) CSTAMP(61);
    if (rep == 0) {
        const int m = c.wave & 3, tok = lane & 15, g = lane >> 4;
        const char* rr = (const char*)p.x + (size_t)(c.i * 64 + m * 16 + tok) * (DM * 2) + (c.j * 72 + 4 * g) * 2;
        const u32x2 r0 = *(const u32x2*)(rr + (2 * kh) * 32), r1 = *(const u32x2*)(rr + (2 * kh + 1) * 32), r2 = *(const u32x2*)(rr + (g < 2 ? 128 : 0));
        xres[0][0] = r0[0]; xres[0][1] = r0[1]; xres[1][0] = r1[0]; xres[1][1] = r1[1]; xres[2][0] = r2[0]; xres[2][1] = r2[1];
    }
    const int vr = vrow_of(r);
    LAS char* kb = c.L + L_RING + lane * 16;
    LAS char* rb = c.L + L_RING + KT_KREM + r * 8;
    LAS char* vb = c.L + L_RING + KT_V + h * KT_VBLK + vr * 16;
    f32x16 sAx, sBx, sAy, sBy, ox = {}, oy = {};
    { const f32x16 zero = {}; KV k0; k0.km = lld<u32x4>(kb); k0.kr = lld<u32x2>(rb);
      const float mx = rowmax(qk(k0, qmx, qrx, zero)), my = rowmax(qk(k0, qmy, qry, zero));
      if (h == 0) { qrx[1] = pk(-mx, 0.f); qry[1] = pk(-my, 0.f); }
      kb += kh * 4 * TILE_B; rb += kh * 4 * TILE_B; vb += kh * 4 * TILE_B;
      k0.km = lld<u32x4>(kb); k0.kr = lld<u32x2>(rb);
      sAx = qk(k0, qmx, qrx, zero); sAy = qk(k0, qmy, qry, zero); }
    u32x4 km = lld<u32x4>(kb + TILE_B), v0 = lld<u32x4>(vb), v1 = lld<u32x4>(vb + 2 * KT_VBLK); u32x2 kr = lld<u32x2>(rb + TILE_B);
    Pipe px, py;
    px.e[0] = px.e[1] = px.e[2] = px.e[3] = 0.f; px.p1 = u32x4{0u, 0u, 0u, 0u}; py = px;
    asm volatile("s_nop 7\n\ts_nop 7" : "+v"(sAx), "+v"(sAy), "+v"(px.p1), "+v"(px.e[0]), "+v"(px.e[1]), "+v"(px.e[2]), "+v"(px.e[3]), "+v"(ox), "+v"(qmx), "+v"(qrx),
                                          "+v"(py.p1), "+v"(py.e[0]), "+v"(py.e[1]), "+v"(py.e[2]), "+v"(py.e[3]), "+v"(oy), "+v"(qmy), "+v"(qry));
    int slot = 0;
    const u64 want = ((u64)c.ehi << 32) | c.elo;
    const u32 fvo = (lane & 31) * 128;
    u64 fl = want;
    if (c.wave == 0) flag_load(fl, ~want, fvo, c.srd, WS_FLAG + (u32)(ph * 256 + ((ch0 + 2) & 7) * 32) * 128);
#pragma unroll 1
    for (int n = 0; n < 8; ++n) {
        const int nslot = slot == 2 ? 0 : slot + 1;
        LAS char* kc = kb + slot * CHUNK_B; LAS char* rc = rb + slot * CHUNK_B; LAS char* vc = vb + slot * CHUNK_B;
        LAS char* kn = kb + nslot * CHUNK_B; LAS char* rn = rb + nslot * CHUNK_B; LAS char* vn = vb + nslot * CHUNK_B;
#define A_TILE(U, SCX, SNX, SCY, SNY, X1, X2) attn_step2(SCX, SNX, ox, px, SCY, SNY, oy, py, km, kr, v0, v1, qmx, qrx, qmy, qry, \
            [&] { km = lld<u32x4>((U) < 2 ? kc + ((U) + 2) * TILE_B : kn + ((U) - 2) * TILE_B); X1; }, \
            [&] { kr = lld<u32x2>((U) < 2 ? rc + ((U) + 2) * TILE_B : rn + ((U) - 2) * TILE_B); }, \
            [&] { v1 = lld<u32x4>(vc + (U) * TILE_B + 2 * KT_VBLK); X2; }, \
            [&] { v0 = lld<u32x4>((U) < 3 ? vc + ((U) + 1) * TILE_B : vn); })
        A_TILE(0, sAx, sBx, sAy, sBy, (void)0, (void)0); A_TILE(1, sBx, sAx, sBy, sAy, (void)0, (void)0);
        asm volatile("s_waitcnt vmcnt(0)" : "+v"(fl) :: "memory");
        if (__builtin_expect(!__all(fl == want), 0)) poll(c, ph, chunk_ids(n + 2));
        BAR();
        if (__builtin_expect(c.wave < 4, 0)) {
            fl = want;
            if (c.wave == 0 && n + 3 < 8) flag_load(fl, ~want, fvo, c.srd, WS_FLAG + (u32)(ph * 256 + ((ch0 + n + 3) & 7) * 32) * 128);
            if (n + 2 < 8) { issue_piece(n + 2, 0); issue_piece(n + 2, 1); issue_piece(n + 2, 2); issue_piece(n + 2, 3); issue_piece(n + 2, 4); }
            if (n >= 5) load_wimage_o4(c, 0, n - 5);
        }
        A_TILE(2, sAx, sBx, sAy, sBy, (void)0, (void)0); A_TILE(3, sBx, sAx, sBy, sAy, (void)0, (void)0);
#undef A_TILE
        slot = nslot;
    }
    attn_finish(ox, px, v1); attn_finish(oy, py, v1);
    { LAS char* mine = c.L + L_RING + kh * 2 * CHUNK_B + a * 3072 + lane * 16; LAS char* theirs = c.L + L_RING + (1 - kh) * 2 * CHUNK_B + a * 3072 + lane * 16;
      f32x16 give = kh ? ox : oy;
#pragma unroll
      for (int e = 0; e < 3; ++e) lst(mine + e * 1024, f32x4{give[4 * e], give[4 * e + 1], give[4 * e + 2], give[4 * e + 3]});
      BAR();
      if (kh) ox = oy;
#pragma unroll
      for (int e = 0; e < 3; ++e) { const f32x4 v = lld<f32x4>(theirs + e * 1024); ox[4 * e] += v[0]; ox[4 * e + 1] += v[1]; ox[4 * e + 2] += v[2]; ox[4 * e + 3] += v[3]; } }
    STAMP(8 + rep * 30); if (rep == 0) CSTAMP(62);
    if (attn_unsafe(ox)) ox = attn_slow(c, kva, 0, 63, false, kh ? qmy : qmx, kh ? qry : qrx);
    store_o(c, WS_OF + (rep * 2) * XF_BYTES, ox, a, kh);
    publish(c, ph + 1);
}

template <bool ISB>
DI void out_proj(const Ctx& c0, const Params& p, int rep, u32 (&xres)[3][2], u32x4 (&w0)[9], u32x4 (&w1)[9]) {
    const Ctx c = fresh(c0);
    const int lane = c.lane, m = c.wave & 3, kh = c.wave >> 2, tok = lane & 15, g = lane >> 4;
    const int layer = rep * 2 + (ISB ? 1 : 0);
    wait_group(c, rep * 6 + (ISB ? 4 : 1));
    STAMP((ISB ? 25 : 10) + rep * 30);
    u32x4 xf[9]; f32x4 acc[5] = {};
    load_xfrags(c, WS_OF + layer * XF_BYTES, xf);
    if (ISB) {
        asm volatile("s_waitcnt vmcnt(9)" ::: "memory"); BAR();
    }
    const u32 wso = WS_WF + (u32)((c.j * 8 + c.wave) * 2) * 18 * 1024;
    u32 csp[2][5];
    g1_mma<false>(c, xf, acc, [&](int sl, const u32x4 (&)[5]) {
        if (!ISB) {
            w0[sl] = ld128(c.rs, lane * 16, wso + sl * 1024); w1[sl] = ld128(c.rs, lane * 16, wso + (18 + sl) * 1024);
            if (sl == 8) {
                const u32 so = WS_CSP + (u32)((c.j * 8 + c.wave) * 2 * 18) * 64;
#pragma unroll
                for (int u = 0; u < 10; ++u) { const int s = g + 4 * (u % 5); csp[u / 5][u % 5] = ld32(c.rs, (s < 18 ? s : 0) * 64 + tok * 4, so + (u / 5) * 18 * 64); }
            }
        } });
    STAMP((ISB ? 26 : 11) + rep * 30);
    g1_exchange(c, acc);
    STAMP((ISB ? 27 : 12) + rep * 30);
    float s1 = 0.f, s2 = 0.f;
#pragma unroll
    for (int u = 0; u < 3; ++u) {
        const float v0 = bflo(xres[u][0]) + acc[u][0], v1 = bfhi(xres[u][0]) + acc[u][1], v2 = bflo(xres[u][1]) + acc[u][2], v3 = bfhi(xres[u][1]) + acc[u][3];
        xres[u][0] = pk(v0, v1); xres[u][1] = pk(v2, v3);
        if (u < 2 || (kh == 1 && g < 2)) {
            const float a0 = bflo(xres[u][0]), a1 = bfhi(xres[u][0]), a2 = bflo(xres[u][1]), a3 = bfhi(xres[u][1]);
            s1 += (a0 + a1) + (a2 + a3); s2 += (a0 * a0 + a1 * a1) + (a2 * a2 + a3 * a3);
        }
    }
    if (ISB && rep == 1) {
        char* o = (char*)p.out + (size_t)(c.i * 64 + m * 16 + tok) * (DM * 2) + (c.j * 72 + 4 * g) * 2;
        *(u32x2*)(o + (2 * kh) * 32) = u32x2{xres[0][0], xres[0][1]};
        *(u32x2*)(o + (2 * kh + 1) * 32) = u32x2{xres[1][0], xres[1][1]};
        if (kh == 1 && g < 2) *(u32x2*)(o + 128) = u32x2{xres[2][0], xres[2][1]};
        return;
    }
    const int xi = layer == 0 ? 0 : (layer == 1 ? 1 : 2);
    const u32 so = WS_XF + xi * XF_BYTES + (u32)(c.i * 18 * 4 + m) * 1024;
    st128(c.rs, u32x4{xres[0][0], xres[0][1], xres[1][0], xres[1][1]}, lane * 16, so + (u32)(2 * c.j + kh) * 4096);
    if (kh == 1 && g < 2) st64(c.rs, u32x2{xres[2][0], xres[2][1]}, ((c.j & 3) * 16 + tok) * 16 + 8 * g, so + (u32)(16 + (c.j >> 2)) * 4096);
    s1 = sum_g(s1); s2 = sum_g(s2);
    if (g == 0) st64(c.rs, u32x2{__builtin_bit_cast(u32, s1), __builtin_bit_cast(u32, s2)}, (u32)(c.i * 64 + m * 16 + tok) * 128 + (2 * c.j + kh) * 8, WS_ST + xi * ST_BYTES);
    publish(c, rep * 6 + (ISB ? 5 : 2));
    if (!ISB) {
#pragma unroll
        for (int slot = 0; slot < 2; ++slot) {
            float cs = (asf(csp[slot][0]) + asf(csp[slot][1])) + (asf(csp[slot][2]) + asf(csp[slot][3])) + (g < 2 ? asf(csp[slot][4]) : 0.f);
            cs = sum_g(cs);
            if (g == 0) lst(c.L + L_CSB + (c.wave * 2 + slot) * 64 + tok * 4, cs);
        }
    }
    if (ISB) load_wimage_o(c, WS_WIMGA);
}

struct T_ { static constexpr bool value = true; };
struct F_ { static constexpr bool value = false; };
DI void b_proj(const Ctx& c0, const Params& p, int rep, u32x4 (&w0)[9], u32x4 (&w1)[9], u32x4& qm, u32x2& qr) {
    const Ctx c = fresh(c0);
    const int lane = c.lane, tok = lane & 15, g = lane >> 4, a = c.wave >> 1, odd = c.wave & 1;
    wait_group(c, rep * 6 + 2);
    STAMP(15 + rep * 30);
    const int xi = rep == 0 ? 0 : 2;
    const u32x4 stv = stats_issue(c, WS_ST + xi * ST_BYTES);
    { const u32 so = WS_XF + xi * XF_BYTES + (u32)(c.i * 72) * 1024;
#pragma unroll
      for (int n = 0; n < 9; ++n) glds_ws(c.srd, lane * 16, so + (c.wave + 8 * n) * 1024, c.Lb + L_R1 + (c.wave + 8 * n) * 1024); }
    asm volatile("s_waitcnt vmcnt(5)" ::: "memory"); BAR();
    STAMP(16 + rep * 30);
    f32x4 a0[4] = {}, a1[4] = {};
    const u32 wso = WS_WF + (u32)((c.j * 8 + c.wave) * 2) * 18 * 1024;
    LAS char* xb = c.L + L_R1 + lane * 16;
    const bool t0 = !odd, t1 = !odd || a == 0, has1 = !odd || a < 2;
    auto body = [&](auto T0, auto T1, auto H1) {
        u32x4 x[2][4];
#pragma unroll
        for (int m = 0; m < 4; ++m) { x[0][m] = lld<u32x4>(xb + m * 1024); asm volatile("" : "+v"(a0[m]), "+v"(a1[m])); }
#pragma unroll
        for (int s = 0; s < 18; ++s) {
            if (s == 7) {
                if (H1.value) asm volatile("s_waitcnt vmcnt(14)" ::: "memory"); else asm volatile("s_waitcnt vmcnt(7)" ::: "memory");
                BAR();
            }
            if (s + 1 < 18) {
#pragma unroll
                for (int m = 0; m < 4; ++m) x[(s + 1) & 1][m] = lld<u32x4>(xb + ((s + 1) * 4 + m) * 1024);
            }
#pragma unroll
            for (int m = 0; m < 4; ++m) {
                if (T0.value) MF16(a0[m], w0[s % 9], x[s & 1][m]); else MF16(a0[m], x[s & 1][m], w0[s % 9]);
                if (H1.value) { if (T1.value) MF16(a1[m], w1[s % 9], x[s & 1][m]); else MF16(a1[m], x[s & 1][m], w1[s % 9]); }
            }
            if (s < 9) { w0[s] = ld128(c.rs, lane * 16, wso + (9 + s) * 1024); if (H1.value) w1[s] = ld128(c.rs, lane * 16, wso + (27 + s) * 1024); }
        }
#pragma unroll
        for (int m = 0; m < 4; ++m) { SETTLE4(a0[m]); SETTLE4(a1[m]); }
    };
    if (!odd) body(T_{}, T_{}, T_{}); else if (a == 0) body(F_{}, T_{}, T_{}); else if (a == 1) body(F_{}, F_{}, T_{}); else body(F_{}, F_{}, F_{});
    (void)t0; (void)t1; (void)has1;
    STAMP(17 + rep * 30);
    stats_finish(c, stv);
    BAR();
    STAMP(23 + rep * 30);
    const u32 kvb = WS_KVB + rep * KVB_BYTES;
    const u32 hb = kvb + (u32)((4 * c.j + a) * 64 + 2 * c.i) * TILE_B;
    auto fin_t = [&](const f32x4 (&acc)[4], int slot, float sc, u32 (&d)[4][2]) {
        const f32x4 cs = lld<f32x4>(c.L + L_CSB + (c.wave * 2 + slot) * 64 + g * 16);
#pragma unroll
        for (int m = 0; m < 4; ++m) {
            const float mean = lld<float>(c.L + L_MEAN + (m * 16 + tok) * 4), rs = lld<float>(c.L + L_MEAN + 256 + (m * 16 + tok) * 4) * sc;
            d[m][0] = pk((acc[m][0] - mean * cs[0]) * rs, (acc[m][1] - mean * cs[1]) * rs);
            d[m][1] = pk((acc[m][2] - mean * cs[2]) * rs, (acc[m][3] - mean * cs[3]) * rs);
        }
    };
    auto fin_p = [&](const f32x4 (&acc)[4], int slot, u32 (&d)[4][2]) {
        const float cs = lld<float>(c.L + L_CSB + (c.wave * 2 + slot) * 64 + tok * 4);
#pragma unroll
        for (int m = 0; m < 4; ++m) {
            const f32x4 mean = lld<f32x4>(c.L + L_MEAN + (m * 16 + 4 * g) * 4), rs = lld<f32x4>(c.L + L_MEAN + 256 + (m * 16 + 4 * g) * 4);
            d[m][0] = pk((acc[m][0] - mean[0] * cs) * rs[0], (acc[m][1] - mean[1] * cs) * rs[1]);
            d[m][1] = pk((acc[m][2] - mean[2] * cs) * rs[2], (acc[m][3] - mean[3] * cs) * rs[3]);
        }
    };
    auto frag16 = [&](const u32 (&d)[4][2], int t) {
        auto x = __builtin_amdgcn_permlane16_swap(d[2 * t][0], d[2 * t + 1][0], false, false);
        auto y = __builtin_amdgcn_permlane16_swap(d[2 * t][1], d[2 * t + 1][1], false, false);
        return u32x4{(u32)x[0], (u32)y[0], (u32)x[1], (u32)y[1]};
    };
    auto frag32 = [&](const u32 (&d)[4][2], int t) {
        auto x = __builtin_amdgcn_permlane32_swap(d[2 * t][0], d[2 * t + 1][0], false, false);
        auto y = __builtin_amdgcn_permlane32_swap(d[2 * t][1], d[2 * t + 1][1], false, false);
        return u32x4{(u32)x[0], (u32)y[0], (u32)x[1], (u32)y[1]};
    };
    u32 d[4][2];
    if (!odd) {
        fin_t(a1, 1, 1.f, d);
        st128(c.rs, frag16(d, 0), lane * 16, hb); st128(c.rs, frag16(d, 1), lane * 16, hb + TILE_B);
        fin_t(a0, 0, QSCALE, d);
        qm = frag16(d, 0);
        lst(c.L + L_QT1 + a * 1024 + lane * 16, frag16(d, 1));
    } else {
        fin_p(a0, 0, d);
        st128(c.rs, frag32(d, 0), KT_V + g * KT_VBLK + tok * 16, hb); st128(c.rs, frag32(d, 1), KT_V + g * KT_VBLK + tok * 16, hb + TILE_B);
        if (a == 0) {
            fin_t(a1, 1, g < 2 ? QSCALE : 1.f, d);
#pragma unroll
            for (int m = 0; m < 4; ++m) {
                if (g < 2) { lst(c.L + L_QREM + (2 * g) * 256 + (m * 16 + tok) * 4, d[m][0]); lst(c.L + L_QREM + (2 * g + 1) * 256 + (m * 16 + tok) * 4, d[m][1]); }
                else {
                    const u32 hb0 = kvb + (u32)(4 * c.j * 64 + 2 * c.i + (m >> 1)) * TILE_B;
                    st64(c.rs, u32x2{d[m][0], 0x00003f80u}, (u32)(2 * (g - 2)) * 64 * TILE_B + KT_KREM + ((m & 1) * 16 + tok) * 8, hb0);
                    st64(c.rs, u32x2{d[m][1], 0x00003f80u}, (u32)(2 * (g - 2) + 1) * 64 * TILE_B + KT_KREM + ((m & 1) * 16 + tok) * 8, hb0);
                }
            }
        } else if (a == 1) {
            fin_p(a1, 1, d);
            const u32 hb0 = kvb + (u32)(4 * c.j * 64 + 2 * c.i) * TILE_B;
            const u32 vo = (u32)(tok >> 1) * 64 * TILE_B + KT_V + g * KT_VBLK + (16 + (tok & 1)) * 16;
            const u32x4 f0 = frag32(d, 0), f1 = frag32(d, 1);
            if (tok < 8) { st128(c.rs, f0, vo, hb0); st128(c.rs, f1, vo, hb0 + TILE_B); }
        } else if (a == 2) {
            const u32 hb0 = kvb + (u32)(4 * c.j * 64 + 2 * c.i) * TILE_B;
            if (lane < 32) st128(c.rs, ones4(),
                                 (u32)(lane >> 3) * 64 * TILE_B + ((lane >> 2) & 1) * TILE_B + KT_V + (lane & 3) * KT_VBLK + 18 * 16, hb0);
        }
    }
    publish(c, rep * 6 + 3);
    load_wimage_part<44, 90>(c, WS_WIMG + 8 * WIMG_BYTES, 1);
    if (odd) qm = lld<u32x4>(c.L + L_QT1 + a * 1024 + lane * 16);
    const u32 rem = lld<u32>(c.L + L_QREM + a * 256 + (32 * odd + (lane & 31)) * 4);
    qr = u32x2{lane < 32 ? rem : 0u, 0u};
}

DI void b_attn(const Ctx& c0, const Params& p, int rep, u32x4 qm, u32x2 qr) {
    const Ctx c = fresh(c0);
    const int lane = c.lane, r = lane & 31, h = lane >> 5, a = c.wave >> 1, t = c.wave & 1;
    const int qt = 2 * c.i + t;
    const f32x16 cd = mask_c(lane, true);
    if (c.wave == 1) {
        const f32x16 cm = mask_c(lane, false);
#pragma unroll
        for (int e = 0; e < 4; ++e) lst(c.L + L_MSK + e * 1024 + lane * 16, f32x4{cm[4 * e], cm[4 * e + 1], cm[4 * e + 2], cm[4 * e + 3]});
    }
    if (c.wave == 0) poll(c, rep * 6 + 3, (lane < 5 && c.i - lane >= 0) ? (c.i - lane) * 8 + c.j : -1);
    BAR();
    STAMP(20 + rep * 30);
    const u32 hb = WS_KVB + rep * KVB_BYTES + (u32)((4 * c.j + a) * 64) * TILE_B;
    { const u32 dst = c.Lb + L_KVB + a * 25600;
#pragma unroll
      for (int n = 0; n < 13; ++n) {
          const int pc = n < 8 ? 24 - t - 2 * n : 9 - t - 2 * (n - 8);
          if (n != 7 || t == 0) {
              u32 vo = lane * 16;
              if (c.i < 4) { const int b = pc * 1024 + lane * 16, tau = b / 2560, kt = 2 * c.i - 8 + tau; vo = (u32)((kt < 0 ? 0 : kt) * 2560 + (b - tau * 2560)); glds_ws(c.srd, vo, hb, dst + pc * 1024); }
              else glds_ws(c.srd, vo, hb + (u32)(2 * c.i - 8) * 2560 + pc * 1024, dst + pc * 1024);
          }
      } }
    asm volatile("s_waitcnt vmcnt(5)" ::: "memory");
    BAR();
    STAMP(21 + rep * 30);
    LAS char* tb = c.L + L_KVB + a * 25600 + (8 + t) * TILE_B;
    LAS char* kb = tb + lane * 16; LAS char* rb = tb + KT_KREM + r * 8; LAS char* vb = tb + KT_V + h * KT_VBLK + vrow_of(r) * 16;
    u32x4 km = lld<u32x4>(kb), v0, v1; u32x2 kr = lld<u32x2>(rb);
    f32x16 sA, sB, o = {};
    { KV k0; k0.km = km; k0.kr = kr;
      const f32x16 s = qk(k0, qm, qr, cd); const float m0 = rowmax(s); if (h == 0) qr[1] = pk(-m0, 0.f);
      sA = qk(k0, qm, qr, cd); }
    km = lld<u32x4>(kb - TILE_B); kr = lld<u32x2>(rb - TILE_B);
    v0 = lld<u32x4>(vb); v1 = lld<u32x4>(vb + 2 * KT_VBLK);
    Pipe pp; pp.e[0] = pp.e[1] = pp.e[2] = pp.e[3] = 0.f; pp.p1 = u32x4{0u, 0u, 0u, 0u};
    asm volatile("s_nop 7\n\ts_nop 7" : "+v"(sA), "+v"(pp.p1), "+v"(pp.e[0]), "+v"(pp.e[1]), "+v"(pp.e[2]), "+v"(pp.e[3]), "+v"(o), "+v"(qm), "+v"(qr));
    auto first = [](f32x16& s, u32x4 k, u32x4 q) { QK1(s, k, q); };
    auto firstm = [&](f32x16& s, u32x4 k, u32x4 q) {
        f32x16 co;
#pragma unroll
        for (int e = 0; e < 4; ++e) { const f32x4 v = lld<f32x4>(c.L + L_MSK + e * 1024 + lane * 16); co[4 * e] = v[0]; co[4 * e + 1] = v[1]; co[4 * e + 2] = v[2]; co[4 * e + 3] = v[3]; }
        QK1C(s, k, q, co); };
    const u32 qoff = h == 0 ? pk(NEG_BIG, 0.f) : 0u;
    kb -= 8 * TILE_B; rb -= 8 * TILE_B; vb -= 8 * TILE_B;
#define B_TILE(U, SC, SN, FIRST) do { u32x2 qs = {qr[0], (U) + 1 <= qt ? qr[1] : qoff}; asm volatile("" : "+v"(qs)); \
        attn_step<((U) < 8)>(SC, SN, o, pp, km, kr, v0, v1, qm, qs, FIRST, \
            [&] { if ((U) + 2 <= 8) km = lld<u32x4>(kb + (8 - ((U) + 2)) * TILE_B); }, [&] { if ((U) + 2 <= 8) kr = lld<u32x2>(rb + (8 - ((U) + 2)) * TILE_B); }, \
            [&] { v1 = lld<u32x4>(vb + (8 - (U)) * TILE_B + 2 * KT_VBLK); }, [&] { if ((U) + 1 <= 8) v0 = lld<u32x4>(vb + (8 - ((U) + 1)) * TILE_B); }); } while (0)
    B_TILE(0, sA, sB, first); B_TILE(1, sB, sA, first);
    DRAIN();
    B_TILE(2, sA, sB, first);
    BAR();
    B_TILE(3, sB, sA, first); B_TILE(4, sA, sB, first); B_TILE(5, sB, sA, first); B_TILE(6, sA, sB, first); B_TILE(7, sB, sA, firstm); B_TILE(8, sA, sB, first);
#undef B_TILE
    attn_finish(o, pp, v1);
    STAMP(22 + rep * 30);
    if (attn_unsafe(o)) o = attn_slow(c, hb, qt - 8 < 0 ? 0 : qt - 8, qt, true, qm, qr);
    store_o(c, WS_OF + (rep * 2 + 1) * XF_BYTES, o, a, t);
    publish(c, rep * 6 + 4);
    load_wimage_part<0, 44>(c, WS_WIMG + 8 * WIMG_BYTES, 1);
}

__global__ void __launch_bounds__(512, 2) fused_block_kernel(Params p) {
    __shared__ __attribute__((aligned(16))) char lds_[LDS_BYTES];
    Ctx c;
    c.L = (LAS char*)lds_; c.Lb = (u32)(size_t)c.L;
    c.ws = p.ws;
    c.tid = threadIdx.x; c.lane = c.tid & 63; c.wave = __builtin_amdgcn_readfirstlane(c.tid >> 6);
    { const int wg = blockIdx.x, slot = wg >> 3; c.i = (wg & 7) * 4 + (slot >> 3); c.j = slot & 7; }
    c.rs = __builtin_amdgcn_make_buffer_rsrc((void*)p.ws, 0, WS_USED, 0x00020000);
    { const u64 b = (u64)p.ws; c.srd = u32x4{(u32)b, (u32)(b >> 32) & 0xffffu, WS_USED, 0x00020000u}; }
    STAMP(0);
    c.elo = 0; c.ehi = 0;
    u32 xres[3][2];
    u32x4 w0[9], w1[9];
#pragma unroll 1
    for (int rep = 0; rep < 2; ++rep) {
        STAMP(1 + rep * 30);
        a_proj(c, p, rep);
        STAMP(5 + rep * 30);
        a_attn(c, p, rep, xres);
        STAMP(9 + rep * 30);
        if (rep == 0) {
            if (blockIdx.x == 0 && c.tid == 0) __hip_atomic_store((GAS u64*)(p.ws + WS_EPOCH), ((u64)c.ehi << 32) | c.elo, __ATOMIC_RELAXED, __HIP_MEMORY_SCOPE_AGENT);
        }
        out_proj<false>(c, p, rep, xres, w0, w1);
        STAMP(14 + rep * 30);
        u32x4 qm; u32x2 qr;
        b_proj(c, p, rep, w0, w1, qm, qr);
        STAMP(19 + rep * 30);
        b_attn(c, p, rep, qm, qr);
        STAMP(24 + rep * 30);
        out_proj<true>(c, p, rep, xres, w0, w1);
        STAMP(29 + rep * 30);
    }
}

extern "C" void kernel_launch(void* const* d_in, const int* in_sizes, int n_in, void* d_out, int out_size, void* d_ws, size_t ws_size, hipStream_t stream) {
    Params p;
    p.x = (const u16*)d_in[0]; p.wq = (const u16*)d_in[1]; p.wk = (const u16*)d_in[2]; p.wv = (const u16*)d_in[3]; p.woA = (const u16*)d_in[4];
    p.wqkv = (const u16*)d_in[6]; p.woB = (const u16*)d_in[7]; p.out = (u16*)d_out; p.ws = (char*)d_ws;
    fused_block_kernel<<<256, 512, 0, stream>>>(p);
}
```

```cpp
#include <hip/hip_runtime.h>
#include <hip/hip_bf16.h>

typedef unsigned short u16;
typedef unsigned u32;
typedef unsigned long long u64;
typedef __bf16 bf16x8 __attribute__((ext_vector_type(8)));
typedef __bf16 bf16x2 __attribute__((ext_vector_type(2)));
typedef short s16x4 __attribute__((ext_vector_type(4)));
typedef float f32x2 __attribute__((ext_vector_type(2)));
typedef float f32x4 __attribute__((ext_vector_type(4)));
typedef float f32x16 __attribute__((ext_vector_type(16)));
typedef u32 u32x2 __attribute__((ext_vector_type(2)));
typedef u32 u32x4 __attribute__((ext_vector_type(4)));
#define LAS __attribute__((address_space(3)))
#define GAS __attribute__((address_space(1)))
#define DI __device__ __forceinline__

constexpr int SEQ = 2048, DM = 576;
constexpr u32 TILE_B = 2560;
constexpr u32 KT_V = 1024, KT_VBLK = 304, KT_KREM = 2240;
constexpr u32 XF_BYTES = 32u * 18 * 4 * 1024;
constexpr u32 WS_EPOCH = 0;
constexpr u32 WS_FLAG = 4096;
constexpr int PH_FMT = 11;
constexpr u32 WS_XF = WS_FLAG + 12u * 256 * 128 + 4096;
constexpr u32 WS_OF = WS_XF + 3 * XF_BYTES;
constexpr u32 ST_BYTES = 2048u * 16 * 8;
constexpr u32 WS_ST = WS_OF + 4 * XF_BYTES;
constexpr u32 KVA_BYTES = 64 * TILE_B;
constexpr u32 WS_KVA = WS_ST + 3 * ST_BYTES;
constexpr u32 KVB_BYTES = 32 * 64 * TILE_B;
constexpr u32 WS_KVB = WS_KVA + 2 * KVA_BYTES;
constexpr u32 WF_BYTES = 8u * 16 * 18 * 1024;
constexpr u32 WS_WF = WS_KVB + 2 * KVB_BYTES;
constexpr u32 WS_CSP = WS_WF + WF_BYTES;
constexpr u32 WIMG_BYTES = 90 * 1024;
constexpr u32 WS_WIMG = WS_CSP + 8u * 16 * 18 * 64;
constexpr u32 WS_WIMGA = WS_WIMG + 16 * WIMG_BYTES;
constexpr u32 WS_STAMP = WS_WIMGA + 8 * WIMG_BYTES;
constexpr u32 WS_STAMP2 = WS_STAMP + 256u * 64 * 8 + 256u * 8 * 8 * 8;
constexpr u32 WS_USED = WS_STAMP2 + 256u * 16 * 8;
constexpr int L_XCH = 0;
constexpr int L_RING = 0;
constexpr int CHUNK_B = 8 * TILE_B;
constexpr int L_STG = 40960;
constexpr int STG_P = 168;
constexpr int L_XIN = 0;
constexpr int L_XIN2 = 153600;
constexpr int L_FMT = 24576;
constexpr int L_KVB = 0;
constexpr int L_MSK = 102400;
constexpr int L_R1 = 61440;
constexpr int WP = 160;
constexpr int L_A1 = 24576;
constexpr int L_STATP = 153600;
constexpr int L_MEAN = L_STATP + 1024;
constexpr int L_CSA = L_MEAN + 512;
constexpr int L_CSB = L_CSA + 1024;
constexpr int L_QREM = L_CSB + 1024;
constexpr int L_QT1 = L_QREM + 1024;
constexpr int LDS_BYTES = 163840;
static_assert(L_QT1 + 4096 <= LDS_BYTES, "lds");

constexpr int RP = 0;
constexpr float QSCALE = 0.23570226039551584f * 1.4426950408889634f;
constexpr float NEG_BIG = -1e30f;
constexpr u32 SPIN_CAP = 1u << 20;
constexpr int AUX_SC1 = 16;

struct Params { const u16* x; const u16* wq; const u16* wk; const u16* wv; const u16* woA; const u16* wqkv; const u16* woB; u16* out; char* ws; };

DI u32 pk(float a, float b) { f32x2 v = {a, b}; return __builtin_bit_cast(u32, __builtin_convertvector(v, bf16x2)); }
DI float asf(u32 d) { return __builtin_bit_cast(float, d); }
DI u32 asu(float f) { return __builtin_bit_cast(u32, f); }
DI float bflo(u32 d) { return __builtin_bit_cast(float, d << 16); }
DI float bfhi(u32 d) { return __builtin_bit_cast(float, d & 0xffff0000u); }
DI bf16x8 asb(u32x4 v) { return __builtin_bit_cast(bf16x8, v); }
DI f32x4 mfma16(u32x4 a, u32x4 b, f32x4 c) { return __builtin_amdgcn_mfma_f32_16x16x32_bf16(asb(a), asb(b), c, 0, 0, 0); }
DI f32x16 mfma32(u32x4 a, u32x4 b, f32x16 c) { return __builtin_amdgcn_mfma_f32_32x32x16_bf16(asb(a), asb(b), c, 0, 0, 0); }
DI void MF16(f32x4& acc, u32x4 a, u32x4 b) { asm volatile("v_mfma_f32_16x16x32_bf16 %0, %1, %2, %0" : "+v"(acc) : "v"(a), "v"(b)); }
#define SETTLE4(x) asm volatile("s_nop 7" : "+v"(x))
template <class T> DI T lld(LAS char* p) { return *(LAS const T*)p; }
template <class T> DI void lst(LAS char* p, T v) { *(LAS T*)p = v; }
DI u32x2 trr(LAS char* p) { return __builtin_bit_cast(u32x2, __builtin_amdgcn_ds_read_tr16_b64_v4i16((LAS s16x4*)p)); }
#define BAR() asm volatile("s_waitcnt lgkmcnt(0)\n\ts_barrier" ::: "memory")
#define DRAIN() asm volatile("s_waitcnt vmcnt(0)" ::: "memory")
typedef __amdgpu_buffer_rsrc_t rsrc_t;
DI u32x4 ld128(rsrc_t rs, u32 voff, u32 soff) { return __builtin_amdgcn_raw_buffer_load_b128(rs, voff, soff, AUX_SC1); }
DI u32x2 ld64(rsrc_t rs, u32 voff, u32 soff) { return __builtin_amdgcn_raw_buffer_load_b64(rs, voff, soff, AUX_SC1); }
DI u32 ld32(rsrc_t rs, u32 voff, u32 soff) { return __builtin_amdgcn_raw_buffer_load_b32(rs, voff, soff, AUX_SC1); }
DI void st128(rsrc_t rs, u32x4 v, u32 voff, u32 soff) { __builtin_amdgcn_raw_buffer_store_b128(v, rs, voff + soff, 0, AUX_SC1); }
DI void st64(rsrc_t rs, u32x2 v, u32 voff, u32 soff) { __builtin_amdgcn_raw_buffer_store_b64(v, rs, voff, soff, AUX_SC1); }
DI void st32(rsrc_t rs, u32 v, u32 voff, u32 soff) { __builtin_amdgcn_raw_buffer_store_b32(v, rs, voff, soff, AUX_SC1); }
DI void glds_ws(u32x4 srd, u32 voff, u32 soff, u32 ldsdst) {
    soff = __builtin_amdgcn_readfirstlane(soff); ldsdst = __builtin_amdgcn_readfirstlane(ldsdst);
    asm volatile("s_mov_b32 m0, %0\n\ts_nop 3\n\tbuffer_load_dwordx4 %1, %2, %3 offen sc1 lds" :: "s"(ldsdst), "v"(voff), "s"(srd), "s"(soff) : "memory");
}
DI void glds_in(const void* g, u32 ldsdst) {
    ldsdst = __builtin_amdgcn_readfirstlane(ldsdst);
    asm volatile("s_mov_b32 m0, %0\n\ts_nop 0\n\tglobal_load_lds_dwordx4 %1, off" :: "s"(ldsdst), "v"(g) : "memory");
}
DI void flag_load(u64& fl, u64 nomatch, u32 voff, u32x4 srd, u32 soff) {
    soff = __builtin_amdgcn_readfirstlane(soff);
    fl = nomatch;
    asm volatile("s_nop 4\n\tbuffer_load_dwordx2 %0, %1, %2, %3 offen sc1" : "+v"(fl) : "v"(voff), "s"(srd), "s"(soff) : "memory");
}
DI float sum_g(float v) {
    auto a = __builtin_amdgcn_permlane32_swap(__builtin_bit_cast(u32, v), __builtin_bit_cast(u32, v), false, false);
    v = __builtin_bit_cast(float, (u32)a[0]) + __builtin_bit_cast(float, (u32)a[1]);
    auto b = __builtin_amdgcn_permlane16_swap(__builtin_bit_cast(u32, v), __builtin_bit_cast(u32, v), false, false);
    return __builtin_bit_cast(float, (u32)b[0]) + __builtin_bit_cast(float, (u32)b[1]);
}
DI float max_h(float v) {
    auto a = __builtin_amdgcn_permlane32_swap(__builtin_bit_cast(u32, v), __builtin_bit_cast(u32, v), false, false);
    return fmaxf(__builtin_bit_cast(float, (u32)a[0]), __builtin_bit_cast(float, (u32)a[1]));
}
DI u32x4 ones4() { u32 o = 0x3f803f80u; asm volatile("" : "+v"(o)); return u32x4{o, o, o, o}; }
DI int crow(int reg, int h) { return (reg & 3) + 8 * (reg >> 2) + 4 * h; }

#define STAMP(n) do {} while (0)
#define CSTAMP(n) do {} while (0)
#define CSTAMPW(n, W) do {} while (0)

struct Ctx {
    LAS char* L; u32 Lb;
    char* ws; rsrc_t rs; u32x4 srd;
    int tid, lane, wave, i, j;
    u32 elo, ehi;
};

DI Ctx fresh(const Ctx& c0) {
    Ctx c = c0;
    asm volatile("" : "+v"(c.lane)); asm volatile("" : "+v"(c.tid));
    asm volatile("" : "+s"(c.wave)); asm volatile("" : "+s"(c.i)); asm volatile("" : "+s"(c.j));
    return c;
}
DI GAS u64* flag_ptr(const Ctx& c, int phase, int id) { return (GAS u64*)(c.ws + WS_FLAG + (u32)(phase * 256 + id) * 128); }
DI void signal(const Ctx& c, int phase) {
    if (c.tid == 0) __hip_atomic_store(flag_ptr(c, phase, c.i * 8 + c.j), ((u64)c.ehi << 32) | c.elo, __ATOMIC_RELAXED, __HIP_MEMORY_SCOPE_AGENT);
}
DI void publish(const Ctx& c, int phase) {
    DRAIN();
    BAR();
    signal(c, phase);
}
DI void poll(const Ctx& c, int phase, int id) {
    const u64 want = ((u64)c.ehi << 32) | c.elo;
    GAS u64* f = flag_ptr(c, phase, id < 0 ? 0 : id);
    for (u32 spins = 0; spins < SPIN_CAP; ++spins) {
        const u64 v = __hip_atomic_load(f, __ATOMIC_RELAXED, __HIP_MEMORY_SCOPE_AGENT);
        if (__all(id < 0 || v == want)) break;
        __builtin_amdgcn_s_sleep(1);
    }
    __builtin_amdgcn_fence(__ATOMIC_ACQUIRE, "wavefront");
}
DI void wait_group(const Ctx& c, int phase) {
    if (c.wave == 0) poll(c, phase, c.lane < 8 ? c.i * 8 + c.lane : -1);
    BAR();
}

DI void load_wimage_a(const Ctx& c, const Params& p, int w0) {
    if (c.wave < w0) return;
    const int jj = c.j, nw = 8 - w0, g0 = c.wave - w0;
    const u16 *pk_ = p.wk, *pv_ = p.wv;
    asm volatile("" : "+s"(pk_), "+s"(pv_));
    const char* xsrc = (const char*)((jj >= 3 && jj < 6) ? pv_ : pk_); const int sub = jj < 6 ? jj % 3 : 0;
    xsrc += sub == 0 ? 0 : (sub == 1 ? 16 : 20);
    const char* msrc = (const char*)p.wq + jj * 144;
    const char* a[5]; u32 st[5];
#pragma unroll
    for (int k = 0; k < 5; ++k) {
        const u32 id = 64 * k + c.lane, row = (id * 6554u) >> 16, ch = id - row * 10;
        const bool ex = ch == 9;
        const u32 s1 = ex ? 32 * 36 : 32 * 1152;
        st[k] = s1 * nw;
        a[k] = (ex ? xsrc : msrc) + (ex ? row * 36 : row * 1152 + ch * 16) + (u32)g0 * s1;
    }
#pragma unroll
    for (int n = 0; n < 3; ++n) {
        const int G = g0 + nw * n;
        if (G < 18) {
#pragma unroll
            for (int k = 0; k < 5; ++k) { glds_in(a[k], c.Lb + L_R1 + (G * 5 + k) * 1024); a[k] += st[k]; }
        }
    }
}
DI void load_wimage_o(const Ctx& c, u32 base) {
    const u32 so = base + (u32)c.j * WIMG_BYTES;
    if (c.wave >= 1) {
#pragma unroll
        for (int n = 0; n < 13; ++n)
            if (n < 12 || c.wave < 7) glds_ws(c.srd, c.lane * 16, so + (c.wave - 1 + 7 * n) * 1024, c.Lb + L_R1 + (c.wave - 1 + 7 * n) * 1024);
    }
}
template <int P0, int P1>
DI void load_wimage_part(const Ctx& c, u32 base, int w0, int ldsbase = L_R1) {
    const u32 so = base + (u32)c.j * WIMG_BYTES; const int nw = 8 - w0;
    if (c.wave >= w0) {
#pragma unroll
        for (int n = 0; n < (P1 - P0 + 6) / 7; ++n) {
            const int pc = P0 + (c.wave - w0) + nw * n;
            if (pc < P1) glds_ws(c.srd, c.lane * 16, so + pc * 1024, c.Lb + ldsbase + pc * 1024);
        }
    }
}
DI void load_wimage_o4(const Ctx& c, int which, int part) {
    const u32 so = WS_WIMG + (u32)(which * 8 + c.j) * WIMG_BYTES + (u32)part * 30 * 1024; const u32 dst = c.Lb + L_R1 + part * 30 * 1024;
    if (c.wave < 4) {
#pragma unroll
        for (int n = 0; n < 8; ++n)
            if (n < 7 || c.wave < 2) glds_ws(c.srd, c.lane * 16, so + (c.wave + 4 * n) * 1024, dst + (c.wave + 4 * n) * 1024);
    }
}
struct Fmt { u32x4 v[2]; u32 g[8]; u32 dst[2], cdst[2]; int kind[2]; };
DI void fmt_issue(const Ctx& c, const Params& p, Fmt& m) {
    const u16 *pw = p.wqkv, *pa = p.woA, *pb = p.woB;
    asm volatile("" : "+s"(pw), "+s"(pa), "+s"(pb));
    const int lane = c.lane, rho = lane >> 1, g = rho >> 3, e = rho & 7;
#pragma unroll
    for (int r = 0; r < 2; ++r) {
        const int fr = blockIdx.x * 8 + c.wave + 2048 * r;
        int col, A, P, Q, pitch; const GAS char* src;
        if (fr < 2016) {
            const int jj = fr / 252, rem = fr - jj * 252, u = rem / 18, s = rem - u * 18;
            const int wv = u < 8 ? u : (u - 8 < 5 ? u - 8 : 6), slot = u < 8 ? 0 : 1, a = wv >> 1;
            const bool isrem = (wv & 1) && slot == 1;
            const u32 fo = (u32)(((jj * 8 + wv) * 2 + slot) * 18 + s);
            m.dst[r] = WS_WF + fo * 1024; m.cdst[r] = WS_CSP + fo * 64; m.kind[r] = isrem ? 3 : 1;
            P = s < 16 ? 16 : 4; Q = 2; pitch = 1728 * 2; src = (const GAS char*)pw;
            if (!isrem) {
                col = ((wv & 1) ? 1152 : (slot ? 576 : 0)) + 18 * (4 * jj + a) + 8 * (lane & 1);
                A = s < 16 ? 72 * (s >> 1) + 32 * (s & 1) + 4 * g : 72 * (4 * (s - 16) + g) + 64;
            } else {
                const int n = lane & 15, g2 = lane >> 4;
                col = (a == 0 ? (n < 8 ? 0 : 576) : 1152) + 18 * (4 * jj + ((n & 7) >> 1)) + 16 + (n & 1);
                A = s < 16 ? 72 * (s >> 1) + 32 * (s & 1) + 4 * g2 : 72 * (4 * (s - 16) + g2) + 64;
#pragma unroll
                for (int q = 0; q < 8; ++q) m.g[q] = *(const GAS u16*)(src + (A + P * (q >> 2) + (q & 3)) * pitch + col * 2);
            }
        } else {
            const bool on = fr < 3456;
            const int q = on ? fr - 2016 : 0, im = q / 720, rem = q - im * 720, jj = rem / 90, st = rem - jj * 90, s = st / 5, t = st - s * 5;
            m.dst[r] = WS_WIMG + (u32)(im * 8 + jj) * WIMG_BYTES + (u32)st * 1024; m.cdst[r] = 0; m.kind[r] = on ? 2 : 0;
            col = 72 * jj + 16 * t + (t == 4 ? 0 : 8 * (lane & 1));
            if (s < 16) { A = 36 * s + 18 * (g >> 1) + 4 * (g & 1); P = 8; Q = 2; } else { A = 72 * (4 * (s - 16) + g) + 16; P = 36; Q = 18; }
            src = (const GAS char*)(im ? pb : pa); pitch = 576 * 2;
        }
        if (m.kind[r] == 1 || m.kind[r] == 2) m.v[r] = *(const GAS u32x4*)(src + (A + P * (e >> 2) + Q * ((e >> 1) & 1) + (e & 1)) * pitch + col * 2);
    }
}
DI void fmt_finish(const Ctx& c, Fmt& m) {
    asm volatile("" : "+v"(m.v[0]), "+v"(m.v[1]), "+v"(m.g[0]), "+v"(m.g[1]), "+v"(m.g[2]), "+v"(m.g[3]), "+v"(m.g[4]), "+v"(m.g[5]), "+v"(m.g[6]), "+v"(m.g[7]));
    const int lane = c.lane, i16 = lane & 15, g = lane >> 4, q = i16 >> 2, p = i16 & 3;
#pragma unroll
    for (int r = 0; r < 2; ++r) {
        if (m.kind[r] != 0) {
            u32x4 f;
            if (m.kind[r] == 3) { const u32* w = m.g; f = u32x4{w[0] | (w[1] << 16), w[2] | (w[3] << 16), w[4] | (w[5] << 16), w[6] | (w[7] << 16)}; }
            else {
                LAS char* t = c.L + L_FMT + c.wave * 2048 + r * 1024;
                lst(t + lane * 16, m.v[r]);
                asm volatile("s_waitcnt lgkmcnt(0)" ::: "memory");
                const u32x2 lo = trr(t + (8 * g + q) * 32 + 8 * p), hi = trr(t + (8 * g + 4 + q) * 32 + 8 * p);
                f = u32x4{lo[0], lo[1], hi[0], hi[1]};
            }
            st128(c.rs, f, lane * 16, m.dst[r]);
            if (m.kind[r] != 2) {
                float cs = ((bflo(f[0]) + bfhi(f[0])) + (bflo(f[1]) + bfhi(f[1]))) + ((bflo(f[2]) + bfhi(f[2])) + (bflo(f[3]) + bfhi(f[3])));
                cs = sum_g(cs);
                if (lane < 16) st32(c.rs, asu(cs), lane * 4, m.cdst[r]);
            }
        }
    }
}

template <bool PROJ, bool NAT = false, class HK>
DI void g1_mma(const Ctx& c, const u32x4 (&xf)[9], f32x4 (&acc)[5], HK hook) {
    const int lane = c.lane, kh = c.wave >> 2, q = (lane & 15) >> 2, p = lane & 3, g = lane >> 4;
    LAS char *bN, *bR, *bNo, *bNt, *bRo, *bRt;
    if (PROJ) {
        if (NAT) { bN = c.L + L_R1 + (kh * 256 + 4 * g + q) * WP + 8 * p; bR = c.L + L_R1 + (512 + 32 * kh + 8 * g + q) * WP + 8 * p; }
        else { bN = c.L + L_A1 + (kh * 288 + 4 * g + q) * WP + 8 * p; bR = c.L + L_A1 + (kh * 288 + 72 * g + q) * WP + 8 * p; }
        bNo = bN + 64 * kh; bNt = bN + 64 - 64 * kh; bRo = bR + 64 * kh; bRt = bR + 64 - 64 * kh;
    } else {
        bN = c.L + L_R1 + kh * 8 * 5120 + lane * 16; bR = c.L + L_R1 + (16 + kh) * 5120 + lane * 16;
        bNo = bN + 2048 * kh; bNt = bN + 2048 - 2048 * kh; bRo = bR + 2048 * kh; bRt = bR + 2048 - 2048 * kh;
    }
    u32x4 w[2][5];
    auto rd = [&](int sl, u32x4 (&d)[5]) {
#pragma unroll
        for (int u = 0; u < 5; ++u) {
            if (PROJ) {
                const int r0 = NAT ? (sl < 8 ? 32 * sl : 0) : (sl < 8 ? 72 * (sl >> 1) + 32 * (sl & 1) : 64), hs = sl < 8 ? 16 : 4;
                LAS char* bb = u < 2 ? (sl < 8 ? bNo : bRo) + 32 * u : (u == 2 ? (sl < 8 ? bN : bR) + 128 : (sl < 8 ? bNt : bRt) + 32 * (u - 3));
                const u32x2 lo = trr(bb + r0 * WP), hi = trr(bb + (r0 + hs) * WP);
                d[u] = u32x4{lo[0], lo[1], hi[0], hi[1]};
            } else {
                LAS char* bb = u < 2 ? (sl < 8 ? bNo : bRo) + 1024 * u : (u == 2 ? (sl < 8 ? bN : bR) + 4096 : (sl < 8 ? bNt : bRt) + 1024 * (u - 3));
                d[u] = lld<u32x4>(bb + (sl < 8 ? sl * 5120 : 0));
            }
        }
    };
    rd(0, w[0]);
#pragma unroll
    for (int u = 0; u < 5; ++u) asm volatile("" : "+v"(acc[u]));
#pragma unroll
    for (int sl = 0; sl < 9; ++sl) {
        if (sl + 1 < 9) rd(sl + 1, w[(sl + 1) & 1]);
#pragma unroll
        for (int u = 0; u < 5; ++u) MF16(acc[u], w[sl & 1][u], xf[sl]);
        hook(sl, w[sl & 1]);
    }
    asm volatile("s_nop 7" : "+v"(acc[0]), "+v"(acc[1]), "+v"(acc[2]), "+v"(acc[3]), "+v"(acc[4]));
}
DI void g1_exchange(const Ctx& c, f32x4 (&acc)[5]) {
    LAS char* xw = c.L + L_XCH + c.wave * 3072 + c.lane * 16;
    lst(xw, acc[3]); lst(xw + 1024, acc[4]); lst(xw + 2048, acc[2]);
    BAR();
    LAS char* xr = c.L + L_XCH + (c.wave ^ 4) * 3072 + c.lane * 16;
    acc[0] += lld<f32x4>(xr); acc[1] += lld<f32x4>(xr + 1024); acc[2] += lld<f32x4>(xr + 2048);
}
DI void load_xfrags(const Ctx& c, u32 buf, u32x4 (&xf)[9]) {
    const int m = c.wave & 3, kh = c.wave >> 2;
    const u32 so = buf + (u32)(c.i * 18 * 4 + m) * 1024;
#pragma unroll
    for (int sl = 0; sl < 9; ++sl) { const int s = sl < 8 ? 8 * kh + sl : 16 + kh; xf[sl] = ld128(c.rs, c.lane * 16, so + (u32)s * 4096); }
}
DI u32x4 stats_issue(const Ctx& c, u32 stbuf) {
    const u32 vo = (u32)((c.i * 16 + 2 * (c.lane >> 3)) * 64 + c.wave * 8 + (c.lane & 7)) * 8;
    const u32x2 a = ld64(c.rs, vo, stbuf), b = ld64(c.rs, vo + 512, stbuf);
    return u32x4{a[0], a[1], b[0], b[1]};
}
DI void stats_finish(const Ctx& c, u32x4 v) {
    const int tok = c.wave * 8 + (c.lane & 7);
    float s = asf(v[0]) + asf(v[2]), s2 = asf(v[1]) + asf(v[3]);
    s += __shfl_xor(s, 8); s2 += __shfl_xor(s2, 8);
    s = sum_g(s); s2 = sum_g(s2);
    if (c.lane < 8) {
        lst(c.L + L_STATP + tok * 8, f32x2{s, s2}); lst(c.L + L_STATP + 512 + tok * 8, f32x2{0.f, 0.f});
        const float mean = s * (1.f / DM), var = s2 * (1.f / DM) - mean * mean;
        lst(c.L + L_MEAN + tok * 4, mean); lst(c.L + L_MEAN + 256 + tok * 4, __builtin_amdgcn_rsqf(fmaxf(var, 0.f) + 1e-5f));
    }
}
DI void load_stats(const Ctx& c, u32 stbuf) { stats_finish(c, stats_issue(c, stbuf)); }

struct KV { u32x4 km; u32x2 kr; u32x4 v0, v1; };
DI int vrow_of(int r) { return r < 18 ? r : 18; }
DI KV load_tile_ws(const Ctx& c, u32 tile) {
    const int r = c.lane & 31, h = c.lane >> 5;
    KV t;
    t.km = ld128(c.rs, c.lane * 16, tile);
    t.kr = ld64(c.rs, KT_KREM + r * 8, tile);
    t.v0 = ld128(c.rs, KT_V + h * KT_VBLK + vrow_of(r) * 16, tile);
    t.v1 = ld128(c.rs, KT_V + (2 + h) * KT_VBLK + vrow_of(r) * 16, tile);
    return t;
}
DI f32x16 mfma32k8(u32x2 a, u32x2 b, f32x16 c) { return __builtin_amdgcn_mfma_f32_32x32x8bf16_1k(__builtin_bit_cast(s16x4, a), __builtin_bit_cast(s16x4, b), c, 0, 0, 0); }
DI f32x16 qk(const KV& t, u32x4 qm, u32x2 qr, f32x16 c0) {
    return mfma32k8(t.kr, qr, mfma32(t.km, qm, c0));
}
DI void QK1(f32x16& s, u32x4 km, u32x4 qm) { asm volatile("v_mfma_f32_32x32x16_bf16 %0, %1, %2, 0" : "=&v"(s) : "v"(km), "v"(qm)); }
DI void QK1C(f32x16& s, u32x4 km, u32x4 qm, const f32x16& c0) { asm volatile("v_mfma_f32_32x32x16_bf16 %0, %1, %2, %3" : "=&v"(s) : "v"(km), "v"(qm), "v"(c0)); }
DI void QK2(f32x16& s, u32x2 kr, u32x2 qr) { asm volatile("v_mfma_f32_32x32x8_bf16 %0, %1, %2, %0" : "+v"(s) : "v"(kr), "v"(qr)); }
DI void PV(f32x16& o, u32x4 v, u32x4 p) { asm volatile("v_mfma_f32_32x32x16_bf16 %0, %1, %2, %0" : "+v"(o) : "v"(v), "v"(p)); }
DI float EX(float x) { float r; asm volatile("v_exp_f32 %0, %1" : "=v"(r) : "v"(x)); return r; }
DI u32 CV(float a, float b) { u32 r; asm volatile("v_cvt_pk_bf16_f32 %0, %1, %2" : "=v"(r) : "v"(a), "v"(b)); return r; }
struct Pipe { float e[4]; u32x4 p1; };
template <bool NEXT = true, class F0, class F1, class F2, class F3, class F4>
DI void attn_step(const f32x16& sc, f32x16& sn, f32x16& o, Pipe& pp, const u32x4& km, const u32x2& kr, const u32x4& v0, const u32x4& v1, u32x4 qm, u32x2 qr, F0 first, F1 LK, F2 LR, F3 LV1, F4 LV0) {
    u32x4 p0; float a0, a1, a2, a3, b0, b1, b2, b3;
    if (NEXT) first(sn, km, qm);
    LK();
    pp.p1[2] = CV(pp.e[0], pp.e[1]); pp.p1[3] = CV(pp.e[2], pp.e[3]);
    a0 = EX(sc[0]); a1 = EX(sc[1]); a2 = EX(sc[2]); a3 = EX(sc[3]);
    if (NEXT) QK2(sn, kr, qr); else asm volatile("s_nop 0");
    LR();
    p0[0] = CV(a0, a1); p0[1] = CV(a2, a3);
    b0 = EX(sc[4]); b1 = EX(sc[5]); b2 = EX(sc[6]); b3 = EX(sc[7]);
    PV(o, v1, pp.p1); LV1();
    p0[2] = CV(b0, b1); p0[3] = CV(b2, b3);
    a0 = EX(sc[8]); a1 = EX(sc[9]); a2 = EX(sc[10]); a3 = EX(sc[11]);
    PV(o, v0, p0); LV0();
    pp.p1[0] = CV(a0, a1); pp.p1[1] = CV(a2, a3);
    pp.e[0] = EX(sc[12]); pp.e[1] = EX(sc[13]); pp.e[2] = EX(sc[14]); pp.e[3] = EX(sc[15]);
}
template <class F1, class F2, class F3, class F4>
DI void attn_step2(const f32x16& scx, f32x16& snx, f32x16& ox, Pipe& px, const f32x16& scy, f32x16& sny, f32x16& oy, Pipe& py,
                   const u32x4& km, const u32x2& kr, const u32x4& v0, const u32x4& v1, u32x4 qmx, u32x2 qrx, u32x4 qmy, u32x2 qry, F1 LK, F2 LR, F3 LV1, F4 LV0) {
    u32x4 p0x, p0y; float a0, a1, a2, a3, b0, b1, b2, b3, c0, c1, c2, c3, d0, d1, d2, d3;
#define GRP(MF, PD0, PD1, E0, E1, E2, E3, D0, D1, D2, D3, SRC, I) MF; PD0 = CV(E0, E1); PD1 = CV(E2, E3); D0 = EX(SRC[I]); D1 = EX(SRC[I + 1]); D2 = EX(SRC[I + 2]); D3 = EX(SRC[I + 3])
    GRP(QK1(snx, km, qmx), px.p1[2], px.p1[3], px.e[0], px.e[1], px.e[2], px.e[3], a0, a1, a2, a3, scx, 0);
    GRP(QK1(sny, km, qmy); LK(), py.p1[2], py.p1[3], py.e[0], py.e[1], py.e[2], py.e[3], b0, b1, b2, b3, scy, 0);
    GRP(QK2(snx, kr, qrx), p0x[0], p0x[1], a0, a1, a2, a3, c0, c1, c2, c3, scx, 4);
    GRP(QK2(sny, kr, qry); LR(), p0y[0], p0y[1], b0, b1, b2, b3, d0, d1, d2, d3, scy, 4);
    GRP(PV(ox, v1, px.p1), p0x[2], p0x[3], c0, c1, c2, c3, a0, a1, a2, a3, scx, 8);
    GRP(PV(oy, v1, py.p1); LV1(), p0y[2], p0y[3], d0, d1, d2, d3, b0, b1, b2, b3, scy, 8);
    GRP(PV(ox, v0, p0x), px.p1[0], px.p1[1], a0, a1, a2, a3, px.e[0], px.e[1], px.e[2], px.e[3], scx, 12);
    GRP(PV(oy, v0, p0y); LV0(), py.p1[0], py.p1[1], b0, b1, b2, b3, py.e[0], py.e[1], py.e[2], py.e[3], scy, 12);
#undef GRP
}
DI void attn_finish(f32x16& o, Pipe& pp, u32x4 v1) {
    asm volatile("s_nop 0");
    pp.p1[2] = CV(pp.e[0], pp.e[1]); pp.p1[3] = CV(pp.e[2], pp.e[3]);
    asm volatile("s_nop 1");
    PV(o, v1, pp.p1);
    asm volatile("s_nop 7\n\ts_nop 3" : "+v"(o));
}
DI void attn_finish2(f32x16& ox, Pipe& px, f32x16& oy, Pipe& py, u32x4 v1) {
    asm volatile("s_nop 0");
    px.p1[2] = CV(px.e[0], px.e[1]); px.p1[3] = CV(px.e[2], px.e[3]);
    py.p1[2] = CV(py.e[0], py.e[1]); py.p1[3] = CV(py.e[2], py.e[3]);
    PV(ox, v1, px.p1);
    asm volatile("s_nop 0");
    PV(oy, v1, py.p1);
    asm volatile("s_nop 7\n\ts_nop 3" : "+v"(ox), "+v"(oy));
}
DI void pexp(const f32x16& s, u32x4& p0, u32x4& p1) {
    float e[16];
#pragma unroll
    for (int r = 0; r < 16; ++r) e[r] = __builtin_amdgcn_exp2f(s[r]);
    p0 = u32x4{pk(e[0], e[1]), pk(e[2], e[3]), pk(e[4], e[5]), pk(e[6], e[7])};
    p1 = u32x4{pk(e[8], e[9]), pk(e[10], e[11]), pk(e[12], e[13]), pk(e[14], e[15])};
}
DI float rowmax(const f32x16& s) {
    float a = fmaxf(fmaxf(s[0], s[1]), s[2]), b = fmaxf(fmaxf(s[3], s[4]), s[5]);
#pragma unroll
    for (int r = 6; r < 16; r += 2) { if (r & 2) a = fmaxf(fmaxf(a, s[r]), s[r + 1]); else b = fmaxf(fmaxf(b, s[r]), s[r + 1]); }
    return max_h(fmaxf(a, b));
}
DI f32x16 mask_c(int lane, bool diag) {
    const int r = lane & 31, h = lane >> 5; f32x16 m;
#pragma unroll
    for (int e = 0; e < 16; ++e) { const bool ok = diag ? (crow(e, h) <= r) : (crow(e, h) > r); m[e] = ok ? 0.f : NEG_BIG; }
    return m;
}
DI f32x16 attn_slow(const Ctx& c, u32 tile0, int n0, int n1, bool masked, u32x4 qm, u32x2 qr) {
    const int h = c.lane >> 5;
    qr[1] = 0u;
    f32x16 o = {}; float m = NEG_BIG;
    for (int n = n0; n <= n1; ++n) {
        const KV t = load_tile_ws(c, tile0 + (u32)n * TILE_B);
        f32x16 z = {};
        f32x16 s = qk(t, qm, qr, z);
        if (masked && (n == n1 || n == n1 - 8)) {
            const int r = c.lane & 31;
#pragma unroll
            for (int e = 0; e < 16; ++e) { const bool ok = (n == n1) ? (crow(e, h) <= r) : (crow(e, h) > r); s[e] = ok ? s[e] : NEG_BIG; }
        }
        const float mn = fmaxf(m, rowmax(s)), al = __builtin_amdgcn_exp2f(m - mn);
        m = mn;
#pragma unroll
        for (int e = 0; e < 16; ++e) { o[e] *= al; s[e] = s[e] <= NEG_BIG ? NEG_BIG : s[e] - mn; }
        u32x4 p0, p1; pexp(s, p0, p1);
        o = mfma32(t.v0, p0, o); o = mfma32(t.v1, p1, o);
    }
    return o;
}
DI bool attn_unsafe(const f32x16& o) {
    float z = 0.f;
#pragma unroll
    for (int e = 0; e < 10; ++e) z = __builtin_fmaf(o[e], 0.f, z);
    const float l = o[10];
    return __any(!(l > 0x1p-40f && l < 0x1p30f && z == 0.f));
}
DI void store_o(const Ctx& c, u32 obuf, const f32x16& o, int a, int t) {
    const int r = c.lane & 31, h = c.lane >> 5;
    const float inv = __builtin_amdgcn_rcpf(o[10]);
    const u32x4 mainp = {pk(o[0] * inv, o[1] * inv), pk(o[2] * inv, o[3] * inv), pk(o[4] * inv, o[5] * inv), pk(o[6] * inv, o[7] * inv)};
    const int m = 2 * t + (r >> 4), tok = r & 15;
    const u32 so = obuf + (u32)(c.i * 18 * 4) * 1024;
    st128(c.rs, mainp, (u32)((2 * c.j + (a >> 1)) * 4 + m) * 1024 + ((2 * (a & 1) + h) * 16 + tok) * 16, so);
    if (h == 0) st32(c.rs, pk(o[8] * inv, o[9] * inv), (u32)((16 + (c.j >> 2)) * 4 + m) * 1024 + ((c.j & 3) * 16 + tok) * 16 + 4 * a, so);
}

DI void a_proj(Ctx& c0, const Params& p, int rep) {
    Ctx c = fresh(c0);
    const int lane = c.lane, m = c.wave & 3, kh = c.wave >> 2, tok = lane & 15, g = lane >> 4;
    u32x4 xf[9]; f32x4 acc[5] = {};
    f32x4 s1 = {}, s2 = {}, ca = {}, cb = {};
    Fmt fm; u64 e0 = 0; u32x4 stv;
    if (rep == 0) {
        e0 = __hip_atomic_load((GAS u64*)(p.ws + WS_EPOCH), __ATOMIC_RELAXED, __HIP_MEMORY_SCOPE_AGENT);
        const char* xt = (const char*)p.x + (size_t)(c.i * 64) * (DM * 2);
#pragma unroll
        for (int n = 0; n < 8; ++n) {
            const int row = c.wave * 8 + n;
            glds_in(xt + row * (DM * 2) + (lane ^ (row & 15)) * 16, c.Lb + (row < 60 ? L_XIN + row * 1024 : L_XIN2 + (row - 60) * 1024));
        }
        xf[8] = *(const u32x4*)(xt + (m * 16 + tok) * (DM * 2) + (512 + 32 * kh + 8 * g) * 2);
        load_wimage_a(c, p, 0);
        STAMP(13);
        DRAIN(); BAR();
        STAMP(18);
        asm volatile("" : "+v"(xf[8]), "+v"(e0));
        fmt_issue(c, p, fm);
        { const int row = m * 16 + tok, t2 = tok ^ (g >> 1);
          LAS char* xb = c.L + (row < 60 ? L_XIN + row * 1024 : L_XIN2 + (row - 60) * 1024) + kh * 512 + 8 * (g & 1);
#pragma unroll
          for (int sl = 0; sl < 8; ++sl) {
              const int K = 4 * (sl & 3);
              const u32x2 lo = lld<u32x2>(xb + (sl >> 2) * 256 + ((K ^ t2) * 16)), hi = lld<u32x2>(xb + (sl >> 2) * 256 + (((K + 2) ^ t2) * 16));
              xf[sl] = u32x4{lo[0], lo[1], hi[0], hi[1]};
          } }
        STAMP(21);
        if (c.tid < 180) st128(c.rs, lld<u32x4>(c.L + L_R1 + c.i * 2880 + c.tid * 16), c.tid * 16, WS_WIMGA + (u32)c.j * WIMG_BYTES + (u32)c.i * 2880);
        BAR();
        STAMP(2);
    } else {
        wait_group(c, 5);
        load_xfrags(c, WS_XF + 1 * XF_BYTES, xf);
        stv = stats_issue(c, WS_ST + 1 * ST_BYTES);
        asm volatile("s_waitcnt vmcnt(11)" ::: "memory"); BAR();
        STAMP(32);
    }
    if (rep != 0) { g1_mma<true>(c, xf, acc, [](int, const u32x4 (&)[5]) {}); stats_finish(c, stv); }
    else {
        u32x4 ones = ones4();
        const int tlc = m < 2 ? 2 * kh + m : 2 * (1 - kh) + (m - 2), q = (lane & 15) >> 2, pp = lane & 3;
        LAS char* bN = c.L + L_R1 + (kh * 256 + 4 * g + q) * WP + 8 * pp + 32 * tlc;
        LAS char* bR = c.L + L_R1 + (512 + 32 * kh + 8 * g + q) * WP + 8 * pp + 32 * tlc;
        asm volatile("" : "+v"(s1), "+v"(s2), "+v"(ca), "+v"(cb), "+v"(ones));
        u32x4 wx[2];
        auto rdx = [&](int sl) {
            const int r0 = sl < 8 ? 32 * sl : 0, hs = sl < 8 ? 16 : 4; LAS char* b = sl < 8 ? bN : bR;
            const u32x2 lo = trr(b + r0 * WP), hi = trr(b + (r0 + hs) * WP);
            wx[sl & 1] = u32x4{lo[0], lo[1], hi[0], hi[1]}; };
        rdx(0);
        g1_mma<true, true>(c, xf, acc, [&](int sl, const u32x4 (&w)[5]) {
            if (sl + 1 < 9) rdx(sl + 1);
            MF16(s1, ones, xf[sl]); MF16(s2, xf[sl], xf[sl]); MF16(cb, w[2], ones); MF16(ca, wx[sl & 1], ones);
        });
        asm volatile("s_nop 7" : "+v"(s1), "+v"(s2), "+v"(ca), "+v"(cb));
        const int rg = tok & 3;
        float d = rg == 0 ? s2[0] : (rg == 1 ? s2[1] : (rg == 2 ? s2[2] : s2[3]));
        d = sum_g(g == (tok >> 2) ? d : 0.f);
        if (g == 0) lst(c.L + L_STATP + kh * 512 + (m * 16 + tok) * 8, f32x2{s1[0], d});
        const int tl = m < 2 ? 2 * kh + m : 2 * (1 - kh) + (m - 2);
        if (tok == 0) { lst(c.L + L_CSA + (kh * 5 + tl) * 64 + g * 16, ca); if (m == 0) lst(c.L + L_CSA + (kh * 5 + 4) * 64 + g * 16, cb); }
    }
    STAMP(3 + rep * 30);
    g1_exchange(c, acc);
    const f32x2 sa = lld<f32x2>(c.L + L_STATP + (m * 16 + tok) * 8), sb = lld<f32x2>(c.L + L_STATP + 512 + (m * 16 + tok) * 8);
    const float mean = (sa[0] + sb[0]) * (1.f / DM), var = (sa[1] + sb[1]) * (1.f / DM) - mean * mean;
    const float rstd = __builtin_amdgcn_rsqf(fmaxf(var, 0.f) + 1e-5f);
    LAS char* stg = c.L + L_STG + (m * 16 + tok) * STG_P + g * 8;
#pragma unroll
    for (int u = 0; u < 3; ++u) {
        if (u < 2 || kh == 1) {
            const int t = u < 2 ? 2 * kh + u : 4;
            f32x4 cs;
            cs = lld<f32x4>(c.L + L_CSA + t * 64 + g * 16) + lld<f32x4>(c.L + L_CSA + (5 + t) * 64 + g * 16);
            const float sc = (u < 2 || g < 2) ? rstd * QSCALE : rstd;
            f32x4 v;
#pragma unroll
            for (int e = 0; e < 4; ++e) v[e] = (acc[u][e] - mean * cs[e]) * sc;
            lst(stg + t * 32, u32x2{pk(v[0], v[1]), pk(v[2], v[3])});
        }
    }
    BAR();
    STAMP(4 + rep * 30);
    if (rep == 0) {
        fmt_finish(c, fm);
        const u64 e = e0 + 1; c0.elo = c.elo = __builtin_amdgcn_readfirstlane((u32)e); c0.ehi = c.ehi = __builtin_amdgcn_readfirstlane((u32)(e >> 32));
    }
    if (c.wave == 0) {
        const u32 kva = WS_KVA + rep * KVA_BYTES + (u32)(2 * c.i) * TILE_B;
        const int j = c.j;
        if (j < 2) {
            LAS char* s = c.L + L_STG + lane * STG_P + 144;
            const u32x2 lo = lld<u32x2>(s), hi = lld<u32x2>(s + 8);
            st128(c.rs, u32x4{lo[0], lo[1], hi[0], hi[1]}, (lane >> 5) * TILE_B + j * 512 + (lane & 31) * 16, kva);
        } else if (j == 2) {
            st64(c.rs, u32x2{lld<u32>(c.L + L_STG + lane * STG_P + 144 + 12), 0x00003f80u}, (lane >> 5) * TILE_B + KT_KREM + (lane & 31) * 8, kva);
        } else if (j < 6) {
            const int s4 = lane >> 4, q = (lane & 15) >> 2, pp = lane & 3;
            LAS char* s = c.L + L_STG + (16 * s4 + q + 4 * (pp >> 1)) * STG_P + 144 + 8 * (pp & 1);
            const u32x2 lo = trr(s), hi = trr(s + 8 * STG_P);
            const int dl = lane & 7, hh = (lane >> 3) & 1, drow = j < 5 ? 8 * (j - 3) + dl : 10 + dl;
            if (j < 5 || dl >= 6) st128(c.rs, u32x4{lo[0], lo[1], hi[0], hi[1]}, (s4 >> 1) * TILE_B + KT_V + ((s4 & 1) * 2 + hh) * KT_VBLK + drow * 16, kva);
        } else if (j == 6) {
            if (lane < 8) st128(c.rs, ones4(), (lane >> 2) * TILE_B + KT_V + (lane & 3) * KT_VBLK + 18 * 16, kva);
        }
        if (rep != 0) { DRAIN(); signal(c, 6); }
    }
    if (rep == 0) publish(c, 0);
}

DI void load_wring(const Ctx& c, u32x4 (&w0)[9], u32x4 (&w1)[9]) {
    const u32 wso = WS_WF + (u32)((c.j * 8 + c.wave) * 2) * 18 * 1024;
#pragma unroll
    for (int s = 0; s < 9; ++s) { w0[s] = ld128(c.rs, c.lane * 16, wso + s * 1024); w1[s] = ld128(c.rs, c.lane * 16, wso + (18 + s) * 1024); }
}
DI void colsums_b(const Ctx& c) {
    const int tok = c.lane & 15, g = c.lane >> 4;
    if (c.wave >= 1) {
        const int extra = c.wave == 1 ? 0 : c.wave;
        u32 v[4][5];
#pragma unroll
        for (int q = 0; q < 4; ++q) {
            const int unit = (q < 2 ? c.wave : extra) * 2 + (q & 1);
#pragma unroll
            for (int u = 0; u < 5; ++u) { const int s = g + 4 * u; v[q][u] = (q < 2 || c.wave == 1) ? ld32(c.rs, (s < 18 ? s : 0) * 64 + tok * 4, WS_CSP + (u32)((c.j * 16 + unit) * 18) * 64) : 0u; }
        }
#pragma unroll
        for (int q = 0; q < 4; ++q) {
            const int unit = (q < 2 ? c.wave : extra) * 2 + (q & 1);
            float cs = (asf(v[q][0]) + asf(v[q][1])) + (asf(v[q][2]) + asf(v[q][3])) + (g < 2 ? asf(v[q][4]) : 0.f);
            cs = sum_g(cs);
            if (g == 0 && (q < 2 || c.wave == 1)) lst(c.L + L_CSB + unit * 64 + tok * 4, cs);
        }
    }
}
struct ATail { f32x16 o; u32x4 qm; u32x2 qr; };
DI void a_attn(const Ctx& c0, const Params& p, int rep, u32 (&xres)[3][2], ATail& st) {
    const Ctx c = fresh(c0);
    const int lane = c.lane, r = lane & 31, h = lane >> 5, a = c.wave & 3, kh = c.wave >> 2;
    const int ph = rep * 6;
    const u32 kva = WS_KVA + rep * KVA_BYTES;
    u32x4 qmx, qmy; u32x2 qrx, qry;
    { LAS char* s = c.L + L_STG + r * STG_P + 36 * a;
      qmx = u32x4{lld<u32>(s + 16 * h), lld<u32>(s + 16 * h + 4), lld<u32>(s + 16 * h + 8), lld<u32>(s + 16 * h + 12)};
      const u32 rx = lld<u32>(s + 32);
      s += 32 * STG_P;
      qmy = u32x4{lld<u32>(s + 16 * h), lld<u32>(s + 16 * h + 4), lld<u32>(s + 16 * h + 8), lld<u32>(s + 16 * h + 12)};
      const u32 ry = lld<u32>(s + 32);
      qrx = u32x2{h == 0 ? rx : 0u, 0u}; qry = u32x2{h == 0 ? ry : 0u, 0u}; }
    const int ch0 = c.i >> 2;
    auto chunk_ids = [&](int n) { const int ch = (ch0 + n) & 7; return ch * 32 + (lane & 31); };
    auto issue_piece = [&](int n, int k) {
        const int ch = (ch0 + n) & 7; const u32 so = kva + (u32)ch * CHUNK_B; const u32 dst = c.Lb + L_RING + (n % 3) * CHUNK_B;
        glds_ws(c.srd, lane * 16, so + (c.wave + 4 * k) * 1024, dst + (c.wave + 4 * k) * 1024);
    };
    auto issue_chunk8 = [&](int n) {
        const int ch = (ch0 + n) & 7; const u32 so = kva + (u32)ch * CHUNK_B; const u32 dst = c.Lb + L_RING + (n % 3) * CHUNK_B;
        glds_ws(c.srd, lane * 16, so + c.wave * 1024, dst + c.wave * 1024);
        glds_ws(c.srd, lane * 16, so + (c.wave + 8) * 1024, dst + (c.wave + 8) * 1024);
        if (c.wave < 4) glds_ws(c.srd, lane * 16, so + (c.wave + 16) * 1024, dst + (c.wave + 16) * 1024);
    };
    if (c.wave == 0) poll(c, ph, lane < 32 ? chunk_ids(0) : chunk_ids(1));
    BAR();
    STAMP(6 + rep * 30);
    issue_chunk8(0);
    if (c.wave < 4) { issue_piece(1, 0); issue_piece(1, 1); issue_piece(1, 2); issue_piece(1, 3); issue_piece(1, 4); asm volatile("s_waitcnt vmcnt(5)" ::: "memory"); }
    else DRAIN();
    BAR();
    STAMP(7 + rep * 30); if (rep == 0) CSTAMP(61);
    if (rep == 1) { CSTAMPW(0, 4); CSTAMPW(8, 0); }
    if (rep == 0) {
        const int m = c.wave & 3, tok = lane & 15, g = lane >> 4;
        const char* rr = (const char*)p.x + (size_t)(c.i * 64 + m * 16 + tok) * (DM * 2) + (c.j * 72 + 4 * g) * 2;
        const u32x2 r0 = *(const u32x2*)(rr + (2 * kh) * 32), r1 = *(const u32x2*)(rr + (2 * kh + 1) * 32), r2 = *(const u32x2*)(rr + (g < 2 ? 128 : 0));
        xres[0][0] = r0[0]; xres[0][1] = r0[1]; xres[1][0] = r1[0]; xres[1][1] = r1[1]; xres[2][0] = r2[0]; xres[2][1] = r2[1];
    }
    if (rep == 0 && c.wave >= 4) {
        u32 z0 = 0u; asm volatile("" : "+v"(z0));
        const u32x4 z = {z0, z0, z0, z0};
#pragma unroll
        for (int n = 0; n < 5; ++n) {
            st128(c.rs, z, lane * 16 + n * 1024, WS_KVB + (u32)((4 * c.j + c.wave - 4) * 64 + 2 * c.i) * TILE_B);
        }
        if (c.wave < 6) st128(c.rs, z, (u32)((16 + (c.j >> 2)) * 4 + (lane >> 4)) * 1024 + ((c.j & 3) * 16 + (lane & 15)) * 16, WS_OF + (c.wave - 4) * XF_BYTES + (u32)(c.i * 72) * 1024);
    }
    const int vr = vrow_of(r);
    LAS char* kb = c.L + L_RING + lane * 16;
    LAS char* rb = c.L + L_RING + KT_KREM + r * 8;
    LAS char* vb = c.L + L_RING + KT_V + h * KT_VBLK + vr * 16;
    f32x16 sAx, sBx, sAy, sBy, ox, oy;
    const u32x4 k0m = lld<u32x4>(kb); const u32x2 k0r = lld<u32x2>(rb);
    kb += kh * 4 * TILE_B; rb += kh * 4 * TILE_B; vb += kh * 4 * TILE_B;
    const u32x4 kom = lld<u32x4>(kb); const u32x2 kor = lld<u32x2>(rb);
    u32x4 km = lld<u32x4>(kb + TILE_B), v0 = lld<u32x4>(vb), v1 = lld<u32x4>(vb + 2 * KT_VBLK); u32x2 kr = lld<u32x2>(rb + TILE_B);
    Pipe px, py;
    { u32 z = 0u; asm volatile("" : "+v"(z));
      px.e[0] = px.e[1] = px.e[2] = px.e[3] = asf(z); px.p1 = u32x4{z, z, z, z}; py = px; }
    asm volatile("" : "+v"(px.p1), "+v"(px.e[0]), "+v"(px.e[1]), "+v"(px.e[2]), "+v"(px.e[3]), "+v"(qmx), "+v"(qrx));
    asm volatile("s_nop 1" : "+v"(py.p1), "+v"(py.e[0]), "+v"(py.e[1]), "+v"(py.e[2]), "+v"(py.e[3]), "+v"(qmy), "+v"(qry));
    QK1(ox, px.p1, px.p1); QK1(oy, px.p1, px.p1);
    QK1(sBx, k0m, qmx); QK1(sBy, k0m, qmy); QK2(sBx, k0r, qrx); QK2(sBy, k0r, qry);
    QK1(sAx, kom, qmx); QK1(sAy, kom, qmy);
    asm volatile("s_nop 7\n\ts_nop 1" : "+v"(sBx), "+v"(sBy));
    { const float mx = rowmax(sBx), my = rowmax(sBy);
      if (h == 0) { qrx[1] = pk(-mx, 0.f); qry[1] = pk(-my, 0.f); } }
    asm volatile("s_nop 1" : "+v"(qrx), "+v"(qry));
    QK2(sAx, kor, qrx); QK2(sAy, kor, qry);
    asm volatile("s_nop 7\n\ts_nop 3" : "+v"(sAx), "+v"(sAy));
    if (rep == 1) { CSTAMPW(1, 4); CSTAMPW(9, 0); }
    int slot = 0;
    const u64 want = ((u64)c.ehi << 32) | c.elo;
    const u32 fvo = (lane & 31) * 128;
    u64 fl = want;
    if (c.wave == 0) flag_load(fl, ~want, fvo, c.srd, WS_FLAG + (u32)(ph * 256 + ((ch0 + 2) & 7) * 32) * 128);
#pragma unroll 1
    for (int n = 0; n < 8; ++n) {
        const int nslot = slot == 2 ? 0 : slot + 1;
        LAS char* kc = kb + slot * CHUNK_B; LAS char* rc = rb + slot * CHUNK_B; LAS char* vc = vb + slot * CHUNK_B;
        LAS char* kn = kb + nslot * CHUNK_B; LAS char* rn = rb + nslot * CHUNK_B; LAS char* vn = vb + nslot * CHUNK_B;
#define A_TILE(U, SCX, SNX, SCY, SNY, X1, X2) attn_step2(SCX, SNX, ox, px, SCY, SNY, oy, py, km, kr, v0, v1, qmx, qrx, qmy, qry, \
            [&] { km = lld<u32x4>((U) < 2 ? kc + ((U) + 2) * TILE_B : kn + ((U) - 2) * TILE_B); X1; }, \
            [&] { kr = lld<u32x2>((U) < 2 ? rc + ((U) + 2) * TILE_B : rn + ((U) - 2) * TILE_B); }, \
            [&] { v1 = lld<u32x4>(vc + (U) * TILE_B + 2 * KT_VBLK); X2; }, \
            [&] { v0 = lld<u32x4>((U) < 3 ? vc + ((U) + 1) * TILE_B : vn); })
        A_TILE(0, sAx, sBx, sAy, sBy, (void)0, (void)0); A_TILE(1, sBx, sAx, sBy, sAy, (void)0, (void)0);
        if (__builtin_expect(c.wave < 4, 0)) {
            asm volatile("s_waitcnt vmcnt(0)" : "+v"(fl) :: "memory");
            if (__builtin_expect(!__all(fl == want), 0)) poll(c, ph, chunk_ids(n + 2));
        }
        BAR();
        if (__builtin_expect(c.wave < 4, 0)) {
            fl = want;
            if (c.wave == 0 && n + 3 < 8) flag_load(fl, ~want, fvo, c.srd, WS_FLAG + (u32)(ph * 256 + ((ch0 + n + 3) & 7) * 32) * 128);
            if (n + 2 < 8) { issue_piece(n + 2, 0); issue_piece(n + 2, 1); issue_piece(n + 2, 2); issue_piece(n + 2, 3); issue_piece(n + 2, 4); }
            if (n >= 5) load_wimage_o4(c, 0, n - 5);
        }
        A_TILE(2, sAx, sBx, sAy, sBy, (void)0, (void)0); A_TILE(3, sBx, sAx, sBy, sAy, (void)0, (void)0);
#undef A_TILE
        slot = nslot;
    }
    if (rep == 1) { CSTAMPW(2, 4); CSTAMPW(10, 0); }
    attn_finish2(ox, px, oy, py, v1);
    { LAS char* mine = c.L + L_RING + kh * 2 * CHUNK_B + a * 3072 + lane * 16;
      const f32x16 give = kh ? ox : oy;
#pragma unroll
      for (int e = 0; e < 3; ++e) lst(mine + e * 1024, f32x4{give[4 * e], give[4 * e + 1], give[4 * e + 2], give[4 * e + 3]}); }
    st.o = kh ? oy : ox; st.qm = kh ? qmy : qmx; st.qr = kh ? qry : qrx;
}

template <bool ISB, class HK>
DI void out_proj(const Ctx& c0, const Params& p, int rep, u32 (&xres)[3][2], HK behind_x) {
    const Ctx c = fresh(c0);
    const int lane = c.lane, m = c.wave & 3, kh = c.wave >> 2, tok = lane & 15, g = lane >> 4;
    const int layer = rep * 2 + (ISB ? 1 : 0);
    wait_group(c, rep * 6 + (ISB ? 4 : 1));
    STAMP((ISB ? 25 : 10) + rep * 30);
    u32x4 xf[9]; f32x4 acc[5] = {};
    load_xfrags(c, WS_OF + (rep * RP * 2 + (ISB ? 1 : 0)) * XF_BYTES, xf);
    __builtin_amdgcn_sched_barrier(0);
    behind_x(c);
    if (ISB) {
        asm volatile("s_waitcnt vmcnt(9)" ::: "memory"); BAR();
    }
    g1_mma<false>(c, xf, acc, [](int, const u32x4 (&)[5]) {});
    STAMP((ISB ? 26 : 11) + rep * 30);
    g1_exchange(c, acc);
    STAMP((ISB ? 27 : 12) + rep * 30);
    float s1 = 0.f, s2 = 0.f;
#pragma unroll
    for (int u = 0; u < 3; ++u) {
        const float v0 = bflo(xres[u][0]) + acc[u][0], v1 = bfhi(xres[u][0]) + acc[u][1], v2 = bflo(xres[u][1]) + acc[u][2], v3 = bfhi(xres[u][1]) + acc[u][3];
        xres[u][0] = pk(v0, v1); xres[u][1] = pk(v2, v3);
        if (u < 2 || (kh == 1 && g < 2)) {
            const float a0 = bflo(xres[u][0]), a1 = bfhi(xres[u][0]), a2 = bflo(xres[u][1]), a3 = bfhi(xres[u][1]);
            s1 += (a0 + a1) + (a2 + a3); s2 += (a0 * a0 + a1 * a1) + (a2 * a2 + a3 * a3);
        }
    }
    if (ISB && rep == 1) {
        char* o = (char*)p.out + (size_t)(c.i * 64 + m * 16 + tok) * (DM * 2) + (c.j * 72 + 4 * g) * 2;
        *(u32x2*)(o + (2 * kh) * 32) = u32x2{xres[0][0], xres[0][1]};
        *(u32x2*)(o + (2 * kh + 1) * 32) = u32x2{xres[1][0], xres[1][1]};
        if (kh == 1 && g < 2) *(u32x2*)(o + 128) = u32x2{xres[2][0], xres[2][1]};
        return;
    }
    const int xi = layer == 0 ? 0 : (layer == 1 ? 1 : 2 * RP);
    const u32 so = WS_XF + xi * XF_BYTES + (u32)(c.i * 18 * 4 + m) * 1024;
    st128(c.rs, u32x4{xres[0][0], xres[0][1], xres[1][0], xres[1][1]}, lane * 16, so + (u32)(2 * c.j + kh) * 4096);
    if (kh == 1 && g < 2) st64(c.rs, u32x2{xres[2][0], xres[2][1]}, ((c.j & 3) * 16 + tok) * 16 + 8 * g, so + (u32)(16 + (c.j >> 2)) * 4096);
    s1 = sum_g(s1); s2 = sum_g(s2);
    if (g == 0) st64(c.rs, u32x2{__builtin_bit_cast(u32, s1), __builtin_bit_cast(u32, s2)}, (u32)((c.i * 16 + 2 * c.j + kh) * 64 + m * 16 + tok) * 8, WS_ST + xi * ST_BYTES);
    publish(c, rep * 6 + (ISB ? 5 : 2));
    if (!ISB && rep == 0) colsums_b(c);
    if (ISB) load_wimage_part<36, 90>(c, WS_WIMGA, 1, L_A1);
}

template <bool EARLY>
DI void a_tail(const Ctx& c0, const Params& p, int rep, ATail& st, u32 (&xres)[3][2], u32x4 (&w0)[9], u32x4 (&w1)[9]) {
    const Ctx c = fresh(c0);
    const int lane = c.lane, a = c.wave & 3, kh = c.wave >> 2;
    BAR();
    LAS char* theirs = c.L + L_RING + (1 - kh) * 2 * CHUNK_B + a * 3072 + lane * 16;
#pragma unroll
    for (int e = 0; e < 3; ++e) { const f32x4 v = lld<f32x4>(theirs + e * 1024); st.o[4 * e] += v[0]; st.o[4 * e + 1] += v[1]; st.o[4 * e + 2] += v[2]; st.o[4 * e + 3] += v[3]; }
    STAMP(8 + rep * 30); if (rep == 0) CSTAMP(62);
    if (rep == 1) { CSTAMPW(3, 4); CSTAMPW(11, 0); }
    if (attn_unsafe(st.o)) st.o = attn_slow(c, WS_KVA + rep * KVA_BYTES, 0, 63, false, st.qm, st.qr);
    store_o(c, WS_OF + (rep * RP * 2) * XF_BYTES, st.o, a, kh);
    if (rep == 1) { CSTAMPW(4, 4); CSTAMPW(12, 0); }
    publish(c, rep * 6 + 1);
    if (EARLY) load_wring(c, w0, w1);
    STAMP(9 + rep * 30);
    out_proj<false>(c, p, rep, xres, [&](const Ctx& cc) { if (!EARLY) load_wring(cc, w0, w1); });
}

struct T_ { static constexpr bool value = true; };
struct F_ { static constexpr bool value = false; };
DI void b_proj(const Ctx& c0, const Params& p, int rep, u32x4 (&w0)[9], u32x4 (&w1)[9], u32x4& qm, u32x2& qr) {
    const Ctx c = fresh(c0);
    const int lane = c.lane, tok = lane & 15, g = lane >> 4, a = c.wave >> 1, odd = c.wave & 1;
    wait_group(c, rep * 6 + 2);
    STAMP(15 + rep * 30);
    const int xi = rep == 0 ? 0 : 2 * RP;
    const u32x4 stv = stats_issue(c, WS_ST + xi * ST_BYTES);
    { const u32 so = WS_XF + xi * XF_BYTES + (u32)(c.i * 72) * 1024;
#pragma unroll
      for (int n = 0; n < 9; ++n) glds_ws(c.srd, lane * 16, so + (c.wave + 8 * n) * 1024, c.Lb + L_R1 + (c.wave + 8 * n) * 1024); }
    asm volatile("s_waitcnt vmcnt(5)" ::: "memory"); BAR();
    STAMP(16 + rep * 30);
    f32x4 a0[4] = {}, a1[4] = {};
    const u32 wso = WS_WF + (u32)((c.j * 8 + c.wave) * 2) * 18 * 1024;
    LAS char* xb = c.L + L_R1 + lane * 16;
    const bool t0 = !odd, t1 = !odd || a == 0, has1 = !odd || a < 2;
    auto body = [&](auto T0, auto T1, auto H1) {
        u32x4 x[2][4];
#pragma unroll
        for (int m = 0; m < 4; ++m) { x[0][m] = lld<u32x4>(xb + m * 1024); asm volatile("" : "+v"(a0[m]), "+v"(a1[m])); }
#pragma unroll
        for (int s = 0; s < 18; ++s) {
            if (s == 7) {
                if (H1.value) asm volatile("s_waitcnt vmcnt(14)" ::: "memory"); else asm volatile("s_waitcnt vmcnt(7)" ::: "memory");
                BAR();
            }
            if (s + 1 < 18) {
#pragma unroll
                for (int m = 0; m < 4; ++m) x[(s + 1) & 1][m] = lld<u32x4>(xb + ((s + 1) * 4 + m) * 1024);
            }
#pragma unroll
            for (int m = 0; m < 4; ++m) {
                if (T0.value) MF16(a0[m], w0[s % 9], x[s & 1][m]); else MF16(a0[m], x[s & 1][m], w0[s % 9]);
                if (H1.value) { if (T1.value) MF16(a1[m], w1[s % 9], x[s & 1][m]); else MF16(a1[m], x[s & 1][m], w1[s % 9]); }
            }
            if (s < 9) { w0[s] = ld128(c.rs, lane * 16, wso + (9 + s) * 1024); if (H1.value) w1[s] = ld128(c.rs, lane * 16, wso + (27 + s) * 1024); }
        }
        asm volatile("s_nop 7" : "+v"(a0[0]), "+v"(a0[1]), "+v"(a0[2]), "+v"(a0[3]), "+v"(a1[0]), "+v"(a1[1]), "+v"(a1[2]), "+v"(a1[3]));
    };
    if (!odd) body(T_{}, T_{}, T_{}); else if (a == 0) body(F_{}, T_{}, T_{}); else if (a == 1) body(F_{}, F_{}, T_{}); else body(F_{}, F_{}, F_{});
    (void)t0; (void)t1; (void)has1;
    STAMP(17 + rep * 30);
    stats_finish(c, stv);
    BAR();
    STAMP(23 + rep * 30);
    const u32 kvb = WS_KVB + rep * RP * KVB_BYTES;
    const u32 hb = kvb + (u32)((4 * c.j + a) * 64 + 2 * c.i) * TILE_B;
    auto fin_t = [&](const f32x4 (&acc)[4], int slot, float sc, u32 (&d)[4][2]) {
        const f32x4 cs = lld<f32x4>(c.L + L_CSB + (c.wave * 2 + slot) * 64 + g * 16);
#pragma unroll
        for (int m = 0; m < 4; ++m) {
            const float mean = lld<float>(c.L + L_MEAN + (m * 16 + tok) * 4), rs = lld<float>(c.L + L_MEAN + 256 + (m * 16 + tok) * 4) * sc;
            d[m][0] = pk((acc[m][0] - mean * cs[0]) * rs, (acc[m][1] - mean * cs[1]) * rs);
            d[m][1] = pk((acc[m][2] - mean * cs[2]) * rs, (acc[m][3] - mean * cs[3]) * rs);
        }
    };
    auto fin_p = [&](const f32x4 (&acc)[4], int slot, u32 (&d)[4][2]) {
        const float cs = lld<float>(c.L + L_CSB + (c.wave * 2 + slot) * 64 + tok * 4);
#pragma unroll
        for (int m = 0; m < 4; ++m) {
            const f32x4 mean = lld<f32x4>(c.L + L_MEAN + (m * 16 + 4 * g) * 4), rs = lld<f32x4>(c.L + L_MEAN + 256 + (m * 16 + 4 * g) * 4);
            d[m][0] = pk((acc[m][0] - mean[0] * cs) * rs[0], (acc[m][1] - mean[1] * cs) * rs[1]);
            d[m][1] = pk((acc[m][2] - mean[2] * cs) * rs[2], (acc[m][3] - mean[3] * cs) * rs[3]);
        }
    };
    auto frag16 = [&](const u32 (&d)[4][2], int t) {
        auto x = __builtin_amdgcn_permlane16_swap(d[2 * t][0], d[2 * t + 1][0], false, false);
        auto y = __builtin_amdgcn_permlane16_swap(d[2 * t][1], d[2 * t + 1][1], false, false);
        return u32x4{(u32)x[0], (u32)y[0], (u32)x[1], (u32)y[1]};
    };
    auto frag32 = [&](const u32 (&d)[4][2], int t) {
        auto x = __builtin_amdgcn_permlane32_swap(d[2 * t][0], d[2 * t + 1][0], false, false);
        auto y = __builtin_amdgcn_permlane32_swap(d[2 * t][1], d[2 * t + 1][1], false, false);
        return u32x4{(u32)x[0], (u32)y[0], (u32)x[1], (u32)y[1]};
    };
    u32 d[4][2];
    if (!odd) {
        fin_t(a1, 1, 1.f, d);
        st128(c.rs, frag16(d, 0), lane * 16, hb); st128(c.rs, frag16(d, 1), lane * 16, hb + TILE_B);
        fin_t(a0, 0, QSCALE, d);
        qm = frag16(d, 0);
        lst(c.L + L_QT1 + a * 1024 + lane * 16, frag16(d, 1));
    } else {
        fin_p(a0, 0, d);
        st128(c.rs, frag32(d, 0), KT_V + g * KT_VBLK + tok * 16, hb); st128(c.rs, frag32(d, 1), KT_V + g * KT_VBLK + tok * 16, hb + TILE_B);
        if (a == 0) {
            fin_t(a1, 1, g < 2 ? QSCALE : 1.f, d);
#pragma unroll
            for (int m = 0; m < 4; ++m) {
                if (g < 2) { lst(c.L + L_QREM + (2 * g) * 256 + (m * 16 + tok) * 4, d[m][0]); lst(c.L + L_QREM + (2 * g + 1) * 256 + (m * 16 + tok) * 4, d[m][1]); }
                else {
                    const u32 hb0 = kvb + (u32)(4 * c.j * 64 + 2 * c.i + (m >> 1)) * TILE_B;
                    st64(c.rs, u32x2{d[m][0], 0x00003f80u}, (u32)(2 * (g - 2)) * 64 * TILE_B + KT_KREM + ((m & 1) * 16 + tok) * 8, hb0);
                    st64(c.rs, u32x2{d[m][1], 0x00003f80u}, (u32)(2 * (g - 2) + 1) * 64 * TILE_B + KT_KREM + ((m & 1) * 16 + tok) * 8, hb0);
                }
            }
        } else if (a == 1) {
            fin_p(a1, 1, d);
            const u32 hb0 = kvb + (u32)(4 * c.j * 64 + 2 * c.i) * TILE_B;
            const u32 vo = (u32)(tok >> 1) * 64 * TILE_B + KT_V + g * KT_VBLK + (16 + (tok & 1)) * 16;
            const u32x4 f0 = frag32(d, 0), f1 = frag32(d, 1);
            if (tok < 8) { st128(c.rs, f0, vo, hb0); st128(c.rs, f1, vo, hb0 + TILE_B); }
        } else if (a == 2) {
            const u32 hb0 = kvb + (u32)(4 * c.j * 64 + 2 * c.i) * TILE_B;
            if (lane < 32) st128(c.rs, ones4(),
                                 (u32)(lane >> 3) * 64 * TILE_B + ((lane >> 2) & 1) * TILE_B + KT_V + (lane & 3) * KT_VBLK + 18 * 16, hb0);
        }
    }
    publish(c, rep * 6 + 3);
    load_wimage_part<44, 90>(c, WS_WIMG + 8 * WIMG_BYTES, 1);
    if (odd) qm = lld<u32x4>(c.L + L_QT1 + a * 1024 + lane * 16);
    const u32 rem = lld<u32>(c.L + L_QREM + a * 256 + (32 * odd + (lane & 31)) * 4);
    qr = u32x2{lane < 32 ? rem : 0u, 0u};
}

DI void b_attn(const Ctx& c0, const Params& p, int rep, u32x4 qm, u32x2 qr) {
    const Ctx c = fresh(c0);
    const int lane = c.lane, r = lane & 31, h = lane >> 5, a = c.wave >> 1, t = c.wave & 1;
    const int qt = 2 * c.i + t;
    const f32x16 cd = mask_c(lane, true);
    if (c.wave == 1) {
        const f32x16 cm = mask_c(lane, false);
#pragma unroll
        for (int e = 0; e < 4; ++e) lst(c.L + L_MSK + e * 1024 + lane * 16, f32x4{cm[4 * e], cm[4 * e + 1], cm[4 * e + 2], cm[4 * e + 3]});
    }
    if (c.wave == 0) poll(c, rep * 6 + 3, (lane < 5 && c.i - lane >= 0) ? (c.i - lane) * 8 + c.j : -1);
    BAR();
    STAMP(20 + rep * 30);
    const u32 hb = WS_KVB + rep * RP * KVB_BYTES + (u32)((4 * c.j + a) * 64) * TILE_B;
    { const u32 dst = c.Lb + L_KVB + a * 25600;
#pragma unroll
      for (int n = 0; n < 13; ++n) {
          const int pc = n < 8 ? 24 - t - 2 * n : 9 - t - 2 * (n - 8);
          if (n != 7 || t == 0) {
              u32 vo = lane * 16;
              if (c.i < 4) { const int b = pc * 1024 + lane * 16, tau = b / 2560, kt = 2 * c.i - 8 + tau; vo = (u32)((kt < 0 ? 0 : kt) * 2560 + (b - tau * 2560)); glds_ws(c.srd, vo, hb, dst + pc * 1024); }
              else glds_ws(c.srd, vo, hb + (u32)(2 * c.i - 8) * 2560 + pc * 1024, dst + pc * 1024);
          }
      } }
    asm volatile("s_waitcnt vmcnt(5)" ::: "memory");
    BAR();
    STAMP(21 + rep * 30);
    LAS char* tb = c.L + L_KVB + a * 25600 + (8 + t) * TILE_B;
    LAS char* kb = tb + lane * 16; LAS char* rb = tb + KT_KREM + r * 8; LAS char* vb = tb + KT_V + h * KT_VBLK + vrow_of(r) * 16;
    const u32x4 k0m = lld<u32x4>(kb); const u32x2 k0r = lld<u32x2>(rb);
    u32x4 km = lld<u32x4>(kb - TILE_B), v0 = lld<u32x4>(vb), v1 = lld<u32x4>(vb + 2 * KT_VBLK); u32x2 kr = lld<u32x2>(rb - TILE_B);
    f32x16 sA, sB, o;
    Pipe pp;
    { u32 z = 0u; asm volatile("" : "+v"(z)); pp.e[0] = pp.e[1] = pp.e[2] = pp.e[3] = asf(z); pp.p1 = u32x4{z, z, z, z}; }
    f32x16 cdp = cd;
    asm volatile("s_nop 1" : "+v"(pp.p1), "+v"(pp.e[0]), "+v"(pp.e[1]), "+v"(pp.e[2]), "+v"(pp.e[3]), "+v"(qm), "+v"(qr), "+v"(cdp));
    QK1(o, pp.p1, pp.p1);
    QK1C(sA, k0m, qm, cdp);
    asm volatile("v_mfma_f32_32x32x8_bf16 %0, %1, %2, %3" : "=&v"(sB) : "v"(k0r), "v"(qr), "v"(sA));
    asm volatile("s_nop 7\n\ts_nop 3" : "+v"(sB), "+v"(sA));
    { const float m0 = rowmax(sB); if (h == 0) qr[1] = pk(-m0, 0.f); }
    asm volatile("s_nop 1" : "+v"(qr));
    QK2(sA, k0r, qr);
    asm volatile("s_nop 7\n\ts_nop 3" : "+v"(sA));
    auto first = [](f32x16& s, u32x4 k, u32x4 q) { QK1(s, k, q); };
    auto firstm = [&](f32x16& s, u32x4 k, u32x4 q) {
        f32x16 co;
#pragma unroll
        for (int e = 0; e < 4; ++e) { const f32x4 v = lld<f32x4>(c.L + L_MSK + e * 1024 + lane * 16); co[4 * e] = v[0]; co[4 * e + 1] = v[1]; co[4 * e + 2] = v[2]; co[4 * e + 3] = v[3]; }
        QK1C(s, k, q, co); };
    const u32 qoff = h == 0 ? pk(NEG_BIG, 0.f) : 0u;
    kb -= 8 * TILE_B; rb -= 8 * TILE_B; vb -= 8 * TILE_B;
#define B_TILE(U, SC, SN, FIRST) do { u32x2 qs = {qr[0], (U) + 1 <= qt ? qr[1] : qoff}; asm volatile("" : "+v"(qs)); \
        attn_step<((U) < 8)>(SC, SN, o, pp, km, kr, v0, v1, qm, qs, FIRST, \
            [&] { if ((U) + 2 <= 8) km = lld<u32x4>(kb + (8 - ((U) + 2)) * TILE_B); }, [&] { if ((U) + 2 <= 8) kr = lld<u32x2>(rb + (8 - ((U) + 2)) * TILE_B); }, \
            [&] { v1 = lld<u32x4>(vb + (8 - (U)) * TILE_B + 2 * KT_VBLK); }, [&] { if ((U) + 1 <= 8) v0 = lld<u32x4>(vb + (8 - ((U) + 1)) * TILE_B); }); } while (0)
    B_TILE(0, sA, sB, first); B_TILE(1, sB, sA, first);
    DRAIN();
    B_TILE(2, sA, sB, first);
    BAR();
    B_TILE(3, sB, sA, first); B_TILE(4, sA, sB, first); B_TILE(5, sB, sA, first); B_TILE(6, sA, sB, first); B_TILE(7, sB, sA, firstm); B_TILE(8, sA, sB, first);
#undef B_TILE
    attn_finish(o, pp, v1);
    STAMP(22 + rep * 30);
    if (attn_unsafe(o)) o = attn_slow(c, hb, qt - 8 < 0 ? 0 : qt - 8, qt, true, qm, qr);
    store_o(c, WS_OF + (rep * RP * 2 + 1) * XF_BYTES, o, a, t);
    publish(c, rep * 6 + 4);
    load_wimage_part<0, 44>(c, WS_WIMG + 8 * WIMG_BYTES, 1);
    if (rep == 0) load_wimage_part<0, 36>(c, WS_WIMGA, 1, L_A1);
}

__global__ void __launch_bounds__(512, 2) fused_block_kernel(Params p) {
    __shared__ __attribute__((aligned(16))) char lds_[LDS_BYTES];
    Ctx c;
    c.L = (LAS char*)lds_; c.Lb = (u32)(size_t)c.L;
    c.ws = p.ws;
    c.tid = threadIdx.x; c.lane = c.tid & 63; c.wave = __builtin_amdgcn_readfirstlane(c.tid >> 6);
    { const int wg = blockIdx.x, slot = wg >> 3; c.i = (wg & 7) * 4 + (slot >> 3); c.j = slot & 7; }
    c.rs = __builtin_amdgcn_make_buffer_rsrc((void*)p.ws, 0, WS_USED, 0x00020000);
    { const u64 b = (u64)p.ws; c.srd = u32x4{(u32)b, (u32)(b >> 32) & 0xffffu, WS_USED, 0x00020000u}; }
    STAMP(0);
    u32 warm;
    { u64 pc; u32 len;
      asm volatile("s_getpc_b64 %0\nfbk_code_first:\n\ts_mov_b32 %1, fbk_code_last-fbk_code_first" : "=s"(pc), "=s"(len));
      u32 off = ((u32)(blockIdx.x >> 3) * 8 + c.wave) * 256 + c.lane * 4;
      off = off < len ? off : 0u;
      warm = *(const GAS u32*)((pc & ~3ull) + off); }
    c.elo = 0; c.ehi = 0;
    u32 xres[3][2];
#pragma unroll 1
    for (int rep = 0; rep < 2; ++rep) {
        STAMP(1 + rep * 30);
        a_proj(c, p, rep);
        if (rep == 0) asm volatile("" :: "v"(warm));
        STAMP(5 + rep * 30);
        u32x4 w0[9], w1[9]; ATail st;
        a_attn(c, p, rep, xres, st);
        if (rep == 0) {
            if (blockIdx.x == 0 && c.tid == 0) __hip_atomic_store((GAS u64*)(p.ws + WS_EPOCH), ((u64)c.ehi << 32) | c.elo, __ATOMIC_RELAXED, __HIP_MEMORY_SCOPE_AGENT);
        }
        if (c.wave >= 1 && c.wave < 4) a_tail<true>(c, p, rep, st, xres, w0, w1); else a_tail<false>(c, p, rep, st, xres, w0, w1);
        STAMP(14 + rep * 30);
        u32x4 qm; u32x2 qr;
        b_proj(c, p, rep, w0, w1, qm, qr);
        STAMP(19 + rep * 30);
        b_attn(c, p, rep, qm, qr);
        STAMP(24 + rep * 30);
        out_proj<true>(c, p, rep, xres, [](const Ctx&) {});
        STAMP(29 + rep * 30);
    }
    asm volatile("fbk_code_last:");
}

extern "C" void kernel_launch(void* const* d_in, const int* in_sizes, int n_in, void* d_out, int out_size, void* d_ws, size_t ws_size, hipStream_t stream) {
    Params p;
    p.x = (const u16*)d_in[0]; p.wq = (const u16*)d_in[1]; p.wk = (const u16*)d_in[2]; p.wv = (const u16*)d_in[3]; p.woA = (const u16*)d_in[4];
    p.wqkv = (const u16*)d_in[6]; p.woB = (const u16*)d_in[7]; p.out = (u16*)d_out;
    p.ws = (char*)d_ws + ((ws_size - WS_USED) & ~(size_t)0x1fffff);
    fused_block_kernel<<<256, 512, 0, stream>>>(p);
}
```

```cpp
#include <hip/hip_runtime.h>
#include <hip/hip_bf16.h>

typedef unsigned short u16;
typedef unsigned u32;
typedef unsigned long long u64;
typedef __bf16 bf16x8 __attribute__((ext_vector_type(8)));
typedef __bf16 bf16x2 __attribute__((ext_vector_type(2)));
typedef short s16x4 __attribute__((ext_vector_type(4)));
typedef float f32x2 __attribute__((ext_vector_type(2)));
typedef float f32x4 __attribute__((ext_vector_type(4)));
typedef float f32x16 __attribute__((ext_vector_type(16)));
typedef u32 u32x2 __attribute__((ext_vector_type(2)));
typedef u32 u32x4 __attribute__((ext_vector_type(4)));
#define LAS __attribute__((address_space(3)))
#define GAS __attribute__((address_space(1)))
#define DI __device__ __forceinline__

constexpr int SEQ = 2048, DM = 576;
constexpr int RP = 0;
constexpr u32 TILE_B = 2560;
constexpr u32 KT_V = 1024, KT_VBLK = 304, KT_KREM = 2240;
constexpr u32 XF_BYTES = 32u * 18 * 4 * 1024;
constexpr u32 WS_EPOCH = 0;
constexpr u32 WS_FLAG = 4096;
constexpr int PH_FMT = 11;
constexpr u32 WS_XF = WS_FLAG + 12u * 256 * 128 + 4096;
constexpr u32 WS_OF = WS_XF + (2 + RP) * XF_BYTES;
constexpr u32 ST_BYTES = 2048u * 16 * 8;
constexpr u32 WS_ST = WS_OF + (2 + 2 * RP) * XF_BYTES;
constexpr u32 KVA_BYTES = 64 * TILE_B;
constexpr u32 WS_KVA = WS_ST + (2 + RP) * ST_BYTES;
constexpr u32 KVB_BYTES = 32 * 64 * TILE_B;
constexpr u32 WS_KVB = WS_KVA + 2 * KVA_BYTES;
constexpr u32 WF_BYTES = 8u * 16 * 18 * 1024;
constexpr u32 WS_WF = WS_KVB + (1 + RP) * KVB_BYTES;
constexpr u32 WS_CSP = WS_WF + WF_BYTES;
constexpr u32 WIMG_BYTES = 90 * 1024;
constexpr u32 WS_WIMG = WS_CSP + 8u * 16 * 18 * 64;
constexpr u32 WS_WIMGA = WS_WIMG + 16 * WIMG_BYTES;
constexpr u32 WS_STAMP = WS_WIMGA + 8 * WIMG_BYTES;
constexpr u32 WS_STAMP2 = WS_STAMP + 256u * 64 * 8 + 256u * 8 * 8 * 8;
constexpr u32 WS_USED = WS_STAMP2 + 256u * 16 * 8;
constexpr int L_XCH = 0;
constexpr int L_RING = 0;
constexpr int CHUNK_B = 8 * TILE_B;
constexpr int L_STG = 40960;
constexpr int STG_P = 168;
constexpr int L_XIN = 0;
constexpr int L_XIN2 = 153600;
constexpr int L_FMT = 24576;
constexpr int L_KVB = 0;
constexpr int L_MSK = 102400;
constexpr int L_R1 = 61440;
constexpr int WP = 160;
constexpr int L_A1 = 24576;
constexpr int L_STATP = 153600;
constexpr int L_MEAN = L_STATP + 1024;
constexpr int L_CSA = L_MEAN + 512;
constexpr int L_CSB = L_CSA + 1024;
constexpr int L_QREM = L_CSB + 1024;
constexpr int L_QT1 = L_QREM + 1024;
constexpr int LDS_BYTES = 163840;
static_assert(L_QT1 + 4096 <= LDS_BYTES, "lds");

constexpr float QSCALE = 0.23570226039551584f * 1.4426950408889634f;
constexpr float NEG_BIG = -1e30f;
constexpr u32 SPIN_CAP = 1u << 20;
constexpr int AUX_SC1 = 16;

struct Params { const u16* x; const u16* wq; const u16* wk; const u16* wv; const u16* woA; const u16* wqkv; const u16* woB; u16* out; char* ws; };

DI u32 pk(float a, float b) { f32x2 v = {a, b}; return __builtin_bit_cast(u32, __builtin_convertvector(v, bf16x2)); }
DI float asf(u32 d) { return __builtin_bit_cast(float, d); }
DI u32 asu(float f) { return __builtin_bit_cast(u32, f); }
DI float bflo(u32 d) { return __builtin_bit_cast(float, d << 16); }
DI float bfhi(u32 d) { return __builtin_bit_cast(float, d & 0xffff0000u); }
DI bf16x8 asb(u32x4 v) { return __builtin_bit_cast(bf16x8, v); }
DI f32x4 mfma16(u32x4 a, u32x4 b, f32x4 c) { return __builtin_amdgcn_mfma_f32_16x16x32_bf16(asb(a), asb(b), c, 0, 0, 0); }
DI f32x16 mfma32(u32x4 a, u32x4 b, f32x16 c) { return __builtin_amdgcn_mfma_f32_32x32x16_bf16(asb(a), asb(b), c, 0, 0, 0); }
DI void MF16(f32x4& acc, u32x4 a, u32x4 b) { asm volatile("v_mfma_f32_16x16x32_bf16 %0, %1, %2, %0" : "+v"(acc) : "v"(a), "v"(b)); }
#define SETTLE4(x) asm volatile("s_nop 7" : "+v"(x))
template <class T> DI T lld(LAS char* p) { return *(LAS const T*)p; }
template <class T> DI void lst(LAS char* p, T v) { *(LAS T*)p = v; }
DI u32x2 trr(LAS char* p) { return __builtin_bit_cast(u32x2, __builtin_amdgcn_ds_read_tr16_b64_v4i16((LAS s16x4*)p)); }
#define BAR() asm volatile("s_waitcnt lgkmcnt(0)\n\ts_barrier" ::: "memory")
#define DRAIN() asm volatile("s_waitcnt vmcnt(0)" ::: "memory")
typedef __amdgpu_buffer_rsrc_t rsrc_t;
DI u32x4 ld128(rsrc_t rs, u32 voff, u32 soff) { return __builtin_amdgcn_raw_buffer_load_b128(rs, voff, soff, AUX_SC1); }
DI u32x2 ld64(rsrc_t rs, u32 voff, u32 soff) { return __builtin_amdgcn_raw_buffer_load_b64(rs, voff, soff, AUX_SC1); }
DI u32 ld32(rsrc_t rs, u32 voff, u32 soff) { return __builtin_amdgcn_raw_buffer_load_b32(rs, voff, soff, AUX_SC1); }
DI void st128(rsrc_t rs, u32x4 v, u32 voff, u32 soff) { __builtin_amdgcn_raw_buffer_store_b128(v, rs, voff + soff, 0, AUX_SC1); }
DI void st64(rsrc_t rs, u32x2 v, u32 voff, u32 soff) { __builtin_amdgcn_raw_buffer_store_b64(v, rs, voff, soff, AUX_SC1); }
DI void st32(rsrc_t rs, u32 v, u32 voff, u32 soff) { __builtin_amdgcn_raw_buffer_store_b32(v, rs, voff, soff, AUX_SC1); }
DI void glds_ws(u32x4 srd, u32 voff, u32 soff, u32 ldsdst) {
    soff = __builtin_amdgcn_readfirstlane(soff); ldsdst = __builtin_amdgcn_readfirstlane(ldsdst);
    asm volatile("s_mov_b32 m0, %0\n\ts_nop 3\n\tbuffer_load_dwordx4 %1, %2, %3 offen sc1 lds" :: "s"(ldsdst), "v"(voff), "s"(srd), "s"(soff) : "memory");
}
DI void glds_in(const void* g, u32 ldsdst) {
    ldsdst = __builtin_amdgcn_readfirstlane(ldsdst);
    asm volatile("s_mov_b32 m0, %0\n\ts_nop 0\n\tglobal_load_lds_dwordx4 %1, off" :: "s"(ldsdst), "v"(g) : "memory");
}
DI void flag_load(u64& fl, u64 nomatch, u32 voff, u32x4 srd, u32 soff) {
    soff = __builtin_amdgcn_readfirstlane(soff);
    fl = nomatch;
    asm volatile("s_nop 4\n\tbuffer_load_dwordx2 %0, %1, %2, %3 offen sc1" : "+v"(fl) : "v"(voff), "s"(srd), "s"(soff) : "memory");
}
DI float sum_g(float v) {
    auto a = __builtin_amdgcn_permlane32_swap(__builtin_bit_cast(u32, v), __builtin_bit_cast(u32, v), false, false);
    v = __builtin_bit_cast(float, (u32)a[0]) + __builtin_bit_cast(float, (u32)a[1]);
    auto b = __builtin_amdgcn_permlane16_swap(__builtin_bit_cast(u32, v), __builtin_bit_cast(u32, v), false, false);
    return __builtin_bit_cast(float, (u32)b[0]) + __builtin_bit_cast(float, (u32)b[1]);
}
DI float max_h(float v) {
    auto a = __builtin_amdgcn_permlane32_swap(__builtin_bit_cast(u32, v), __builtin_bit_cast(u32, v), false, false);
    return fmaxf(__builtin_bit_cast(float, (u32)a[0]), __builtin_bit_cast(float, (u32)a[1]));
}
DI u32x4 ones4() { u32 o = 0x3f803f80u; asm volatile("" : "+v"(o)); return u32x4{o, o, o, o}; }
DI int crow(int reg, int h) { return (reg & 3) + 8 * (reg >> 2) + 4 * h; }

#define STAMP(n) do {} while (0)
#define CSTAMP(n) do {} while (0)
#define CSTAMPW(n, W) do {} while (0)

struct Ctx {
    LAS char* L; u32 Lb;
    char* ws; rsrc_t rs; u32x4 srd;
    int tid, lane, wave, i, j;
    u32 elo, ehi;
};

DI Ctx fresh(const Ctx& c0) {
    Ctx c = c0;
    asm volatile("" : "+v"(c.lane)); asm volatile("" : "+v"(c.tid));
    asm volatile("" : "+s"(c.wave)); asm volatile("" : "+s"(c.i)); asm volatile("" : "+s"(c.j));
    return c;
}
DI GAS u64* flag_ptr(const Ctx& c, int phase, int id) { return (GAS u64*)(c.ws + WS_FLAG + (u32)(phase * 256 + id) * 128); }
DI void signal(const Ctx& c, int phase) {
    if (c.tid == 0) __hip_atomic_store(flag_ptr(c, phase, c.i * 8 + c.j), ((u64)c.ehi << 32) | c.elo, __ATOMIC_RELAXED, __HIP_MEMORY_SCOPE_AGENT);
}
DI void publish(const Ctx& c, int phase) {
    DRAIN();
    BAR();
    signal(c, phase);
}
DI void poll(const Ctx& c, int phase, int id) {
    const u64 want = ((u64)c.ehi << 32) | c.elo;
    GAS u64* f = flag_ptr(c, phase, id < 0 ? 0 : id);
    for (u32 spins = 0; spins < SPIN_CAP; ++spins) {
        const u64 v = __hip_atomic_load(f, __ATOMIC_RELAXED, __HIP_MEMORY_SCOPE_AGENT);
        if (__all(id < 0 || v == want)) break;
        __builtin_amdgcn_s_sleep(1);
    }
    __builtin_amdgcn_fence(__ATOMIC_ACQUIRE, "wavefront");
}
DI void wait_group(const Ctx& c, int phase) {
    if (c.wave == 0) poll(c, phase, c.lane < 8 ? c.i * 8 + c.lane : -1);
    BAR();
}

DI void load_wimage_a(const Ctx& c, const Params& p, int w0) {
    if (c.wave < w0) return;
    const int jj = c.j, nw = 8 - w0, g0 = c.wave - w0;
    const u16 *pk_ = p.wk, *pv_ = p.wv;
    asm volatile("" : "+s"(pk_), "+s"(pv_));
    const char* xsrc = (const char*)((jj >= 3 && jj < 6) ? pv_ : pk_); const int sub = jj < 6 ? jj % 3 : 0;
    xsrc += sub == 0 ? 0 : (sub == 1 ? 16 : 20);
    const char* msrc = (const char*)p.wq + jj * 144;
    const char* a[5]; u32 st[5];
#pragma unroll
    for (int k = 0; k < 5; ++k) {
        const u32 id = 64 * k + c.lane, row = (id * 6554u) >> 16, ch = id - row * 10;
        const bool ex = ch == 9;
        const u32 s1 = ex ? 32 * 36 : 32 * 1152;
        st[k] = s1 * nw;
        a[k] = (ex ? xsrc : msrc) + (ex ? row * 36 : row * 1152 + ch * 16) + (u32)g0 * s1;
    }
#pragma unroll
    for (int n = 0; n < 3; ++n) {
        const int G = g0 + nw * n;
        if (G < 18) {
#pragma unroll
            for (int k = 0; k < 5; ++k) { glds_in(a[k], c.Lb + L_R1 + (G * 5 + k) * 1024); a[k] += st[k]; }
        }
    }
}
DI void load_wimage_o(const Ctx& c, u32 base) {
    const u32 so = base + (u32)c.j * WIMG_BYTES;
    if (c.wave >= 1) {
#pragma unroll
        for (int n = 0; n < 13; ++n)
            if (n < 12 || c.wave < 7) glds_ws(c.srd, c.lane * 16, so + (c.wave - 1 + 7 * n) * 1024, c.Lb + L_R1 + (c.wave - 1 + 7 * n) * 1024);
    }
}
template <int P0, int P1>
DI void load_wimage_part(const Ctx& c, u32 base, int w0, int ldsbase = L_R1) {
    const u32 so = base + (u32)c.j * WIMG_BYTES; const int nw = 8 - w0;
    if (c.wave >= w0) {
#pragma unroll
        for (int n = 0; n < (P1 - P0 + 6) / 7; ++n) {
            const int pc = P0 + (c.wave - w0) + nw * n;
            if (pc < P1) glds_ws(c.srd, c.lane * 16, so + pc * 1024, c.Lb + ldsbase + pc * 1024);
        }
    }
}
DI void load_wimage_o4(const Ctx& c, int which, int part) {
    const u32 so = WS_WIMG + (u32)(which * 8 + c.j) * WIMG_BYTES + (u32)part * 30 * 1024; const u32 dst = c.Lb + L_R1 + part * 30 * 1024;
    if (c.wave < 4) {
#pragma unroll
        for (int n = 0; n < 8; ++n)
            if (n < 7 || c.wave < 2) glds_ws(c.srd, c.lane * 16, so + (c.wave + 4 * n) * 1024, dst + (c.wave + 4 * n) * 1024);
    }
}
struct Fmt { u32x4 v[2]; u32 g[8]; u32 dst[2], cdst[2]; int kind[2]; };
DI void fmt_issue(const Ctx& c, const Params& p, Fmt& m) {
    const u16 *pw = p.wqkv, *pa = p.woA, *pb = p.woB;
    asm volatile("" : "+s"(pw), "+s"(pa), "+s"(pb));
    const int lane = c.lane, rho = lane >> 1, g = rho >> 3, e = rho & 7;
#pragma unroll
    for (int r = 0; r < 2; ++r) {
        const int fr = blockIdx.x * 8 + c.wave + 2048 * r;
        int col, A, P, Q, pitch; const GAS char* src;
        if (fr < 2016) {
            const int jj = fr / 252, rem = fr - jj * 252, u = rem / 18, s = rem - u * 18;
            const int wv = u < 8 ? u : (u - 8 < 5 ? u - 8 : 6), slot = u < 8 ? 0 : 1, a = wv >> 1;
            const bool isrem = (wv & 1) && slot == 1;
            const u32 fo = (u32)(((jj * 8 + wv) * 2 + slot) * 18 + s);
            m.dst[r] = WS_WF + fo * 1024; m.cdst[r] = WS_CSP + fo * 64; m.kind[r] = isrem ? 3 : 1;
            P = s < 16 ? 16 : 4; Q = 2; pitch = 1728 * 2; src = (const GAS char*)pw;
            if (!isrem) {
                col = ((wv & 1) ? 1152 : (slot ? 576 : 0)) + 18 * (4 * jj + a) + 8 * (lane & 1);
                A = s < 16 ? 72 * (s >> 1) + 32 * (s & 1) + 4 * g : 72 * (4 * (s - 16) + g) + 64;
            } else {
                const int n = lane & 15, g2 = lane >> 4;
                col = (a == 0 ? (n < 8 ? 0 : 576) : 1152) + 18 * (4 * jj + ((n & 7) >> 1)) + 16 + (n & 1);
                A = s < 16 ? 72 * (s >> 1) + 32 * (s & 1) + 4 * g2 : 72 * (4 * (s - 16) + g2) + 64;
#pragma unroll
                for (int q = 0; q < 8; ++q) m.g[q] = *(const GAS u16*)(src + (A + P * (q >> 2) + (q & 3)) * pitch + col * 2);
            }
        } else {
            const bool on = fr < 3456;
            const int q = on ? fr - 2016 : 0, im = q / 720, rem = q - im * 720, jj = rem / 90, st = rem - jj * 90, s = st / 5, t = st - s * 5;
            m.dst[r] = WS_WIMG + (u32)(im * 8 + jj) * WIMG_BYTES + (u32)st * 1024; m.cdst[r] = 0; m.kind[r] = on ? 2 : 0;
            col = 72 * jj + 16 * t + (t == 4 ? 0 : 8 * (lane & 1));
            if (s < 16) { A = 36 * s + 18 * (g >> 1) + 4 * (g & 1); P = 8; Q = 2; } else { A = 72 * (4 * (s - 16) + g) + 16; P = 36; Q = 18; }
            src = (const GAS char*)(im ? pb : pa); pitch = 576 * 2;
        }
        if (m.kind[r] == 1 || m.kind[r] == 2) m.v[r] = *(const GAS u32x4*)(src + (A + P * (e >> 2) + Q * ((e >> 1) & 1) + (e & 1)) * pitch + col * 2);
    }
}
DI void fmt_finish(const Ctx& c, Fmt& m) {
    asm volatile("" : "+v"(m.v[0]), "+v"(m.v[1]), "+v"(m.g[0]), "+v"(m.g[1]), "+v"(m.g[2]), "+v"(m.g[3]), "+v"(m.g[4]), "+v"(m.g[5]), "+v"(m.g[6]), "+v"(m.g[7]));
    const int lane = c.lane, i16 = lane & 15, g = lane >> 4, q = i16 >> 2, p = i16 & 3;
#pragma unroll
    for (int r = 0; r < 2; ++r) {
        if (m.kind[r] != 0) {
            u32x4 f;
            if (m.kind[r] == 3) { const u32* w = m.g; f = u32x4{w[0] | (w[1] << 16), w[2] | (w[3] << 16), w[4] | (w[5] << 16), w[6] | (w[7] << 16)}; }
            else {
                LAS char* t = c.L + L_FMT + c.wave * 2048 + r * 1024;
                lst(t + lane * 16, m.v[r]);
                asm volatile("s_waitcnt lgkmcnt(0)" ::: "memory");
                const u32x2 lo = trr(t + (8 * g + q) * 32 + 8 * p), hi = trr(t + (8 * g + 4 + q) * 32 + 8 * p);
                f = u32x4{lo[0], lo[1], hi[0], hi[1]};
            }
            st128(c.rs, f, lane * 16, m.dst[r]);
            if (m.kind[r] != 2) {
                float cs = ((bflo(f[0]) + bfhi(f[0])) + (bflo(f[1]) + bfhi(f[1]))) + ((bflo(f[2]) + bfhi(f[2])) + (bflo(f[3]) + bfhi(f[3])));
                cs = sum_g(cs);
                if (lane < 16) st32(c.rs, asu(cs), lane * 4, m.cdst[r]);
            }
        }
    }
}

template <bool PROJ, bool NAT = false, class HK>
DI void g1_mma(const Ctx& c, const u32x4 (&xf)[9], f32x4 (&acc)[5], HK hook) {
    const int lane = c.lane, kh = c.wave >> 2, q = (lane & 15) >> 2, p = lane & 3, g = lane >> 4;
    LAS char *bN, *bR, *bNo, *bNt, *bRo, *bRt;
    if (PROJ) {
        if (NAT) { bN = c.L + L_R1 + (kh * 256 + 4 * g + q) * WP + 8 * p; bR = c.L + L_R1 + (512 + 32 * kh + 8 * g + q) * WP + 8 * p; }
        else { bN = c.L + L_A1 + (kh * 288 + 4 * g + q) * WP + 8 * p; bR = c.L + L_A1 + (kh * 288 + 72 * g + q) * WP + 8 * p; }
        bNo = bN + 64 * kh; bNt = bN + 64 - 64 * kh; bRo = bR + 64 * kh; bRt = bR + 64 - 64 * kh;
    } else {
        bN = c.L + L_R1 + kh * 8 * 5120 + lane * 16; bR = c.L + L_R1 + (16 + kh) * 5120 + lane * 16;
        bNo = bN + 2048 * kh; bNt = bN + 2048 - 2048 * kh; bRo = bR + 2048 * kh; bRt = bR + 2048 - 2048 * kh;
    }
    u32x4 w[2][5];
    auto rd = [&](int sl, u32x4 (&d)[5]) {
#pragma unroll
        for (int u = 0; u < 5; ++u) {
            if (PROJ) {
                const int r0 = NAT ? (sl < 8 ? 32 * sl : 0) : (sl < 8 ? 72 * (sl >> 1) + 32 * (sl & 1) : 64), hs = sl < 8 ? 16 : 4;
                LAS char* bb = u < 2 ? (sl < 8 ? bNo : bRo) + 32 * u : (u == 2 ? (sl < 8 ? bN : bR) + 128 : (sl < 8 ? bNt : bRt) + 32 * (u - 3));
                const u32x2 lo = trr(bb + r0 * WP), hi = trr(bb + (r0 + hs) * WP);
                d[u] = u32x4{lo[0], lo[1], hi[0], hi[1]};
            } else {
                LAS char* bb = u < 2 ? (sl < 8 ? bNo : bRo) + 1024 * u : (u == 2 ? (sl < 8 ? bN : bR) + 4096 : (sl < 8 ? bNt : bRt) + 1024 * (u - 3));
                d[u] = lld<u32x4>(bb + (sl < 8 ? sl * 5120 : 0));
            }
        }
    };
    rd(0, w[0]);
#pragma unroll
    for (int u = 0; u < 5; ++u) asm volatile("" : "+v"(acc[u]));
#pragma unroll
    for (int sl = 0; sl < 9; ++sl) {
        if (sl + 1 < 9) rd(sl + 1, w[(sl + 1) & 1]);
#pragma unroll
        for (int u = 0; u < 5; ++u) MF16(acc[u], w[sl & 1][u], xf[sl]);
        hook(sl, w[sl & 1]);
    }
    asm volatile("s_nop 7" : "+v"(acc[0]), "+v"(acc[1]), "+v"(acc[2]), "+v"(acc[3]), "+v"(acc[4]));
}
DI void g1_exchange(const Ctx& c, f32x4 (&acc)[5]) {
    LAS char* xw = c.L + L_XCH + c.wave * 3072 + c.lane * 16;
    lst(xw, acc[3]); lst(xw + 1024, acc[4]); lst(xw + 2048, acc[2]);
    BAR();
    LAS char* xr = c.L + L_XCH + (c.wave ^ 4) * 3072 + c.lane * 16;
    acc[0] += lld<f32x4>(xr); acc[1] += lld<f32x4>(xr + 1024); acc[2] += lld<f32x4>(xr + 2048);
}
DI void load_xfrags(const Ctx& c, u32 buf, u32x4 (&xf)[9]) {
    const int m = c.wave & 3, kh = c.wave >> 2;
    const u32 so = buf + (u32)(c.i * 18 * 4 + m) * 1024;
#pragma unroll
    for (int sl = 0; sl < 9; ++sl) { const int s = sl < 8 ? 8 * kh + sl : 16 + kh; xf[sl] = ld128(c.rs, c.lane * 16, so + (u32)s * 4096); }
}
DI u32x4 stats_issue(const Ctx& c, u32 stbuf) {
    const u32 vo = (u32)((c.i * 16 + 2 * (c.lane >> 3)) * 64 + c.wave * 8 + (c.lane & 7)) * 8;
    const u32x2 a = ld64(c.rs, vo, stbuf), b = ld64(c.rs, vo + 512, stbuf);
    return u32x4{a[0], a[1], b[0], b[1]};
}
DI void stats_finish(const Ctx& c, u32x4 v) {
    const int tok = c.wave * 8 + (c.lane & 7);
    float s = asf(v[0]) + asf(v[2]), s2 = asf(v[1]) + asf(v[3]);
    s += __shfl_xor(s, 8); s2 += __shfl_xor(s2, 8);
    s = sum_g(s); s2 = sum_g(s2);
    if (c.lane < 8) {
        lst(c.L + L_STATP + tok * 8, f32x2{s, s2}); lst(c.L + L_STATP + 512 + tok * 8, f32x2{0.f, 0.f});
        const float mean = s * (1.f / DM), var = s2 * (1.f / DM) - mean * mean;
        lst(c.L + L_MEAN + tok * 4, mean); lst(c.L + L_MEAN + 256 + tok * 4, __builtin_amdgcn_rsqf(fmaxf(var, 0.f) + 1e-5f));
    }
}
DI void load_stats(const Ctx& c, u32 stbuf) { stats_finish(c, stats_issue(c, stbuf)); }

struct KV { u32x4 km; u32x2 kr; u32x4 v0, v1; };
DI int vrow_of(int r) { return r < 18 ? r : 18; }
DI KV load_tile_ws(const Ctx& c, u32 tile) {
    const int r = c.lane & 31, h = c.lane >> 5;
    KV t;
    t.km = ld128(c.rs, c.lane * 16, tile);
    t.kr = ld64(c.rs, KT_KREM + r * 8, tile);
    t.v0 = ld128(c.rs, KT_V + h * KT_VBLK + vrow_of(r) * 16, tile);
    t.v1 = ld128(c.rs, KT_V + (2 + h) * KT_VBLK + vrow_of(r) * 16, tile);
    return t;
}
DI f32x16 mfma32k8(u32x2 a, u32x2 b, f32x16 c) { return __builtin_amdgcn_mfma_f32_32x32x8bf16_1k(__builtin_bit_cast(s16x4, a), __builtin_bit_cast(s16x4, b), c, 0, 0, 0); }
DI f32x16 qk(const KV& t, u32x4 qm, u32x2 qr, f32x16 c0) {
    return mfma32k8(t.kr, qr, mfma32(t.km, qm, c0));
}
DI void QK1(f32x16& s, u32x4 km, u32x4 qm) { asm volatile("v_mfma_f32_32x32x16_bf16 %0, %1, %2, 0" : "=&v"(s) : "v"(km), "v"(qm)); }
DI void QK1C(f32x16& s, u32x4 km, u32x4 qm, const f32x16& c0) { asm volatile("v_mfma_f32_32x32x16_bf16 %0, %1, %2, %3" : "=&v"(s) : "v"(km), "v"(qm), "v"(c0)); }
DI void QK2(f32x16& s, u32x2 kr, u32x2 qr) { asm volatile("v_mfma_f32_32x32x8_bf16 %0, %1, %2, %0" : "+v"(s) : "v"(kr), "v"(qr)); }
DI void PV(f32x16& o, u32x4 v, u32x4 p) { asm volatile("v_mfma_f32_32x32x16_bf16 %0, %1, %2, %0" : "+v"(o) : "v"(v), "v"(p)); }
DI float EX(float x) { float r; asm volatile("v_exp_f32 %0, %1" : "=v"(r) : "v"(x)); return r; }
DI u32 CV(float a, float b) { u32 r; asm volatile("v_cvt_pk_bf16_f32 %0, %1, %2" : "=v"(r) : "v"(a), "v"(b)); return r; }
struct Pipe { float e[4]; u32x4 p1; };
template <bool NEXT = true, class F0, class F1, class F2, class F3, class F4>
DI void attn_step(const f32x16& sc, f32x16& sn, f32x16& o, Pipe& pp, const u32x4& km, const u32x2& kr, const u32x4& v0, const u32x4& v1, u32x4 qm, u32x2 qr, F0 first, F1 LK, F2 LR, F3 LV1, F4 LV0) {
    u32x4 p0; float a0, a1, a2, a3, b0, b1, b2, b3;
    if (NEXT) first(sn, km, qm);
    LK();
    pp.p1[2] = CV(pp.e[0], pp.e[1]); pp.p1[3] = CV(pp.e[2], pp.e[3]);
    a0 = EX(sc[0]); a1 = EX(sc[1]); a2 = EX(sc[2]); a3 = EX(sc[3]);
    if (NEXT) QK2(sn, kr, qr); else asm volatile("s_nop 0");
    LR();
    p0[0] = CV(a0, a1); p0[1] = CV(a2, a3);
    b0 = EX(sc[4]); b1 = EX(sc[5]); b2 = EX(sc[6]); b3 = EX(sc[7]);
    PV(o, v1, pp.p1); LV1();
    p0[2] = CV(b0, b1); p0[3] = CV(b2, b3);
    a0 = EX(sc[8]); a1 = EX(sc[9]); a2 = EX(sc[10]); a3 = EX(sc[11]);
    PV(o, v0, p0); LV0();
    pp.p1[0] = CV(a0, a1); pp.p1[1] = CV(a2, a3);
    pp.e[0] = EX(sc[12]); pp.e[1] = EX(sc[13]); pp.e[2] = EX(sc[14]); pp.e[3] = EX(sc[15]);
}
template <class F1, class F2, class F3, class F4>
DI void attn_step2(const f32x16& scx, f32x16& snx, f32x16& ox, Pipe& px, const f32x16& scy, f32x16& sny, f32x16& oy, Pipe& py,
                   const u32x4& km, const u32x2& kr, const u32x4& v0, const u32x4& v1, u32x4 qmx, u32x2 qrx, u32x4 qmy, u32x2 qry, F1 LK, F2 LR, F3 LV1, F4 LV0) {
    u32x4 p0x, p0y; float a0, a1, a2, a3, b0, b1, b2, b3, c0, c1, c2, c3, d0, d1, d2, d3;
#define GRP(MF, PD0, PD1, E0, E1, E2, E3, D0, D1, D2, D3, SRC, I) MF; PD0 = CV(E0, E1); PD1 = CV(E2, E3); D0 = EX(SRC[I]); D1 = EX(SRC[I + 1]); D2 = EX(SRC[I + 2]); D3 = EX(SRC[I + 3])
    GRP(QK1(snx, km, qmx), px.p1[2], px.p1[3], px.e[0], px.e[1], px.e[2], px.e[3], a0, a1, a2, a3, scx, 0);
    GRP(QK1(sny, km, qmy); LK(), py.p1[2], py.p1[3], py.e[0], py.e[1], py.e[2], py.e[3], b0, b1, b2, b3, scy, 0);
    GRP(QK2(snx, kr, qrx), p0x[0], p0x[1], a0, a1, a2, a3, c0, c1, c2, c3, scx, 4);
    GRP(QK2(sny, kr, qry); LR(), p0y[0], p0y[1], b0, b1, b2, b3, d0, d1, d2, d3, scy, 4);
    GRP(PV(ox, v1, px.p1), p0x[2], p0x[3], c0, c1, c2, c3, a0, a1, a2, a3, scx, 8);
    GRP(PV(oy, v1, py.p1); LV1(), p0y[2], p0y[3], d0, d1, d2, d3, b0, b1, b2, b3, scy, 8);
    GRP(PV(ox, v0, p0x), px.p1[0], px.p1[1], a0, a1, a2, a3, px.e[0], px.e[1], px.e[2], px.e[3], scx, 12);
    GRP(PV(oy, v0, p0y); LV0(), py.p1[0], py.p1[1], b0, b1, b2, b3, py.e[0], py.e[1], py.e[2], py.e[3], scy, 12);
#undef GRP
}
DI void attn_finish(f32x16& o, Pipe& pp, u32x4 v1) {
    asm volatile("s_nop 0");
    pp.p1[2] = CV(pp.e[0], pp.e[1]); pp.p1[3] = CV(pp.e[2], pp.e[3]);
    asm volatile("s_nop 1");
    PV(o, v1, pp.p1);
    asm volatile("s_nop 7\n\ts_nop 3" : "+v"(o));
}
DI void attn_finish2(f32x16& ox, Pipe& px, f32x16& oy, Pipe& py, u32x4 v1) {
    asm volatile("s_nop 0");
    px.p1[2] = CV(px.e[0], px.e[1]); px.p1[3] = CV(px.e[2], px.e[3]);
    py.p1[2] = CV(py.e[0], py.e[1]); py.p1[3] = CV(py.e[2], py.e[3]);
    PV(ox, v1, px.p1);
    asm volatile("s_nop 0");
    PV(oy, v1, py.p1);
    asm volatile("s_nop 7\n\ts_nop 3" : "+v"(ox), "+v"(oy));
}
DI void pexp(const f32x16& s, u32x4& p0, u32x4& p1) {
    float e[16];
#pragma unroll
    for (int r = 0; r < 16; ++r) e[r] = __builtin_amdgcn_exp2f(s[r]);
    p0 = u32x4{pk(e[0], e[1]), pk(e[2], e[3]), pk(e[4], e[5]), pk(e[6], e[7])};
    p1 = u32x4{pk(e[8], e[9]), pk(e[10], e[11]), pk(e[12], e[13]), pk(e[14], e[15])};
}
DI float rowmax(const f32x16& s) {
    float a = fmaxf(fmaxf(s[0], s[1]), s[2]), b = fmaxf(fmaxf(s[3], s[4]), s[5]);
#pragma unroll
    for (int r = 6; r < 16; r += 2) { if (r & 2) a = fmaxf(fmaxf(a, s[r]), s[r + 1]); else b = fmaxf(fmaxf(b, s[r]), s[r + 1]); }
    return max_h(fmaxf(a, b));
}
DI f32x16 mask_c(int lane, bool diag) {
    const int r = lane & 31, h = lane >> 5; f32x16 m;
#pragma unroll
    for (int e = 0; e < 16; ++e) { const bool ok = diag ? (crow(e, h) <= r) : (crow(e, h) > r); m[e] = ok ? 0.f : NEG_BIG; }
    return m;
}
DI f32x16 attn_slow(const Ctx& c, u32 tile0, int n0, int n1, bool masked, u32x4 qm, u32x2 qr) {
    const int h = c.lane >> 5;
    qr[1] = 0u;
    f32x16 o = {}; float m = NEG_BIG;
    for (int n = n0; n <= n1; ++n) {
        const KV t = load_tile_ws(c, tile0 + (u32)n * TILE_B);
        f32x16 z = {};
        f32x16 s = qk(t, qm, qr, z);
        if (masked && (n == n1 || n == n1 - 8)) {
            const int r = c.lane & 31;
#pragma unroll
            for (int e = 0; e < 16; ++e) { const bool ok = (n == n1) ? (crow(e, h) <= r) : (crow(e, h) > r); s[e] = ok ? s[e] : NEG_BIG; }
        }
        const float mn = fmaxf(m, rowmax(s)), al = __builtin_amdgcn_exp2f(m - mn);
        m = mn;
#pragma unroll
        for (int e = 0; e < 16; ++e) { o[e] *= al; s[e] = s[e] <= NEG_BIG ? NEG_BIG : s[e] - mn; }
        u32x4 p0, p1; pexp(s, p0, p1);
        o = mfma32(t.v0, p0, o); o = mfma32(t.v1, p1, o);
    }
    return o;
}
DI bool attn_unsafe(const f32x16& o) {
    float z = 0.f;
#pragma unroll
    for (int e = 0; e < 10; ++e) z = __builtin_fmaf(o[e], 0.f, z);
    const float l = o[10];
    return __any(!(l > 0x1p-40f && l < 0x1p30f && z == 0.f));
}
DI void store_o(const Ctx& c, u32 obuf, const f32x16& o, int a, int t) {
    const int r = c.lane & 31, h = c.lane >> 5;
    const float inv = __builtin_amdgcn_rcpf(o[10]);
    const u32x4 mainp = {pk(o[0] * inv, o[1] * inv), pk(o[2] * inv, o[3] * inv), pk(o[4] * inv, o[5] * inv), pk(o[6] * inv, o[7] * inv)};
    const int m = 2 * t + (r >> 4), tok = r & 15;
    const u32 so = obuf + (u32)(c.i * 18 * 4) * 1024;
    st128(c.rs, mainp, (u32)((2 * c.j + (a >> 1)) * 4 + m) * 1024 + ((2 * (a & 1) + h) * 16 + tok) * 16, so);
    if (h == 0) st32(c.rs, pk(o[8] * inv, o[9] * inv), (u32)((16 + (c.j >> 2)) * 4 + m) * 1024 + ((c.j & 3) * 16 + tok) * 16 + 4 * a, so);
}

DI void a_proj(Ctx& c0, const Params& p, int rep) {
    Ctx c = fresh(c0);
    const int lane = c.lane, m = c.wave & 3, kh = c.wave >> 2, tok = lane & 15, g = lane >> 4;
    u32x4 xf[9]; f32x4 acc[5] = {};
    f32x4 s1 = {}, s2 = {}, ca = {}, cb = {};
    Fmt fm; u64 e0 = 0; u32x4 stv;
    if (rep == 0) {
        e0 = __hip_atomic_load((GAS u64*)(p.ws + WS_EPOCH), __ATOMIC_RELAXED, __HIP_MEMORY_SCOPE_AGENT);
        const char* xt = (const char*)p.x + (size_t)(c.i * 64) * (DM * 2);
#pragma unroll
        for (int n = 0; n < 8; ++n) {
            const int row = c.wave * 8 + n;
            glds_in(xt + row * (DM * 2) + (lane ^ (row & 15)) * 16, c.Lb + (row < 60 ? L_XIN + row * 1024 : L_XIN2 + (row - 60) * 1024));
        }
        xf[8] = *(const u32x4*)(xt + (m * 16 + tok) * (DM * 2) + (512 + 32 * kh + 8 * g) * 2);
        load_wimage_a(c, p, 0);
        STAMP(13);
        DRAIN(); BAR();
        STAMP(18);
        asm volatile("" : "+v"(xf[8]), "+v"(e0));
        fmt_issue(c, p, fm);
        { const int row = m * 16 + tok, t2 = tok ^ (g >> 1);
          LAS char* xb = c.L + (row < 60 ? L_XIN + row * 1024 : L_XIN2 + (row - 60) * 1024) + kh * 512 + 8 * (g & 1);
#pragma unroll
          for (int sl = 0; sl < 8; ++sl) {
              const int K = 4 * (sl & 3);
              const u32x2 lo = lld<u32x2>(xb + (sl >> 2) * 256 + ((K ^ t2) * 16)), hi = lld<u32x2>(xb + (sl >> 2) * 256 + (((K + 2) ^ t2) * 16));
              xf[sl] = u32x4{lo[0], lo[1], hi[0], hi[1]};
          } }
        STAMP(21);
        if (c.tid < 180) st128(c.rs, lld<u32x4>(c.L + L_R1 + c.i * 2880 + c.tid * 16), c.tid * 16, WS_WIMGA + (u32)c.j * WIMG_BYTES + (u32)c.i * 2880);
        BAR();
        STAMP(2);
    } else {
        wait_group(c, 5);
        load_xfrags(c, WS_XF + 1 * XF_BYTES, xf);
        stv = stats_issue(c, WS_ST + 1 * ST_BYTES);
        asm volatile("s_waitcnt vmcnt(11)" ::: "memory"); BAR();
        STAMP(32);
    }
    if (rep != 0) { g1_mma<true>(c, xf, acc, [](int, const u32x4 (&)[5]) {}); stats_finish(c, stv); }
    else {
        u32x4 ones = ones4();
        const int tlc = m < 2 ? 2 * kh + m : 2 * (1 - kh) + (m - 2), q = (lane & 15) >> 2, pp = lane & 3;
        LAS char* bN = c.L + L_R1 + (kh * 256 + 4 * g + q) * WP + 8 * pp + 32 * tlc;
        LAS char* bR = c.L + L_R1 + (512 + 32 * kh + 8 * g + q) * WP + 8 * pp + 32 * tlc;
        asm volatile("" : "+v"(s1), "+v"(s2), "+v"(ca), "+v"(cb), "+v"(ones));
        u32x4 wx[2];
        auto rdx = [&](int sl) {
            const int r0 = sl < 8 ? 32 * sl : 0, hs = sl < 8 ? 16 : 4; LAS char* b = sl < 8 ? bN : bR;
            const u32x2 lo = trr(b + r0 * WP), hi = trr(b + (r0 + hs) * WP);
            wx[sl & 1] = u32x4{lo[0], lo[1], hi[0], hi[1]}; };
        rdx(0);
        g1_mma<true, true>(c, xf, acc, [&](int sl, const u32x4 (&w)[5]) {
            if (sl + 1 < 9) rdx(sl + 1);
            MF16(s1, ones, xf[sl]); MF16(s2, xf[sl], xf[sl]); MF16(cb, w[2], ones); MF16(ca, wx[sl & 1], ones);
        });
        asm volatile("s_nop 7" : "+v"(s1), "+v"(s2), "+v"(ca), "+v"(cb));
        const int rg = tok & 3;
        float d = rg == 0 ? s2[0] : (rg == 1 ? s2[1] : (rg == 2 ? s2[2] : s2[3]));
        d = sum_g(g == (tok >> 2) ? d : 0.f);
        if (g == 0) lst(c.L + L_STATP + kh * 512 + (m * 16 + tok) * 8, f32x2{s1[0], d});
        const int tl = m < 2 ? 2 * kh + m : 2 * (1 - kh) + (m - 2);
        if (tok == 0) { lst(c.L + L_CSA + (kh * 5 + tl) * 64 + g * 16, ca); if (m == 0) lst(c.L + L_CSA + (kh * 5 + 4) * 64 + g * 16, cb); }
    }
    STAMP(3 + rep * 30);
    g1_exchange(c, acc);
    const f32x2 sa = lld<f32x2>(c.L + L_STATP + (m * 16 + tok) * 8), sb = lld<f32x2>(c.L + L_STATP + 512 + (m * 16 + tok) * 8);
    const float mean = (sa[0] + sb[0]) * (1.f / DM), var = (sa[1] + sb[1]) * (1.f / DM) - mean * mean;
    const float rstd = __builtin_amdgcn_rsqf(fmaxf(var, 0.f) + 1e-5f);
    LAS char* stg = c.L + L_STG + (m * 16 + tok) * STG_P + g * 8;
#pragma unroll
    for (int u = 0; u < 3; ++u) {
        if (u < 2 || kh == 1) {
            const int t = u < 2 ? 2 * kh + u : 4;
            f32x4 cs;
            cs = lld<f32x4>(c.L + L_CSA + t * 64 + g * 16) + lld<f32x4>(c.L + L_CSA + (5 + t) * 64 + g * 16);
            const float sc = (u < 2 || g < 2) ? rstd * QSCALE : rstd;
            f32x4 v;
#pragma unroll
            for (int e = 0; e < 4; ++e) v[e] = (acc[u][e] - mean * cs[e]) * sc;
            lst(stg + t * 32, u32x2{pk(v[0], v[1]), pk(v[2], v[3])});
        }
    }
    BAR();
    STAMP(4 + rep * 30);
    if (rep == 0) {
        fmt_finish(c, fm);
        const u64 e = e0 + 1; c0.elo = c.elo = __builtin_amdgcn_readfirstlane((u32)e); c0.ehi = c.ehi = __builtin_amdgcn_readfirstlane((u32)(e >> 32));
    }
    if (c.wave == 0) {
        const u32 kva = WS_KVA + rep * KVA_BYTES + (u32)(2 * c.i) * TILE_B;
        const int j = c.j;
        if (j < 2) {
            LAS char* s = c.L + L_STG + lane * STG_P + 144;
            const u32x2 lo = lld<u32x2>(s), hi = lld<u32x2>(s + 8);
            st128(c.rs, u32x4{lo[0], lo[1], hi[0], hi[1]}, (lane >> 5) * TILE_B + j * 512 + (lane & 31) * 16, kva);
        } else if (j == 2) {
            st64(c.rs, u32x2{lld<u32>(c.L + L_STG + lane * STG_P + 144 + 12), 0x00003f80u}, (lane >> 5) * TILE_B + KT_KREM + (lane & 31) * 8, kva);
        } else if (j < 6) {
            const int s4 = lane >> 4, q = (lane & 15) >> 2, pp = lane & 3;
            LAS char* s = c.L + L_STG + (16 * s4 + q + 4 * (pp >> 1)) * STG_P + 144 + 8 * (pp & 1);
            const u32x2 lo = trr(s), hi = trr(s + 8 * STG_P);
            const int dl = lane & 7, hh = (lane >> 3) & 1, drow = j < 5 ? 8 * (j - 3) + dl : 10 + dl;
            if (j < 5 || dl >= 6) st128(c.rs, u32x4{lo[0], lo[1], hi[0], hi[1]}, (s4 >> 1) * TILE_B + KT_V + ((s4 & 1) * 2 + hh) * KT_VBLK + drow * 16, kva);
        } else if (j == 6) {
            if (lane < 8) st128(c.rs, ones4(), (lane >> 2) * TILE_B + KT_V + (lane & 3) * KT_VBLK + 18 * 16, kva);
        }
        if (rep != 0) { DRAIN(); signal(c, 6); }
    }
    if (rep == 0) publish(c, 0);
}

DI void load_wring(const Ctx& c, u32x4 (&w0)[9], u32x4 (&w1)[9]) {
    const u32 wso = WS_WF + (u32)((c.j * 8 + c.wave) * 2) * 18 * 1024;
#pragma unroll
    for (int s = 0; s < 9; ++s) { w0[s] = ld128(c.rs, c.lane * 16, wso + s * 1024); w1[s] = ld128(c.rs, c.lane * 16, wso + (18 + s) * 1024); }
}
DI void colsums_b(const Ctx& c) {
    const int tok = c.lane & 15, g = c.lane >> 4;
    if (c.wave >= 1) {
        const int extra = c.wave == 1 ? 0 : c.wave;
        u32 v[4][5];
#pragma unroll
        for (int q = 0; q < 4; ++q) {
            const int unit = (q < 2 ? c.wave : extra) * 2 + (q & 1);
#pragma unroll
            for (int u = 0; u < 5; ++u) { const int s = g + 4 * u; v[q][u] = (q < 2 || c.wave == 1) ? ld32(c.rs, (s < 18 ? s : 0) * 64 + tok * 4, WS_CSP + (u32)((c.j * 16 + unit) * 18) * 64) : 0u; }
        }
#pragma unroll
        for (int q = 0; q < 4; ++q) {
            const int unit = (q < 2 ? c.wave : extra) * 2 + (q & 1);
            float cs = (asf(v[q][0]) + asf(v[q][1])) + (asf(v[q][2]) + asf(v[q][3])) + (g < 2 ? asf(v[q][4]) : 0.f);
            cs = sum_g(cs);
            if (g == 0 && (q < 2 || c.wave == 1)) lst(c.L + L_CSB + unit * 64 + tok * 4, cs);
        }
    }
}
struct ATail { f32x16 o; u32x4 qm; u32x2 qr; };
DI void a_attn(const Ctx& c0, const Params& p, int rep, u32 (&xres)[3][2], ATail& st) {
    const Ctx c = fresh(c0);
    const int lane = c.lane, r = lane & 31, h = lane >> 5, a = c.wave & 3, kh = c.wave >> 2;
    const int ph = rep * 6;
    const u32 kva = WS_KVA + rep * KVA_BYTES;
    u32x4 qmx, qmy; u32x2 qrx, qry;
    { LAS char* s = c.L + L_STG + r * STG_P + 36 * a;
      qmx = u32x4{lld<u32>(s + 16 * h), lld<u32>(s + 16 * h + 4), lld<u32>(s + 16 * h + 8), lld<u32>(s + 16 * h + 12)};
      const u32 rx = lld<u32>(s + 32);
      s += 32 * STG_P;
      qmy = u32x4{lld<u32>(s + 16 * h), lld<u32>(s + 16 * h + 4), lld<u32>(s + 16 * h + 8), lld<u32>(s + 16 * h + 12)};
      const u32 ry = lld<u32>(s + 32);
      qrx = u32x2{h == 0 ? rx : 0u, 0u}; qry = u32x2{h == 0 ? ry : 0u, 0u}; }
    const int ch0 = c.i >> 2;
    auto chunk_ids = [&](int n) { const int ch = (ch0 + n) & 7; return ch * 32 + (lane & 31); };
    auto issue_piece = [&](int n, int k) {
        const int ch = (ch0 + n) & 7; const u32 so = kva + (u32)ch * CHUNK_B; const u32 dst = c.Lb + L_RING + (n % 3) * CHUNK_B;
        glds_ws(c.srd, lane * 16, so + (c.wave + 4 * k) * 1024, dst + (c.wave + 4 * k) * 1024);
    };
    auto issue_chunk8 = [&](int n) {
        const int ch = (ch0 + n) & 7; const u32 so = kva + (u32)ch * CHUNK_B; const u32 dst = c.Lb + L_RING + (n % 3) * CHUNK_B;
        glds_ws(c.srd, lane * 16, so + c.wave * 1024, dst + c.wave * 1024);
        glds_ws(c.srd, lane * 16, so + (c.wave + 8) * 1024, dst + (c.wave + 8) * 1024);
        if (c.wave < 4) glds_ws(c.srd, lane * 16, so + (c.wave + 16) * 1024, dst + (c.wave + 16) * 1024);
    };
    if (c.wave == 0) poll(c, ph, lane < 32 ? chunk_ids(0) : chunk_ids(1));
    BAR();
    STAMP(6 + rep * 30);
    issue_chunk8(0);
    if (c.wave < 4) { issue_piece(1, 0); issue_piece(1, 1); issue_piece(1, 2); issue_piece(1, 3); issue_piece(1, 4); asm volatile("s_waitcnt vmcnt(5)" ::: "memory"); }
    else DRAIN();
    BAR();
    STAMP(7 + rep * 30); if (rep == 0) CSTAMP(61);
    if (rep == 1) { CSTAMPW(0, 4); CSTAMPW(8, 0); }
    if (rep == 0) {
        const int m = c.wave & 3, tok = lane & 15, g = lane >> 4;
        const char* rr = (const char*)p.x + (size_t)(c.i * 64 + m * 16 + tok) * (DM * 2) + (c.j * 72 + 4 * g) * 2;
        const u32x2 r0 = *(const u32x2*)(rr + (2 * kh) * 32), r1 = *(const u32x2*)(rr + (2 * kh + 1) * 32), r2 = *(const u32x2*)(rr + (g < 2 ? 128 : 0));
        xres[0][0] = r0[0]; xres[0][1] = r0[1]; xres[1][0] = r1[0]; xres[1][1] = r1[1]; xres[2][0] = r2[0]; xres[2][1] = r2[1];
    }
    if (rep == 0 && c.wave >= 4) {
        u32 z0 = 0u; asm volatile("" : "+v"(z0));
        const u32x4 z = {z0, z0, z0, z0};
#pragma unroll
        for (int n = 0; n < 5; ++n) {
            st128(c.rs, z, lane * 16 + n * 1024, WS_KVB + (u32)((4 * c.j + c.wave - 4) * 64 + 2 * c.i) * TILE_B);
        }
        if (c.wave < 6) st128(c.rs, z, (u32)((16 + (c.j >> 2)) * 4 + (lane >> 4)) * 1024 + ((c.j & 3) * 16 + (lane & 15)) * 16, WS_OF + (c.wave - 4) * XF_BYTES + (u32)(c.i * 72) * 1024);
    }
    const int vr = vrow_of(r);
    LAS char* kb = c.L + L_RING + lane * 16;
    LAS char* rb = c.L + L_RING + KT_KREM + r * 8;
    LAS char* vb = c.L + L_RING + KT_V + h * KT_VBLK + vr * 16;
    f32x16 sAx, sBx, sAy, sBy, ox, oy;
    const u32x4 k0m = lld<u32x4>(kb); const u32x2 k0r = lld<u32x2>(rb);
    kb += kh * 4 * TILE_B; rb += kh * 4 * TILE_B; vb += kh * 4 * TILE_B;
    const u32x4 kom = lld<u32x4>(kb); const u32x2 kor = lld<u32x2>(rb);
    u32x4 km = lld<u32x4>(kb + TILE_B), v0 = lld<u32x4>(vb), v1 = lld<u32x4>(vb + 2 * KT_VBLK); u32x2 kr = lld<u32x2>(rb + TILE_B);
    Pipe px, py;
    { u32 z = 0u; asm volatile("" : "+v"(z));
      px.e[0] = px.e[1] = px.e[2] = px.e[3] = asf(z); px.p1 = u32x4{z, z, z, z}; py = px; }
    asm volatile("" : "+v"(px.p1), "+v"(px.e[0]), "+v"(px.e[1]), "+v"(px.e[2]), "+v"(px.e[3]), "+v"(qmx), "+v"(qrx));
    asm volatile("s_nop 1" : "+v"(py.p1), "+v"(py.e[0]), "+v"(py.e[1]), "+v"(py.e[2]), "+v"(py.e[3]), "+v"(qmy), "+v"(qry));
    QK1(ox, px.p1, px.p1); QK1(oy, px.p1, px.p1);
    QK1(sBx, k0m, qmx); QK1(sBy, k0m, qmy); QK2(sBx, k0r, qrx); QK2(sBy, k0r, qry);
    QK1(sAx, kom, qmx); QK1(sAy, kom, qmy);
    asm volatile("s_nop 7\n\ts_nop 1" : "+v"(sBx), "+v"(sBy));
    { const float mx = rowmax(sBx), my = rowmax(sBy);
      if (h == 0) { qrx[1] = pk(-mx, 0.f); qry[1] = pk(-my, 0.f); } }
    asm volatile("s_nop 1" : "+v"(qrx), "+v"(qry));
    QK2(sAx, kor, qrx); QK2(sAy, kor, qry);
    asm volatile("s_nop 7\n\ts_nop 3" : "+v"(sAx), "+v"(sAy));
    if (rep == 1) { CSTAMPW(1, 4); CSTAMPW(9, 0); }
    int slot = 0;
    const u64 want = ((u64)c.ehi << 32) | c.elo;
    const u32 fvo = (lane & 31) * 128;
    u64 fl = want;
    if (c.wave == 0) flag_load(fl, ~want, fvo, c.srd, WS_FLAG + (u32)(ph * 256 + ((ch0 + 2) & 7) * 32) * 128);
#pragma unroll 1
    for (int n = 0; n < 8; ++n) {
        const int nslot = slot == 2 ? 0 : slot + 1;
        LAS char* kc = kb + slot * CHUNK_B; LAS char* rc = rb + slot * CHUNK_B; LAS char* vc = vb + slot * CHUNK_B;
        LAS char* kn = kb + nslot * CHUNK_B; LAS char* rn = rb + nslot * CHUNK_B; LAS char* vn = vb + nslot * CHUNK_B;
#define A_TILE(U, SCX, SNX, SCY, SNY, X1, X2) attn_step2(SCX, SNX, ox, px, SCY, SNY, oy, py, km, kr, v0, v1, qmx, qrx, qmy, qry, \
            [&] { km = lld<u32x4>((U) < 2 ? kc + ((U) + 2) * TILE_B : kn + ((U) - 2) * TILE_B); X1; }, \
            [&] { kr = lld<u32x2>((U) < 2 ? rc + ((U) + 2) * TILE_B : rn + ((U) - 2) * TILE_B); }, \
            [&] { v1 = lld<u32x4>(vc + (U) * TILE_B + 2 * KT_VBLK); X2; }, \
            [&] { v0 = lld<u32x4>((U) < 3 ? vc + ((U) + 1) * TILE_B : vn); })
        A_TILE(0, sAx, sBx, sAy, sBy, (void)0, (void)0); A_TILE(1, sBx, sAx, sBy, sAy, (void)0, (void)0);
        if (__builtin_expect(c.wave < 4, 0)) {
            asm volatile("s_waitcnt vmcnt(0)" : "+v"(fl) :: "memory");
            if (__builtin_expect(!__all(fl == want), 0)) poll(c, ph, chunk_ids(n + 2));
        }
        BAR();
        if (__builtin_expect(c.wave < 4, 0)) {
            fl = want;
            if (c.wave == 0 && n + 3 < 8) flag_load(fl, ~want, fvo, c.srd, WS_FLAG + (u32)(ph * 256 + ((ch0 + n + 3) & 7) * 32) * 128);
            if (n + 2 < 8) { issue_piece(n + 2, 0); issue_piece(n + 2, 1); issue_piece(n + 2, 2); issue_piece(n + 2, 3); issue_piece(n + 2, 4); }
            if (n >= 5) load_wimage_o4(c, 0, n - 5);
        }
        A_TILE(2, sAx, sBx, sAy, sBy, (void)0, (void)0); A_TILE(3, sBx, sAx, sBy, sAy, (void)0, (void)0);
#undef A_TILE
        slot = nslot;
    }
    if (rep == 1) { CSTAMPW(2, 4); CSTAMPW(10, 0); }
    attn_finish2(ox, px, oy, py, v1);
    { LAS char* mine = c.L + L_RING + kh * 2 * CHUNK_B + a * 3072 + lane * 16;
      const f32x16 give = kh ? ox : oy;
#pragma unroll
      for (int e = 0; e < 3; ++e) lst(mine + e * 1024, f32x4{give[4 * e], give[4 * e + 1], give[4 * e + 2], give[4 * e + 3]}); }
    st.o = kh ? oy : ox; st.qm = kh ? qmy : qmx; st.qr = kh ? qry : qrx;
}

template <bool ISB, class HK>
DI void out_proj(const Ctx& c0, const Params& p, int rep, u32 (&xres)[3][2], HK behind_x) {
    const Ctx c = fresh(c0);
    const int lane = c.lane, m = c.wave & 3, kh = c.wave >> 2, tok = lane & 15, g = lane >> 4;
    const int layer = rep * 2 + (ISB ? 1 : 0);
    wait_group(c, rep * 6 + (ISB ? 4 : 1));
    STAMP((ISB ? 25 : 10) + rep * 30);
    u32x4 xf[9]; f32x4 acc[5] = {};
    load_xfrags(c, WS_OF + (rep * RP * 2 + (ISB ? 1 : 0)) * XF_BYTES, xf);
    __builtin_amdgcn_sched_barrier(0);
    behind_x(c);
    if (ISB) {
        asm volatile("s_waitcnt vmcnt(9)" ::: "memory"); BAR();
    }
    g1_mma<false>(c, xf, acc, [](int, const u32x4 (&)[5]) {});
    STAMP((ISB ? 26 : 11) + rep * 30);
    g1_exchange(c, acc);
    STAMP((ISB ? 27 : 12) + rep * 30);
#pragma unroll
    for (int u = 0; u < 3; ++u) {
        const float v0 = bflo(xres[u][0]) + acc[u][0], v1 = bfhi(xres[u][0]) + acc[u][1], v2 = bflo(xres[u][1]) + acc[u][2], v3 = bfhi(xres[u][1]) + acc[u][3];
        xres[u][0] = pk(v0, v1); xres[u][1] = pk(v2, v3);
    }
    if (ISB && rep == 1) {
        char* o = (char*)p.out + (size_t)(c.i * 64 + m * 16 + tok) * (DM * 2) + (c.j * 72 + 4 * g) * 2;
        *(u32x2*)(o + (2 * kh) * 32) = u32x2{xres[0][0], xres[0][1]};
        *(u32x2*)(o + (2 * kh + 1) * 32) = u32x2{xres[1][0], xres[1][1]};
        if (kh == 1 && g < 2) *(u32x2*)(o + 128) = u32x2{xres[2][0], xres[2][1]};
        return;
    }
    const int xi = layer == 0 ? 0 : (layer == 1 ? 1 : 2 * RP);
    const u32 so = WS_XF + xi * XF_BYTES + (u32)(c.i * 18 * 4 + m) * 1024;
    st128(c.rs, u32x4{xres[0][0], xres[0][1], xres[1][0], xres[1][1]}, lane * 16, so + (u32)(2 * c.j + kh) * 4096);
    if (kh == 1 && g < 2) st64(c.rs, u32x2{xres[2][0], xres[2][1]}, ((c.j & 3) * 16 + tok) * 16 + 8 * g, so + (u32)(16 + (c.j >> 2)) * 4096);
    __builtin_amdgcn_sched_barrier(0);
    float s1 = 0.f, s2 = 0.f;
#pragma unroll
    for (int u = 0; u < 3; ++u) {
        u32 d0 = xres[u][0], d1 = xres[u][1];
        asm volatile("" : "+v"(d0), "+v"(d1));
        if (u == 2 && !(kh == 1 && g < 2)) { d0 = 0u; d1 = 0u; }
        const float a0 = bflo(d0), a1 = bfhi(d0), a2 = bflo(d1), a3 = bfhi(d1);
        s1 += (a0 + a1) + (a2 + a3); s2 += (a0 * a0 + a1 * a1) + (a2 * a2 + a3 * a3);
    }
    s1 = sum_g(s1); s2 = sum_g(s2);
    if (g == 0) st64(c.rs, u32x2{asu(s1), asu(s2)}, (u32)((c.i * 16 + 2 * c.j + kh) * 64 + m * 16 + tok) * 8, WS_ST + xi * ST_BYTES);
    publish(c, rep * 6 + (ISB ? 5 : 2));
    if (!ISB && rep == 0) colsums_b(c);
    if (ISB) load_wimage_part<36, 90>(c, WS_WIMGA, 1, L_A1);
}

template <bool EARLY>
DI void a_tail(const Ctx& c0, const Params& p, int rep, ATail& st, u32 (&xres)[3][2], u32x4 (&w0)[9], u32x4 (&w1)[9]) {
    const Ctx c = fresh(c0);
    const int lane = c.lane, a = c.wave & 3, kh = c.wave >> 2;
    BAR();
    LAS char* theirs = c.L + L_RING + (1 - kh) * 2 * CHUNK_B + a * 3072 + lane * 16;
#pragma unroll
    for (int e = 0; e < 3; ++e) { const f32x4 v = lld<f32x4>(theirs + e * 1024); st.o[4 * e] += v[0]; st.o[4 * e + 1] += v[1]; st.o[4 * e + 2] += v[2]; st.o[4 * e + 3] += v[3]; }
    STAMP(8 + rep * 30); if (rep == 0) CSTAMP(62);
    if (rep == 1) { CSTAMPW(3, 4); CSTAMPW(11, 0); }
    if (attn_unsafe(st.o)) st.o = attn_slow(c, WS_KVA + rep * KVA_BYTES, 0, 63, false, st.qm, st.qr);
    store_o(c, WS_OF + (rep * RP * 2) * XF_BYTES, st.o, a, kh);
    if (rep == 1) { CSTAMPW(4, 4); CSTAMPW(12, 0); }
    publish(c, rep * 6 + 1);
    if (EARLY) load_wring(c, w0, w1);
    STAMP(9 + rep * 30);
    out_proj<false>(c, p, rep, xres, [&](const Ctx& cc) { if (!EARLY) load_wring(cc, w0, w1); });
}

struct T_ { static constexpr bool value = true; };
struct F_ { static constexpr bool value = false; };
DI void b_proj(const Ctx& c0, const Params& p, int rep, u32x4 (&w0)[9], u32x4 (&w1)[9], u32x4& qm, u32x2& qr) {
    const Ctx c = fresh(c0);
    const int lane = c.lane, tok = lane & 15, g = lane >> 4, a = c.wave >> 1, odd = c.wave & 1;
    wait_group(c, rep * 6 + 2);
    STAMP(15 + rep * 30);
    const int xi = rep == 0 ? 0 : 2 * RP;
    const u32x4 stv = stats_issue(c, WS_ST + xi * ST_BYTES);
    { const u32 so = WS_XF + xi * XF_BYTES + (u32)(c.i * 72) * 1024;
#pragma unroll
      for (int n = 0; n < 9; ++n) glds_ws(c.srd, lane * 16, so + (c.wave + 8 * n) * 1024, c.Lb + L_R1 + (c.wave + 8 * n) * 1024); }
    asm volatile("s_waitcnt vmcnt(5)" ::: "memory"); BAR();
    STAMP(16 + rep * 30);
    f32x4 a0[4] = {}, a1[4] = {};
    const u32 wso = WS_WF + (u32)((c.j * 8 + c.wave) * 2) * 18 * 1024;
    LAS char* xb = c.L + L_R1 + lane * 16;
    const bool t0 = !odd, t1 = !odd || a == 0, has1 = !odd || a < 2;
    auto body = [&](auto T0, auto T1, auto H1) {
        u32x4 x[2][4];
#pragma unroll
        for (int m = 0; m < 4; ++m) { x[0][m] = lld<u32x4>(xb + m * 1024); asm volatile("" : "+v"(a0[m]), "+v"(a1[m])); }
#pragma unroll
        for (int s = 0; s < 18; ++s) {
            if (s == 7) {
                if (H1.value) asm volatile("s_waitcnt vmcnt(14)" ::: "memory"); else asm volatile("s_waitcnt vmcnt(7)" ::: "memory");
                BAR();
            }
            if (s + 1 < 18) {
#pragma unroll
                for (int m = 0; m < 4; ++m) x[(s + 1) & 1][m] = lld<u32x4>(xb + ((s + 1) * 4 + m) * 1024);
            }
#pragma unroll
            for (int m = 0; m < 4; ++m) {
                if (T0.value) MF16(a0[m], w0[s % 9], x[s & 1][m]); else MF16(a0[m], x[s & 1][m], w0[s % 9]);
                if (H1.value) { if (T1.value) MF16(a1[m], w1[s % 9], x[s & 1][m]); else MF16(a1[m], x[s & 1][m], w1[s % 9]); }
            }
            if (s < 9) { w0[s] = ld128(c.rs, lane * 16, wso + (9 + s) * 1024); if (H1.value) w1[s] = ld128(c.rs, lane * 16, wso + (27 + s) * 1024); }
        }
        asm volatile("s_nop 7" : "+v"(a0[0]), "+v"(a0[1]), "+v"(a0[2]), "+v"(a0[3]), "+v"(a1[0]), "+v"(a1[1]), "+v"(a1[2]), "+v"(a1[3]));
    };
    if (!odd) body(T_{}, T_{}, T_{}); else if (a == 0) body(F_{}, T_{}, T_{}); else if (a == 1) body(F_{}, F_{}, T_{}); else body(F_{}, F_{}, F_{});
    (void)t0; (void)t1; (void)has1;
    STAMP(17 + rep * 30);
    stats_finish(c, stv);
    BAR();
    STAMP(23 + rep * 30);
    const u32 kvb = WS_KVB + rep * RP * KVB_BYTES;
    const u32 hb = kvb + (u32)((4 * c.j + a) * 64 + 2 * c.i) * TILE_B;
    auto fin_t = [&](const f32x4 (&acc)[4], int slot, float sc, u32 (&d)[4][2]) {
        const f32x4 cs = lld<f32x4>(c.L + L_CSB + (c.wave * 2 + slot) * 64 + g * 16);
#pragma unroll
        for (int m = 0; m < 4; ++m) {
            const float mean = lld<float>(c.L + L_MEAN + (m * 16 + tok) * 4), rs = lld<float>(c.L + L_MEAN + 256 + (m * 16 + tok) * 4) * sc;
            d[m][0] = pk((acc[m][0] - mean * cs[0]) * rs, (acc[m][1] - mean * cs[1]) * rs);
            d[m][1] = pk((acc[m][2] - mean * cs[2]) * rs, (acc[m][3] - mean * cs[3]) * rs);
        }
    };
    auto fin_p = [&](const f32x4 (&acc)[4], int slot, u32 (&d)[4][2]) {
        const float cs = lld<float>(c.L + L_CSB + (c.wave * 2 + slot) * 64 + tok * 4);
#pragma unroll
        for (int m = 0; m < 4; ++m) {
            const f32x4 mean = lld<f32x4>(c.L + L_MEAN + (m * 16 + 4 * g) * 4), rs = lld<f32x4>(c.L + L_MEAN + 256 + (m * 16 + 4 * g) * 4);
            d[m][0] = pk((acc[m][0] - mean[0] * cs) * rs[0], (acc[m][1] - mean[1] * cs) * rs[1]);
            d[m][1] = pk((acc[m][2] - mean[2] * cs) * rs[2], (acc[m][3] - mean[3] * cs) * rs[3]);
        }
    };
    auto frag16 = [&](const u32 (&d)[4][2], int t) {
        auto x = __builtin_amdgcn_permlane16_swap(d[2 * t][0], d[2 * t + 1][0], false, false);
        auto y = __builtin_amdgcn_permlane16_swap(d[2 * t][1], d[2 * t + 1][1], false, false);
        return u32x4{(u32)x[0], (u32)y[0], (u32)x[1], (u32)y[1]};
    };
    auto frag32 = [&](const u32 (&d)[4][2], int t) {
        auto x = __builtin_amdgcn_permlane32_swap(d[2 * t][0], d[2 * t + 1][0], false, false);
        auto y = __builtin_amdgcn_permlane32_swap(d[2 * t][1], d[2 * t + 1][1], false, false);
        return u32x4{(u32)x[0], (u32)y[0], (u32)x[1], (u32)y[1]};
    };
    u32 d[4][2];
    if (!odd) {
        fin_t(a1, 1, 1.f, d);
        st128(c.rs, frag16(d, 0), lane * 16, hb); st128(c.rs, frag16(d, 1), lane * 16, hb + TILE_B);
        fin_t(a0, 0, QSCALE, d);
        qm = frag16(d, 0);
        lst(c.L + L_QT1 + a * 1024 + lane * 16, frag16(d, 1));
    } else {
        fin_p(a0, 0, d);
        st128(c.rs, frag32(d, 0), KT_V + g * KT_VBLK + tok * 16, hb); st128(c.rs, frag32(d, 1), KT_V + g * KT_VBLK + tok * 16, hb + TILE_B);
        if (a == 0) {
            fin_t(a1, 1, g < 2 ? QSCALE : 1.f, d);
#pragma unroll
            for (int m = 0; m < 4; ++m) {
                if (g < 2) { lst(c.L + L_QREM + (2 * g) * 256 + (m * 16 + tok) * 4, d[m][0]); lst(c.L + L_QREM + (2 * g + 1) * 256 + (m * 16 + tok) * 4, d[m][1]); }
                else {
                    const u32 hb0 = kvb + (u32)(4 * c.j * 64 + 2 * c.i + (m >> 1)) * TILE_B;
                    st64(c.rs, u32x2{d[m][0], 0x00003f80u}, (u32)(2 * (g - 2)) * 64 * TILE_B + KT_KREM + ((m & 1) * 16 + tok) * 8, hb0);
                    st64(c.rs, u32x2{d[m][1], 0x00003f80u}, (u32)(2 * (g - 2) + 1) * 64 * TILE_B + KT_KREM + ((m & 1) * 16 + tok) * 8, hb0);
                }
            }
        } else if (a == 1) {
            fin_p(a1, 1, d);
            const u32 hb0 = kvb + (u32)(4 * c.j * 64 + 2 * c.i) * TILE_B;
            const u32 vo = (u32)(tok >> 1) * 64 * TILE_B + KT_V + g * KT_VBLK + (16 + (tok & 1)) * 16;
            const u32x4 f0 = frag32(d, 0), f1 = frag32(d, 1);
            if (tok < 8) { st128(c.rs, f0, vo, hb0); st128(c.rs, f1, vo, hb0 + TILE_B); }
        } else if (a == 2) {
            const u32 hb0 = kvb + (u32)(4 * c.j * 64 + 2 * c.i) * TILE_B;
            if (lane < 32) st128(c.rs, ones4(),
                                 (u32)(lane >> 3) * 64 * TILE_B + ((lane >> 2) & 1) * TILE_B + KT_V + (lane & 3) * KT_VBLK + 18 * 16, hb0);
        }
    }
    publish(c, rep * 6 + 3);
    load_wimage_part<44, 90>(c, WS_WIMG + 8 * WIMG_BYTES, 1);
    if (odd) qm = lld<u32x4>(c.L + L_QT1 + a * 1024 + lane * 16);
    const u32 rem = lld<u32>(c.L + L_QREM + a * 256 + (32 * odd + (lane & 31)) * 4);
    qr = u32x2{lane < 32 ? rem : 0u, 0u};
}

DI void b_attn(const Ctx& c0, const Params& p, int rep, u32x4 qm, u32x2 qr) {
    const Ctx c = fresh(c0);
    const int lane = c.lane, r = lane & 31, h = lane >> 5, a = c.wave >> 1, t = c.wave & 1;
    const int qt = 2 * c.i + t;
    const f32x16 cd = mask_c(lane, true);
    if (c.wave == 1) {
        const f32x16 cm = mask_c(lane, false);
#pragma unroll
        for (int e = 0; e < 4; ++e) lst(c.L + L_MSK + e * 1024 + lane * 16, f32x4{cm[4 * e], cm[4 * e + 1], cm[4 * e + 2], cm[4 * e + 3]});
    }
    if (c.wave == 0) poll(c, rep * 6 + 3, (lane < 5 && c.i - lane >= 0) ? (c.i - lane) * 8 + c.j : -1);
    BAR();
    STAMP(20 + rep * 30);
    const u32 hb = WS_KVB + rep * RP * KVB_BYTES + (u32)((4 * c.j + a) * 64) * TILE_B;
    { const u32 dst = c.Lb + L_KVB + a * 25600;
#pragma unroll
      for (int n = 0; n < 13; ++n) {
          const int pc = n < 8 ? 24 - t - 2 * n : 9 - t - 2 * (n - 8);
          if (n != 7 || t == 0) {
              u32 vo = lane * 16;
              if (c.i < 4) { const int b = pc * 1024 + lane * 16, tau = b / 2560, kt = 2 * c.i - 8 + tau; vo = (u32)((kt < 0 ? 0 : kt) * 2560 + (b - tau * 2560)); glds_ws(c.srd, vo, hb, dst + pc * 1024); }
              else glds_ws(c.srd, vo, hb + (u32)(2 * c.i - 8) * 2560 + pc * 1024, dst + pc * 1024);
          }
      } }
    asm volatile("s_waitcnt vmcnt(5)" ::: "memory");
    BAR();
    STAMP(21 + rep * 30);
    LAS char* tb = c.L + L_KVB + a * 25600 + (8 + t) * TILE_B;
    LAS char* kb = tb + lane * 16; LAS char* rb = tb + KT_KREM + r * 8; LAS char* vb = tb + KT_V + h * KT_VBLK + vrow_of(r) * 16;
    const u32x4 k0m = lld<u32x4>(kb); const u32x2 k0r = lld<u32x2>(rb);
    u32x4 km = lld<u32x4>(kb - TILE_B), v0 = lld<u32x4>(vb), v1 = lld<u32x4>(vb + 2 * KT_VBLK); u32x2 kr = lld<u32x2>(rb - TILE_B);
    f32x16 sA, sB, o;
    Pipe pp;
    { u32 z = 0u; asm volatile("" : "+v"(z)); pp.e[0] = pp.e[1] = pp.e[2] = pp.e[3] = asf(z); pp.p1 = u32x4{z, z, z, z}; }
    f32x16 cdp = cd;
    asm volatile("s_nop 1" : "+v"(pp.p1), "+v"(pp.e[0]), "+v"(pp.e[1]), "+v"(pp.e[2]), "+v"(pp.e[3]), "+v"(qm), "+v"(qr), "+v"(cdp));
    QK1(o, pp.p1, pp.p1);
    QK1C(sA, k0m, qm, cdp);
    asm volatile("v_mfma_f32_32x32x8_bf16 %0, %1, %2, %3" : "=&v"(sB) : "v"(k0r), "v"(qr), "v"(sA));
    asm volatile("s_nop 7\n\ts_nop 3" : "+v"(sB), "+v"(sA));
    { const float m0 = rowmax(sB); if (h == 0) qr[1] = pk(-m0, 0.f); }
    asm volatile("s_nop 1" : "+v"(qr));
    QK2(sA, k0r, qr);
    asm volatile("s_nop 7\n\ts_nop 3" : "+v"(sA));
    auto first = [](f32x16& s, u32x4 k, u32x4 q) { QK1(s, k, q); };
    auto firstm = [&](f32x16& s, u32x4 k, u32x4 q) {
        f32x16 co;
#pragma unroll
        for (int e = 0; e < 4; ++e) { const f32x4 v = lld<f32x4>(c.L + L_MSK + e * 1024 + lane * 16); co[4 * e] = v[0]; co[4 * e + 1] = v[1]; co[4 * e + 2] = v[2]; co[4 * e + 3] = v[3]; }
        QK1C(s, k, q, co); };
    const u32 qoff = h == 0 ? pk(NEG_BIG, 0.f) : 0u;
    kb -= 8 * TILE_B; rb -= 8 * TILE_B; vb -= 8 * TILE_B;
#define B_TILE(U, SC, SN, FIRST) do { u32x2 qs = {qr[0], (U) + 1 <= qt ? qr[1] : qoff}; asm volatile("" : "+v"(qs)); \
        attn_step<((U) < 8)>(SC, SN, o, pp, km, kr, v0, v1, qm, qs, FIRST, \
            [&] { if ((U) + 2 <= 8) km = lld<u32x4>(kb + (8 - ((U) + 2)) * TILE_B); }, [&] { if ((U) + 2 <= 8) kr = lld<u32x2>(rb + (8 - ((U) + 2)) * TILE_B); }, \
            [&] { v1 = lld<u32x4>(vb + (8 - (U)) * TILE_B + 2 * KT_VBLK); }, [&] { if ((U) + 1 <= 8) v0 = lld<u32x4>(vb + (8 - ((U) + 1)) * TILE_B); }); } while (0)
    B_TILE(0, sA, sB, first); B_TILE(1, sB, sA, first);
    DRAIN();
    B_TILE(2, sA, sB, first);
    BAR();
    B_TILE(3, sB, sA, first); B_TILE(4, sA, sB, first); B_TILE(5, sB, sA, first); B_TILE(6, sA, sB, first); B_TILE(7, sB, sA, firstm); B_TILE(8, sA, sB, first);
#undef B_TILE
    attn_finish(o, pp, v1);
    STAMP(22 + rep * 30);
    if (attn_unsafe(o)) o = attn_slow(c, hb, qt - 8 < 0 ? 0 : qt - 8, qt, true, qm, qr);
    store_o(c, WS_OF + (rep * RP * 2 + 1) * XF_BYTES, o, a, t);
    publish(c, rep * 6 + 4);
    load_wimage_part<0, 44>(c, WS_WIMG + 8 * WIMG_BYTES, 1);
    if (rep == 0) load_wimage_part<0, 36>(c, WS_WIMGA, 1, L_A1);
}

__global__ void __launch_bounds__(512, 2) fused_block_kernel(Params p) {
    __shared__ __attribute__((aligned(16))) char lds_[LDS_BYTES];
    Ctx c;
    c.L = (LAS char*)lds_; c.Lb = (u32)(size_t)c.L;
    c.ws = p.ws;
    c.tid = threadIdx.x; c.lane = c.tid & 63; c.wave = __builtin_amdgcn_readfirstlane(c.tid >> 6);
    { const int wg = blockIdx.x, slot = wg >> 3; c.i = (wg & 7) * 4 + (slot >> 3); c.j = slot & 7; }
    c.rs = __builtin_amdgcn_make_buffer_rsrc((void*)p.ws, 0, WS_USED, 0x00020000);
    { const u64 b = (u64)p.ws; c.srd = u32x4{(u32)b, (u32)(b >> 32) & 0xffffu, WS_USED, 0x00020000u}; }
    STAMP(0);
    u32 warm;
    { u64 pc; u32 len;
      asm volatile("s_getpc_b64 %0\nfbk_code_first:\n\ts_mov_b32 %1, fbk_code_last-fbk_code_first" : "=s"(pc), "=s"(len));
      u32 off = ((u32)(blockIdx.x >> 3) * 8 + c.wave) * 256 + c.lane * 4;
      off = off < len ? off : 0u;
      warm = *(const GAS u32*)((pc & ~3ull) + off); }
    c.elo = 0; c.ehi = 0;
    u32 xres[3][2];
#pragma unroll 1
    for (int rep = 0; rep < 2; ++rep) {
        STAMP(1 + rep * 30);
        a_proj(c, p, rep);
        if (rep == 0) asm volatile("" :: "v"(warm));
        STAMP(5 + rep * 30);
        u32x4 w0[9], w1[9]; ATail st;
        a_attn(c, p, rep, xres, st);
        if (rep == 0) {
            if (blockIdx.x == 0 && c.tid == 0) __hip_atomic_store((GAS u64*)(p.ws + WS_EPOCH), ((u64)c.ehi << 32) | c.elo, __ATOMIC_RELAXED, __HIP_MEMORY_SCOPE_AGENT);
        }
        if (c.wave >= 1 && c.wave < 4) a_tail<true>(c, p, rep, st, xres, w0, w1); else a_tail<false>(c, p, rep, st, xres, w0, w1);
        STAMP(14 + rep * 30);
        u32x4 qm; u32x2 qr;
        b_proj(c, p, rep, w0, w1, qm, qr);
        STAMP(19 + rep * 30);
        b_attn(c, p, rep, qm, qr);
        STAMP(24 + rep * 30);
        out_proj<true>(c, p, rep, xres, [](const Ctx&) {});
        STAMP(29 + rep * 30);
    }
    asm volatile("fbk_code_last:");
}

static size_t ws_offset(size_t ws_size) { return (ws_size - WS_USED) & ~(size_t)0x1fffff; }

extern "C" void kernel_launch(void* const* d_in, const int* in_sizes, int n_in, void* d_out, int out_size, void* d_ws, size_t ws_size, hipStream_t stream) {
    Params p;
    p.x = (const u16*)d_in[0]; p.wq = (const u16*)d_in[1]; p.wk = (const u16*)d_in[2]; p.wv = (const u16*)d_in[3]; p.woA = (const u16*)d_in[4];
    p.wqkv = (const u16*)d_in[6]; p.woB = (const u16*)d_in[7]; p.out = (u16*)d_out;
    p.ws = (char*)d_ws + ws_offset(ws_size);
    fused_block_kernel<<<256, 512, 0, stream>>>(p);
}
```

```cpp
#include <hip/hip_runtime.h>
#include <hip/hip_bf16.h>

typedef unsigned short u16;
typedef unsigned u32;
typedef unsigned long long u64;
typedef __bf16 bf16x8 __attribute__((ext_vector_type(8)));
typedef __bf16 bf16x2 __attribute__((ext_vector_type(2)));
typedef short s16x4 __attribute__((ext_vector_type(4)));
typedef float f32x2 __attribute__((ext_vector_type(2)));
typedef float f32x4 __attribute__((ext_vector_type(4)));
typedef float f32x16 __attribute__((ext_vector_type(16)));
typedef u32 u32x2 __attribute__((ext_vector_type(2)));
typedef u32 u32x4 __attribute__((ext_vector_type(4)));
#define LAS __attribute__((address_space(3)))
#define GAS __attribute__((address_space(1)))
#define DI __device__ __forceinline__

constexpr int SEQ = 2048, DM = 576;
constexpr int RP = 0;
constexpr u32 TILE_B = 2560;
constexpr u32 KT_V = 1024, KT_VBLK = 304, KT_KREM = 2240;
constexpr u32 KA_KREM = 1024, KA_V0 = 1280, KA_V8 = 1792, KA_VX = 2304;
constexpr u32 XF_BYTES = 32u * 18 * 4 * 1024;
constexpr u32 WS_EPOCH = 0;
constexpr u32 WS_FLAG = 4096;
constexpr int PH_FMT = 11;
constexpr u32 WS_XF = WS_FLAG + 12u * 256 * 128 + 4096;
constexpr u32 WS_OF = WS_XF + (2 + RP) * XF_BYTES;
constexpr u32 ST_BYTES = 2048u * 16 * 8;
constexpr u32 WS_ST = WS_OF + (2 + 2 * RP) * XF_BYTES;
constexpr u32 KVA_BYTES = 64 * TILE_B;
constexpr u32 WS_KVA = WS_ST + (2 + RP) * ST_BYTES;
constexpr u32 KVB_BYTES = 32 * 64 * TILE_B;
constexpr u32 WS_KVB = WS_KVA + 2 * KVA_BYTES;
constexpr u32 WF_BYTES = 8u * 16 * 18 * 1024;
constexpr u32 WS_WF = WS_KVB + (1 + RP) * KVB_BYTES;
constexpr u32 WS_CSP = WS_WF + WF_BYTES;
constexpr u32 WIMG_BYTES = 90 * 1024;
constexpr u32 WS_WIMG = WS_CSP + 8u * 16 * 18 * 64;
constexpr u32 WS_WIMGA = WS_WIMG + 16 * WIMG_BYTES;
constexpr u32 WS_STAMP = WS_WIMGA + 8 * WIMG_BYTES;
constexpr u32 WS_STAMP2 = WS_STAMP + 256u * 64 * 8 + 256u * 8 * 8 * 8;
constexpr u32 WS_USED = WS_STAMP2 + 256u * 16 * 8;
constexpr int L_XCH = 0;
constexpr int L_RING = 0;
constexpr int CHUNK_B = 8 * TILE_B;
constexpr int L_STG = 40960;
constexpr int STG_P = 168;
constexpr int L_XIN = 0;
constexpr int L_XIN2 = 153600;
constexpr int L_FMT = 24576;
constexpr int L_KVB = 0;
constexpr int L_MSK = 102400;
constexpr int L_R1 = 61440;
constexpr int WP = 160;
constexpr int L_XB = 76800;
constexpr int L_A1 = 24576;
constexpr int L_STATP = 153600;
constexpr int L_MEAN = L_STATP + 1024;
constexpr int L_CSA = L_MEAN + 512;
constexpr int L_CSB = L_CSA + 1024;
constexpr int L_QREM = L_CSB + 1024;
constexpr int L_QT1 = L_QREM + 1024;
constexpr int LDS_BYTES = 163840;
static_assert(L_QT1 + 4096 <= LDS_BYTES, "lds");

constexpr float QSCALE = 0.23570226039551584f * 1.4426950408889634f;
constexpr float NEG_BIG = -1e30f;
constexpr u32 SPIN_CAP = 1u << 20;
constexpr int AUX_SC1 = 16;

struct Params { const u16* x; const u16* wq; const u16* wk; const u16* wv; const u16* woA; const u16* wqkv; const u16* woB; u16* out; char* ws; };

DI u32 pk(float a, float b) { f32x2 v = {a, b}; return __builtin_bit_cast(u32, __builtin_convertvector(v, bf16x2)); }
DI float asf(u32 d) { return __builtin_bit_cast(float, d); }
DI u32 asu(float f) { return __builtin_bit_cast(u32, f); }
DI float bflo(u32 d) { return __builtin_bit_cast(float, d << 16); }
DI float bfhi(u32 d) { return __builtin_bit_cast(float, d & 0xffff0000u); }
DI bf16x8 asb(u32x4 v) { return __builtin_bit_cast(bf16x8, v); }
DI f32x4 mfma16(u32x4 a, u32x4 b, f32x4 c) { return __builtin_amdgcn_mfma_f32_16x16x32_bf16(asb(a), asb(b), c, 0, 0, 0); }
DI f32x16 mfma32(u32x4 a, u32x4 b, f32x16 c) { return __builtin_amdgcn_mfma_f32_32x32x16_bf16(asb(a), asb(b), c, 0, 0, 0); }
DI void MF16(f32x4& acc, u32x4 a, u32x4 b) { asm volatile("v_mfma_f32_16x16x32_bf16 %0, %1, %2, %0" : "+v"(acc) : "v"(a), "v"(b)); }
#define SETTLE4(x) asm volatile("s_nop 7" : "+v"(x))
template <class T> DI T lld(LAS char* p) { return *(LAS const T*)p; }
template <class T> DI void lst(LAS char* p, T v) { *(LAS T*)p = v; }
DI u32x2 trr(LAS char* p) { return __builtin_bit_cast(u32x2, __builtin_amdgcn_ds_read_tr16_b64_v4i16((LAS s16x4*)p)); }
#define BAR() asm volatile("s_waitcnt lgkmcnt(0)\n\ts_barrier" ::: "memory")
#define DRAIN() asm volatile("s_waitcnt vmcnt(0)" ::: "memory")
typedef __amdgpu_buffer_rsrc_t rsrc_t;
DI u32x4 ld128(rsrc_t rs, u32 voff, u32 soff) { return __builtin_amdgcn_raw_buffer_load_b128(rs, voff, soff, AUX_SC1); }
DI u32x2 ld64(rsrc_t rs, u32 voff, u32 soff) { return __builtin_amdgcn_raw_buffer_load_b64(rs, voff, soff, AUX_SC1); }
DI u32 ld32(rsrc_t rs, u32 voff, u32 soff) { return __builtin_amdgcn_raw_buffer_load_b32(rs, voff, soff, AUX_SC1); }
DI void st128(rsrc_t rs, u32x4 v, u32 voff, u32 soff) { __builtin_amdgcn_raw_buffer_store_b128(v, rs, voff + soff, 0, AUX_SC1); }
DI void st64(rsrc_t rs, u32x2 v, u32 voff, u32 soff) { __builtin_amdgcn_raw_buffer_store_b64(v, rs, voff, soff, AUX_SC1); }
DI void st32(rsrc_t rs, u32 v, u32 voff, u32 soff) { __builtin_amdgcn_raw_buffer_store_b32(v, rs, voff, soff, AUX_SC1); }
DI void glds_ws(u32x4 srd, u32 voff, u32 soff, u32 ldsdst) {
    soff = __builtin_amdgcn_readfirstlane(soff); ldsdst = __builtin_amdgcn_readfirstlane(ldsdst);
    asm volatile("s_mov_b32 m0, %0\n\ts_nop 3\n\tbuffer_load_dwordx4 %1, %2, %3 offen sc1 lds" :: "s"(ldsdst), "v"(voff), "s"(srd), "s"(soff) : "memory");
}
DI void glds_in(const void* g, u32 ldsdst) {
    ldsdst = __builtin_amdgcn_readfirstlane(ldsdst);
    asm volatile("s_mov_b32 m0, %0\n\ts_nop 0\n\tglobal_load_lds_dwordx4 %1, off" :: "s"(ldsdst), "v"(g) : "memory");
}
DI void flag_load(u64& fl, u64 nomatch, u32 voff, u32x4 srd, u32 soff) {
    soff = __builtin_amdgcn_readfirstlane(soff);
    fl = nomatch;
    asm volatile("s_nop 4\n\tbuffer_load_dwordx2 %0, %1, %2, %3 offen sc1" : "+v"(fl) : "v"(voff), "s"(srd), "s"(soff) : "memory");
}
DI float sum_g(float v) {
    auto a = __builtin_amdgcn_permlane32_swap(__builtin_bit_cast(u32, v), __builtin_bit_cast(u32, v), false, false);
    v = __builtin_bit_cast(float, (u32)a[0]) + __builtin_bit_cast(float, (u32)a[1]);
    auto b = __builtin_amdgcn_permlane16_swap(__builtin_bit_cast(u32, v), __builtin_bit_cast(u32, v), false, false);
    return __builtin_bit_cast(float, (u32)b[0]) + __builtin_bit_cast(float, (u32)b[1]);
}
DI float max_h(float v) {
    auto a = __builtin_amdgcn_permlane32_swap(__builtin_bit_cast(u32, v), __builtin_bit_cast(u32, v), false, false);
    return fmaxf(__builtin_bit_cast(float, (u32)a[0]), __builtin_bit_cast(float, (u32)a[1]));
}
DI u32x4 ones4() { u32 o = 0x3f803f80u; asm volatile("" : "+v"(o)); return u32x4{o, o, o, o}; }
DI int crow(int reg, int h) { return (reg & 3) + 8 * (reg >> 2) + 4 * h; }

#define STAMP(n) do {} while (0)
#define CSTAMP(n) do {} while (0)
#define CSTAMPW(n, W) do {} while (0)

struct Ctx {
    LAS char* L; u32 Lb;
    char* ws; rsrc_t rs; u32x4 srd;
    int tid, lane, wave, i, j;
    u32 elo, ehi;
};

DI Ctx fresh(const Ctx& c0) {
    Ctx c = c0;
    asm volatile("" : "+v"(c.lane)); asm volatile("" : "+v"(c.tid));
    asm volatile("" : "+s"(c.wave)); asm volatile("" : "+s"(c.i)); asm volatile("" : "+s"(c.j));
    return c;
}
DI GAS u64* flag_ptr(const Ctx& c, int phase, int id) { return (GAS u64*)(c.ws + WS_FLAG + (u32)(phase * 256 + id) * 128); }
DI void signal(const Ctx& c, int phase) {
    if (c.tid == 0) __hip_atomic_store(flag_ptr(c, phase, c.i * 8 + c.j), ((u64)c.ehi << 32) | c.elo, __ATOMIC_RELAXED, __HIP_MEMORY_SCOPE_AGENT);
}
DI void publish(const Ctx& c, int phase) {
    DRAIN();
    BAR();
    signal(c, phase);
}
DI void poll(const Ctx& c, int phase, int id) {
    const u64 want = ((u64)c.ehi << 32) | c.elo;
    GAS u64* f = flag_ptr(c, phase, id < 0 ? 0 : id);
    for (u32 spins = 0; spins < SPIN_CAP; ++spins) {
        const u64 v = __hip_atomic_load(f, __ATOMIC_RELAXED, __HIP_MEMORY_SCOPE_AGENT);
        if (__all(id < 0 || v == want)) break;
        __builtin_amdgcn_s_sleep(1);
    }
    __builtin_amdgcn_fence(__ATOMIC_ACQUIRE, "wavefront");
}
DI void wait_group(const Ctx& c, int phase) {
    if (c.wave == 0) poll(c, phase, c.lane < 8 ? c.i * 8 + c.lane : -1);
    BAR();
}

DI void load_wimage_a(const Ctx& c, const Params& p, int w0) {
    if (c.wave < w0) return;
    const int jj = c.j, nw = 8 - w0, g0 = c.wave - w0;
    const u16 *pk_ = p.wk, *pv_ = p.wv;
    asm volatile("" : "+s"(pk_), "+s"(pv_));
    const char* xsrc = (const char*)((jj >= 3 && jj < 6) ? pv_ : pk_); const int sub = jj < 6 ? jj % 3 : 0;
    xsrc += sub == 0 ? 0 : (sub == 1 ? 16 : 20);
    const char* msrc = (const char*)p.wq + jj * 144;
    const char* a[5]; u32 st[5];
#pragma unroll
    for (int k = 0; k < 5; ++k) {
        const u32 id = 64 * k + c.lane, row = (id * 6554u) >> 16, ch = id - row * 10;
        const bool ex = ch == 9;
        const u32 s1 = ex ? 32 * 36 : 32 * 1152;
        st[k] = s1 * nw;
        a[k] = (ex ? xsrc : msrc) + (ex ? row * 36 : row * 1152 + ch * 16) + (u32)g0 * s1;
    }
#pragma unroll
    for (int n = 0; n < 3; ++n) {
        const int G = g0 + nw * n;
        if (G < 18) {
#pragma unroll
            for (int k = 0; k < 5; ++k) { glds_in(a[k], c.Lb + L_R1 + (G * 5 + k) * 1024); a[k] += st[k]; }
        }
    }
}
DI void load_wimage_o(const Ctx& c, u32 base) {
    const u32 so = base + (u32)c.j * WIMG_BYTES;
    if (c.wave >= 1) {
#pragma unroll
        for (int n = 0; n < 13; ++n)
            if (n < 12 || c.wave < 7) glds_ws(c.srd, c.lane * 16, so + (c.wave - 1 + 7 * n) * 1024, c.Lb + L_R1 + (c.wave - 1 + 7 * n) * 1024);
    }
}
template <int P0, int P1>
DI void load_wimage_part(const Ctx& c, u32 base, int w0, int ldsbase = L_R1) {
    const u32 so = base + (u32)c.j * WIMG_BYTES; const int nw = 8 - w0;
    if (c.wave >= w0) {
#pragma unroll
        for (int n = 0; n < (P1 - P0 + 6) / 7; ++n) {
            const int pc = P0 + (c.wave - w0) + nw * n;
            if (pc < P1) glds_ws(c.srd, c.lane * 16, so + pc * 1024, c.Lb + ldsbase + pc * 1024);
        }
    }
}
DI void load_wimage_o4(const Ctx& c, int which, int part) {
    const u32 so = WS_WIMG + (u32)(which * 8 + c.j) * WIMG_BYTES + (u32)part * 30 * 1024; const u32 dst = c.Lb + L_R1 + part * 30 * 1024;
    if (c.wave < 4) {
#pragma unroll
        for (int n = 0; n < 8; ++n)
            if (n < 7 || c.wave < 2) glds_ws(c.srd, c.lane * 16, so + (c.wave + 4 * n) * 1024, dst + (c.wave + 4 * n) * 1024);
    }
}
struct Fmt { u32x4 v[2]; u32 g[8]; u32 dst[2], cdst[2]; int kind[2]; };
DI void fmt_issue(const Ctx& c, const Params& p, Fmt& m) {
    const u16 *pw = p.wqkv, *pa = p.woA, *pb = p.woB;
    asm volatile("" : "+s"(pw), "+s"(pa), "+s"(pb));
    const int lane = c.lane, rho = lane >> 1, g = rho >> 3, e = rho & 7;
#pragma unroll
    for (int r = 0; r < 2; ++r) {
        const int fr = blockIdx.x * 8 + c.wave + 2048 * r;
        int col, A, P, Q, pitch; const GAS char* src;
        if (fr < 2016) {
            const int jj = fr / 252, rem = fr - jj * 252, u = rem / 18, s = rem - u * 18;
            const int wv = u < 8 ? u : (u - 8 < 5 ? u - 8 : 6), slot = u < 8 ? 0 : 1, a = wv >> 1;
            const bool isrem = (wv & 1) && slot == 1;
            const u32 fo = (u32)(((jj * 8 + wv) * 2 + slot) * 18 + s);
            m.dst[r] = WS_WF + fo * 1024; m.cdst[r] = WS_CSP + fo * 64; m.kind[r] = isrem ? 3 : 1;
            P = s < 16 ? 16 : 4; Q = 2; pitch = 1728 * 2; src = (const GAS char*)pw;
            if (!isrem) {
                col = ((wv & 1) ? 1152 : (slot ? 576 : 0)) + 18 * (4 * jj + a) + 8 * (lane & 1);
                A = s < 16 ? 72 * (s >> 1) + 32 * (s & 1) + 4 * g : 72 * (4 * (s - 16) + g) + 64;
            } else {
                const int n = lane & 15, g2 = lane >> 4;
                col = (a == 0 ? (n < 8 ? 0 : 576) : 1152) + 18 * (4 * jj + ((n & 7) >> 1)) + 16 + (n & 1);
                A = s < 16 ? 72 * (s >> 1) + 32 * (s & 1) + 4 * g2 : 72 * (4 * (s - 16) + g2) + 64;
#pragma unroll
                for (int q = 0; q < 8; ++q) m.g[q] = *(const GAS u16*)(src + (A + P * (q >> 2) + (q & 3)) * pitch + col * 2);
            }
        } else {
            const bool on = fr < 3456;
            const int q = on ? fr - 2016 : 0, im = q / 720, rem = q - im * 720, jj = rem / 90, st = rem - jj * 90, s = st / 5, t = st - s * 5;
            m.dst[r] = WS_WIMG + (u32)(im * 8 + jj) * WIMG_BYTES + (u32)st * 1024; m.cdst[r] = 0; m.kind[r] = on ? 2 : 0;
            col = 72 * jj + 16 * t + (t == 4 ? 0 : 8 * (lane & 1));
            if (s < 16) { A = 36 * s + 18 * (g >> 1) + 4 * (g & 1); P = 8; Q = 2; } else { A = 72 * (4 * (s - 16) + g) + 16; P = 36; Q = 18; }
            src = (const GAS char*)(im ? pb : pa); pitch = 576 * 2;
        }
        if (m.kind[r] == 1 || m.kind[r] == 2) m.v[r] = *(const GAS u32x4*)(src + (A + P * (e >> 2) + Q * ((e >> 1) & 1) + (e & 1)) * pitch + col * 2);
    }
}
DI void fmt_finish(const Ctx& c, Fmt& m) {
    asm volatile("" : "+v"(m.v[0]), "+v"(m.v[1]), "+v"(m.g[0]), "+v"(m.g[1]), "+v"(m.g[2]), "+v"(m.g[3]), "+v"(m.g[4]), "+v"(m.g[5]), "+v"(m.g[6]), "+v"(m.g[7]));
    const int lane = c.lane, i16 = lane & 15, g = lane >> 4, q = i16 >> 2, p = i16 & 3;
#pragma unroll
    for (int r = 0; r < 2; ++r) {
        if (m.kind[r] != 0) {
            u32x4 f;
            if (m.kind[r] == 3) { const u32* w = m.g; f = u32x4{w[0] | (w[1] << 16), w[2] | (w[3] << 16), w[4] | (w[5] << 16), w[6] | (w[7] << 16)}; }
            else {
                LAS char* t = c.L + L_FMT + c.wave * 2048 + r * 1024;
                lst(t + lane * 16, m.v[r]);
                asm volatile("s_waitcnt lgkmcnt(0)" ::: "memory");
                const u32x2 lo = trr(t + (8 * g + q) * 32 + 8 * p), hi = trr(t + (8 * g + 4 + q) * 32 + 8 * p);
                f = u32x4{lo[0], lo[1], hi[0], hi[1]};
            }
            st128(c.rs, f, lane * 16, m.dst[r]);
            if (m.kind[r] != 2) {
                float cs = ((bflo(f[0]) + bfhi(f[0])) + (bflo(f[1]) + bfhi(f[1]))) + ((bflo(f[2]) + bfhi(f[2])) + (bflo(f[3]) + bfhi(f[3])));
                cs = sum_g(cs);
                if (lane < 16) st32(c.rs, asu(cs), lane * 4, m.cdst[r]);
            }
        }
    }
}

template <bool PROJ, bool NAT = false, class HK>
DI void g1_mma(const Ctx& c, const u32x4 (&xf)[9], f32x4 (&acc)[5], HK hook) {
    const int lane = c.lane, kh = c.wave >> 2, q = (lane & 15) >> 2, p = lane & 3, g = lane >> 4;
    LAS char *bN, *bR, *bNo, *bNt, *bRo, *bRt;
    if (PROJ) {
        if (NAT) { bN = c.L + L_R1 + (kh * 256 + 4 * g + q) * WP + 8 * p; bR = c.L + L_R1 + (512 + 32 * kh + 8 * g + q) * WP + 8 * p; }
        else { bN = c.L + L_A1 + (kh * 288 + 4 * g + q) * WP + 8 * p; bR = c.L + L_A1 + (kh * 288 + 72 * g + q) * WP + 8 * p; }
        bNo = bN + 64 * kh; bNt = bN + 64 - 64 * kh; bRo = bR + 64 * kh; bRt = bR + 64 - 64 * kh;
    } else {
        bN = c.L + L_R1 + kh * 8 * 5120 + lane * 16; bR = c.L + L_R1 + (16 + kh) * 5120 + lane * 16;
        bNo = bN + 2048 * kh; bNt = bN + 2048 - 2048 * kh; bRo = bR + 2048 * kh; bRt = bR + 2048 - 2048 * kh;
    }
    u32x4 w[2][5];
    auto rd = [&](int sl, u32x4 (&d)[5]) {
#pragma unroll
        for (int u = 0; u < 5; ++u) {
            if (PROJ) {
                const int r0 = NAT ? (sl < 8 ? 32 * sl : 0) : (sl < 8 ? 72 * (sl >> 1) + 32 * (sl & 1) : 64), hs = sl < 8 ? 16 : 4;
                LAS char* bb = u < 2 ? (sl < 8 ? bNo : bRo) + 32 * u : (u == 2 ? (sl < 8 ? bN : bR) + 128 : (sl < 8 ? bNt : bRt) + 32 * (u - 3));
                const u32x2 lo = trr(bb + r0 * WP), hi = trr(bb + (r0 + hs) * WP);
                d[u] = u32x4{lo[0], lo[1], hi[0], hi[1]};
            } else {
                LAS char* bb = u < 2 ? (sl < 8 ? bNo : bRo) + 1024 * u : (u == 2 ? (sl < 8 ? bN : bR) + 4096 : (sl < 8 ? bNt : bRt) + 1024 * (u - 3));
                d[u] = lld<u32x4>(bb + (sl < 8 ? sl * 5120 : 0));
            }
        }
    };
    rd(0, w[0]);
#pragma unroll
    for (int u = 0; u < 5; ++u) asm volatile("" : "+v"(acc[u]));
#pragma unroll
    for (int sl = 0; sl < 9; ++sl) {
        if (sl + 1 < 9) rd(sl + 1, w[(sl + 1) & 1]);
#pragma unroll
        for (int u = 0; u < 5; ++u) MF16(acc[u], w[sl & 1][u], xf[sl]);
        hook(sl, w[sl & 1]);
    }
    asm volatile("s_nop 7" : "+v"(acc[0]), "+v"(acc[1]), "+v"(acc[2]), "+v"(acc[3]), "+v"(acc[4]));
}
DI void g1_exchange(const Ctx& c, f32x4 (&acc)[5]) {
    LAS char* xw = c.L + L_XCH + c.wave * 3072 + c.lane * 16;
    lst(xw, acc[3]); lst(xw + 1024, acc[4]); lst(xw + 2048, acc[2]);
    BAR();
    LAS char* xr = c.L + L_XCH + (c.wave ^ 4) * 3072 + c.lane * 16;
    acc[0] += lld<f32x4>(xr); acc[1] += lld<f32x4>(xr + 1024); acc[2] += lld<f32x4>(xr + 2048);
}
DI void load_xfrags(const Ctx& c, u32 buf, u32x4 (&xf)[9]) {
    const int m = c.wave & 3, kh = c.wave >> 2;
    const u32 so = buf + (u32)(c.i * 18 * 4 + m) * 1024;
#pragma unroll
    for (int sl = 0; sl < 9; ++sl) { const int s = sl < 8 ? 8 * kh + sl : 16 + kh; xf[sl] = ld128(c.rs, c.lane * 16, so + (u32)s * 4096); }
}
DI u32x4 stats_issue(const Ctx& c, u32 stbuf) {
    const u32 vo = (u32)((c.i * 16 + 2 * (c.lane >> 3)) * 64 + c.wave * 8 + (c.lane & 7)) * 8;
    const u32x2 a = ld64(c.rs, vo, stbuf), b = ld64(c.rs, vo + 512, stbuf);
    return u32x4{a[0], a[1], b[0], b[1]};
}
DI void stats_finish(const Ctx& c, u32x4 v) {
    const int tok = c.wave * 8 + (c.lane & 7);
    float s = asf(v[0]) + asf(v[2]), s2 = asf(v[1]) + asf(v[3]);
    s += __shfl_xor(s, 8); s2 += __shfl_xor(s2, 8);
    s = sum_g(s); s2 = sum_g(s2);
    if (c.lane < 8) {
        lst(c.L + L_STATP + tok * 8, f32x2{s, s2}); lst(c.L + L_STATP + 512 + tok * 8, f32x2{0.f, 0.f});
        const float mean = s * (1.f / DM), var = s2 * (1.f / DM) - mean * mean;
        lst(c.L + L_MEAN + tok * 4, mean); lst(c.L + L_MEAN + 256 + tok * 4, __builtin_amdgcn_rsqf(fmaxf(var, 0.f) + 1e-5f));
    }
}
DI void stats_issue32(const Ctx& c, u32 stbuf, u32x4 (&v)[4]) {
    const u32 vo = (u32)((c.i * 16 + (c.lane >> 4)) * 64 + (c.wave == 7 ? 32 : 0) + 2 * (c.lane & 15)) * 8;
#pragma unroll
    for (int n = 0; n < 4; ++n) v[n] = ld128(c.rs, vo + n * 4 * 512, stbuf);
}
DI void stats_finish32(const Ctx& c, const u32x4 (&v)[4]) {
    float s[4];
#pragma unroll
    for (int e = 0; e < 4; ++e) s[e] = sum_g((asf(v[0][e]) + asf(v[1][e])) + (asf(v[2][e]) + asf(v[3][e])));
    if (c.lane < 16) {
        const int tok = (c.wave == 7 ? 32 : 0) + 2 * c.lane;
        const float m0 = s[0] * (1.f / DM), m1 = s[2] * (1.f / DM);
        lst(c.L + L_MEAN + tok * 4, f32x2{m0, m1});
        lst(c.L + L_MEAN + 256 + tok * 4, f32x2{__builtin_amdgcn_rsqf(fmaxf(s[1] * (1.f / DM) - m0 * m0, 0.f) + 1e-5f), __builtin_amdgcn_rsqf(fmaxf(s[3] * (1.f / DM) - m1 * m1, 0.f) + 1e-5f)});
    }
}

struct KV { u32x4 km; u32x2 kr; u32x4 v0, v1; };
DI int vrow_of(int r) { return r < 18 ? r : 18; }
DI u32 ka_vrow(int blk, int row) { return row < 8 ? KA_V0 + blk * 128 + row * 16 : (row < 16 ? KA_V8 + blk * 128 + (row - 8) * 16 : KA_VX + blk * 64 + (row - 16) * 16); }
DI KV load_tile_ws(const Ctx& c, u32 tile, bool shared) {
    const int r = c.lane & 31, h = c.lane >> 5;
    KV t;
    if (shared) {
        t.km = ld128(c.rs, c.lane * 16, tile);
        t.kr = ld64(c.rs, KA_KREM + r * 8, tile);
        t.v0 = ld128(c.rs, ka_vrow(h, vrow_of(r)), tile);
        t.v1 = ld128(c.rs, ka_vrow(2 + h, vrow_of(r)), tile);
        return t;
    }
    t.km = ld128(c.rs, c.lane * 16, tile);
    t.kr = ld64(c.rs, KT_KREM + r * 8, tile);
    t.v0 = ld128(c.rs, KT_V + h * KT_VBLK + vrow_of(r) * 16, tile);
    t.v1 = ld128(c.rs, KT_V + (2 + h) * KT_VBLK + vrow_of(r) * 16, tile);
    return t;
}
DI f32x16 mfma32k8(u32x2 a, u32x2 b, f32x16 c) { return __builtin_amdgcn_mfma_f32_32x32x8bf16_1k(__builtin_bit_cast(s16x4, a), __builtin_bit_cast(s16x4, b), c, 0, 0, 0); }
DI f32x16 qk(const KV& t, u32x4 qm, u32x2 qr, f32x16 c0) {
    return mfma32k8(t.kr, qr, mfma32(t.km, qm, c0));
}
DI void QK1(f32x16& s, u32x4 km, u32x4 qm) { asm volatile("v_mfma_f32_32x32x16_bf16 %0, %1, %2, 0" : "=&v"(s) : "v"(km), "v"(qm)); }
DI void QK1C(f32x16& s, u32x4 km, u32x4 qm, const f32x16& c0) { asm volatile("v_mfma_f32_32x32x16_bf16 %0, %1, %2, %3" : "=&v"(s) : "v"(km), "v"(qm), "v"(c0)); }
DI void QK2(f32x16& s, u32x2 kr, u32x2 qr) { asm volatile("v_mfma_f32_32x32x8_bf16 %0, %1, %2, %0" : "+v"(s) : "v"(kr), "v"(qr)); }
DI void PV(f32x16& o, u32x4 v, u32x4 p) { asm volatile("v_mfma_f32_32x32x16_bf16 %0, %1, %2, %0" : "+v"(o) : "v"(v), "v"(p)); }
DI float EX(float x) { float r; asm volatile("v_exp_f32 %0, %1" : "=v"(r) : "v"(x)); return r; }
DI u32 CV(float a, float b) { u32 r; asm volatile("v_cvt_pk_bf16_f32 %0, %1, %2" : "=v"(r) : "v"(a), "v"(b)); return r; }
struct Pipe { float e[4]; u32x4 p1; };
template <bool NEXT = true, class F0, class F1, class F2, class F3, class F4>
DI void attn_step(const f32x16& sc, f32x16& sn, f32x16& o, Pipe& pp, const u32x4& km, const u32x2& kr, const u32x4& v0, const u32x4& v1, u32x4 qm, u32x2 qr, F0 first, F1 LK, F2 LR, F3 LV1, F4 LV0) {
    u32x4 p0; float a0, a1, a2, a3, b0, b1, b2, b3;
    if (NEXT) first(sn, km, qm);
    LK();
    pp.p1[2] = CV(pp.e[0], pp.e[1]); pp.p1[3] = CV(pp.e[2], pp.e[3]);
    a0 = EX(sc[0]); a1 = EX(sc[1]); a2 = EX(sc[2]); a3 = EX(sc[3]);
    if (NEXT) QK2(sn, kr, qr); else asm volatile("s_nop 0");
    LR();
    p0[0] = CV(a0, a1); p0[1] = CV(a2, a3);
    b0 = EX(sc[4]); b1 = EX(sc[5]); b2 = EX(sc[6]); b3 = EX(sc[7]);
    PV(o, v1, pp.p1); LV1();
    p0[2] = CV(b0, b1); p0[3] = CV(b2, b3);
    a0 = EX(sc[8]); a1 = EX(sc[9]); a2 = EX(sc[10]); a3 = EX(sc[11]);
    PV(o, v0, p0); LV0();
    pp.p1[0] = CV(a0, a1); pp.p1[1] = CV(a2, a3);
    pp.e[0] = EX(sc[12]); pp.e[1] = EX(sc[13]); pp.e[2] = EX(sc[14]); pp.e[3] = EX(sc[15]);
}
template <class F1, class F2, class F3, class F4>
DI void attn_step2(const f32x16& scx, f32x16& snx, f32x16& ox, Pipe& px, const f32x16& scy, f32x16& sny, f32x16& oy, Pipe& py,
                   const u32x4& km, const u32x2& kr, const u32x4& v0, const u32x4& v1, u32x4 qmx, u32x2 qrx, u32x4 qmy, u32x2 qry, F1 LK, F2 LR, F3 LV1, F4 LV0) {
    u32x4 p0x, p0y; float a0, a1, a2, a3, b0, b1, b2, b3, c0, c1, c2, c3, d0, d1, d2, d3;
#define GRP(MF, PD0, PD1, E0, E1, E2, E3, D0, D1, D2, D3, SRC, I) MF; PD0 = CV(E0, E1); PD1 = CV(E2, E3); D0 = EX(SRC[I]); D1 = EX(SRC[I + 1]); D2 = EX(SRC[I + 2]); D3 = EX(SRC[I + 3])
    GRP(QK1(snx, km, qmx), px.p1[2], px.p1[3], px.e[0], px.e[1], px.e[2], px.e[3], a0, a1, a2, a3, scx, 0);
    GRP(QK1(sny, km, qmy); LK(), py.p1[2], py.p1[3], py.e[0], py.e[1], py.e[2], py.e[3], b0, b1, b2, b3, scy, 0);
    GRP(QK2(snx, kr, qrx), p0x[0], p0x[1], a0, a1, a2, a3, c0, c1, c2, c3, scx, 4);
    GRP(QK2(sny, kr, qry); LR(), p0y[0], p0y[1], b0, b1, b2, b3, d0, d1, d2, d3, scy, 4);
    GRP(PV(ox, v1, px.p1), p0x[2], p0x[3], c0, c1, c2, c3, a0, a1, a2, a3, scx, 8);
    GRP(PV(oy, v1, py.p1); LV1(), p0y[2], p0y[3], d0, d1, d2, d3, b0, b1, b2, b3, scy, 8);
    GRP(PV(ox, v0, p0x), px.p1[0], px.p1[1], a0, a1, a2, a3, px.e[0], px.e[1], px.e[2], px.e[3], scx, 12);
    GRP(PV(oy, v0, p0y); LV0(), py.p1[0], py.p1[1], b0, b1, b2, b3, py.e[0], py.e[1], py.e[2], py.e[3], scy, 12);
#undef GRP
}
DI void attn_finish(f32x16& o, Pipe& pp, u32x4 v1) {
    asm volatile("s_nop 0");
    pp.p1[2] = CV(pp.e[0], pp.e[1]); pp.p1[3] = CV(pp.e[2], pp.e[3]);
    asm volatile("s_nop 1");
    PV(o, v1, pp.p1);
    asm volatile("s_nop 7\n\ts_nop 3" : "+v"(o));
}
DI void attn_finish2(f32x16& ox, Pipe& px, f32x16& oy, Pipe& py, u32x4 v1) {
    asm volatile("s_nop 0");
    px.p1[2] = CV(px.e[0], px.e[1]); px.p1[3] = CV(px.e[2], px.e[3]);
    py.p1[2] = CV(py.e[0], py.e[1]); py.p1[3] = CV(py.e[2], py.e[3]);
    PV(ox, v1, px.p1);
    asm volatile("s_nop 0");
    PV(oy, v1, py.p1);
    asm volatile("s_nop 7\n\ts_nop 3" : "+v"(ox), "+v"(oy));
}
DI void pexp(const f32x16& s, u32x4& p0, u32x4& p1) {
    float e[16];
#pragma unroll
    for (int r = 0; r < 16; ++r) e[r] = __builtin_amdgcn_exp2f(s[r]);
    p0 = u32x4{pk(e[0], e[1]), pk(e[2], e[3]), pk(e[4], e[5]), pk(e[6], e[7])};
    p1 = u32x4{pk(e[8], e[9]), pk(e[10], e[11]), pk(e[12], e[13]), pk(e[14], e[15])};
}
DI float rowmax(const f32x16& s) {
    float a = fmaxf(fmaxf(s[0], s[1]), s[2]), b = fmaxf(fmaxf(s[3], s[4]), s[5]);
#pragma unroll
    for (int r = 6; r < 16; r += 2) { if (r & 2) a = fmaxf(fmaxf(a, s[r]), s[r + 1]); else b = fmaxf(fmaxf(b, s[r]), s[r + 1]); }
    return max_h(fmaxf(a, b));
}
DI f32x16 mask_c(int lane, bool diag) {
    const int r = lane & 31, h = lane >> 5; f32x16 m;
#pragma unroll
    for (int e = 0; e < 16; ++e) { const bool ok = diag ? (crow(e, h) <= r) : (crow(e, h) > r); m[e] = ok ? 0.f : NEG_BIG; }
    return m;
}
DI f32x16 attn_slow(const Ctx& c, u32 tile0, int n0, int n1, bool masked, u32x4 qm, u32x2 qr) {
    const int h = c.lane >> 5;
    qr[1] = 0u;
    f32x16 o = {}; float m = NEG_BIG;
    for (int n = n0; n <= n1; ++n) {
        const KV t = load_tile_ws(c, tile0 + (u32)n * TILE_B, !masked);
        f32x16 z = {};
        f32x16 s = qk(t, qm, qr, z);
        if (masked && (n == n1 || n == n1 - 8)) {
            const int r = c.lane & 31;
#pragma unroll
            for (int e = 0; e < 16; ++e) { const bool ok = (n == n1) ? (crow(e, h) <= r) : (crow(e, h) > r); s[e] = ok ? s[e] : NEG_BIG; }
        }
        const float mn = fmaxf(m, rowmax(s)), al = __builtin_amdgcn_exp2f(m - mn);
        m = mn;
#pragma unroll
        for (int e = 0; e < 16; ++e) { o[e] *= al; s[e] = s[e] <= NEG_BIG ? NEG_BIG : s[e] - mn; }
        u32x4 p0, p1; pexp(s, p0, p1);
        o = mfma32(t.v0, p0, o); o = mfma32(t.v1, p1, o);
    }
    return o;
}
DI bool attn_unsafe(const f32x16& o) {
    float z = 0.f;
#pragma unroll
    for (int e = 0; e < 10; ++e) z = __builtin_fmaf(o[e], 0.f, z);
    const float l = o[10];
    return __any(!(l > 0x1p-40f && l < 0x1p30f && z == 0.f));
}
DI void store_o(const Ctx& c, u32 obuf, const f32x16& o, int a, int t) {
    const int r = c.lane & 31, h = c.lane >> 5;
    const float inv = __builtin_amdgcn_rcpf(o[10]);
    const u32x4 mainp = {pk(o[0] * inv, o[1] * inv), pk(o[2] * inv, o[3] * inv), pk(o[4] * inv, o[5] * inv), pk(o[6] * inv, o[7] * inv)};
    const int m = 2 * t + (r >> 4), tok = r & 15;
    const u32 so = obuf + (u32)(c.i * 18 * 4) * 1024;
    st128(c.rs, mainp, (u32)((2 * c.j + (a >> 1)) * 4 + m) * 1024 + ((2 * (a & 1) + h) * 16 + tok) * 16, so);
    if (h == 0) st32(c.rs, pk(o[8] * inv, o[9] * inv), (u32)((16 + (c.j >> 2)) * 4 + m) * 1024 + ((c.j & 3) * 16 + tok) * 16 + 4 * a, so);
}

DI void a_proj(Ctx& c0, const Params& p, int rep) {
    Ctx c = fresh(c0);
    const int lane = c.lane, m = c.wave & 3, kh = c.wave >> 2, tok = lane & 15, g = lane >> 4;
    u32x4 xf[9]; f32x4 acc[5] = {};
    f32x4 s1 = {}, s2 = {}, ca = {}, cb = {};
    Fmt fm; u64 e0 = 0; u32x4 stv;
    if (rep == 0) {
        e0 = __hip_atomic_load((GAS u64*)(p.ws + WS_EPOCH), __ATOMIC_RELAXED, __HIP_MEMORY_SCOPE_AGENT);
        const char* xt = (const char*)p.x + (size_t)(c.i * 64) * (DM * 2);
#pragma unroll
        for (int n = 0; n < 8; ++n) {
            const int row = c.wave * 8 + n;
            glds_in(xt + row * (DM * 2) + (lane ^ (row & 15)) * 16, c.Lb + (row < 60 ? L_XIN + row * 1024 : L_XIN2 + (row - 60) * 1024));
        }
        xf[8] = *(const u32x4*)(xt + (m * 16 + tok) * (DM * 2) + (512 + 32 * kh + 8 * g) * 2);
        load_wimage_a(c, p, 0);
        fmt_issue(c, p, fm);
        STAMP(13);
        DRAIN(); BAR();
        STAMP(18);
        asm volatile("" : "+v"(xf[8]), "+v"(e0));
        { const int row = m * 16 + tok, t2 = tok ^ (g >> 1);
          LAS char* xb = c.L + (row < 60 ? L_XIN + row * 1024 : L_XIN2 + (row - 60) * 1024) + kh * 512 + 8 * (g & 1);
#pragma unroll
          for (int sl = 0; sl < 8; ++sl) {
              const int K = 4 * (sl & 3);
              const u32x2 lo = lld<u32x2>(xb + (sl >> 2) * 256 + ((K ^ t2) * 16)), hi = lld<u32x2>(xb + (sl >> 2) * 256 + (((K + 2) ^ t2) * 16));
              xf[sl] = u32x4{lo[0], lo[1], hi[0], hi[1]};
          } }
        STAMP(21);
        if (c.tid < 180) st128(c.rs, lld<u32x4>(c.L + L_R1 + c.i * 2880 + c.tid * 16), c.tid * 16, WS_WIMGA + (u32)c.j * WIMG_BYTES + (u32)c.i * 2880);
        BAR();
        STAMP(2);
    } else {
        wait_group(c, 5);
        load_xfrags(c, WS_XF + 1 * XF_BYTES, xf);
        stv = stats_issue(c, WS_ST + 1 * ST_BYTES);
        asm volatile("s_waitcnt vmcnt(11)" ::: "memory"); BAR();
        STAMP(32);
    }
    if (rep != 0) { g1_mma<true>(c, xf, acc, [](int, const u32x4 (&)[5]) {}); stats_finish(c, stv); }
    else {
        u32x4 ones = ones4();
        const int tlc = m < 2 ? 2 * kh + m : 2 * (1 - kh) + (m - 2), q = (lane & 15) >> 2, pp = lane & 3;
        LAS char* bN = c.L + L_R1 + (kh * 256 + 4 * g + q) * WP + 8 * pp + 32 * tlc;
        LAS char* bR = c.L + L_R1 + (512 + 32 * kh + 8 * g + q) * WP + 8 * pp + 32 * tlc;
        asm volatile("" : "+v"(s1), "+v"(s2), "+v"(ca), "+v"(cb), "+v"(ones));
        u32x4 wx[2];
        auto rdx = [&](int sl) {
            const int r0 = sl < 8 ? 32 * sl : 0, hs = sl < 8 ? 16 : 4; LAS char* b = sl < 8 ? bN : bR;
            const u32x2 lo = trr(b + r0 * WP), hi = trr(b + (r0 + hs) * WP);
            wx[sl & 1] = u32x4{lo[0], lo[1], hi[0], hi[1]}; };
        rdx(0);
        g1_mma<true, true>(c, xf, acc, [&](int sl, const u32x4 (&w)[5]) {
            if (sl + 1 < 9) rdx(sl + 1);
            MF16(s1, ones, xf[sl]); MF16(s2, xf[sl], xf[sl]); MF16(cb, w[2], ones); MF16(ca, wx[sl & 1], ones);
        });
        asm volatile("s_nop 7" : "+v"(s1), "+v"(s2), "+v"(ca), "+v"(cb));
        const int rg = tok & 3;
        float d = rg == 0 ? s2[0] : (rg == 1 ? s2[1] : (rg == 2 ? s2[2] : s2[3]));
        d = sum_g(g == (tok >> 2) ? d : 0.f);
        if (g == 0) lst(c.L + L_STATP + kh * 512 + (m * 16 + tok) * 8, f32x2{s1[0], d});
        const int tl = m < 2 ? 2 * kh + m : 2 * (1 - kh) + (m - 2);
        if (tok == 0) { lst(c.L + L_CSA + (kh * 5 + tl) * 64 + g * 16, ca); if (m == 0) lst(c.L + L_CSA + (kh * 5 + 4) * 64 + g * 16, cb); }
    }
    STAMP(3 + rep * 30);
    g1_exchange(c, acc);
    const f32x2 sa = lld<f32x2>(c.L + L_STATP + (m * 16 + tok) * 8), sb = lld<f32x2>(c.L + L_STATP + 512 + (m * 16 + tok) * 8);
    const float mean = (sa[0] + sb[0]) * (1.f / DM), var = (sa[1] + sb[1]) * (1.f / DM) - mean * mean;
    const float rstd = __builtin_amdgcn_rsqf(fmaxf(var, 0.f) + 1e-5f);
    LAS char* stg = c.L + L_STG + (m * 16 + tok) * STG_P + g * 8;
    auto fin = [&](int u, int t, float sc) {
        const f32x4 cs = lld<f32x4>(c.L + L_CSA + t * 64 + g * 16) + lld<f32x4>(c.L + L_CSA + (5 + t) * 64 + g * 16);
        f32x4 v;
#pragma unroll
        for (int e = 0; e < 4; ++e) v[e] = (acc[u][e] - mean * cs[e]) * sc;
        return u32x2{pk(v[0], v[1]), pk(v[2], v[3])};
    };
    if (kh == 1) {
        const u32x2 d4 = fin(2, 4, g < 2 ? rstd * QSCALE : rstd);
        lst(stg + 128, d4);
        const u32 kva = WS_KVA + rep * KVA_BYTES + (u32)(2 * c.i + (m >> 1)) * TILE_B;
        const int j = c.j, key = (m & 1) * 16 + tok;
        if (j < 2) { if (g >= 2) st64(c.rs, d4, j * 512 + key * 16 + (g - 2) * 8, kva); }
        else if (j == 2) { if (g == 3) st64(c.rs, u32x2{d4[1], 0x00003f80u}, KA_KREM + key * 8, kva); }
        else if (j < 6) {
            asm volatile("s_waitcnt lgkmcnt(0)" ::: "memory");
            const int q = (lane & 15) >> 2, pp = lane & 3;
            LAS char* s = c.L + L_STG + (16 * m + q + 4 * (pp >> 1)) * STG_P + 144 + 8 * (pp & 1);
            const u32x2 lo = trr(s), hi = trr(s + 8 * STG_P);
            const int dl = lane & 7, blk = (m & 1) * 2 + ((lane >> 3) & 1);
            if (j < 5) { if (lane < 16) st128(c.rs, u32x4{lo[0], lo[1], hi[0], hi[1]}, (j == 3 ? KA_V0 : KA_V8) + blk * 128 + dl * 16, kva); }
            else {
                const u32x4 on = ones4();
                const u32x4 v = dl >= 6 ? u32x4{lo[0], lo[1], hi[0], hi[1]} : on;
                if (lane < 16 && (dl >= 6 || dl < 2)) st128(c.rs, v, KA_VX + blk * 64 + (dl >= 6 ? dl - 6 : 2 + dl) * 16, kva);
            }
        }
    }
    lst(stg + (2 * kh) * 32, fin(0, 2 * kh, rstd * QSCALE));
    lst(stg + (2 * kh + 1) * 32, fin(1, 2 * kh + 1, rstd * QSCALE));
    if (rep == 0) {
        fmt_finish(c, fm);
        const u64 e = e0 + 1; c0.elo = c.elo = __builtin_amdgcn_readfirstlane((u32)e); c0.ehi = c.ehi = __builtin_amdgcn_readfirstlane((u32)(e >> 32));
    }
    STAMP(4 + rep * 30);
    publish(c, rep * 6);
}

DI void load_wring(const Ctx& c, u32x4 (&w0)[9], u32x4 (&w1)[9]) {
    const u32 wso = WS_WF + (u32)((c.j * 8 + c.wave) * 2) * 18 * 1024;
#pragma unroll
    for (int s = 0; s < 9; ++s) { w0[s] = ld128(c.rs, c.lane * 16, wso + s * 1024); w1[s] = ld128(c.rs, c.lane * 16, wso + (18 + s) * 1024); }
}
DI void colsums_b(const Ctx& c) {
    const int tok = c.lane & 15, g = c.lane >> 4;
    if (c.wave >= 1) {
        const int extra = c.wave == 1 ? 0 : c.wave;
        u32 v[4][5];
#pragma unroll
        for (int q = 0; q < 4; ++q) {
            const int unit = (q < 2 ? c.wave : extra) * 2 + (q & 1);
#pragma unroll
            for (int u = 0; u < 5; ++u) { const int s = g + 4 * u; v[q][u] = (q < 2 || c.wave == 1) ? ld32(c.rs, (s < 18 ? s : 0) * 64 + tok * 4, WS_CSP + (u32)((c.j * 16 + unit) * 18) * 64) : 0u; }
        }
#pragma unroll
        for (int q = 0; q < 4; ++q) {
            const int unit = (q < 2 ? c.wave : extra) * 2 + (q & 1);
            float cs = (asf(v[q][0]) + asf(v[q][1])) + (asf(v[q][2]) + asf(v[q][3])) + (g < 2 ? asf(v[q][4]) : 0.f);
            cs = sum_g(cs);
            if (g == 0 && (q < 2 || c.wave == 1)) lst(c.L + L_CSB + unit * 64 + tok * 4, cs);
        }
    }
}
struct ATail { f32x16 o; u32x4 qm; u32x2 qr; };
DI void a_attn(const Ctx& c0, const Params& p, int rep, ATail& st) {
    const Ctx c = fresh(c0);
    const int lane = c.lane, r = lane & 31, h = lane >> 5, a = c.wave & 3, kh = c.wave >> 2;
    const int ph = rep * 6;
    const u32 kva = WS_KVA + rep * KVA_BYTES;
    u32x4 qmx, qmy; u32x2 qrx, qry;
    { LAS char* s = c.L + L_STG + (32 * kh + r) * STG_P + 36 * a;
      qmx = u32x4{lld<u32>(s + 16 * h), lld<u32>(s + 16 * h + 4), lld<u32>(s + 16 * h + 8), lld<u32>(s + 16 * h + 12)};
      const u32 rx = lld<u32>(s + 32);
      s += (32 - 64 * kh) * STG_P;
      qmy = u32x4{lld<u32>(s + 16 * h), lld<u32>(s + 16 * h + 4), lld<u32>(s + 16 * h + 8), lld<u32>(s + 16 * h + 12)};
      const u32 ry = lld<u32>(s + 32);
      qrx = u32x2{h == 0 ? rx : 0u, 0u}; qry = u32x2{h == 0 ? ry : 0u, 0u}; }
    const int ch0 = c.i >> 2;
    auto chunk_ids = [&](int n) { const int ch = (ch0 + n) & 7; return ch * 32 + (lane & 31); };
    u32 pvo[5];
#pragma unroll
    for (int k = 0; k < 5; ++k) {
        const u32 S = (u32)((c.wave & 3) + 4 * k) * 64 + lane, tile = (S * 52429u) >> 23, ps = S - tile * 160;
        const u32 v = ps - 64, blk = (v * 3450u) >> 16, row = v - blk * 19;
        const u32 src = ps < 64 ? ps * 16 : (ps < 140 ? ka_vrow(blk, row) : (ps < 156 ? KA_KREM + (ps - 140) * 16 : ps * 16));
        pvo[k] = tile * TILE_B + src;
    }
    auto issue_piece = [&](int n, int k) {
        const int ch = (ch0 + n) & 7; const u32 so = kva + (u32)ch * CHUNK_B; const u32 dst = c.Lb + L_RING + (n % 3) * CHUNK_B;
        glds_ws(c.srd, pvo[k], so, dst + ((c.wave & 3) + 4 * k) * 1024);
    };
    auto issue_chunk8 = [&](int n) {
        if (c.wave < 4) { issue_piece(n, 0); issue_piece(n, 2); issue_piece(n, 4); } else { issue_piece(n, 1); issue_piece(n, 3); }
    };
    if (c.wave == 0) poll(c, ph, lane < 32 ? chunk_ids(0) : chunk_ids(1));
    BAR();
    STAMP(6 + rep * 30);
    issue_chunk8(0);
    if (c.wave < 4) { issue_piece(1, 0); issue_piece(1, 1); issue_piece(1, 2); issue_piece(1, 3); issue_piece(1, 4); asm volatile("s_waitcnt vmcnt(5)" ::: "memory"); }
    else DRAIN();
    BAR();
    STAMP(7 + rep * 30); if (rep == 0) CSTAMP(61);
    if (rep == 1) { CSTAMPW(0, 4); CSTAMPW(8, 0); }
    const int vr = vrow_of(r);
    LAS char* kb = c.L + L_RING + lane * 16;
    LAS char* rb = c.L + L_RING + KT_KREM + r * 8;
    LAS char* vb = c.L + L_RING + KT_V + h * KT_VBLK + vr * 16;
    f32x16 sAx, sBx, sAy, sBy, ox, oy;
    const u32x4 k0m = lld<u32x4>(kb); const u32x2 k0r = lld<u32x2>(rb);
    kb += kh * 4 * TILE_B; rb += kh * 4 * TILE_B; vb += kh * 4 * TILE_B;
    const u32x4 kom = lld<u32x4>(kb); const u32x2 kor = lld<u32x2>(rb);
    u32x4 km = lld<u32x4>(kb + TILE_B), v0 = lld<u32x4>(vb), v1 = lld<u32x4>(vb + 2 * KT_VBLK); u32x2 kr = lld<u32x2>(rb + TILE_B);
    Pipe px, py;
    { u32 z = 0u; asm volatile("" : "+v"(z));
      px.e[0] = px.e[1] = px.e[2] = px.e[3] = asf(z); px.p1 = u32x4{z, z, z, z}; py = px; }
    asm volatile("" : "+v"(px.p1), "+v"(px.e[0]), "+v"(px.e[1]), "+v"(px.e[2]), "+v"(px.e[3]), "+v"(qmx), "+v"(qrx));
    asm volatile("s_nop 1" : "+v"(py.p1), "+v"(py.e[0]), "+v"(py.e[1]), "+v"(py.e[2]), "+v"(py.e[3]), "+v"(qmy), "+v"(qry));
    QK1(ox, px.p1, px.p1); QK1(oy, px.p1, px.p1);
    QK1(sBx, k0m, qmx); QK1(sBy, k0m, qmy); QK2(sBx, k0r, qrx); QK2(sBy, k0r, qry);
    QK1(sAx, kom, qmx); QK1(sAy, kom, qmy);
    asm volatile("s_nop 7\n\ts_nop 1" : "+v"(sBx), "+v"(sBy));
    { const float mx = rowmax(sBx), my = rowmax(sBy);
      if (h == 0) { qrx[1] = pk(-mx, 0.f); qry[1] = pk(-my, 0.f); } }
    asm volatile("s_nop 1" : "+v"(qrx), "+v"(qry));
    QK2(sAx, kor, qrx); QK2(sAy, kor, qry);
    asm volatile("s_nop 7\n\ts_nop 3" : "+v"(sAx), "+v"(sAy));
    if (rep == 1) { CSTAMPW(1, 4); CSTAMPW(9, 0); }
    int slot = 0;
    const u64 want = ((u64)c.ehi << 32) | c.elo;
    const u32 fvo = (lane & 31) * 128;
    u64 fl = want;
    if (c.wave == 0) flag_load(fl, ~want, fvo, c.srd, WS_FLAG + (u32)(ph * 256 + ((ch0 + 2) & 7) * 32) * 128);
#pragma unroll 1
    for (int n = 0; n < 8; ++n) {
        const int nslot = slot == 2 ? 0 : slot + 1;
        LAS char* kc = kb + slot * CHUNK_B; LAS char* rc = rb + slot * CHUNK_B; LAS char* vc = vb + slot * CHUNK_B;
        LAS char* kn = kb + nslot * CHUNK_B; LAS char* rn = rb + nslot * CHUNK_B; LAS char* vn = vb + nslot * CHUNK_B;
#define A_TILE(U, SCX, SNX, SCY, SNY, X1, X2) attn_step2(SCX, SNX, ox, px, SCY, SNY, oy, py, km, kr, v0, v1, qmx, qrx, qmy, qry, \
            [&] { km = lld<u32x4>((U) < 2 ? kc + ((U) + 2) * TILE_B : kn + ((U) - 2) * TILE_B); X1; }, \
            [&] { kr = lld<u32x2>((U) < 2 ? rc + ((U) + 2) * TILE_B : rn + ((U) - 2) * TILE_B); }, \
            [&] { v1 = lld<u32x4>(vc + (U) * TILE_B + 2 * KT_VBLK); X2; }, \
            [&] { v0 = lld<u32x4>((U) < 3 ? vc + ((U) + 1) * TILE_B : vn); })
        A_TILE(0, sAx, sBx, sAy, sBy, (void)0, (void)0); A_TILE(1, sBx, sAx, sBy, sAy, (void)0, (void)0);
        if (__builtin_expect(c.wave < 4, 0)) {
            asm volatile("s_waitcnt vmcnt(0)" : "+v"(fl) :: "memory");
            if (__builtin_expect(!__all(fl == want), 0)) poll(c, ph, chunk_ids(n + 2));
        }
        BAR();
        if (__builtin_expect(c.wave < 4, 0)) {
            fl = want;
            if (c.wave == 0 && n + 3 < 8) flag_load(fl, ~want, fvo, c.srd, WS_FLAG + (u32)(ph * 256 + ((ch0 + n + 3) & 7) * 32) * 128);
            if (n + 2 < 8) { issue_piece(n + 2, 0); issue_piece(n + 2, 1); issue_piece(n + 2, 2); issue_piece(n + 2, 3); issue_piece(n + 2, 4); }
            if (n >= 5) load_wimage_o4(c, 0, n - 5);
            if (rep == 0 && n == 0) {
                u32 z0 = 0u; asm volatile("" : "+v"(z0));
                const u32x4 z = {z0, z0, z0, z0};
#pragma unroll
                for (int q = 0; q < 5; ++q)
                    st128(c.rs, z, lane * 16 + q * 1024, WS_KVB + (u32)((4 * c.j + c.wave) * 64 + 2 * c.i) * TILE_B);
                if (c.wave < 2) st128(c.rs, z, (u32)((16 + (c.j >> 2)) * 4 + (lane >> 4)) * 1024 + ((c.j & 3) * 16 + (lane & 15)) * 16, WS_OF + c.wave * XF_BYTES + (u32)(c.i * 72) * 1024);
            }
        }
        A_TILE(2, sAx, sBx, sAy, sBy, (void)0, (void)0); A_TILE(3, sBx, sAx, sBy, sAy, (void)0, (void)0);
#undef A_TILE
        slot = nslot;
    }
    if (rep == 1) { CSTAMPW(2, 4); CSTAMPW(10, 0); }
    attn_finish2(ox, px, oy, py, v1);
    { LAS char* mine = c.L + L_RING + kh * 2 * CHUNK_B + a * 3072 + lane * 16;
#pragma unroll
      for (int e = 0; e < 3; ++e) lst(mine + e * 1024, f32x4{oy[4 * e], oy[4 * e + 1], oy[4 * e + 2], oy[4 * e + 3]}); }
    st.o = ox; st.qm = qmx; st.qr = qrx;
}

template <bool ISB, class HK>
DI void out_proj(const Ctx& c0, const Params& p, int rep, u32 (&xres)[3][2], HK behind_x) {
    const Ctx c = fresh(c0);
    const int lane = c.lane, m = c.wave & 3, kh = c.wave >> 2, tok = lane & 15, g = lane >> 4;
    const int layer = rep * 2 + (ISB ? 1 : 0);
    wait_group(c, rep * 6 + (ISB ? 4 : 1));
    STAMP((ISB ? 25 : 10) + rep * 30);
    u32x4 xf[9]; f32x4 acc[5] = {};
    load_xfrags(c, WS_OF + (rep * RP * 2 + (ISB ? 1 : 0)) * XF_BYTES, xf);
    __builtin_amdgcn_sched_barrier(0);
    behind_x(c);
    if (ISB) {
        asm volatile("s_waitcnt vmcnt(9)" ::: "memory"); BAR();
    }
    g1_mma<false>(c, xf, acc, [](int, const u32x4 (&)[5]) {});
    STAMP((ISB ? 26 : 11) + rep * 30);
    g1_exchange(c, acc);
    STAMP((ISB ? 27 : 12) + rep * 30);
#pragma unroll
    for (int u = 0; u < 3; ++u) {
        const float v0 = bflo(xres[u][0]) + acc[u][0], v1 = bfhi(xres[u][0]) + acc[u][1], v2 = bflo(xres[u][1]) + acc[u][2], v3 = bfhi(xres[u][1]) + acc[u][3];
        xres[u][0] = pk(v0, v1); xres[u][1] = pk(v2, v3);
    }
    if (ISB && rep == 1) {
        char* o = (char*)p.out + (size_t)(c.i * 64 + m * 16 + tok) * (DM * 2) + (c.j * 72 + 4 * g) * 2;
        *(u32x2*)(o + (2 * kh) * 32) = u32x2{xres[0][0], xres[0][1]};
        *(u32x2*)(o + (2 * kh + 1) * 32) = u32x2{xres[1][0], xres[1][1]};
        if (kh == 1 && g < 2) *(u32x2*)(o + 128) = u32x2{xres[2][0], xres[2][1]};
        return;
    }
    const int xi = layer == 0 ? 0 : (layer == 1 ? 1 : 2 * RP);
    const u32 so = WS_XF + xi * XF_BYTES + (u32)(c.i * 18 * 4 + m) * 1024;
    st128(c.rs, u32x4{xres[0][0], xres[0][1], xres[1][0], xres[1][1]}, lane * 16, so + (u32)(2 * c.j + kh) * 4096);
    if (kh == 1 && g < 2) st64(c.rs, u32x2{xres[2][0], xres[2][1]}, ((c.j & 3) * 16 + tok) * 16 + 8 * g, so + (u32)(16 + (c.j >> 2)) * 4096);
    __builtin_amdgcn_sched_barrier(0);
    float s1 = 0.f, s2 = 0.f;
#pragma unroll
    for (int u = 0; u < 3; ++u) {
        u32 d0 = xres[u][0], d1 = xres[u][1];
        asm volatile("" : "+v"(d0), "+v"(d1));
        if (u == 2 && !(kh == 1 && g < 2)) { d0 = 0u; d1 = 0u; }
        const float a0 = bflo(d0), a1 = bfhi(d0), a2 = bflo(d1), a3 = bfhi(d1);
        s1 += (a0 + a1) + (a2 + a3); s2 += (a0 * a0 + a1 * a1) + (a2 * a2 + a3 * a3);
    }
    s1 = sum_g(s1); s2 = sum_g(s2);
    if (g == 0) st64(c.rs, u32x2{asu(s1), asu(s2)}, (u32)((c.i * 16 + 2 * c.j + kh) * 64 + m * 16 + tok) * 8, WS_ST + xi * ST_BYTES);
    publish(c, rep * 6 + (ISB ? 5 : 2));
    if (!ISB && rep == 0) colsums_b(c);
    if (ISB) load_wimage_part<36, 90>(c, WS_WIMGA, 1, L_A1);
}

template <bool EARLY>
DI void a_tail(const Ctx& c0, const Params& p, int rep, ATail& st, u32 (&xres)[3][2], u32x4 (&w0)[9], u32x4 (&w1)[9]) {
    const Ctx c = fresh(c0);
    const int lane = c.lane, a = c.wave & 3, kh = c.wave >> 2;
    BAR();
    LAS char* theirs = c.L + L_RING + (1 - kh) * 2 * CHUNK_B + a * 3072 + lane * 16;
#pragma unroll
    for (int e = 0; e < 3; ++e) { const f32x4 v = lld<f32x4>(theirs + e * 1024); st.o[4 * e] += v[0]; st.o[4 * e + 1] += v[1]; st.o[4 * e + 2] += v[2]; st.o[4 * e + 3] += v[3]; }
    STAMP(8 + rep * 30); if (rep == 0) CSTAMP(62);
    if (rep == 1) { CSTAMPW(3, 4); CSTAMPW(11, 0); }
    if (attn_unsafe(st.o)) st.o = attn_slow(c, WS_KVA + rep * KVA_BYTES, 0, 63, false, st.qm, st.qr);
    store_o(c, WS_OF + (rep * RP * 2) * XF_BYTES, st.o, a, kh);
    if (rep == 1) { CSTAMPW(4, 4); CSTAMPW(12, 0); }
    publish(c, rep * 6 + 1);
    if (EARLY) load_wring(c, w0, w1);
    STAMP(9 + rep * 30);
    out_proj<false>(c, p, rep, xres, [&](const Ctx& cc) {
        if (rep == 0) {
            const int m = cc.wave & 3, k2 = cc.wave >> 2, tok = cc.lane & 15, g = cc.lane >> 4;
            const char* rr = (const char*)p.x + (size_t)(cc.i * 64 + m * 16 + tok) * (DM * 2) + (cc.j * 72 + 4 * g) * 2;
            const u32x2 r0 = *(const u32x2*)(rr + (2 * k2) * 32), r1 = *(const u32x2*)(rr + (2 * k2 + 1) * 32), r2 = *(const u32x2*)(rr + (g < 2 ? 128 : 0));
            xres[0][0] = r0[0]; xres[0][1] = r0[1]; xres[1][0] = r1[0]; xres[1][1] = r1[1]; xres[2][0] = r2[0]; xres[2][1] = r2[1];
        }
        if (!EARLY) load_wring(cc, w0, w1); });
}

struct T_ { static constexpr bool value = true; };
struct F_ { static constexpr bool value = false; };
DI void b_proj(const Ctx& c0, const Params& p, int rep, u32x4 (&w0)[9], u32x4 (&w1)[9], u32x4& qm, u32x2& qr) {
    const Ctx c = fresh(c0);
    const int lane = c.lane, tok = lane & 15, g = lane >> 4, a = c.wave >> 1, odd = c.wave & 1;
    wait_group(c, rep * 6 + 2);
    STAMP(15 + rep * 30);
    const int xi = rep == 0 ? 0 : 2 * RP;
    u32x4 stv[4];
    if (c.wave == 5 || c.wave == 7) stats_issue32(c, WS_ST + xi * ST_BYTES, stv);
    { const u32 so = WS_XF + xi * XF_BYTES + (u32)(c.i * 72) * 1024;
#pragma unroll
      for (int n = 0; n < 9; ++n) glds_ws(c.srd, lane * 16, so + (c.wave + 8 * n) * 1024, c.Lb + L_XB + (c.wave + 8 * n) * 1024); }
    asm volatile("s_waitcnt vmcnt(5)" ::: "memory"); BAR();
    STAMP(16 + rep * 30);
    f32x4 a0[4] = {}, a1[4] = {};
    const u32 wso = WS_WF + (u32)((c.j * 8 + c.wave) * 2) * 18 * 1024;
    LAS char* xb = c.L + L_XB + lane * 16;
    const bool t0 = !odd, t1 = !odd || a == 0, has1 = !odd || a < 2;
    auto body = [&](auto T0, auto T1, auto H1) {
        u32x4 x[2][4];
#pragma unroll
        for (int m = 0; m < 4; ++m) { x[0][m] = lld<u32x4>(xb + m * 1024); asm volatile("" : "+v"(a0[m]), "+v"(a1[m])); }
        if (!H1.value) stats_finish32(c, stv);
#pragma unroll
        for (int s = 0; s < 18; ++s) {
            if (s == 7) {
                if (H1.value) asm volatile("s_waitcnt vmcnt(14)" ::: "memory"); else asm volatile("s_waitcnt vmcnt(7)" ::: "memory");
                BAR();
            }
            if (s + 1 < 18) {
#pragma unroll
                for (int m = 0; m < 4; ++m) x[(s + 1) & 1][m] = lld<u32x4>(xb + ((s + 1) * 4 + m) * 1024);
            }
#pragma unroll
            for (int m = 0; m < 4; ++m) {
                if (T0.value) MF16(a0[m], w0[s % 9], x[s & 1][m]); else MF16(a0[m], x[s & 1][m], w0[s % 9]);
                if (H1.value) { if (T1.value) MF16(a1[m], w1[s % 9], x[s & 1][m]); else MF16(a1[m], x[s & 1][m], w1[s % 9]); }
            }
            if (s < 9) { w0[s] = ld128(c.rs, lane * 16, wso + (9 + s) * 1024); if (H1.value) w1[s] = ld128(c.rs, lane * 16, wso + (27 + s) * 1024); }
        }
        asm volatile("s_nop 7" : "+v"(a0[0]), "+v"(a0[1]), "+v"(a0[2]), "+v"(a0[3]), "+v"(a1[0]), "+v"(a1[1]), "+v"(a1[2]), "+v"(a1[3]));
    };
    if (!odd) body(T_{}, T_{}, T_{}); else if (a == 0) body(F_{}, T_{}, T_{}); else if (a == 1) body(F_{}, F_{}, T_{}); else body(F_{}, F_{}, F_{});
    (void)t0; (void)t1; (void)has1;
    STAMP(17 + rep * 30);
    const u32 kvb = WS_KVB + rep * RP * KVB_BYTES;
    const u32 hb = kvb + (u32)((4 * c.j + a) * 64 + 2 * c.i) * TILE_B;
    auto fin_t = [&](const f32x4 (&acc)[4], int slot, float sc, u32 (&d)[4][2]) {
        const f32x4 cs = lld<f32x4>(c.L + L_CSB + (c.wave * 2 + slot) * 64 + g * 16);
#pragma unroll
        for (int m = 0; m < 4; ++m) {
            const float mean = lld<float>(c.L + L_MEAN + (m * 16 + tok) * 4), rs = lld<float>(c.L + L_MEAN + 256 + (m * 16 + tok) * 4) * sc;
            d[m][0] = pk((acc[m][0] - mean * cs[0]) * rs, (acc[m][1] - mean * cs[1]) * rs);
            d[m][1] = pk((acc[m][2] - mean * cs[2]) * rs, (acc[m][3] - mean * cs[3]) * rs);
        }
    };
    auto fin_p = [&](const f32x4 (&acc)[4], int slot, u32 (&d)[4][2]) {
        const float cs = lld<float>(c.L + L_CSB + (c.wave * 2 + slot) * 64 + tok * 4);
#pragma unroll
        for (int m = 0; m < 4; ++m) {
            const f32x4 mean = lld<f32x4>(c.L + L_MEAN + (m * 16 + 4 * g) * 4), rs = lld<f32x4>(c.L + L_MEAN + 256 + (m * 16 + 4 * g) * 4);
            d[m][0] = pk((acc[m][0] - mean[0] * cs) * rs[0], (acc[m][1] - mean[1] * cs) * rs[1]);
            d[m][1] = pk((acc[m][2] - mean[2] * cs) * rs[2], (acc[m][3] - mean[3] * cs) * rs[3]);
        }
    };
    auto frag16 = [&](const u32 (&d)[4][2], int t) {
        auto x = __builtin_amdgcn_permlane16_swap(d[2 * t][0], d[2 * t + 1][0], false, false);
        auto y = __builtin_amdgcn_permlane16_swap(d[2 * t][1], d[2 * t + 1][1], false, false);
        return u32x4{(u32)x[0], (u32)y[0], (u32)x[1], (u32)y[1]};
    };
    auto frag32 = [&](const u32 (&d)[4][2], int t) {
        auto x = __builtin_amdgcn_permlane32_swap(d[2 * t][0], d[2 * t + 1][0], false, false);
        auto y = __builtin_amdgcn_permlane32_swap(d[2 * t][1], d[2 * t + 1][1], false, false);
        return u32x4{(u32)x[0], (u32)y[0], (u32)x[1], (u32)y[1]};
    };
    u32 d[4][2];
    LAS char* lh = c.L + L_KVB + a * 25600 + 8 * TILE_B; LAS char* l0 = c.L + L_KVB + 8 * TILE_B;
    if (!odd) {
        fin_t(a1, 1, 1.f, d);
        { const u32x4 f0 = frag16(d, 0), f1 = frag16(d, 1);
          st128(c.rs, f0, lane * 16, hb); st128(c.rs, f1, lane * 16, hb + TILE_B);
          lst(lh + lane * 16, f0); lst(lh + TILE_B + lane * 16, f1); }
        fin_t(a0, 0, QSCALE, d);
        qm = frag16(d, 0);
        lst(c.L + L_QT1 + a * 1024 + lane * 16, frag16(d, 1));
    } else {
        fin_p(a0, 0, d);
        { const u32x4 f0 = frag32(d, 0), f1 = frag32(d, 1);
          st128(c.rs, f0, KT_V + g * KT_VBLK + tok * 16, hb); st128(c.rs, f1, KT_V + g * KT_VBLK + tok * 16, hb + TILE_B);
          lst(lh + KT_V + g * KT_VBLK + tok * 16, f0); lst(lh + TILE_B + KT_V + g * KT_VBLK + tok * 16, f1); }
        if (a == 0) {
            fin_t(a1, 1, g < 2 ? QSCALE : 1.f, d);
#pragma unroll
            for (int m = 0; m < 4; ++m) {
                if (g < 2) { lst(c.L + L_QREM + (2 * g) * 256 + (m * 16 + tok) * 4, d[m][0]); lst(c.L + L_QREM + (2 * g + 1) * 256 + (m * 16 + tok) * 4, d[m][1]); }
                else {
                    const u32 hb0 = kvb + (u32)(4 * c.j * 64 + 2 * c.i + (m >> 1)) * TILE_B;
                    st64(c.rs, u32x2{d[m][0], 0x00003f80u}, (u32)(2 * (g - 2)) * 64 * TILE_B + KT_KREM + ((m & 1) * 16 + tok) * 8, hb0);
                    st64(c.rs, u32x2{d[m][1], 0x00003f80u}, (u32)(2 * (g - 2) + 1) * 64 * TILE_B + KT_KREM + ((m & 1) * 16 + tok) * 8, hb0);
                    LAS char* lk = l0 + (2 * (g - 2)) * 25600 + (m >> 1) * TILE_B + KT_KREM + ((m & 1) * 16 + tok) * 8;
                    lst(lk, u32x2{d[m][0], 0x00003f80u}); lst(lk + 25600, u32x2{d[m][1], 0x00003f80u});
                }
            }
        } else if (a == 1) {
            fin_p(a1, 1, d);
            const u32 hb0 = kvb + (u32)(4 * c.j * 64 + 2 * c.i) * TILE_B;
            const u32 vo = (u32)(tok >> 1) * 64 * TILE_B + KT_V + g * KT_VBLK + (16 + (tok & 1)) * 16;
            const u32x4 f0 = frag32(d, 0), f1 = frag32(d, 1);
            if (tok < 8) {
                st128(c.rs, f0, vo, hb0); st128(c.rs, f1, vo, hb0 + TILE_B);
                LAS char* lv = l0 + (tok >> 1) * 25600 + KT_V + g * KT_VBLK + (16 + (tok & 1)) * 16;
                lst(lv, f0); lst(lv + TILE_B, f1);
            }
        } else if (a == 2) {
            const u32 hb0 = kvb + (u32)(4 * c.j * 64 + 2 * c.i) * TILE_B;
            if (lane < 32) {
                const u32x4 on = ones4();
                st128(c.rs, on, (u32)(lane >> 3) * 64 * TILE_B + ((lane >> 2) & 1) * TILE_B + KT_V + (lane & 3) * KT_VBLK + 18 * 16, hb0);
                lst(l0 + (lane >> 3) * 25600 + ((lane >> 2) & 1) * TILE_B + KT_V + (lane & 3) * KT_VBLK + 18 * 16, on);
            }
        }
    }
    publish(c, rep * 6 + 3);
    load_wimage_part<44, 90>(c, WS_WIMG + 8 * WIMG_BYTES, 1);
    if (odd) qm = lld<u32x4>(c.L + L_QT1 + a * 1024 + lane * 16);
    const u32 rem = lld<u32>(c.L + L_QREM + a * 256 + (32 * odd + (lane & 31)) * 4);
    qr = u32x2{lane < 32 ? rem : 0u, 0u};
}

DI void b_attn(const Ctx& c0, const Params& p, int rep, u32x4 qm, u32x2 qr) {
    const Ctx c = fresh(c0);
    const int lane = c.lane, r = lane & 31, h = lane >> 5, a = c.wave >> 1, t = c.wave & 1;
    const int qt = 2 * c.i + t;
    const f32x16 cd = mask_c(lane, true);
    if (c.wave == 1) {
        const f32x16 cm = mask_c(lane, false);
#pragma unroll
        for (int e = 0; e < 4; ++e) lst(c.L + L_MSK + e * 1024 + lane * 16, f32x4{cm[4 * e], cm[4 * e + 1], cm[4 * e + 2], cm[4 * e + 3]});
    }
    LAS char* tb = c.L + L_KVB + a * 25600 + (8 + t) * TILE_B;
    LAS char* kb = tb + lane * 16; LAS char* rb = tb + KT_KREM + r * 8; LAS char* vb = tb + KT_V + h * KT_VBLK + vrow_of(r) * 16;
    const u32x4 k0m = lld<u32x4>(kb); const u32x2 k0r = lld<u32x2>(rb);
    u32x4 km, v0 = lld<u32x4>(vb), v1 = lld<u32x4>(vb + 2 * KT_VBLK); u32x2 kr;
    f32x16 sA, sB, o;
    Pipe pp;
    { u32 z = 0u; asm volatile("" : "+v"(z)); pp.e[0] = pp.e[1] = pp.e[2] = pp.e[3] = asf(z); pp.p1 = u32x4{z, z, z, z}; }
    f32x16 cdp = cd;
    asm volatile("s_nop 1" : "+v"(pp.p1), "+v"(pp.e[0]), "+v"(pp.e[1]), "+v"(pp.e[2]), "+v"(pp.e[3]), "+v"(qm), "+v"(qr), "+v"(cdp));
    QK1(o, pp.p1, pp.p1);
    QK1C(sA, k0m, qm, cdp);
    asm volatile("v_mfma_f32_32x32x8_bf16 %0, %1, %2, %3" : "=&v"(sB) : "v"(k0r), "v"(qr), "v"(sA));
    asm volatile("s_nop 7\n\ts_nop 3" : "+v"(sB), "+v"(sA));
    { const float m0 = rowmax(sB); if (h == 0) qr[1] = pk(-m0, 0.f); }
    asm volatile("s_nop 1" : "+v"(qr));
    QK2(sA, k0r, qr);
    asm volatile("s_nop 7\n\ts_nop 3" : "+v"(sA), "+v"(o), "+v"(v0), "+v"(v1));
    if (c.wave == 0) poll(c, rep * 6 + 3, (lane < 5 && c.i - lane >= 0) ? (c.i - lane) * 8 + c.j : -1);
    BAR();
    STAMP(20 + rep * 30);
    const u32 hb = WS_KVB + rep * RP * KVB_BYTES + (u32)((4 * c.j + a) * 64) * TILE_B;
    { const u32 dst = c.Lb + L_KVB + a * 25600;
#pragma unroll
      for (int n = 0; n < 10; ++n) {
          const int pc = 19 - t - 2 * n;
          u32 vo = lane * 16;
          if (c.i < 4) { const int b = pc * 1024 + lane * 16, tau = b / 2560, kt = 2 * c.i - 8 + tau; vo = (u32)((kt < 0 ? 0 : kt) * 2560 + (b - tau * 2560)); glds_ws(c.srd, vo, hb, dst + pc * 1024); }
          else glds_ws(c.srd, vo, hb + (u32)(2 * c.i - 8) * 2560 + pc * 1024, dst + pc * 1024);
      } }
    asm volatile("s_waitcnt vmcnt(5)" ::: "memory");
    BAR();
    STAMP(21 + rep * 30);
    km = lld<u32x4>(kb - TILE_B); kr = lld<u32x2>(rb - TILE_B);
    auto first = [](f32x16& s, u32x4 k, u32x4 q) { QK1(s, k, q); };
    auto firstm = [&](f32x16& s, u32x4 k, u32x4 q) {
        f32x16 co;
#pragma unroll
        for (int e = 0; e < 4; ++e) { const f32x4 v = lld<f32x4>(c.L + L_MSK + e * 1024 + lane * 16); co[4 * e] = v[0]; co[4 * e + 1] = v[1]; co[4 * e + 2] = v[2]; co[4 * e + 3] = v[3]; }
        QK1C(s, k, q, co); };
    const u32 qoff = h == 0 ? pk(NEG_BIG, 0.f) : 0u;
    kb -= 8 * TILE_B; rb -= 8 * TILE_B; vb -= 8 * TILE_B;
#define B_TILE(U, SC, SN, FIRST) do { u32x2 qs = {qr[0], (U) + 1 <= qt ? qr[1] : qoff}; asm volatile("" : "+v"(qs)); \
        attn_step<((U) < 8)>(SC, SN, o, pp, km, kr, v0, v1, qm, qs, FIRST, \
            [&] { if ((U) + 2 <= 8) km = lld<u32x4>(kb + (8 - ((U) + 2)) * TILE_B); }, [&] { if ((U) + 2 <= 8) kr = lld<u32x2>(rb + (8 - ((U) + 2)) * TILE_B); }, \
            [&] { v1 = lld<u32x4>(vb + (8 - (U)) * TILE_B + 2 * KT_VBLK); }, [&] { if ((U) + 1 <= 8) v0 = lld<u32x4>(vb + (8 - ((U) + 1)) * TILE_B); }); } while (0)
    B_TILE(0, sA, sB, first); B_TILE(1, sB, sA, first);
    DRAIN();
    B_TILE(2, sA, sB, first);
    BAR();
    B_TILE(3, sB, sA, first); B_TILE(4, sA, sB, first); B_TILE(5, sB, sA, first); B_TILE(6, sA, sB, first); B_TILE(7, sB, sA, firstm); B_TILE(8, sA, sB, first);
#undef B_TILE
    attn_finish(o, pp, v1);
    STAMP(22 + rep * 30);
    if (attn_unsafe(o)) o = attn_slow(c, hb, qt - 8 < 0 ? 0 : qt - 8, qt, true, qm, qr);
    store_o(c, WS_OF + (rep * RP * 2 + 1) * XF_BYTES, o, a, t);
    publish(c, rep * 6 + 4);
    load_wimage_part<0, 44>(c, WS_WIMG + 8 * WIMG_BYTES, 1);
    if (rep == 0) load_wimage_part<0, 36>(c, WS_WIMGA, 1, L_A1);
}

__global__ void __launch_bounds__(512, 2) fused_block_kernel(Params p) {
    __shared__ __attribute__((aligned(16))) char lds_[LDS_BYTES];
    Ctx c;
    c.L = (LAS char*)lds_; c.Lb = (u32)(size_t)c.L;
    c.ws = p.ws;
    c.tid = threadIdx.x; c.lane = c.tid & 63; c.wave = __builtin_amdgcn_readfirstlane(c.tid >> 6);
    { const int wg = blockIdx.x, slot = wg >> 3; c.i = (wg & 7) * 4 + (slot >> 3); c.j = slot & 7; }
    c.rs = __builtin_amdgcn_make_buffer_rsrc((void*)p.ws, 0, WS_USED, 0x00020000);
    { const u64 b = (u64)p.ws; c.srd = u32x4{(u32)b, (u32)(b >> 32) & 0xffffu, WS_USED, 0x00020000u}; }
    STAMP(0);
    u32 warm;
    { u64 pc; u32 len;
      asm volatile("s_getpc_b64 %0\nfbk_code_first:\n\ts_mov_b32 %1, fbk_code_last-fbk_code_first" : "=s"(pc), "=s"(len));
      u32 off = ((u32)(blockIdx.x >> 3) * 8 + c.wave) * 256 + c.lane * 4;
      off = off < len ? off : 0u;
      warm = *(const GAS u32*)((pc & ~3ull) + off); }
    c.elo = 0; c.ehi = 0;
    u32 xres[3][2];
#pragma unroll 1
    for (int rep = 0; rep < 2; ++rep) {
        STAMP(1 + rep * 30);
        a_proj(c, p, rep);
        if (rep == 0) asm volatile("" :: "v"(warm));
        STAMP(5 + rep * 30);
        u32x4 w0[9], w1[9]; ATail st;
        a_attn(c, p, rep, st);
        if (rep == 0) {
            if (blockIdx.x == 0 && c.tid == 0) __hip_atomic_store((GAS u64*)(p.ws + WS_EPOCH), ((u64)c.ehi << 32) | c.elo, __ATOMIC_RELAXED, __HIP_MEMORY_SCOPE_AGENT);
        }
        if (c.wave >= 1 && c.wave < 4) a_tail<true>(c, p, rep, st, xres, w0, w1); else a_tail<false>(c, p, rep, st, xres, w0, w1);
        STAMP(14 + rep * 30);
        u32x4 qm; u32x2 qr;
        b_proj(c, p, rep, w0, w1, qm, qr);
        STAMP(19 + rep * 30);
        b_attn(c, p, rep, qm, qr);
        STAMP(24 + rep * 30);
        out_proj<true>(c, p, rep, xres, [](const Ctx&) {});
        STAMP(29 + rep * 30);
    }
    asm volatile("fbk_code_last:");
}

static size_t ws_offset(size_t ws_size) { return (ws_size - WS_USED) & ~(size_t)0x1fffff; }

extern "C" void kernel_launch(void* const* d_in, const int* in_sizes, int n_in, void* d_out, int out_size, void* d_ws, size_t ws_size, hipStream_t stream) {
    Params p;
    p.x = (const u16*)d_in[0]; p.wq = (const u16*)d_in[1]; p.wk = (const u16*)d_in[2]; p.wv = (const u16*)d_in[3]; p.woA = (const u16*)d_in[4];
    p.wqkv = (const u16*)d_in[6]; p.woB = (const u16*)d_in[7]; p.out = (u16*)d_out;
    p.ws = (char*)d_ws + ws_offset(ws_size);
    fused_block_kernel<<<256, 512, 0, stream>>>(p);
}
```

```cpp
#include <hip/hip_runtime.h>
#include <hip/hip_bf16.h>

typedef unsigned short u16;
typedef unsigned u32;
typedef unsigned long long u64;
typedef __bf16 bf16x8 __attribute__((ext_vector_type(8)));
typedef __bf16 bf16x2 __attribute__((ext_vector_type(2)));
typedef short s16x4 __attribute__((ext_vector_type(4)));
typedef float f32x2 __attribute__((ext_vector_type(2)));
typedef float f32x4 __attribute__((ext_vector_type(4)));
typedef float f32x16 __attribute__((ext_vector_type(16)));
typedef u32 u32x2 __attribute__((ext_vector_type(2)));
typedef u32 u32x4 __attribute__((ext_vector_type(4)));
#define LAS __attribute__((address_space(3)))
#define GAS __attribute__((address_space(1)))
#define DI __device__ __forceinline__

constexpr int SEQ = 2048, DM = 576;
constexpr int RP = 0;
constexpr u32 TILE_B = 2560;
constexpr u32 KT_V = 1024, KT_VBLK = 304, KT_KREM = 2240;
constexpr u32 KA_KREM = 1024, KA_V0 = 1280, KA_V8 = 1792, KA_VX = 2304;
constexpr u32 XF_BYTES = 32u * 18 * 4 * 1024;
constexpr u32 WS_EPOCH = 0;
constexpr u32 WS_FLAG = 4096;
constexpr int PH_FMT = 11;
constexpr u32 WS_XF = WS_FLAG + 12u * 256 * 128 + 4096;
constexpr u32 WS_OF = WS_XF + (2 + RP) * XF_BYTES;
constexpr u32 ST_BYTES = 2048u * 16 * 8;
constexpr u32 WS_ST = WS_OF + (2 + 2 * RP) * XF_BYTES;
constexpr u32 KVA_BYTES = 64 * TILE_B;
constexpr u32 WS_KVA = WS_ST + (2 + RP) * ST_BYTES;
constexpr u32 KVB_BYTES = 32 * 64 * TILE_B;
constexpr u32 WS_KVB = WS_KVA + 2 * KVA_BYTES;
constexpr u32 WF_BYTES = 8u * 16 * 18 * 1024;
constexpr u32 WS_WF = WS_KVB + (1 + RP) * KVB_BYTES;
constexpr u32 WS_CSP = WS_WF + WF_BYTES;
constexpr u32 WIMG_BYTES = 90 * 1024;
constexpr u32 WS_WIMG = WS_CSP + 8u * 16 * 18 * 64;
constexpr u32 WS_WIMGA = WS_WIMG + 16 * WIMG_BYTES;
constexpr u32 WS_STAMP = WS_WIMGA + 8 * WIMG_BYTES;
constexpr u32 WS_STAMP2 = WS_STAMP + 256u * 64 * 8 + 256u * 8 * 8 * 8;
constexpr u32 WS_USED = WS_STAMP2 + 256u * 16 * 8;
constexpr int L_XCH = 0;
constexpr int L_RING = 0;
constexpr int CHUNK_B = 8 * TILE_B;
constexpr int L_STG = 40960;
constexpr int STG_P = 168;
constexpr int L_XIN = 0;
constexpr int L_XIN2 = 153600;
constexpr int L_FMT = 24576;
constexpr int L_KVB = 0;
constexpr int L_MSK = 102400;
constexpr int L_R1 = 61440;
constexpr int WP = 160;
constexpr int L_XB = 76800;
constexpr int L_A1 = 24576;
constexpr int L_STATP = 153600;
constexpr int L_MEAN = L_STATP + 1024;
constexpr int L_CSA = L_MEAN + 512;
constexpr int L_CSB = L_CSA + 1024;
constexpr int L_QREM = L_CSB + 1024;
constexpr int L_QT1 = L_QREM + 1024;
constexpr int LDS_BYTES = 163840;
static_assert(L_QT1 + 4096 <= LDS_BYTES, "lds");

constexpr float QSCALE = 0.23570226039551584f * 1.4426950408889634f;
constexpr float NEG_BIG = -1e30f;
constexpr u32 SPIN_CAP = 1u << 20;
constexpr int AUX_SC1 = 16;

struct Params { const u16* x; const u16* wq; const u16* wk; const u16* wv; const u16* woA; const u16* wqkv; const u16* woB; u16* out; char* ws; };

DI u32 pk(float a, float b) { f32x2 v = {a, b}; return __builtin_bit_cast(u32, __builtin_convertvector(v, bf16x2)); }
DI float asf(u32 d) { return __builtin_bit_cast(float, d); }
DI u32 asu(float f) { return __builtin_bit_cast(u32, f); }
DI float bflo(u32 d) { return __builtin_bit_cast(float, d << 16); }
DI float bfhi(u32 d) { return __builtin_bit_cast(float, d & 0xffff0000u); }
DI bf16x8 asb(u32x4 v) { return __builtin_bit_cast(bf16x8, v); }
DI f32x4 mfma16(u32x4 a, u32x4 b, f32x4 c) { return __builtin_amdgcn_mfma_f32_16x16x32_bf16(asb(a), asb(b), c, 0, 0, 0); }
DI f32x16 mfma32(u32x4 a, u32x4 b, f32x16 c) { return __builtin_amdgcn_mfma_f32_32x32x16_bf16(asb(a), asb(b), c, 0, 0, 0); }
DI void MF16(f32x4& acc, u32x4 a, u32x4 b) { asm volatile("v_mfma_f32_16x16x32_bf16 %0, %1, %2, %0" : "+v"(acc) : "v"(a), "v"(b)); }
#define SETTLE4(x) asm volatile("s_nop 7" : "+v"(x))
template <class T> DI T lld(LAS char* p) { return *(LAS const T*)p; }
template <class T> DI void lst(LAS char* p, T v) { *(LAS T*)p = v; }
DI u32x2 trr(LAS char* p) { return __builtin_bit_cast(u32x2, __builtin_amdgcn_ds_read_tr16_b64_v4i16((LAS s16x4*)p)); }
#define BAR() asm volatile("s_waitcnt lgkmcnt(0)\n\ts_barrier" ::: "memory")
#define DRAIN() asm volatile("s_waitcnt vmcnt(0)" ::: "memory")
typedef __amdgpu_buffer_rsrc_t rsrc_t;
DI u32x4 ld128(rsrc_t rs, u32 voff, u32 soff) { return __builtin_amdgcn_raw_buffer_load_b128(rs, voff, soff, AUX_SC1); }
DI u32x2 ld64(rsrc_t rs, u32 voff, u32 soff) { return __builtin_amdgcn_raw_buffer_load_b64(rs, voff, soff, AUX_SC1); }
DI u32 ld32(rsrc_t rs, u32 voff, u32 soff) { return __builtin_amdgcn_raw_buffer_load_b32(rs, voff, soff, AUX_SC1); }
DI void st128(rsrc_t rs, u32x4 v, u32 voff, u32 soff) { __builtin_amdgcn_raw_buffer_store_b128(v, rs, voff + soff, 0, AUX_SC1); }
DI void st64(rsrc_t rs, u32x2 v, u32 voff, u32 soff) { __builtin_amdgcn_raw_buffer_store_b64(v, rs, voff, soff, AUX_SC1); }
DI void st32(rsrc_t rs, u32 v, u32 voff, u32 soff) { __builtin_amdgcn_raw_buffer_store_b32(v, rs, voff, soff, AUX_SC1); }
DI void glds_ws(u32x4 srd, u32 voff, u32 soff, u32 ldsdst) {
    soff = __builtin_amdgcn_readfirstlane(soff); ldsdst = __builtin_amdgcn_readfirstlane(ldsdst);
    asm volatile("s_mov_b32 m0, %0\n\ts_nop 3\n\tbuffer_load_dwordx4 %1, %2, %3 offen sc1 lds" :: "s"(ldsdst), "v"(voff), "s"(srd), "s"(soff) : "memory");
}
DI void glds_in(const void* g, u32 ldsdst) {
    ldsdst = __builtin_amdgcn_readfirstlane(ldsdst);
    asm volatile("s_mov_b32 m0, %0\n\ts_nop 0\n\tglobal_load_lds_dwordx4 %1, off" :: "s"(ldsdst), "v"(g) : "memory");
}
DI void flag_load(u64& fl, u64 nomatch, u32 voff, u32x4 srd, u32 soff) {
    soff = __builtin_amdgcn_readfirstlane(soff);
    fl = nomatch;
    asm volatile("s_nop 4\n\tbuffer_load_dwordx2 %0, %1, %2, %3 offen sc1" : "+v"(fl) : "v"(voff), "s"(srd), "s"(soff) : "memory");
}
DI float sum_g(float v) {
    auto a = __builtin_amdgcn_permlane32_swap(__builtin_bit_cast(u32, v), __builtin_bit_cast(u32, v), false, false);
    v = __builtin_bit_cast(float, (u32)a[0]) + __builtin_bit_cast(float, (u32)a[1]);
    auto b = __builtin_amdgcn_permlane16_swap(__builtin_bit_cast(u32, v), __builtin_bit_cast(u32, v), false, false);
    return __builtin_bit_cast(float, (u32)b[0]) + __builtin_bit_cast(float, (u32)b[1]);
}
DI float max_h(float v) {
    auto a = __builtin_amdgcn_permlane32_swap(__builtin_bit_cast(u32, v), __builtin_bit_cast(u32, v), false, false);
    return fmaxf(__builtin_bit_cast(float, (u32)a[0]), __builtin_bit_cast(float, (u32)a[1]));
}
DI u32x4 ones4() { u32 o = 0x3f803f80u; asm volatile("" : "+v"(o)); return u32x4{o, o, o, o}; }
DI int crow(int reg, int h) { return (reg & 3) + 8 * (reg >> 2) + 4 * h; }

#define STAMP(n) do {} while (0)
#define CSTAMP(n) do {} while (0)
#define CSTAMPW(n, W) do {} while (0)

struct Ctx {
    LAS char* L; u32 Lb;
    char* ws; rsrc_t rs; u32x4 srd;
    int tid, lane, wave, i, j;
    u32 elo, ehi;
};

DI Ctx fresh(const Ctx& c0) {
    Ctx c = c0;
    asm volatile("" : "+v"(c.lane)); asm volatile("" : "+v"(c.tid));
    asm volatile("" : "+s"(c.wave)); asm volatile("" : "+s"(c.i)); asm volatile("" : "+s"(c.j));
    return c;
}
DI GAS u64* flag_ptr(const Ctx& c, int phase, int id) { return (GAS u64*)(c.ws + WS_FLAG + (u32)(phase * 256 + id) * 128); }
DI void signal(const Ctx& c, int phase) {
    if (c.tid == 0) __hip_atomic_store(flag_ptr(c, phase, c.i * 8 + c.j), ((u64)c.ehi << 32) | c.elo, __ATOMIC_RELAXED, __HIP_MEMORY_SCOPE_AGENT);
}
DI void publish(const Ctx& c, int phase) {
    DRAIN();
    BAR();
    signal(c, phase);
}
DI void poll(const Ctx& c, int phase, int id) {
    const u64 want = ((u64)c.ehi << 32) | c.elo;
    GAS u64* f = flag_ptr(c, phase, id < 0 ? 0 : id);
    for (u32 spins = 0; spins < SPIN_CAP; ++spins) {
        const u64 v = __hip_atomic_load(f, __ATOMIC_RELAXED, __HIP_MEMORY_SCOPE_AGENT);
        if (__all(id < 0 || v == want)) break;
        __builtin_amdgcn_s_sleep(1);
    }
    __builtin_amdgcn_fence(__ATOMIC_ACQUIRE, "wavefront");
}
DI void wait_group(const Ctx& c, int phase) {
    if (c.wave == 0) poll(c, phase, c.lane < 8 ? c.i * 8 + c.lane : -1);
    BAR();
}

DI void load_wimage_a(const Ctx& c, const Params& p, int w0) {
    if (c.wave < w0) return;
    const int jj = c.j, nw = 8 - w0, g0 = c.wave - w0;
    const u16 *pk_ = p.wk, *pv_ = p.wv;
    asm volatile("" : "+s"(pk_), "+s"(pv_));
    const char* xsrc = (const char*)((jj >= 3 && jj < 6) ? pv_ : pk_); const int sub = jj < 6 ? jj % 3 : 0;
    xsrc += sub == 0 ? 0 : (sub == 1 ? 16 : 20);
    const char* msrc = (const char*)p.wq + jj * 144;
    const char* a[5]; u32 st[5];
#pragma unroll
    for (int k = 0; k < 5; ++k) {
        const u32 id = 64 * k + c.lane, row = (id * 6554u) >> 16, ch = id - row * 10;
        const bool ex = ch == 9;
        const u32 s1 = ex ? 32 * 36 : 32 * 1152;
        st[k] = s1 * nw;
        a[k] = (ex ? xsrc : msrc) + (ex ? row * 36 : row * 1152 + ch * 16) + (u32)g0 * s1;
    }
#pragma unroll
    for (int n = 0; n < 3; ++n) {
        const int G = g0 + nw * n;
        if (G < 18) {
#pragma unroll
            for (int k = 0; k < 5; ++k) { glds_in(a[k], c.Lb + L_R1 + (G * 5 + k) * 1024); a[k] += st[k]; }
        }
    }
}
DI void load_wimage_o(const Ctx& c, u32 base) {
    const u32 so = base + (u32)c.j * WIMG_BYTES;
    if (c.wave >= 1) {
#pragma unroll
        for (int n = 0; n < 13; ++n)
            if (n < 12 || c.wave < 7) glds_ws(c.srd, c.lane * 16, so + (c.wave - 1 + 7 * n) * 1024, c.Lb + L_R1 + (c.wave - 1 + 7 * n) * 1024);
    }
}
template <int P0, int P1>
DI void load_wimage_part(const Ctx& c, u32 base, int w0, int ldsbase = L_R1) {
    const u32 so = base + (u32)c.j * WIMG_BYTES; const int nw = 8 - w0;
    if (c.wave >= w0) {
#pragma unroll
        for (int n = 0; n < (P1 - P0 + 6) / 7; ++n) {
            const int pc = P0 + (c.wave - w0) + nw * n;
            if (pc < P1) glds_ws(c.srd, c.lane * 16, so + pc * 1024, c.Lb + ldsbase + pc * 1024);
        }
    }
}
DI void load_wimage_o4(const Ctx& c, int which, int part) {
    const u32 so = WS_WIMG + (u32)(which * 8 + c.j) * WIMG_BYTES + (u32)part * 30 * 1024; const u32 dst = c.Lb + L_R1 + part * 30 * 1024;
    if (c.wave < 4) {
#pragma unroll
        for (int n = 0; n < 8; ++n)
            if (n < 7 || c.wave < 2) glds_ws(c.srd, c.lane * 16, so + (c.wave + 4 * n) * 1024, dst + (c.wave + 4 * n) * 1024);
    }
}
struct Fmt { u32x4 v[2]; u32 g[8]; u32 dst[2], cdst[2]; int kind[2]; };
DI void fmt_issue(const Ctx& c, const Params& p, Fmt& m) {
    const u16 *pw = p.wqkv, *pa = p.woA, *pb = p.woB;
    asm volatile("" : "+s"(pw), "+s"(pa), "+s"(pb));
    const int lane = c.lane, rho = lane >> 1, g = rho >> 3, e = rho & 7;
#pragma unroll
    for (int r = 0; r < 2; ++r) {
        const int fr = blockIdx.x * 8 + c.wave + 2048 * r;
        int col, A, P, Q, pitch; const GAS char* src;
        if (fr < 2016) {
            const int jj = fr / 252, rem = fr - jj * 252, u = rem / 18, s = rem - u * 18;
            const int wv = u < 8 ? u : (u - 8 < 5 ? u - 8 : 6), slot = u < 8 ? 0 : 1, a = wv >> 1;
            const bool isrem = (wv & 1) && slot == 1;
            const u32 fo = (u32)(((jj * 8 + wv) * 2 + slot) * 18 + s);
            m.dst[r] = WS_WF + fo * 1024; m.cdst[r] = WS_CSP + fo * 64; m.kind[r] = isrem ? 3 : 1;
            P = s < 16 ? 16 : 4; Q = 2; pitch = 1728 * 2; src = (const GAS char*)pw;
            if (!isrem) {
                col = ((wv & 1) ? 1152 : (slot ? 576 : 0)) + 18 * (4 * jj + a) + 8 * (lane & 1);
                A = s < 16 ? 72 * (s >> 1) + 32 * (s & 1) + 4 * g : 72 * (4 * (s - 16) + g) + 64;
            } else {
                const int n = lane & 15, g2 = lane >> 4;
                col = (a == 0 ? (n < 8 ? 0 : 576) : 1152) + 18 * (4 * jj + ((n & 7) >> 1)) + 16 + (n & 1);
                A = s < 16 ? 72 * (s >> 1) + 32 * (s & 1) + 4 * g2 : 72 * (4 * (s - 16) + g2) + 64;
#pragma unroll
                for (int q = 0; q < 8; ++q) m.g[q] = *(const GAS u16*)(src + (A + P * (q >> 2) + (q & 3)) * pitch + col * 2);
            }
        } else {
            const bool on = fr < 3456;
            const int q = on ? fr - 2016 : 0, im = q / 720, rem = q - im * 720, jj = rem / 90, st = rem - jj * 90, s = st / 5, t = st - s * 5;
            m.dst[r] = WS_WIMG + (u32)(im * 8 + jj) * WIMG_BYTES + (u32)st * 1024; m.cdst[r] = 0; m.kind[r] = on ? 2 : 0;
            col = 72 * jj + 16 * t + (t == 4 ? 0 : 8 * (lane & 1));
            if (s < 16) { A = 36 * s + 18 * (g >> 1) + 4 * (g & 1); P = 8; Q = 2; } else { A = 72 * (4 * (s - 16) + g) + 16; P = 36; Q = 18; }
            src = (const GAS char*)(im ? pb : pa); pitch = 576 * 2;
        }
        if (m.kind[r] == 1 || m.kind[r] == 2) m.v[r] = *(const GAS u32x4*)(src + (A + P * (e >> 2) + Q * ((e >> 1) & 1) + (e & 1)) * pitch + col * 2);
    }
}
DI void fmt_finish(const Ctx& c, Fmt& m) {
    asm volatile("" : "+v"(m.v[0]), "+v"(m.v[1]), "+v"(m.g[0]), "+v"(m.g[1]), "+v"(m.g[2]), "+v"(m.g[3]), "+v"(m.g[4]), "+v"(m.g[5]), "+v"(m.g[6]), "+v"(m.g[7]));
    const int lane = c.lane, i16 = lane & 15, g = lane >> 4, q = i16 >> 2, p = i16 & 3;
#pragma unroll
    for (int r = 0; r < 2; ++r) {
        if (m.kind[r] != 0) {
            u32x4 f;
            if (m.kind[r] == 3) { const u32* w = m.g; f = u32x4{w[0] | (w[1] << 16), w[2] | (w[3] << 16), w[4] | (w[5] << 16), w[6] | (w[7] << 16)}; }
            else {
                LAS char* t = c.L + L_FMT + c.wave * 2048 + r * 1024;
                lst(t + lane * 16, m.v[r]);
                asm volatile("s_waitcnt lgkmcnt(0)" ::: "memory");
                const u32x2 lo = trr(t + (8 * g + q) * 32 + 8 * p), hi = trr(t + (8 * g + 4 + q) * 32 + 8 * p);
                f = u32x4{lo[0], lo[1], hi[0], hi[1]};
            }
            st128(c.rs, f, lane * 16, m.dst[r]);
            if (m.kind[r] != 2) {
                float cs = ((bflo(f[0]) + bfhi(f[0])) + (bflo(f[1]) + bfhi(f[1]))) + ((bflo(f[2]) + bfhi(f[2])) + (bflo(f[3]) + bfhi(f[3])));
                cs = sum_g(cs);
                if (lane < 16) st32(c.rs, asu(cs), lane * 4, m.cdst[r]);
            }
        }
    }
}

template <bool PROJ, bool NAT = false, class HK>
DI void g1_mma(const Ctx& c, const u32x4 (&xf)[9], f32x4 (&acc)[5], HK hook) {
    const int lane = c.lane, kh = c.wave >> 2, q = (lane & 15) >> 2, p = lane & 3, g = lane >> 4;
    LAS char *bN, *bR, *bNo, *bNt, *bRo, *bRt;
    if (PROJ) {
        if (NAT) { bN = c.L + L_R1 + (kh * 256 + 4 * g + q) * WP + 8 * p; bR = c.L + L_R1 + (512 + 32 * kh + 8 * g + q) * WP + 8 * p; }
        else { bN = c.L + L_A1 + (kh * 288 + 4 * g + q) * WP + 8 * p; bR = c.L + L_A1 + (kh * 288 + 72 * g + q) * WP + 8 * p; }
        bNo = bN + 64 * kh; bNt = bN + 64 - 64 * kh; bRo = bR + 64 * kh; bRt = bR + 64 - 64 * kh;
    } else {
        bN = c.L + L_R1 + kh * 8 * 5120 + lane * 16; bR = c.L + L_R1 + (16 + kh) * 5120 + lane * 16;
        bNo = bN + 2048 * kh; bNt = bN + 2048 - 2048 * kh; bRo = bR + 2048 * kh; bRt = bR + 2048 - 2048 * kh;
    }
    u32x4 w[2][5];
    auto rd = [&](int sl, u32x4 (&d)[5]) {
#pragma unroll
        for (int u = 0; u < 5; ++u) {
            if (PROJ) {
                const int r0 = NAT ? (sl < 8 ? 32 * sl : 0) : (sl < 8 ? 72 * (sl >> 1) + 32 * (sl & 1) : 64), hs = sl < 8 ? 16 : 4;
                LAS char* bb = u < 2 ? (sl < 8 ? bNo : bRo) + 32 * u : (u == 2 ? (sl < 8 ? bN : bR) + 128 : (sl < 8 ? bNt : bRt) + 32 * (u - 3));
                const u32x2 lo = trr(bb + r0 * WP), hi = trr(bb + (r0 + hs) * WP);
                d[u] = u32x4{lo[0], lo[1], hi[0], hi[1]};
            } else {
                LAS char* bb = u < 2 ? (sl < 8 ? bNo : bRo) + 1024 * u : (u == 2 ? (sl < 8 ? bN : bR) + 4096 : (sl < 8 ? bNt : bRt) + 1024 * (u - 3));
                d[u] = lld<u32x4>(bb + (sl < 8 ? sl * 5120 : 0));
            }
        }
    };
    rd(0, w[0]);
#pragma unroll
    for (int u = 0; u < 5; ++u) asm volatile("" : "+v"(acc[u]));
#pragma unroll
    for (int sl = 0; sl < 9; ++sl) {
        if (sl + 1 < 9) rd(sl + 1, w[(sl + 1) & 1]);
#pragma unroll
        for (int u = 0; u < 5; ++u) MF16(acc[u], w[sl & 1][u], xf[sl]);
        hook(sl, w[sl & 1]);
    }
    asm volatile("s_nop 7" : "+v"(acc[0]), "+v"(acc[1]), "+v"(acc[2]), "+v"(acc[3]), "+v"(acc[4]));
}
DI void g1_exchange(const Ctx& c, f32x4 (&acc)[5]) {
    LAS char* xw = c.L + L_XCH + c.wave * 3072 + c.lane * 16;
    lst(xw, acc[3]); lst(xw + 1024, acc[4]); lst(xw + 2048, acc[2]);
    BAR();
    LAS char* xr = c.L + L_XCH + (c.wave ^ 4) * 3072 + c.lane * 16;
    acc[0] += lld<f32x4>(xr); acc[1] += lld<f32x4>(xr + 1024); acc[2] += lld<f32x4>(xr + 2048);
}
DI void load_xfrags(const Ctx& c, u32 buf, u32x4 (&xf)[9]) {
    const int m = c.wave & 3, kh = c.wave >> 2;
    const u32 so = buf + (u32)(c.i * 18 * 4 + m) * 1024;
#pragma unroll
    for (int sl = 0; sl < 9; ++sl) { const int s = sl < 8 ? 8 * kh + sl : 16 + kh; xf[sl] = ld128(c.rs, c.lane * 16, so + (u32)s * 4096); }
}
DI u32x4 stats_issue(const Ctx& c, u32 stbuf) {
    const u32 vo = (u32)((c.i * 16 + 2 * (c.lane >> 3)) * 64 + c.wave * 8 + (c.lane & 7)) * 8;
    const u32x2 a = ld64(c.rs, vo, stbuf), b = ld64(c.rs, vo + 512, stbuf);
    return u32x4{a[0], a[1], b[0], b[1]};
}
DI void stats_finish(const Ctx& c, u32x4 v) {
    const int tok = c.wave * 8 + (c.lane & 7);
    float s = asf(v[0]) + asf(v[2]), s2 = asf(v[1]) + asf(v[3]);
    s += __shfl_xor(s, 8); s2 += __shfl_xor(s2, 8);
    s = sum_g(s); s2 = sum_g(s2);
    if (c.lane < 8) {
        lst(c.L + L_STATP + tok * 8, f32x2{s, s2}); lst(c.L + L_STATP + 512 + tok * 8, f32x2{0.f, 0.f});
        const float mean = s * (1.f / DM), var = s2 * (1.f / DM) - mean * mean;
        lst(c.L + L_MEAN + tok * 4, mean); lst(c.L + L_MEAN + 256 + tok * 4, __builtin_amdgcn_rsqf(fmaxf(var, 0.f) + 1e-5f));
    }
}
DI void stats_issue32(const Ctx& c, u32 stbuf, u32x4 (&v)[4]) {
    const u32 vo = (u32)((c.i * 16 + (c.lane >> 4)) * 64 + (c.wave == 7 ? 32 : 0) + 2 * (c.lane & 15)) * 8;
#pragma unroll
    for (int n = 0; n < 4; ++n) v[n] = ld128(c.rs, vo + n * 4 * 512, stbuf);
}
DI void stats_finish32(const Ctx& c, const u32x4 (&v)[4]) {
    float s[4];
#pragma unroll
    for (int e = 0; e < 4; ++e) s[e] = sum_g((asf(v[0][e]) + asf(v[1][e])) + (asf(v[2][e]) + asf(v[3][e])));
    if (c.lane < 16) {
        const int tok = (c.wave == 7 ? 32 : 0) + 2 * c.lane;
        const float m0 = s[0] * (1.f / DM), m1 = s[2] * (1.f / DM);
        lst(c.L + L_MEAN + tok * 4, f32x2{m0, m1});
        lst(c.L + L_MEAN + 256 + tok * 4, f32x2{__builtin_amdgcn_rsqf(fmaxf(s[1] * (1.f / DM) - m0 * m0, 0.f) + 1e-5f), __builtin_amdgcn_rsqf(fmaxf(s[3] * (1.f / DM) - m1 * m1, 0.f) + 1e-5f)});
    }
}

struct KV { u32x4 km; u32x2 kr; u32x4 v0, v1; };
DI int vrow_of(int r) { return r < 18 ? r : 18; }
DI u32 ka_vrow(int blk, int row) { return row < 8 ? KA_V0 + blk * 128 + row * 16 : (row < 16 ? KA_V8 + blk * 128 + (row - 8) * 16 : KA_VX + blk * 64 + (row - 16) * 16); }
DI KV load_tile_ws(const Ctx& c, u32 tile, bool shared) {
    const int r = c.lane & 31, h = c.lane >> 5;
    KV t;
    if (shared) {
        t.km = ld128(c.rs, c.lane * 16, tile);
        t.kr = ld64(c.rs, KA_KREM + r * 8, tile);
        t.v0 = ld128(c.rs, ka_vrow(h, vrow_of(r)), tile);
        t.v1 = ld128(c.rs, ka_vrow(2 + h, vrow_of(r)), tile);
        return t;
    }
    t.km = ld128(c.rs, c.lane * 16, tile);
    t.kr = ld64(c.rs, KT_KREM + r * 8, tile);
    t.v0 = ld128(c.rs, KT_V + h * KT_VBLK + vrow_of(r) * 16, tile);
    t.v1 = ld128(c.rs, KT_V + (2 + h) * KT_VBLK + vrow_of(r) * 16, tile);
    return t;
}
DI f32x16 mfma32k8(u32x2 a, u32x2 b, f32x16 c) { return __builtin_amdgcn_mfma_f32_32x32x8bf16_1k(__builtin_bit_cast(s16x4, a), __builtin_bit_cast(s16x4, b), c, 0, 0, 0); }
DI f32x16 qk(const KV& t, u32x4 qm, u32x2 qr, f32x16 c0) {
    return mfma32k8(t.kr, qr, mfma32(t.km, qm, c0));
}
DI void QK1(f32x16& s, u32x4 km, u32x4 qm) { asm volatile("v_mfma_f32_32x32x16_bf16 %0, %1, %2, 0" : "=&v"(s) : "v"(km), "v"(qm)); }
DI void QK1C(f32x16& s, u32x4 km, u32x4 qm, const f32x16& c0) { asm volatile("v_mfma_f32_32x32x16_bf16 %0, %1, %2, %3" : "=&v"(s) : "v"(km), "v"(qm), "v"(c0)); }
DI void QK2(f32x16& s, u32x2 kr, u32x2 qr) { asm volatile("v_mfma_f32_32x32x8_bf16 %0, %1, %2, %0" : "+v"(s) : "v"(kr), "v"(qr)); }
DI void PV(f32x16& o, u32x4 v, u32x4 p) { asm volatile("v_mfma_f32_32x32x16_bf16 %0, %1, %2, %0" : "+v"(o) : "v"(v), "v"(p)); }
DI float EX(float x) { float r; asm volatile("v_exp_f32 %0, %1" : "=v"(r) : "v"(x)); return r; }
DI u32 CV(float a, float b) { u32 r; asm volatile("v_cvt_pk_bf16_f32 %0, %1, %2" : "=v"(r) : "v"(a), "v"(b)); return r; }
struct Pipe { float e[4]; u32x4 p1; };
template <bool NEXT = true, class F0, class F1, class F2, class F3, class F4>
DI void attn_step(const f32x16& sc, f32x16& sn, f32x16& o, Pipe& pp, const u32x4& km, const u32x2& kr, const u32x4& v0, const u32x4& v1, u32x4 qm, u32x2 qr, F0 first, F1 LK, F2 LR, F3 LV1, F4 LV0) {
    u32x4 p0; float a0, a1, a2, a3, b0, b1, b2, b3;
    if (NEXT) first(sn, km, qm);
    LK();
    pp.p1[2] = CV(pp.e[0], pp.e[1]); pp.p1[3] = CV(pp.e[2], pp.e[3]);
    a0 = EX(sc[0]); a1 = EX(sc[1]); a2 = EX(sc[2]); a3 = EX(sc[3]);
    if (NEXT) QK2(sn, kr, qr); else asm volatile("s_nop 0");
    LR();
    p0[0] = CV(a0, a1); p0[1] = CV(a2, a3);
    b0 = EX(sc[4]); b1 = EX(sc[5]); b2 = EX(sc[6]); b3 = EX(sc[7]);
    PV(o, v1, pp.p1); LV1();
    p0[2] = CV(b0, b1); p0[3] = CV(b2, b3);
    a0 = EX(sc[8]); a1 = EX(sc[9]); a2 = EX(sc[10]); a3 = EX(sc[11]);
    PV(o, v0, p0); LV0();
    pp.p1[0] = CV(a0, a1); pp.p1[1] = CV(a2, a3);
    pp.e[0] = EX(sc[12]); pp.e[1] = EX(sc[13]); pp.e[2] = EX(sc[14]); pp.e[3] = EX(sc[15]);
}
template <class F1, class F2, class F3, class F4>
DI void attn_step2(const f32x16& scx, f32x16& snx, f32x16& ox, Pipe& px, const f32x16& scy, f32x16& sny, f32x16& oy, Pipe& py,
                   const u32x4& km, const u32x2& kr, const u32x4& v0, const u32x4& v1, u32x4 qmx, u32x2 qrx, u32x4 qmy, u32x2 qry, F1 LK, F2 LR, F3 LV1, F4 LV0) {
    u32x4 p0x, p0y; float a0, a1, a2, a3, b0, b1, b2, b3, c0, c1, c2, c3, d0, d1, d2, d3;
#define GRP(MF, PD0, PD1, E0, E1, E2, E3, D0, D1, D2, D3, SRC, I) MF; PD0 = CV(E0, E1); PD1 = CV(E2, E3); D0 = EX(SRC[I]); D1 = EX(SRC[I + 1]); D2 = EX(SRC[I + 2]); D3 = EX(SRC[I + 3])
    GRP(QK1(snx, km, qmx), px.p1[2], px.p1[3], px.e[0], px.e[1], px.e[2], px.e[3], a0, a1, a2, a3, scx, 0);
    GRP(QK1(sny, km, qmy); LK(), py.p1[2], py.p1[3], py.e[0], py.e[1], py.e[2], py.e[3], b0, b1, b2, b3, scy, 0);
    GRP(QK2(snx, kr, qrx), p0x[0], p0x[1], a0, a1, a2, a3, c0, c1, c2, c3, scx, 4);
    GRP(QK2(sny, kr, qry); LR(), p0y[0], p0y[1], b0, b1, b2, b3, d0, d1, d2, d3, scy, 4);
    GRP(PV(ox, v1, px.p1), p0x[2], p0x[3], c0, c1, c2, c3, a0, a1, a2, a3, scx, 8);
    GRP(PV(oy, v1, py.p1); LV1(), p0y[2], p0y[3], d0, d1, d2, d3, b0, b1, b2, b3, scy, 8);
    GRP(PV(ox, v0, p0x), px.p1[0], px.p1[1], a0, a1, a2, a3, px.e[0], px.e[1], px.e[2], px.e[3], scx, 12);
    GRP(PV(oy, v0, p0y); LV0(), py.p1[0], py.p1[1], b0, b1, b2, b3, py.e[0], py.e[1], py.e[2], py.e[3], scy, 12);
#undef GRP
}
DI void attn_finish(f32x16& o, Pipe& pp, u32x4 v1) {
    asm volatile("s_nop 0");
    pp.p1[2] = CV(pp.e[0], pp.e[1]); pp.p1[3] = CV(pp.e[2], pp.e[3]);
    asm volatile("s_nop 1");
    PV(o, v1, pp.p1);
    asm volatile("s_nop 7\n\ts_nop 3" : "+v"(o));
}
DI void attn_finish2(f32x16& ox, Pipe& px, f32x16& oy, Pipe& py, u32x4 v1) {
    asm volatile("s_nop 0");
    px.p1[2] = CV(px.e[0], px.e[1]); px.p1[3] = CV(px.e[2], px.e[3]);
    py.p1[2] = CV(py.e[0], py.e[1]); py.p1[3] = CV(py.e[2], py.e[3]);
    PV(ox, v1, px.p1);
    asm volatile("s_nop 0");
    PV(oy, v1, py.p1);
    asm volatile("s_nop 7\n\ts_nop 3" : "+v"(ox), "+v"(oy));
}
DI void pexp(const f32x16& s, u32x4& p0, u32x4& p1) {
    float e[16];
#pragma unroll
    for (int r = 0; r < 16; ++r) e[r] = __builtin_amdgcn_exp2f(s[r]);
    p0 = u32x4{pk(e[0], e[1]), pk(e[2], e[3]), pk(e[4], e[5]), pk(e[6], e[7])};
    p1 = u32x4{pk(e[8], e[9]), pk(e[10], e[11]), pk(e[12], e[13]), pk(e[14], e[15])};
}
DI float rowmax(const f32x16& s) {
    float a = fmaxf(fmaxf(s[0], s[1]), s[2]), b = fmaxf(fmaxf(s[3], s[4]), s[5]);
#pragma unroll
    for (int r = 6; r < 16; r += 2) { if (r & 2) a = fmaxf(fmaxf(a, s[r]), s[r + 1]); else b = fmaxf(fmaxf(b, s[r]), s[r + 1]); }
    return max_h(fmaxf(a, b));
}
DI f32x16 mask_c(int lane, bool diag) {
    const int r = lane & 31, h = lane >> 5; f32x16 m;
#pragma unroll
    for (int e = 0; e < 16; ++e) { const bool ok = diag ? (crow(e, h) <= r) : (crow(e, h) > r); m[e] = ok ? 0.f : NEG_BIG; }
    return m;
}
DI f32x16 attn_slow(const Ctx& c, u32 tile0, int n0, int n1, bool masked, u32x4 qm, u32x2 qr) {
    const int h = c.lane >> 5;
    qr[1] = 0u;
    f32x16 o = {}; float m = NEG_BIG;
    for (int n = n0; n <= n1; ++n) {
        const KV t = load_tile_ws(c, tile0 + (u32)n * TILE_B, !masked);
        f32x16 z = {};
        f32x16 s = qk(t, qm, qr, z);
        if (masked && (n == n1 || n == n1 - 8)) {
            const int r = c.lane & 31;
#pragma unroll
            for (int e = 0; e < 16; ++e) { const bool ok = (n == n1) ? (crow(e, h) <= r) : (crow(e, h) > r); s[e] = ok ? s[e] : NEG_BIG; }
        }
        const float mn = fmaxf(m, rowmax(s)), al = __builtin_amdgcn_exp2f(m - mn);
        m = mn;
#pragma unroll
        for (int e = 0; e < 16; ++e) { o[e] *= al; s[e] = s[e] <= NEG_BIG ? NEG_BIG : s[e] - mn; }
        u32x4 p0, p1; pexp(s, p0, p1);
        o = mfma32(t.v0, p0, o); o = mfma32(t.v1, p1, o);
    }
    return o;
}
DI bool attn_unsafe(const f32x16& o) {
    float z = 0.f;
#pragma unroll
    for (int e = 0; e < 10; ++e) z = __builtin_fmaf(o[e], 0.f, z);
    const float l = o[10];
    return __any(!(l > 0x1p-40f && l < 0x1p30f && z == 0.f));
}
DI void store_o(const Ctx& c, u32 obuf, const f32x16& o, int a, int t) {
    const int r = c.lane & 31, h = c.lane >> 5;
    const float inv = __builtin_amdgcn_rcpf(o[10]);
    const u32x4 mainp = {pk(o[0] * inv, o[1] * inv), pk(o[2] * inv, o[3] * inv), pk(o[4] * inv, o[5] * inv), pk(o[6] * inv, o[7] * inv)};
    const int m = 2 * t + (r >> 4), tok = r & 15;
    const u32 so = obuf + (u32)(c.i * 18 * 4) * 1024;
    st128(c.rs, mainp, (u32)((2 * c.j + (a >> 1)) * 4 + m) * 1024 + ((2 * (a & 1) + h) * 16 + tok) * 16, so);
    if (h == 0) st32(c.rs, pk(o[8] * inv, o[9] * inv), (u32)((16 + (c.j >> 2)) * 4 + m) * 1024 + ((c.j & 3) * 16 + tok) * 16 + 4 * a, so);
}

template <int rep>
DI void a_proj(Ctx& c0, const Params& p) {
    Ctx c = fresh(c0);
    const int lane = c.lane, m = c.wave & 3, kh = c.wave >> 2, tok = lane & 15, g = lane >> 4;
    u32x4 xf[9]; f32x4 acc[5] = {};
    f32x4 s1 = {}, s2 = {}, ca = {}, cb = {};
    Fmt fm; u64 e0 = 0; u32x4 stv;
    if (rep == 0) {
        e0 = __hip_atomic_load((GAS u64*)(p.ws + WS_EPOCH), __ATOMIC_RELAXED, __HIP_MEMORY_SCOPE_AGENT);
        const char* xt = (const char*)p.x + (size_t)(c.i * 64) * (DM * 2);
#pragma unroll
        for (int n = 0; n < 8; ++n) {
            const int row = c.wave * 8 + n;
            glds_in(xt + row * (DM * 2) + (lane ^ (row & 15)) * 16, c.Lb + (row < 60 ? L_XIN + row * 1024 : L_XIN2 + (row - 60) * 1024));
        }
        xf[8] = *(const u32x4*)(xt + (m * 16 + tok) * (DM * 2) + (512 + 32 * kh + 8 * g) * 2);
        load_wimage_a(c, p, 0);
        fmt_issue(c, p, fm);
        STAMP(13);
        DRAIN(); BAR();
        STAMP(18);
        asm volatile("" : "+v"(xf[8]), "+v"(e0));
        { const int row = m * 16 + tok, t2 = tok ^ (g >> 1);
          LAS char* xb = c.L + (row < 60 ? L_XIN + row * 1024 : L_XIN2 + (row - 60) * 1024) + kh * 512 + 8 * (g & 1);
#pragma unroll
          for (int sl = 0; sl < 8; ++sl) {
              const int K = 4 * (sl & 3);
              const u32x2 lo = lld<u32x2>(xb + (sl >> 2) * 256 + ((K ^ t2) * 16)), hi = lld<u32x2>(xb + (sl >> 2) * 256 + (((K + 2) ^ t2) * 16));
              xf[sl] = u32x4{lo[0], lo[1], hi[0], hi[1]};
          } }
        STAMP(21);
        if (c.tid < 180) st128(c.rs, lld<u32x4>(c.L + L_R1 + c.i * 2880 + c.tid * 16), c.tid * 16, WS_WIMGA + (u32)c.j * WIMG_BYTES + (u32)c.i * 2880);
        BAR();
        STAMP(2);
    } else {
        wait_group(c, 5);
        load_xfrags(c, WS_XF + 1 * XF_BYTES, xf);
        stv = stats_issue(c, WS_ST + 1 * ST_BYTES);
        asm volatile("s_waitcnt vmcnt(11)" ::: "memory"); BAR();
        STAMP(32);
    }
    if (rep != 0) { g1_mma<true>(c, xf, acc, [](int, const u32x4 (&)[5]) {}); stats_finish(c, stv); }
    else {
        u32x4 ones = ones4();
        const int tlc = m < 2 ? 2 * kh + m : 2 * (1 - kh) + (m - 2), q = (lane & 15) >> 2, pp = lane & 3;
        LAS char* bN = c.L + L_R1 + (kh * 256 + 4 * g + q) * WP + 8 * pp + 32 * tlc;
        LAS char* bR = c.L + L_R1 + (512 + 32 * kh + 8 * g + q) * WP + 8 * pp + 32 * tlc;
        asm volatile("" : "+v"(s1), "+v"(s2), "+v"(ca), "+v"(cb), "+v"(ones));
        u32x4 wx[2];
        auto rdx = [&](int sl) {
            const int r0 = sl < 8 ? 32 * sl : 0, hs = sl < 8 ? 16 : 4; LAS char* b = sl < 8 ? bN : bR;
            const u32x2 lo = trr(b + r0 * WP), hi = trr(b + (r0 + hs) * WP);
            wx[sl & 1] = u32x4{lo[0], lo[1], hi[0], hi[1]}; };
        rdx(0);
        g1_mma<true, true>(c, xf, acc, [&](int sl, const u32x4 (&w)[5]) {
            if (sl + 1 < 9) rdx(sl + 1);
            MF16(s1, ones, xf[sl]); MF16(s2, xf[sl], xf[sl]); MF16(cb, w[2], ones); MF16(ca, wx[sl & 1], ones);
        });
        asm volatile("s_nop 7" : "+v"(s1), "+v"(s2), "+v"(ca), "+v"(cb));
        const int rg = tok & 3;
        float d = rg == 0 ? s2[0] : (rg == 1 ? s2[1] : (rg == 2 ? s2[2] : s2[3]));
        d = sum_g(g == (tok >> 2) ? d : 0.f);
        if (g == 0) lst(c.L + L_STATP + kh * 512 + (m * 16 + tok) * 8, f32x2{s1[0], d});
        const int tl = m < 2 ? 2 * kh + m : 2 * (1 - kh) + (m - 2);
        if (tok == 0) { lst(c.L + L_CSA + (kh * 5 + tl) * 64 + g * 16, ca); if (m == 0) lst(c.L + L_CSA + (kh * 5 + 4) * 64 + g * 16, cb); }
    }
    STAMP(3 + rep * 30);
    g1_exchange(c, acc);
    const f32x2 sa = lld<f32x2>(c.L + L_STATP + (m * 16 + tok) * 8), sb = lld<f32x2>(c.L + L_STATP + 512 + (m * 16 + tok) * 8);
    const float mean = (sa[0] + sb[0]) * (1.f / DM), var = (sa[1] + sb[1]) * (1.f / DM) - mean * mean;
    const float rstd = __builtin_amdgcn_rsqf(fmaxf(var, 0.f) + 1e-5f);
    LAS char* stg = c.L + L_STG + (m * 16 + tok) * STG_P + g * 8;
    auto fin = [&](int u, int t, float sc) {
        const f32x4 cs = lld<f32x4>(c.L + L_CSA + t * 64 + g * 16) + lld<f32x4>(c.L + L_CSA + (5 + t) * 64 + g * 16);
        f32x4 v;
#pragma unroll
        for (int e = 0; e < 4; ++e) v[e] = (acc[u][e] - mean * cs[e]) * sc;
        return u32x2{pk(v[0], v[1]), pk(v[2], v[3])};
    };
    if (kh == 1) {
        const u32x2 d4 = fin(2, 4, g < 2 ? rstd * QSCALE : rstd);
        lst(stg + 128, d4);
        const u32 kva = WS_KVA + rep * KVA_BYTES + (u32)(2 * c.i + (m >> 1)) * TILE_B;
        const int j = c.j, key = (m & 1) * 16 + tok;
        if (j < 2) { if (g >= 2) st64(c.rs, d4, j * 512 + key * 16 + (g - 2) * 8, kva); }
        else if (j == 2) { if (g == 3) st64(c.rs, u32x2{d4[1], 0x00003f80u}, KA_KREM + key * 8, kva); }
        else if (j < 6) {
            asm volatile("s_waitcnt lgkmcnt(0)" ::: "memory");
            const int q = (lane & 15) >> 2, pp = lane & 3;
            LAS char* s = c.L + L_STG + (16 * m + q + 4 * (pp >> 1)) * STG_P + 144 + 8 * (pp & 1);
            const u32x2 lo = trr(s), hi = trr(s + 8 * STG_P);
            const int dl = lane & 7, blk = (m & 1) * 2 + ((lane >> 3) & 1);
            if (j < 5) { if (lane < 16) st128(c.rs, u32x4{lo[0], lo[1], hi[0], hi[1]}, (j == 3 ? KA_V0 : KA_V8) + blk * 128 + dl * 16, kva); }
            else {
                const u32x4 on = ones4();
                const u32x4 v = dl >= 6 ? u32x4{lo[0], lo[1], hi[0], hi[1]} : on;
                if (lane < 16 && (dl >= 6 || dl < 2)) st128(c.rs, v, KA_VX + blk * 64 + (dl >= 6 ? dl - 6 : 2 + dl) * 16, kva);
            }
        }
    }
    lst(stg + (2 * kh) * 32, fin(0, 2 * kh, rstd * QSCALE));
    lst(stg + (2 * kh + 1) * 32, fin(1, 2 * kh + 1, rstd * QSCALE));
    if (rep == 0) {
        fmt_finish(c, fm);
        const u64 e = e0 + 1; c0.elo = c.elo = __builtin_amdgcn_readfirstlane((u32)e); c0.ehi = c.ehi = __builtin_amdgcn_readfirstlane((u32)(e >> 32));
    }
    STAMP(4 + rep * 30);
    publish(c, rep * 6);
}

DI void load_wring(const Ctx& c, u32x4 (&w0)[9], u32x4 (&w1)[9]) {
    const u32 wso = WS_WF + (u32)((c.j * 8 + c.wave) * 2) * 18 * 1024;
#pragma unroll
    for (int s = 0; s < 9; ++s) { w0[s] = ld128(c.rs, c.lane * 16, wso + s * 1024); w1[s] = ld128(c.rs, c.lane * 16, wso + (18 + s) * 1024); }
}
DI void colsums_b(const Ctx& c) {
    const int tok = c.lane & 15, g = c.lane >> 4;
    if (c.wave >= 1) {
        const int extra = c.wave == 1 ? 0 : c.wave;
        u32 v[4][5];
#pragma unroll
        for (int q = 0; q < 4; ++q) {
            const int unit = (q < 2 ? c.wave : extra) * 2 + (q & 1);
#pragma unroll
            for (int u = 0; u < 5; ++u) { const int s = g + 4 * u; v[q][u] = (q < 2 || c.wave == 1) ? ld32(c.rs, (s < 18 ? s : 0) * 64 + tok * 4, WS_CSP + (u32)((c.j * 16 + unit) * 18) * 64) : 0u; }
        }
#pragma unroll
        for (int q = 0; q < 4; ++q) {
            const int unit = (q < 2 ? c.wave : extra) * 2 + (q & 1);
            float cs = (asf(v[q][0]) + asf(v[q][1])) + (asf(v[q][2]) + asf(v[q][3])) + (g < 2 ? asf(v[q][4]) : 0.f);
            cs = sum_g(cs);
            if (g == 0 && (q < 2 || c.wave == 1)) lst(c.L + L_CSB + unit * 64 + tok * 4, cs);
        }
    }
}
struct ATail { f32x16 o; u32x4 qm; u32x2 qr; };
DI void a_attn(const Ctx& c0, const Params& p, int rep, ATail& st) {
    const Ctx c = fresh(c0);
    const int lane = c.lane, r = lane & 31, h = lane >> 5, a = c.wave & 3, kh = c.wave >> 2;
    const int ph = rep * 6;
    const u32 kva = WS_KVA + rep * KVA_BYTES;
    u32x4 qmx, qmy; u32x2 qrx, qry;
    { LAS char* s = c.L + L_STG + (32 * kh + r) * STG_P + 36 * a;
      qmx = u32x4{lld<u32>(s + 16 * h), lld<u32>(s + 16 * h + 4), lld<u32>(s + 16 * h + 8), lld<u32>(s + 16 * h + 12)};
      const u32 rx = lld<u32>(s + 32);
      s += (32 - 64 * kh) * STG_P;
      qmy = u32x4{lld<u32>(s + 16 * h), lld<u32>(s + 16 * h + 4), lld<u32>(s + 16 * h + 8), lld<u32>(s + 16 * h + 12)};
      const u32 ry = lld<u32>(s + 32);
      qrx = u32x2{h == 0 ? rx : 0u, 0u}; qry = u32x2{h == 0 ? ry : 0u, 0u}; }
    const int ch0 = c.i >> 2;
    auto chunk_ids = [&](int n) { const int ch = (ch0 + n) & 7; return ch * 32 + (lane & 31); };
    u32 pvo[5];
#pragma unroll
    for (int k = 0; k < 5; ++k) {
        const u32 S = (u32)((c.wave & 3) + 4 * k) * 64 + lane, tile = (S * 52429u) >> 23, ps = S - tile * 160;
        const u32 v = ps - 64, blk = (v * 3450u) >> 16, row = v - blk * 19;
        const u32 src = ps < 64 ? ps * 16 : (ps < 140 ? ka_vrow(blk, row) : (ps < 156 ? KA_KREM + (ps - 140) * 16 : ps * 16));
        pvo[k] = tile * TILE_B + src;
    }
    auto issue_piece = [&](int n, int k) {
        const int ch = (ch0 + n) & 7; const u32 so = kva + (u32)ch * CHUNK_B; const u32 dst = c.Lb + L_RING + (n % 3) * CHUNK_B;
        glds_ws(c.srd, pvo[k], so, dst + ((c.wave & 3) + 4 * k) * 1024);
    };
    auto issue_chunk8 = [&](int n) {
        if (c.wave < 4) { issue_piece(n, 0); issue_piece(n, 2); issue_piece(n, 4); } else { issue_piece(n, 1); issue_piece(n, 3); }
    };
    if (c.wave == 0) poll(c, ph, lane < 32 ? chunk_ids(0) : chunk_ids(1));
    BAR();
    STAMP(6 + rep * 30);
    issue_chunk8(0);
    if (c.wave < 4) { issue_piece(1, 0); issue_piece(1, 1); issue_piece(1, 2); issue_piece(1, 3); issue_piece(1, 4); asm volatile("s_waitcnt vmcnt(5)" ::: "memory"); }
    else DRAIN();
    BAR();
    STAMP(7 + rep * 30); if (rep == 0) CSTAMP(61);
    if (rep == 1) { CSTAMPW(0, 4); CSTAMPW(8, 0); }
    const int vr = vrow_of(r);
    LAS char* kb = c.L + L_RING + lane * 16;
    LAS char* rb = c.L + L_RING + KT_KREM + r * 8;
    LAS char* vb = c.L + L_RING + KT_V + h * KT_VBLK + vr * 16;
    f32x16 sAx, sBx, sAy, sBy, ox, oy;
    const u32x4 k0m = lld<u32x4>(kb); const u32x2 k0r = lld<u32x2>(rb);
    kb += kh * 4 * TILE_B; rb += kh * 4 * TILE_B; vb += kh * 4 * TILE_B;
    const u32x4 kom = lld<u32x4>(kb); const u32x2 kor = lld<u32x2>(rb);
    u32x4 km = lld<u32x4>(kb + TILE_B), v0 = lld<u32x4>(vb), v1 = lld<u32x4>(vb + 2 * KT_VBLK); u32x2 kr = lld<u32x2>(rb + TILE_B);
    Pipe px, py;
    { u32 z = 0u; asm volatile("" : "+v"(z));
      px.e[0] = px.e[1] = px.e[2] = px.e[3] = asf(z); px.p1 = u32x4{z, z, z, z}; py = px; }
    asm volatile("" : "+v"(px.p1), "+v"(px.e[0]), "+v"(px.e[1]), "+v"(px.e[2]), "+v"(px.e[3]), "+v"(qmx), "+v"(qrx));
    asm volatile("s_nop 1" : "+v"(py.p1), "+v"(py.e[0]), "+v"(py.e[1]), "+v"(py.e[2]), "+v"(py.e[3]), "+v"(qmy), "+v"(qry));
    QK1(ox, px.p1, px.p1); QK1(oy, px.p1, px.p1);
    QK1(sBx, k0m, qmx); QK1(sBy, k0m, qmy); QK2(sBx, k0r, qrx); QK2(sBy, k0r, qry);
    QK1(sAx, kom, qmx); QK1(sAy, kom, qmy);
    asm volatile("s_nop 7\n\ts_nop 1" : "+v"(sBx), "+v"(sBy));
    { const float mx = rowmax(sBx), my = rowmax(sBy);
      if (h == 0) { qrx[1] = pk(-mx, 0.f); qry[1] = pk(-my, 0.f); } }
    asm volatile("s_nop 1" : "+v"(qrx), "+v"(qry));
    QK2(sAx, kor, qrx); QK2(sAy, kor, qry);
    asm volatile("s_nop 7\n\ts_nop 3" : "+v"(sAx), "+v"(sAy));
    if (rep == 1) { CSTAMPW(1, 4); CSTAMPW(9, 0); }
    int slot = 0;
    const u64 want = ((u64)c.ehi << 32) | c.elo;
    const u32 fvo = (lane & 31) * 128;
    u64 fl = want;
    if (c.wave == 0) flag_load(fl, ~want, fvo, c.srd, WS_FLAG + (u32)(ph * 256 + ((ch0 + 2) & 7) * 32) * 128);
#pragma unroll 1
    for (int n = 0; n < 8; ++n) {
        const int nslot = slot == 2 ? 0 : slot + 1;
        LAS char* kc = kb + slot * CHUNK_B; LAS char* rc = rb + slot * CHUNK_B; LAS char* vc = vb + slot * CHUNK_B;
        LAS char* kn = kb + nslot * CHUNK_B; LAS char* rn = rb + nslot * CHUNK_B; LAS char* vn = vb + nslot * CHUNK_B;
#define A_TILE(U, SCX, SNX, SCY, SNY, X1, X2) attn_step2(SCX, SNX, ox, px, SCY, SNY, oy, py, km, kr, v0, v1, qmx, qrx, qmy, qry, \
            [&] { km = lld<u32x4>((U) < 2 ? kc + ((U) + 2) * TILE_B : kn + ((U) - 2) * TILE_B); X1; }, \
            [&] { kr = lld<u32x2>((U) < 2 ? rc + ((U) + 2) * TILE_B : rn + ((U) - 2) * TILE_B); }, \
            [&] { v1 = lld<u32x4>(vc + (U) * TILE_B + 2 * KT_VBLK); X2; }, \
            [&] { v0 = lld<u32x4>((U) < 3 ? vc + ((U) + 1) * TILE_B : vn); })
        A_TILE(0, sAx, sBx, sAy, sBy, (void)0, (void)0); A_TILE(1, sBx, sAx, sBy, sAy, (void)0, (void)0);
        if (__builtin_expect(c.wave < 4, 0)) {
            asm volatile("s_waitcnt vmcnt(0)" : "+v"(fl) :: "memory");
            if (__builtin_expect(!__all(fl == want), 0)) poll(c, ph, chunk_ids(n + 2));
        }
        BAR();
        if (__builtin_expect(c.wave < 4, 0)) {
            fl = want;
            if (c.wave == 0 && n + 3 < 8) flag_load(fl, ~want, fvo, c.srd, WS_FLAG + (u32)(ph * 256 + ((ch0 + n + 3) & 7) * 32) * 128);
            if (n + 2 < 8) { issue_piece(n + 2, 0); issue_piece(n + 2, 1); issue_piece(n + 2, 2); issue_piece(n + 2, 3); issue_piece(n + 2, 4); }
            if (n >= 5) load_wimage_o4(c, 0, n - 5);
            if (rep == 0 && n == 0) {
                u32 z0 = 0u; asm volatile("" : "+v"(z0));
                const u32x4 z = {z0, z0, z0, z0};
#pragma unroll
                for (int q = 0; q < 5; ++q)
                    st128(c.rs, z, lane * 16 + q * 1024, WS_KVB + (u32)((4 * c.j + c.wave) * 64 + 2 * c.i) * TILE_B);
                if (c.wave < 2) st128(c.rs, z, (u32)((16 + (c.j >> 2)) * 4 + (lane >> 4)) * 1024 + ((c.j & 3) * 16 + (lane & 15)) * 16, WS_OF + c.wave * XF_BYTES + (u32)(c.i * 72) * 1024);
            }
        }
        A_TILE(2, sAx, sBx, sAy, sBy, (void)0, (void)0); A_TILE(3, sBx, sAx, sBy, sAy, (void)0, (void)0);
#undef A_TILE
        slot = nslot;
    }
    if (rep == 1) { CSTAMPW(2, 4); CSTAMPW(10, 0); }
    attn_finish2(ox, px, oy, py, v1);
    { LAS char* mine = c.L + L_RING + kh * 2 * CHUNK_B + a * 3072 + lane * 16;
#pragma unroll
      for (int e = 0; e < 3; ++e) lst(mine + e * 1024, f32x4{oy[4 * e], oy[4 * e + 1], oy[4 * e + 2], oy[4 * e + 3]}); }
    st.o = ox; st.qm = qmx; st.qr = qrx;
}

template <bool ISB, class HK>
DI void out_proj(const Ctx& c0, const Params& p, int rep, u32 (&xres)[3][2], HK behind_x) {
    const Ctx c = fresh(c0);
    const int lane = c.lane, m = c.wave & 3, kh = c.wave >> 2, tok = lane & 15, g = lane >> 4;
    const int layer = rep * 2 + (ISB ? 1 : 0);
    wait_group(c, rep * 6 + (ISB ? 4 : 1));
    STAMP((ISB ? 25 : 10) + rep * 30);
    u32x4 xf[9]; f32x4 acc[5] = {};
    load_xfrags(c, WS_OF + (rep * RP * 2 + (ISB ? 1 : 0)) * XF_BYTES, xf);
    __builtin_amdgcn_sched_barrier(0);
    behind_x(c);
    if (ISB) {
        asm volatile("s_waitcnt vmcnt(9)" ::: "memory"); BAR();
    }
    g1_mma<false>(c, xf, acc, [](int, const u32x4 (&)[5]) {});
    STAMP((ISB ? 26 : 11) + rep * 30);
    g1_exchange(c, acc);
    STAMP((ISB ? 27 : 12) + rep * 30);
#pragma unroll
    for (int u = 0; u < 3; ++u) {
        const float v0 = bflo(xres[u][0]) + acc[u][0], v1 = bfhi(xres[u][0]) + acc[u][1], v2 = bflo(xres[u][1]) + acc[u][2], v3 = bfhi(xres[u][1]) + acc[u][3];
        xres[u][0] = pk(v0, v1); xres[u][1] = pk(v2, v3);
    }
    if (ISB && rep == 1) {
        char* o = (char*)p.out + (size_t)(c.i * 64 + m * 16 + tok) * (DM * 2) + (c.j * 72 + 4 * g) * 2;
        *(u32x2*)(o + (2 * kh) * 32) = u32x2{xres[0][0], xres[0][1]};
        *(u32x2*)(o + (2 * kh + 1) * 32) = u32x2{xres[1][0], xres[1][1]};
        if (kh == 1 && g < 2) *(u32x2*)(o + 128) = u32x2{xres[2][0], xres[2][1]};
        return;
    }
    const int xi = layer == 0 ? 0 : (layer == 1 ? 1 : 2 * RP);
    const u32 so = WS_XF + xi * XF_BYTES + (u32)(c.i * 18 * 4 + m) * 1024;
    st128(c.rs, u32x4{xres[0][0], xres[0][1], xres[1][0], xres[1][1]}, lane * 16, so + (u32)(2 * c.j + kh) * 4096);
    if (kh == 1 && g < 2) st64(c.rs, u32x2{xres[2][0], xres[2][1]}, ((c.j & 3) * 16 + tok) * 16 + 8 * g, so + (u32)(16 + (c.j >> 2)) * 4096);
    __builtin_amdgcn_sched_barrier(0);
    float s1 = 0.f, s2 = 0.f;
#pragma unroll
    for (int u = 0; u < 3; ++u) {
        u32 d0 = xres[u][0], d1 = xres[u][1];
        asm volatile("" : "+v"(d0), "+v"(d1));
        if (u == 2 && !(kh == 1 && g < 2)) { d0 = 0u; d1 = 0u; }
        const float a0 = bflo(d0), a1 = bfhi(d0), a2 = bflo(d1), a3 = bfhi(d1);
        s1 += (a0 + a1) + (a2 + a3); s2 += (a0 * a0 + a1 * a1) + (a2 * a2 + a3 * a3);
    }
    s1 = sum_g(s1); s2 = sum_g(s2);
    if (g == 0) st64(c.rs, u32x2{asu(s1), asu(s2)}, (u32)((c.i * 16 + 2 * c.j + kh) * 64 + m * 16 + tok) * 8, WS_ST + xi * ST_BYTES);
    publish(c, rep * 6 + (ISB ? 5 : 2));
    if (!ISB && rep == 0) colsums_b(c);
    if (ISB) load_wimage_part<36, 90>(c, WS_WIMGA, 1, L_A1);
}

template <bool EARLY>
DI void a_tail(const Ctx& c0, const Params& p, int rep, ATail& st, u32 (&xres)[3][2], u32x4 (&w0)[9], u32x4 (&w1)[9]) {
    const Ctx c = fresh(c0);
    const int lane = c.lane, a = c.wave & 3, kh = c.wave >> 2;
    BAR();
    LAS char* theirs = c.L + L_RING + (1 - kh) * 2 * CHUNK_B + a * 3072 + lane * 16;
#pragma unroll
    for (int e = 0; e < 3; ++e) { const f32x4 v = lld<f32x4>(theirs + e * 1024); st.o[4 * e] += v[0]; st.o[4 * e + 1] += v[1]; st.o[4 * e + 2] += v[2]; st.o[4 * e + 3] += v[3]; }
    STAMP(8 + rep * 30); if (rep == 0) CSTAMP(62);
    if (rep == 1) { CSTAMPW(3, 4); CSTAMPW(11, 0); }
    if (attn_unsafe(st.o)) st.o = attn_slow(c, WS_KVA + rep * KVA_BYTES, 0, 63, false, st.qm, st.qr);
    store_o(c, WS_OF + (rep * RP * 2) * XF_BYTES, st.o, a, kh);
    if (rep == 1) { CSTAMPW(4, 4); CSTAMPW(12, 0); }
    publish(c, rep * 6 + 1);
    if (EARLY) load_wring(c, w0, w1);
    STAMP(9 + rep * 30);
    out_proj<false>(c, p, rep, xres, [&](const Ctx& cc) {
        if (rep == 0) {
            const int m = cc.wave & 3, k2 = cc.wave >> 2, tok = cc.lane & 15, g = cc.lane >> 4;
            const char* rr = (const char*)p.x + (size_t)(cc.i * 64 + m * 16 + tok) * (DM * 2) + (cc.j * 72 + 4 * g) * 2;
            const u32x2 r0 = *(const u32x2*)(rr + (2 * k2) * 32), r1 = *(const u32x2*)(rr + (2 * k2 + 1) * 32), r2 = *(const u32x2*)(rr + (g < 2 ? 128 : 0));
            xres[0][0] = r0[0]; xres[0][1] = r0[1]; xres[1][0] = r1[0]; xres[1][1] = r1[1]; xres[2][0] = r2[0]; xres[2][1] = r2[1];
        }
        if (!EARLY) load_wring(cc, w0, w1); });
}

struct T_ { static constexpr bool value = true; };
struct F_ { static constexpr bool value = false; };
DI void b_proj(const Ctx& c0, const Params& p, int rep, u32x4 (&w0)[9], u32x4 (&w1)[9], u32x4& qm, u32x2& qr) {
    const Ctx c = fresh(c0);
    const int lane = c.lane, tok = lane & 15, g = lane >> 4, a = c.wave >> 1, odd = c.wave & 1;
    wait_group(c, rep * 6 + 2);
    STAMP(15 + rep * 30);
    const int xi = rep == 0 ? 0 : 2 * RP;
    u32x4 stv[4];
    if (c.wave == 5 || c.wave == 7) stats_issue32(c, WS_ST + xi * ST_BYTES, stv);
    { const u32 so = WS_XF + xi * XF_BYTES + (u32)(c.i * 72) * 1024;
#pragma unroll
      for (int n = 0; n < 9; ++n) glds_ws(c.srd, lane * 16, so + (c.wave + 8 * n) * 1024, c.Lb + L_XB + (c.wave + 8 * n) * 1024); }
    asm volatile("s_waitcnt vmcnt(5)" ::: "memory"); BAR();
    STAMP(16 + rep * 30);
    f32x4 a0[4] = {}, a1[4] = {};
    const u32 wso = WS_WF + (u32)((c.j * 8 + c.wave) * 2) * 18 * 1024;
    LAS char* xb = c.L + L_XB + lane * 16;
    const bool t0 = !odd, t1 = !odd || a == 0, has1 = !odd || a < 2;
    auto body = [&](auto T0, auto T1, auto H1) {
        u32x4 x[2][4];
#pragma unroll
        for (int m = 0; m < 4; ++m) { x[0][m] = lld<u32x4>(xb + m * 1024); asm volatile("" : "+v"(a0[m]), "+v"(a1[m])); }
        if (!H1.value) stats_finish32(c, stv);
#pragma unroll
        for (int s = 0; s < 18; ++s) {
            if (s == 7) {
                if (H1.value) asm volatile("s_waitcnt vmcnt(14)" ::: "memory"); else asm volatile("s_waitcnt vmcnt(7)" ::: "memory");
                BAR();
            }
            if (s + 1 < 18) {
#pragma unroll
                for (int m = 0; m < 4; ++m) x[(s + 1) & 1][m] = lld<u32x4>(xb + ((s + 1) * 4 + m) * 1024);
            }
#pragma unroll
            for (int m = 0; m < 4; ++m) {
                if (T0.value) MF16(a0[m], w0[s % 9], x[s & 1][m]); else MF16(a0[m], x[s & 1][m], w0[s % 9]);
                if (H1.value) { if (T1.value) MF16(a1[m], w1[s % 9], x[s & 1][m]); else MF16(a1[m], x[s & 1][m], w1[s % 9]); }
            }
            if (s < 9) { w0[s] = ld128(c.rs, lane * 16, wso + (9 + s) * 1024); if (H1.value) w1[s] = ld128(c.rs, lane * 16, wso + (27 + s) * 1024); }
        }
        asm volatile("s_nop 7" : "+v"(a0[0]), "+v"(a0[1]), "+v"(a0[2]), "+v"(a0[3]), "+v"(a1[0]), "+v"(a1[1]), "+v"(a1[2]), "+v"(a1[3]));
    };
    if (!odd) body(T_{}, T_{}, T_{}); else if (a == 0) body(F_{}, T_{}, T_{}); else if (a == 1) body(F_{}, F_{}, T_{}); else body(F_{}, F_{}, F_{});
    (void)t0; (void)t1; (void)has1;
    STAMP(17 + rep * 30);
    const u32 kvb = WS_KVB + rep * RP * KVB_BYTES;
    const u32 hb = kvb + (u32)((4 * c.j + a) * 64 + 2 * c.i) * TILE_B;
    auto fin_t = [&](const f32x4 (&acc)[4], int slot, float sc, u32 (&d)[4][2]) {
        const f32x4 cs = lld<f32x4>(c.L + L_CSB + (c.wave * 2 + slot) * 64 + g * 16);
#pragma unroll
        for (int m = 0; m < 4; ++m) {
            const float mean = lld<float>(c.L + L_MEAN + (m * 16 + tok) * 4), rs = lld<float>(c.L + L_MEAN + 256 + (m * 16 + tok) * 4) * sc;
            d[m][0] = pk((acc[m][0] - mean * cs[0]) * rs, (acc[m][1] - mean * cs[1]) * rs);
            d[m][1] = pk((acc[m][2] - mean * cs[2]) * rs, (acc[m][3] - mean * cs[3]) * rs);
        }
    };
    auto fin_p = [&](const f32x4 (&acc)[4], int slot, u32 (&d)[4][2]) {
        const float cs = lld<float>(c.L + L_CSB + (c.wave * 2 + slot) * 64 + tok * 4);
#pragma unroll
        for (int m = 0; m < 4; ++m) {
            const f32x4 mean = lld<f32x4>(c.L + L_MEAN + (m * 16 + 4 * g) * 4), rs = lld<f32x4>(c.L + L_MEAN + 256 + (m * 16 + 4 * g) * 4);
            d[m][0] = pk((acc[m][0] - mean[0] * cs) * rs[0], (acc[m][1] - mean[1] * cs) * rs[1]);
            d[m][1] = pk((acc[m][2] - mean[2] * cs) * rs[2], (acc[m][3] - mean[3] * cs) * rs[3]);
        }
    };
    auto frag16 = [&](const u32 (&d)[4][2], int t) {
        auto x = __builtin_amdgcn_permlane16_swap(d[2 * t][0], d[2 * t + 1][0], false, false);
        auto y = __builtin_amdgcn_permlane16_swap(d[2 * t][1], d[2 * t + 1][1], false, false);
        return u32x4{(u32)x[0], (u32)y[0], (u32)x[1], (u32)y[1]};
    };
    auto frag32 = [&](const u32 (&d)[4][2], int t) {
        auto x = __builtin_amdgcn_permlane32_swap(d[2 * t][0], d[2 * t + 1][0], false, false);
        auto y = __builtin_amdgcn_permlane32_swap(d[2 * t][1], d[2 * t + 1][1], false, false);
        return u32x4{(u32)x[0], (u32)y[0], (u32)x[1], (u32)y[1]};
    };
    u32 d[4][2];
    LAS char* lh = c.L + L_KVB + a * 25600 + 8 * TILE_B; LAS char* l0 = c.L + L_KVB + 8 * TILE_B;
    if (!odd) {
        fin_t(a1, 1, 1.f, d);
        { const u32x4 f0 = frag16(d, 0), f1 = frag16(d, 1);
          st128(c.rs, f0, lane * 16, hb); st128(c.rs, f1, lane * 16, hb + TILE_B);
          lst(lh + lane * 16, f0); lst(lh + TILE_B + lane * 16, f1); }
        fin_t(a0, 0, QSCALE, d);
        qm = frag16(d, 0);
        lst(c.L + L_QT1 + a * 1024 + lane * 16, frag16(d, 1));
    } else {
        fin_p(a0, 0, d);
        { const u32x4 f0 = frag32(d, 0), f1 = frag32(d, 1);
          st128(c.rs, f0, KT_V + g * KT_VBLK + tok * 16, hb); st128(c.rs, f1, KT_V + g * KT_VBLK + tok * 16, hb + TILE_B);
          lst(lh + KT_V + g * KT_VBLK + tok * 16, f0); lst(lh + TILE_B + KT_V + g * KT_VBLK + tok * 16, f1); }
        if (a == 0) {
            fin_t(a1, 1, g < 2 ? QSCALE : 1.f, d);
#pragma unroll
            for (int m = 0; m < 4; ++m) {
                if (g < 2) { lst(c.L + L_QREM + (2 * g) * 256 + (m * 16 + tok) * 4, d[m][0]); lst(c.L + L_QREM + (2 * g + 1) * 256 + (m * 16 + tok) * 4, d[m][1]); }
                else {
                    const u32 hb0 = kvb + (u32)(4 * c.j * 64 + 2 * c.i + (m >> 1)) * TILE_B;
                    st64(c.rs, u32x2{d[m][0], 0x00003f80u}, (u32)(2 * (g - 2)) * 64 * TILE_B + KT_KREM + ((m & 1) * 16 + tok) * 8, hb0);
                    st64(c.rs, u32x2{d[m][1], 0x00003f80u}, (u32)(2 * (g - 2) + 1) * 64 * TILE_B + KT_KREM + ((m & 1) * 16 + tok) * 8, hb0);
                    LAS char* lk = l0 + (2 * (g - 2)) * 25600 + (m >> 1) * TILE_B + KT_KREM + ((m & 1) * 16 + tok) * 8;
                    lst(lk, u32x2{d[m][0], 0x00003f80u}); lst(lk + 25600, u32x2{d[m][1], 0x00003f80u});
                }
            }
        } else if (a == 1) {
            fin_p(a1, 1, d);
            const u32 hb0 = kvb + (u32)(4 * c.j * 64 + 2 * c.i) * TILE_B;
            const u32 vo = (u32)(tok >> 1) * 64 * TILE_B + KT_V + g * KT_VBLK + (16 + (tok & 1)) * 16;
            const u32x4 f0 = frag32(d, 0), f1 = frag32(d, 1);
            if (tok < 8) {
                st128(c.rs, f0, vo, hb0); st128(c.rs, f1, vo, hb0 + TILE_B);
                LAS char* lv = l0 + (tok >> 1) * 25600 + KT_V + g * KT_VBLK + (16 + (tok & 1)) * 16;
                lst(lv, f0); lst(lv + TILE_B, f1);
            }
        } else if (a == 2) {
            const u32 hb0 = kvb + (u32)(4 * c.j * 64 + 2 * c.i) * TILE_B;
            if (lane < 32) {
                const u32x4 on = ones4();
                st128(c.rs, on, (u32)(lane >> 3) * 64 * TILE_B + ((lane >> 2) & 1) * TILE_B + KT_V + (lane & 3) * KT_VBLK + 18 * 16, hb0);
                lst(l0 + (lane >> 3) * 25600 + ((lane >> 2) & 1) * TILE_B + KT_V + (lane & 3) * KT_VBLK + 18 * 16, on);
            }
        }
    }
    publish(c, rep * 6 + 3);
    load_wimage_part<44, 90>(c, WS_WIMG + 8 * WIMG_BYTES, 1);
    if (odd) qm = lld<u32x4>(c.L + L_QT1 + a * 1024 + lane * 16);
    const u32 rem = lld<u32>(c.L + L_QREM + a * 256 + (32 * odd + (lane & 31)) * 4);
    qr = u32x2{lane < 32 ? rem : 0u, 0u};
}

DI void b_attn(const Ctx& c0, const Params& p, int rep, u32x4 qm, u32x2 qr) {
    const Ctx c = fresh(c0);
    const int lane = c.lane, r = lane & 31, h = lane >> 5, a = c.wave >> 1, t = c.wave & 1;
    const int qt = 2 * c.i + t;
    const f32x16 cd = mask_c(lane, true);
    if (c.wave == 1) {
        const f32x16 cm = mask_c(lane, false);
#pragma unroll
        for (int e = 0; e < 4; ++e) lst(c.L + L_MSK + e * 1024 + lane * 16, f32x4{cm[4 * e], cm[4 * e + 1], cm[4 * e + 2], cm[4 * e + 3]});
    }
    LAS char* tb = c.L + L_KVB + a * 25600 + (8 + t) * TILE_B;
    LAS char* kb = tb + lane * 16; LAS char* rb = tb + KT_KREM + r * 8; LAS char* vb = tb + KT_V + h * KT_VBLK + vrow_of(r) * 16;
    const u32x4 k0m = lld<u32x4>(kb); const u32x2 k0r = lld<u32x2>(rb);
    u32x4 km, v0 = lld<u32x4>(vb), v1 = lld<u32x4>(vb + 2 * KT_VBLK); u32x2 kr;
    f32x16 sA, sB, o;
    Pipe pp;
    { u32 z = 0u; asm volatile("" : "+v"(z)); pp.e[0] = pp.e[1] = pp.e[2] = pp.e[3] = asf(z); pp.p1 = u32x4{z, z, z, z}; }
    f32x16 cdp = cd;
    asm volatile("s_nop 1" : "+v"(pp.p1), "+v"(pp.e[0]), "+v"(pp.e[1]), "+v"(pp.e[2]), "+v"(pp.e[3]), "+v"(qm), "+v"(qr), "+v"(cdp));
    QK1(o, pp.p1, pp.p1);
    QK1C(sA, k0m, qm, cdp);
    asm volatile("v_mfma_f32_32x32x8_bf16 %0, %1, %2, %3" : "=&v"(sB) : "v"(k0r), "v"(qr), "v"(sA));
    asm volatile("s_nop 7\n\ts_nop 3" : "+v"(sB), "+v"(sA));
    { const float m0 = rowmax(sB); if (h == 0) qr[1] = pk(-m0, 0.f); }
    asm volatile("s_nop 1" : "+v"(qr));
    QK2(sA, k0r, qr);
    asm volatile("s_nop 7\n\ts_nop 3" : "+v"(sA), "+v"(o), "+v"(v0), "+v"(v1));
    if (c.wave == 0) poll(c, rep * 6 + 3, (lane < 5 && c.i - lane >= 0) ? (c.i - lane) * 8 + c.j : -1);
    BAR();
    STAMP(20 + rep * 30);
    const u32 hb = WS_KVB + rep * RP * KVB_BYTES + (u32)((4 * c.j + a) * 64) * TILE_B;
    { const u32 dst = c.Lb + L_KVB + a * 25600;
#pragma unroll
      for (int n = 0; n < 10; ++n) {
          const int pc = 19 - t - 2 * n;
          u32 vo = lane * 16;
          if (c.i < 4) { const int b = pc * 1024 + lane * 16, tau = b / 2560, kt = 2 * c.i - 8 + tau; vo = (u32)((kt < 0 ? 0 : kt) * 2560 + (b - tau * 2560)); glds_ws(c.srd, vo, hb, dst + pc * 1024); }
          else glds_ws(c.srd, vo, hb + (u32)(2 * c.i - 8) * 2560 + pc * 1024, dst + pc * 1024);
      } }
    asm volatile("s_waitcnt vmcnt(5)" ::: "memory");
    BAR();
    STAMP(21 + rep * 30);
    km = lld<u32x4>(kb - TILE_B); kr = lld<u32x2>(rb - TILE_B);
    auto first = [](f32x16& s, u32x4 k, u32x4 q) { QK1(s, k, q); };
    auto firstm = [&](f32x16& s, u32x4 k, u32x4 q) {
        f32x16 co;
#pragma unroll
        for (int e = 0; e < 4; ++e) { const f32x4 v = lld<f32x4>(c.L + L_MSK + e * 1024 + lane * 16); co[4 * e] = v[0]; co[4 * e + 1] = v[1]; co[4 * e + 2] = v[2]; co[4 * e + 3] = v[3]; }
        QK1C(s, k, q, co); };
    const u32 qoff = h == 0 ? pk(NEG_BIG, 0.f) : 0u;
    kb -= 8 * TILE_B; rb -= 8 * TILE_B; vb -= 8 * TILE_B;
#define B_TILE(U, SC, SN, FIRST) do { u32x2 qs = {qr[0], (U) + 1 <= qt ? qr[1] : qoff}; asm volatile("" : "+v"(qs)); \
        attn_step<((U) < 8)>(SC, SN, o, pp, km, kr, v0, v1, qm, qs, FIRST, \
            [&] { if ((U) + 2 <= 8) km = lld<u32x4>(kb + (8 - ((U) + 2)) * TILE_B); }, [&] { if ((U) + 2 <= 8) kr = lld<u32x2>(rb + (8 - ((U) + 2)) * TILE_B); }, \
            [&] { v1 = lld<u32x4>(vb + (8 - (U)) * TILE_B + 2 * KT_VBLK); }, [&] { if ((U) + 1 <= 8) v0 = lld<u32x4>(vb + (8 - ((U) + 1)) * TILE_B); }); } while (0)
    B_TILE(0, sA, sB, first); B_TILE(1, sB, sA, first);
    DRAIN();
    B_TILE(2, sA, sB, first);
    BAR();
    B_TILE(3, sB, sA, first); B_TILE(4, sA, sB, first); B_TILE(5, sB, sA, first); B_TILE(6, sA, sB, first); B_TILE(7, sB, sA, firstm); B_TILE(8, sA, sB, first);
#undef B_TILE
    attn_finish(o, pp, v1);
    STAMP(22 + rep * 30);
    if (attn_unsafe(o)) o = attn_slow(c, hb, qt - 8 < 0 ? 0 : qt - 8, qt, true, qm, qr);
    store_o(c, WS_OF + (rep * RP * 2 + 1) * XF_BYTES, o, a, t);
    publish(c, rep * 6 + 4);
    load_wimage_part<0, 44>(c, WS_WIMG + 8 * WIMG_BYTES, 1);
    if (rep == 0) load_wimage_part<0, 36>(c, WS_WIMGA, 1, L_A1);
}

__global__ void __launch_bounds__(512, 2) fused_block_kernel(Params p) {
    __shared__ __attribute__((aligned(16))) char lds_[LDS_BYTES];
    Ctx c;
    c.L = (LAS char*)lds_; c.Lb = (u32)(size_t)c.L;
    c.ws = p.ws;
    c.tid = threadIdx.x; c.lane = c.tid & 63; c.wave = __builtin_amdgcn_readfirstlane(c.tid >> 6);
    { const int wg = blockIdx.x, slot = wg >> 3; c.i = (wg & 7) * 4 + (slot >> 3); c.j = slot & 7; }
    c.rs = __builtin_amdgcn_make_buffer_rsrc((void*)p.ws, 0, WS_USED, 0x00020000);
    { const u64 b = (u64)p.ws; c.srd = u32x4{(u32)b, (u32)(b >> 32) & 0xffffu, WS_USED, 0x00020000u}; }
    STAMP(0);
    u32 warm;
    { u64 pc; u32 len;
      asm volatile("s_getpc_b64 %0\nfbk_code_first:\n\ts_mov_b32 %1, fbk_code_last-fbk_code_first" : "=s"(pc), "=s"(len));
      u32 off = ((u32)(blockIdx.x >> 3) * 8 + c.wave) * 256 + c.lane * 4;
      off = off < len ? off : 0u;
      warm = *(const GAS u32*)((pc & ~3ull) + off); }
    c.elo = 0; c.ehi = 0;
    u32 xres[3][2];
    STAMP(1);
    a_proj<0>(c, p);
    asm volatile("" :: "v"(warm));
    STAMP(5);
#pragma unroll 1
    for (int rep = 0; rep < 2; ++rep) {
        u32x4 w0[9], w1[9]; ATail st;
        a_attn(c, p, rep, st);
        if (rep == 0) {
            if (blockIdx.x == 0 && c.tid == 0) __hip_atomic_store((GAS u64*)(p.ws + WS_EPOCH), ((u64)c.ehi << 32) | c.elo, __ATOMIC_RELAXED, __HIP_MEMORY_SCOPE_AGENT);
        }
        if (c.wave >= 1 && c.wave < 4) a_tail<true>(c, p, rep, st, xres, w0, w1); else a_tail<false>(c, p, rep, st, xres, w0, w1);
        STAMP(14 + rep * 30);
        u32x4 qm; u32x2 qr;
        b_proj(c, p, rep, w0, w1, qm, qr);
        STAMP(19 + rep * 30);
        b_attn(c, p, rep, qm, qr);
        STAMP(24 + rep * 30);
        out_proj<true>(c, p, rep, xres, [](const Ctx&) {});
        STAMP(29 + rep * 30);
        if (rep == 1) break;
        STAMP(31);
        a_proj<1>(c, p);
        STAMP(35);
    }
    asm volatile("fbk_code_last:");
}

static size_t ws_offset(size_t ws_size) { return (ws_size - WS_USED) & ~(size_t)0x1fffff; }

extern "C" void kernel_launch(void* const* d_in, const int* in_sizes, int n_in, void* d_out, int out_size, void* d_ws, size_t ws_size, hipStream_t stream) {
    Params p;
    p.x = (const u16*)d_in[0]; p.wq = (const u16*)d_in[1]; p.wk = (const u16*)d_in[2]; p.wv = (const u16*)d_in[3]; p.woA = (const u16*)d_in[4];
    p.wqkv = (const u16*)d_in[6]; p.woB = (const u16*)d_in[7]; p.out = (u16*)d_out;
    p.ws = (char*)d_ws + ws_offset(ws_size);
    fused_block_kernel<<<256, 512, 0, stream>>>(p);
}
```

```cpp
#include <hip/hip_runtime.h>
#include <hip/hip_bf16.h>

typedef unsigned short u16;
typedef unsigned u32;
typedef unsigned long long u64;
typedef __bf16 bf16x8 __attribute__((ext_vector_type(8)));
typedef __bf16 bf16x2 __attribute__((ext_vector_type(2)));
typedef short s16x4 __attribute__((ext_vector_type(4)));
typedef float f32x2 __attribute__((ext_vector_type(2)));
typedef float f32x4 __attribute__((ext_vector_type(4)));
typedef float f32x16 __attribute__((ext_vector_type(16)));
typedef u32 u32x2 __attribute__((ext_vector_type(2)));
typedef u32 u32x4 __attribute__((ext_vector_type(4)));
#define LAS __attribute__((address_space(3)))
#define GAS __attribute__((address_space(1)))
#define DI __device__ __forceinline__

constexpr int SEQ = 2048, DM = 576;
constexpr int RP = 0;
constexpr u32 TILE_B = 2560;
constexpr u32 KT_V = 1024, KT_VBLK = 304, KT_KREM = 2240;
constexpr u32 KA_KREM = 1024, KA_V0 = 1280, KA_V8 = 1792, KA_VX = 2304;
constexpr u32 XF_BYTES = 32u * 18 * 4 * 1024;
constexpr u32 WS_EPOCH = 0;
constexpr u32 WS_FLAG = 4096;
constexpr int PH_FMT = 11;
constexpr u32 WS_XF = WS_FLAG + 12u * 256 * 128 + 4096;
constexpr u32 WS_OF = WS_XF + (2 + RP) * XF_BYTES;
constexpr u32 ST_BYTES = 2048u * 16 * 8;
constexpr u32 WS_ST = WS_OF + (2 + 2 * RP) * XF_BYTES;
constexpr u32 KVA_BYTES = 64 * TILE_B;
constexpr u32 WS_KVA = WS_ST + (2 + RP) * ST_BYTES;
constexpr u32 KVB_BYTES = 32 * 64 * TILE_B;
constexpr u32 WS_KVB = WS_KVA + 2 * KVA_BYTES;
constexpr u32 WF_BYTES = 8u * 16 * 18 * 1024;
constexpr u32 WS_WF = WS_KVB + (1 + RP) * KVB_BYTES;
constexpr u32 WS_CSP = WS_WF + WF_BYTES;
constexpr u32 WIMG_BYTES = 90 * 1024;
constexpr u32 WS_WIMG = WS_CSP + 8u * 16 * 18 * 64;
constexpr u32 WS_WIMGA = WS_WIMG + 16 * WIMG_BYTES;
constexpr u32 WS_STAMP = WS_WIMGA + 8 * WIMG_BYTES;
constexpr u32 WS_STAMP2 = WS_STAMP + 256u * 64 * 8 + 256u * 8 * 8 * 8;
constexpr u32 WS_USED = WS_STAMP2 + 256u * 16 * 8;
constexpr int L_XCH = 0;
constexpr int L_RING = 0;
constexpr int CHUNK_B = 8 * TILE_B;
constexpr int L_STG = 40960;
constexpr int STG_P = 168;
constexpr int L_XIN = 0;
constexpr int L_XIN2 = 153600;
constexpr int L_FMT = 24576;
constexpr int L_KVB = 0;
constexpr int L_MSK = 102400;
constexpr int L_R1 = 61440;
constexpr int WP = 160;
constexpr int L_XB = 76800;
constexpr int L_A1 = 24576;
constexpr int L_STATP = 153600;
constexpr int L_MEAN = L_STATP + 1024;
constexpr int L_CSA = L_MEAN + 512;
constexpr int L_CSB = L_CSA + 1024;
constexpr int L_QREM = L_CSB + 1024;
constexpr int L_QT1 = L_QREM + 1024;
constexpr int LDS_BYTES = 163840;
static_assert(L_QT1 + 4096 <= LDS_BYTES, "lds");

#define WRING 4
constexpr float QSCALE = 0.23570226039551584f * 1.4426950408889634f;
constexpr float NEG_BIG = -1e30f;
constexpr u32 SPIN_CAP = 1u << 20;
constexpr int AUX_SC1 = 16;

struct Params { const u16* x; const u16* wq; const u16* wk; const u16* wv; const u16* woA; const u16* wqkv; const u16* woB; u16* out; char* ws; };

DI u32 pk(float a, float b) { f32x2 v = {a, b}; return __builtin_bit_cast(u32, __builtin_convertvector(v, bf16x2)); }
DI float asf(u32 d) { return __builtin_bit_cast(float, d); }
DI u32 asu(float f) { return __builtin_bit_cast(u32, f); }
DI float bflo(u32 d) { return __builtin_bit_cast(float, d << 16); }
DI float bfhi(u32 d) { return __builtin_bit_cast(float, d & 0xffff0000u); }
DI bf16x8 asb(u32x4 v) { return __builtin_bit_cast(bf16x8, v); }
DI f32x4 mfma16(u32x4 a, u32x4 b, f32x4 c) { return __builtin_amdgcn_mfma_f32_16x16x32_bf16(asb(a), asb(b), c, 0, 0, 0); }
DI f32x16 mfma32(u32x4 a, u32x4 b, f32x16 c) { return __builtin_amdgcn_mfma_f32_32x32x16_bf16(asb(a), asb(b), c, 0, 0, 0); }
DI void MF16(f32x4& acc, u32x4 a, u32x4 b) { asm volatile("v_mfma_f32_16x16x32_bf16 %0, %1, %2, %0" : "+v"(acc) : "v"(a), "v"(b)); }
#define SETTLE4(x) asm volatile("s_nop 7" : "+v"(x))
template <class T> DI T lld(LAS char* p) { return *(LAS const T*)p; }
template <class T> DI void lst(LAS char* p, T v) { *(LAS T*)p = v; }
DI u32x2 trr(LAS char* p) { return __builtin_bit_cast(u32x2, __builtin_amdgcn_ds_read_tr16_b64_v4i16((LAS s16x4*)p)); }
#define BAR() asm volatile("s_waitcnt lgkmcnt(0)\n\ts_barrier" ::: "memory")
#define DRAIN() asm volatile("s_waitcnt vmcnt(0)" ::: "memory")
typedef __amdgpu_buffer_rsrc_t rsrc_t;
DI u32x4 ld128(rsrc_t rs, u32 voff, u32 soff) { return __builtin_amdgcn_raw_buffer_load_b128(rs, voff, soff, AUX_SC1); }
DI u32x2 ld64(rsrc_t rs, u32 voff, u32 soff) { return __builtin_amdgcn_raw_buffer_load_b64(rs, voff, soff, AUX_SC1); }
DI u32 ld32(rsrc_t rs, u32 voff, u32 soff) { return __builtin_amdgcn_raw_buffer_load_b32(rs, voff, soff, AUX_SC1); }
DI void st128(rsrc_t rs, u32x4 v, u32 voff, u32 soff) { __builtin_amdgcn_raw_buffer_store_b128(v, rs, voff + soff, 0, AUX_SC1); }
DI void st64(rsrc_t rs, u32x2 v, u32 voff, u32 soff) { __builtin_amdgcn_raw_buffer_store_b64(v, rs, voff, soff, AUX_SC1); }
DI void st32(rsrc_t rs, u32 v, u32 voff, u32 soff) { __builtin_amdgcn_raw_buffer_store_b32(v, rs, voff, soff, AUX_SC1); }
DI void glds_ws(u32x4 srd, u32 voff, u32 soff, u32 ldsdst) {
    soff = __builtin_amdgcn_readfirstlane(soff); ldsdst = __builtin_amdgcn_readfirstlane(ldsdst);
    asm volatile("s_mov_b32 m0, %0\n\ts_nop 3\n\tbuffer_load_dwordx4 %1, %2, %3 offen sc1 lds" :: "s"(ldsdst), "v"(voff), "s"(srd), "s"(soff) : "memory");
}
DI void glds_in(const void* g, u32 ldsdst) {
    ldsdst = __builtin_amdgcn_readfirstlane(ldsdst);
    asm volatile("s_mov_b32 m0, %0\n\ts_nop 0\n\tglobal_load_lds_dwordx4 %1, off" :: "s"(ldsdst), "v"(g) : "memory");
}
DI void flag_load(u64& fl, u64 nomatch, u32 voff, u32x4 srd, u32 soff) {
    soff = __builtin_amdgcn_readfirstlane(soff);
    fl = nomatch;
    asm volatile("s_nop 4\n\tbuffer_load_dwordx2 %0, %1, %2, %3 offen sc1" : "+v"(fl) : "v"(voff), "s"(srd), "s"(soff) : "memory");
}
DI float sum_g(float v) {
    auto a = __builtin_amdgcn_permlane32_swap(__builtin_bit_cast(u32, v), __builtin_bit_cast(u32, v), false, false);
    v = __builtin_bit_cast(float, (u32)a[0]) + __builtin_bit_cast(float, (u32)a[1]);
    auto b = __builtin_amdgcn_permlane16_swap(__builtin_bit_cast(u32, v), __builtin_bit_cast(u32, v), false, false);
    return __builtin_bit_cast(float, (u32)b[0]) + __builtin_bit_cast(float, (u32)b[1]);
}
DI float max_h(float v) {
    auto a = __builtin_amdgcn_permlane32_swap(__builtin_bit_cast(u32, v), __builtin_bit_cast(u32, v), false, false);
    return fmaxf(__builtin_bit_cast(float, (u32)a[0]), __builtin_bit_cast(float, (u32)a[1]));
}
DI u32x4 ones4() { u32 o = 0x3f803f80u; asm volatile("" : "+v"(o)); return u32x4{o, o, o, o}; }
DI int crow(int reg, int h) { return (reg & 3) + 8 * (reg >> 2) + 4 * h; }

#define STAMP(n) do {} while (0)
#define CSTAMP(n) do {} while (0)
#define CSTAMPW(n, W) do {} while (0)

struct Ctx {
    LAS char* L; u32 Lb;
    char* ws; rsrc_t rs; u32x4 srd;
    int tid, lane, wave, i, j;
    u32 elo, ehi;
};

DI Ctx fresh(const Ctx& c0) {
    Ctx c = c0;
    asm volatile("" : "+v"(c.lane)); asm volatile("" : "+v"(c.tid));
    asm volatile("" : "+s"(c.wave)); asm volatile("" : "+s"(c.i)); asm volatile("" : "+s"(c.j));
    return c;
}
DI GAS u64* flag_ptr(const Ctx& c, int phase, int id) { return (GAS u64*)(c.ws + WS_FLAG + (u32)(phase * 256 + id) * 128); }
DI void signal(const Ctx& c, int phase) {
    if (c.tid == 0) __hip_atomic_store(flag_ptr(c, phase, c.i * 8 + c.j), ((u64)c.ehi << 32) | c.elo, __ATOMIC_RELAXED, __HIP_MEMORY_SCOPE_AGENT);
}
DI void publish(const Ctx& c, int phase) {
    DRAIN();
    BAR();
    signal(c, phase);
}
DI void poll(const Ctx& c, int phase, int id) {
    const u64 want = ((u64)c.ehi << 32) | c.elo;
    GAS u64* f = flag_ptr(c, phase, id < 0 ? 0 : id);
    for (u32 spins = 0; spins < SPIN_CAP; ++spins) {
        const u64 v = __hip_atomic_load(f, __ATOMIC_RELAXED, __HIP_MEMORY_SCOPE_AGENT);
        if (__all(id < 0 || v == want)) break;
        __builtin_amdgcn_s_sleep(1);
    }
    __builtin_amdgcn_fence(__ATOMIC_ACQUIRE, "wavefront");
}
DI void wait_group(const Ctx& c, int phase) {
    if (c.wave == 0) poll(c, phase, c.lane < 8 ? c.i * 8 + c.lane : -1);
    BAR();
}

DI void load_wimage_a(const Ctx& c, const Params& p) {
    const int jj = c.j, w = c.wave;
    const u16 *pk_ = p.wk, *pv_ = p.wv;
    asm volatile("" : "+s"(pk_), "+s"(pv_));
    const char* xsrc = (const char*)((jj >= 3 && jj < 6) ? pv_ : pk_); const int sub = jj < 6 ? jj % 3 : 0;
    xsrc += sub == 0 ? 0 : (sub == 1 ? 16 : 20);
    const char* msrc = (const char*)p.wq + jj * 144;
    const char* a[5]; u32 st[5];
#pragma unroll
    for (int k = 0; k < 5; ++k) {
        const u32 id = 64 * k + c.lane, row = (id * 6554u) >> 16, ch = id - row * 10;
        const bool ex = ch == 9;
        st[k] = ex ? 32 * 36 : 32 * 1152;
        a[k] = (ex ? xsrc : msrc) + (ex ? row * 36 : row * 1152 + ch * 16);
    }
#pragma unroll
    for (int n = 0; n < 3; ++n) {
        const int G = n < 2 ? (w & 3) + 8 * (w >> 2) + 4 * n : 16 + w;
        if (n < 2 || w < 2) {
#pragma unroll
            for (int k = 0; k < 5; ++k) glds_in(a[k] + (u32)G * st[k], c.Lb + L_R1 + (G * 5 + k) * 1024);
        }
    }
}
DI void load_wimage_o(const Ctx& c, u32 base) {
    const u32 so = base + (u32)c.j * WIMG_BYTES;
    if (c.wave >= 1) {
#pragma unroll
        for (int n = 0; n < 13; ++n)
            if (n < 12 || c.wave < 7) glds_ws(c.srd, c.lane * 16, so + (c.wave - 1 + 7 * n) * 1024, c.Lb + L_R1 + (c.wave - 1 + 7 * n) * 1024);
    }
}
template <int P0, int P1>
DI void load_wimage_part(const Ctx& c, u32 base, int w0, int ldsbase = L_R1) {
    const u32 so = base + (u32)c.j * WIMG_BYTES; const int nw = 8 - w0;
    if (c.wave >= w0) {
#pragma unroll
        for (int n = 0; n < (P1 - P0 + 6) / 7; ++n) {
            const int pc = P0 + (c.wave - w0) + nw * n;
            if (pc < P1) glds_ws(c.srd, c.lane * 16, so + pc * 1024, c.Lb + ldsbase + pc * 1024);
        }
    }
}
DI void load_wimage_o4(const Ctx& c, int which, int part) {
    const u32 so = WS_WIMG + (u32)(which * 8 + c.j) * WIMG_BYTES + (u32)part * 30 * 1024; const u32 dst = c.Lb + L_R1 + part * 30 * 1024;
    if (c.wave < 4) {
#pragma unroll
        for (int n = 0; n < 8; ++n)
            if (n < 7 || c.wave < 2) glds_ws(c.srd, c.lane * 16, so + (c.wave + 4 * n) * 1024, dst + (c.wave + 4 * n) * 1024);
    }
}
struct Fmt { u32x4 v[2]; u32 g[8]; u32 dst[2], cdst[2]; int kind[2]; };
DI void fmt_issue(const Ctx& c, const Params& p, Fmt& m) {
    const u16 *pw = p.wqkv, *pa = p.woA, *pb = p.woB;
    asm volatile("" : "+s"(pw), "+s"(pa), "+s"(pb));
    const int lane = c.lane, rho = lane >> 1, g = rho >> 3, e = rho & 7;
#pragma unroll
    for (int r = 0; r < 2; ++r) {
        const int fr = blockIdx.x * 8 + c.wave + 2048 * r;
        int col, A, P, Q, pitch; const GAS char* src;
        if (fr < 2016) {
            const int jj = fr / 252, rem = fr - jj * 252, u = rem / 18, s = rem - u * 18;
            const int wv = u < 8 ? u : (u - 8 < 5 ? u - 8 : 6), slot = u < 8 ? 0 : 1, a = wv >> 1;
            const bool isrem = (wv & 1) && slot == 1;
            const u32 fo = (u32)(((jj * 8 + wv) * 2 + slot) * 18 + s);
            m.dst[r] = WS_WF + fo * 1024; m.cdst[r] = WS_CSP + fo * 64; m.kind[r] = isrem ? 3 : 1;
            P = s < 16 ? 16 : 4; Q = 2; pitch = 1728 * 2; src = (const GAS char*)pw;
            if (!isrem) {
                col = ((wv & 1) ? 1152 : (slot ? 576 : 0)) + 18 * (4 * jj + a) + 8 * (lane & 1);
                A = s < 16 ? 72 * (s >> 1) + 32 * (s & 1) + 4 * g : 72 * (4 * (s - 16) + g) + 64;
            } else {
                const int n = lane & 15, g2 = lane >> 4;
                col = (a == 0 ? (n < 8 ? 0 : 576) : 1152) + 18 * (4 * jj + ((n & 7) >> 1)) + 16 + (n & 1);
                A = s < 16 ? 72 * (s >> 1) + 32 * (s & 1) + 4 * g2 : 72 * (4 * (s - 16) + g2) + 64;
#pragma unroll
                for (int q = 0; q < 8; ++q) m.g[q] = *(const GAS u16*)(src + (A + P * (q >> 2) + (q & 3)) * pitch + col * 2);
            }
        } else {
            const bool on = fr < 3456;
            const int q = on ? fr - 2016 : 0, im = q / 720, rem = q - im * 720, jj = rem / 90, st = rem - jj * 90, s = st / 5, t = st - s * 5;
            m.dst[r] = WS_WIMG + (u32)(im * 8 + jj) * WIMG_BYTES + (u32)st * 1024; m.cdst[r] = 0; m.kind[r] = on ? 2 : 0;
            col = 72 * jj + 16 * t + (t == 4 ? 0 : 8 * (lane & 1));
            if (s < 16) { A = 36 * s + 18 * (g >> 1) + 4 * (g & 1); P = 8; Q = 2; } else { A = 72 * (4 * (s - 16) + g) + 16; P = 36; Q = 18; }
            src = (const GAS char*)(im ? pb : pa); pitch = 576 * 2;
        }
        if (r == 1 || m.kind[r] != 3) m.v[r] = *(const GAS u32x4*)(src + (A + P * (e >> 2) + Q * ((e >> 1) & 1) + (e & 1)) * pitch + col * 2);
    }
}
DI void fmt_finish(const Ctx& c, Fmt& m) {
    asm volatile("" : "+v"(m.v[0]), "+v"(m.v[1]), "+v"(m.g[0]), "+v"(m.g[1]), "+v"(m.g[2]), "+v"(m.g[3]), "+v"(m.g[4]), "+v"(m.g[5]), "+v"(m.g[6]), "+v"(m.g[7]));
    const int lane = c.lane, i16 = lane & 15, g = lane >> 4, q = i16 >> 2, p = i16 & 3;
#pragma unroll
    for (int r = 0; r < 2; ++r) {
        if (m.kind[r] != 0) {
            u32x4 f;
            if (m.kind[r] == 3) { const u32* w = m.g; f = u32x4{w[0] | (w[1] << 16), w[2] | (w[3] << 16), w[4] | (w[5] << 16), w[6] | (w[7] << 16)}; }
            else {
                LAS char* t = c.L + L_FMT + c.wave * 2048 + r * 1024;
                lst(t + lane * 16, m.v[r]);
                asm volatile("s_waitcnt lgkmcnt(0)" ::: "memory");
                const u32x2 lo = trr(t + (8 * g + q) * 32 + 8 * p), hi = trr(t + (8 * g + 4 + q) * 32 + 8 * p);
                f = u32x4{lo[0], lo[1], hi[0], hi[1]};
            }
            st128(c.rs, f, lane * 16, m.dst[r]);
            if (m.kind[r] != 2) {
                float cs = ((bflo(f[0]) + bfhi(f[0])) + (bflo(f[1]) + bfhi(f[1]))) + ((bflo(f[2]) + bfhi(f[2])) + (bflo(f[3]) + bfhi(f[3])));
                cs = sum_g(cs);
                if (lane < 16) st32(c.rs, asu(cs), lane * 4, m.cdst[r]);
            }
        }
    }
}

template <bool PROJ, bool NAT = false, class HK, class PRE>
DI void g1_mma(const Ctx& c, const u32x4 (&xf)[9], f32x4 (&acc)[5], HK hook, PRE pre) {
    const int lane = c.lane, kh = c.wave >> 2, q = (lane & 15) >> 2, p = lane & 3, g = lane >> 4;
    LAS char *bN, *bR, *bNo, *bNt, *bRo, *bRt;
    if (PROJ) {
        if (NAT) { bN = c.L + L_R1 + (kh * 256 + 4 * g + q) * WP + 8 * p; bR = c.L + L_R1 + (512 + 32 * kh + 8 * g + q) * WP + 8 * p; }
        else { bN = c.L + L_A1 + (kh * 288 + 4 * g + q) * WP + 8 * p; bR = c.L + L_A1 + (kh * 288 + 72 * g + q) * WP + 8 * p; }
        bNo = bN + 64 * kh; bNt = bN + 64 - 64 * kh; bRo = bR + 64 * kh; bRt = bR + 64 - 64 * kh;
    } else {
        bN = c.L + L_R1 + kh * 8 * 5120 + lane * 16; bR = c.L + L_R1 + (16 + kh) * 5120 + lane * 16;
        bNo = bN + 2048 * kh; bNt = bN + 2048 - 2048 * kh; bRo = bR + 2048 * kh; bRt = bR + 2048 - 2048 * kh;
    }
    u32x4 w[2][5];
    auto rd = [&](int sl, u32x4 (&d)[5]) {
#pragma unroll
        for (int u = 0; u < 5; ++u) {
            if (PROJ) {
                const int r0 = NAT ? (sl < 8 ? 32 * sl : 0) : (sl < 8 ? 72 * (sl >> 1) + 32 * (sl & 1) : 64), hs = sl < 8 ? 16 : 4;
                LAS char* bb = u < 2 ? (sl < 8 ? bNo : bRo) + 32 * u : (u == 2 ? (sl < 8 ? bN : bR) + 128 : (sl < 8 ? bNt : bRt) + 32 * (u - 3));
                const u32x2 lo = trr(bb + r0 * WP), hi = trr(bb + (r0 + hs) * WP);
                d[u] = u32x4{lo[0], lo[1], hi[0], hi[1]};
            } else {
                LAS char* bb = u < 2 ? (sl < 8 ? bNo : bRo) + 1024 * u : (u == 2 ? (sl < 8 ? bN : bR) + 4096 : (sl < 8 ? bNt : bRt) + 1024 * (u - 3));
                d[u] = lld<u32x4>(bb + (sl < 8 ? sl * 5120 : 0));
            }
        }
    };
    rd(0, w[0]);
#pragma unroll
    for (int u = 0; u < 5; ++u) asm volatile("" : "+v"(acc[u]));
#pragma unroll
    for (int sl = 0; sl < 9; ++sl) {
        pre(sl);
        if (sl + 1 < 9) rd(sl + 1, w[(sl + 1) & 1]);
#pragma unroll
        for (int u = 0; u < 5; ++u) MF16(acc[u], w[sl & 1][u], xf[sl]);
        hook(sl, w[sl & 1]);
    }
    asm volatile("s_nop 7" : "+v"(acc[0]), "+v"(acc[1]), "+v"(acc[2]), "+v"(acc[3]), "+v"(acc[4]));
}
DI void g1_exchange(const Ctx& c, f32x4 (&acc)[5]) {
    LAS char* xw = c.L + L_XCH + c.wave * 3072 + c.lane * 16;
    lst(xw, acc[3]); lst(xw + 1024, acc[4]); lst(xw + 2048, acc[2]);
    BAR();
    LAS char* xr = c.L + L_XCH + (c.wave ^ 4) * 3072 + c.lane * 16;
    acc[0] += lld<f32x4>(xr); acc[1] += lld<f32x4>(xr + 1024); acc[2] += lld<f32x4>(xr + 2048);
}
DI void load_xfrags(const Ctx& c, u32 buf, u32x4 (&xf)[9]) {
    const int m = c.wave & 3, kh = c.wave >> 2;
    const u32 so = buf + (u32)(c.i * 18 * 4 + m) * 1024;
#pragma unroll
    for (int sl = 0; sl < 9; ++sl) { const int s = sl < 8 ? 8 * kh + sl : 16 + kh; xf[sl] = ld128(c.rs, c.lane * 16, so + (u32)s * 4096); }
}
DI u32x4 stats_issue(const Ctx& c, u32 stbuf) {
    const u32 vo = (u32)((c.i * 16 + 2 * (c.lane >> 3)) * 64 + c.wave * 8 + (c.lane & 7)) * 8;
    const u32x2 a = ld64(c.rs, vo, stbuf), b = ld64(c.rs, vo + 512, stbuf);
    return u32x4{a[0], a[1], b[0], b[1]};
}
DI void stats_finish(const Ctx& c, u32x4 v) {
    const int tok = c.wave * 8 + (c.lane & 7);
    float s = asf(v[0]) + asf(v[2]), s2 = asf(v[1]) + asf(v[3]);
    s += __shfl_xor(s, 8); s2 += __shfl_xor(s2, 8);
    s = sum_g(s); s2 = sum_g(s2);
    if (c.lane < 8) {
        lst(c.L + L_STATP + tok * 8, f32x2{s, s2}); lst(c.L + L_STATP + 512 + tok * 8, f32x2{0.f, 0.f});
        const float mean = s * (1.f / DM), var = s2 * (1.f / DM) - mean * mean;
        lst(c.L + L_MEAN + tok * 4, mean); lst(c.L + L_MEAN + 256 + tok * 4, __builtin_amdgcn_rsqf(fmaxf(var, 0.f) + 1e-5f));
    }
}
DI void stats_issue32(const Ctx& c, u32 stbuf, u32x4 (&v)[4]) {
    const u32 vo = (u32)((c.i * 16 + (c.lane >> 4)) * 64 + (c.wave == 7 ? 32 : 0) + 2 * (c.lane & 15)) * 8;
#pragma unroll
    for (int n = 0; n < 4; ++n) v[n] = ld128(c.rs, vo + n * 4 * 512, stbuf);
}
DI void stats_finish32(const Ctx& c, const u32x4 (&v)[4]) {
    float s[4];
#pragma unroll
    for (int e = 0; e < 4; ++e) s[e] = sum_g((asf(v[0][e]) + asf(v[1][e])) + (asf(v[2][e]) + asf(v[3][e])));
    if (c.lane < 16) {
        const int tok = (c.wave == 7 ? 32 : 0) + 2 * c.lane;
        const float m0 = s[0] * (1.f / DM), m1 = s[2] * (1.f / DM);
        lst(c.L + L_MEAN + tok * 4, f32x2{m0, m1});
        lst(c.L + L_MEAN + 256 + tok * 4, f32x2{__builtin_amdgcn_rsqf(fmaxf(s[1] * (1.f / DM) - m0 * m0, 0.f) + 1e-5f), __builtin_amdgcn_rsqf(fmaxf(s[3] * (1.f / DM) - m1 * m1, 0.f) + 1e-5f)});
    }
}

struct KV { u32x4 km; u32x2 kr; u32x4 v0, v1; };
DI int vrow_of(int r) { return r < 18 ? r : 18; }
DI u32 ka_vrow(int blk, int row) { return row < 8 ? KA_V0 + blk * 128 + row * 16 : (row < 16 ? KA_V8 + blk * 128 + (row - 8) * 16 : KA_VX + blk * 64 + (row - 16) * 16); }
DI KV load_tile_ws(const Ctx& c, u32 tile, bool shared) {
    const int r = c.lane & 31, h = c.lane >> 5;
    KV t;
    if (shared) {
        t.km = ld128(c.rs, c.lane * 16, tile);
        t.kr = ld64(c.rs, KA_KREM + r * 8, tile);
        t.v0 = ld128(c.rs, ka_vrow(h, vrow_of(r)), tile);
        t.v1 = ld128(c.rs, ka_vrow(2 + h, vrow_of(r)), tile);
        return t;
    }
    t.km = ld128(c.rs, c.lane * 16, tile);
    t.kr = ld64(c.rs, KT_KREM + r * 8, tile);
    t.v0 = ld128(c.rs, KT_V + h * KT_VBLK + vrow_of(r) * 16, tile);
    t.v1 = ld128(c.rs, KT_V + (2 + h) * KT_VBLK + vrow_of(r) * 16, tile);
    return t;
}
DI f32x16 mfma32k8(u32x2 a, u32x2 b, f32x16 c) { return __builtin_amdgcn_mfma_f32_32x32x8bf16_1k(__builtin_bit_cast(s16x4, a), __builtin_bit_cast(s16x4, b), c, 0, 0, 0); }
DI f32x16 qk(const KV& t, u32x4 qm, u32x2 qr, f32x16 c0) {
    return mfma32k8(t.kr, qr, mfma32(t.km, qm, c0));
}
DI void QK1(f32x16& s, u32x4 km, u32x4 qm) { asm volatile("v_mfma_f32_32x32x16_bf16 %0, %1, %2, 0" : "=&v"(s) : "v"(km), "v"(qm)); }
DI void QK1C(f32x16& s, u32x4 km, u32x4 qm, const f32x16& c0) { asm volatile("v_mfma_f32_32x32x16_bf16 %0, %1, %2, %3" : "=&v"(s) : "v"(km), "v"(qm), "v"(c0)); }
DI void QK2(f32x16& s, u32x2 kr, u32x2 qr) { asm volatile("v_mfma_f32_32x32x8_bf16 %0, %1, %2, %0" : "+v"(s) : "v"(kr), "v"(qr)); }
DI void PV(f32x16& o, u32x4 v, u32x4 p) { asm volatile("v_mfma_f32_32x32x16_bf16 %0, %1, %2, %0" : "+v"(o) : "v"(v), "v"(p)); }
DI float EX(float x) { float r; asm volatile("v_exp_f32 %0, %1" : "=v"(r) : "v"(x)); return r; }
DI u32 CV(float a, float b) { u32 r; asm volatile("v_cvt_pk_bf16_f32 %0, %1, %2" : "=v"(r) : "v"(a), "v"(b)); return r; }
struct Pipe { float e[4]; u32x4 p1; };
template <bool NEXT = true, class F0, class F1, class F2, class F3, class F4>
DI void attn_step(const f32x16& sc, f32x16& sn, f32x16& o, Pipe& pp, const u32x4& km, const u32x2& kr, const u32x4& v0, const u32x4& v1, u32x4 qm, u32x2 qr, F0 first, F1 LK, F2 LR, F3 LV1, F4 LV0) {
    u32x4 p0; float a0, a1, a2, a3, b0, b1, b2, b3;
    if (NEXT) first(sn, km, qm);
    LK();
    pp.p1[2] = CV(pp.e[0], pp.e[1]); pp.p1[3] = CV(pp.e[2], pp.e[3]);
    a0 = EX(sc[0]); a1 = EX(sc[1]); a2 = EX(sc[2]); a3 = EX(sc[3]);
    if (NEXT) QK2(sn, kr, qr); else asm volatile("s_nop 0");
    LR();
    p0[0] = CV(a0, a1); p0[1] = CV(a2, a3);
    b0 = EX(sc[4]); b1 = EX(sc[5]); b2 = EX(sc[6]); b3 = EX(sc[7]);
    PV(o, v1, pp.p1); LV1();
    p0[2] = CV(b0, b1); p0[3] = CV(b2, b3);
    a0 = EX(sc[8]); a1 = EX(sc[9]); a2 = EX(sc[10]); a3 = EX(sc[11]);
    PV(o, v0, p0); LV0();
    pp.p1[0] = CV(a0, a1); pp.p1[1] = CV(a2, a3);
    pp.e[0] = EX(sc[12]); pp.e[1] = EX(sc[13]); pp.e[2] = EX(sc[14]); pp.e[3] = EX(sc[15]);
}
template <class F1, class F2, class F3, class F4>
DI void attn_step2(const f32x16& scx, f32x16& snx, f32x16& ox, Pipe& px, const f32x16& scy, f32x16& sny, f32x16& oy, Pipe& py,
                   const u32x4& km, const u32x2& kr, const u32x4& v0, const u32x4& v1, u32x4 qmx, u32x2 qrx, u32x4 qmy, u32x2 qry, F1 LK, F2 LR, F3 LV1, F4 LV0) {
    u32x4 p0x, p0y; float a0, a1, a2, a3, b0, b1, b2, b3, c0, c1, c2, c3, d0, d1, d2, d3;
#define GRP(MF, PD0, PD1, E0, E1, E2, E3, D0, D1, D2, D3, SRC, I) MF; PD0 = CV(E0, E1); PD1 = CV(E2, E3); D0 = EX(SRC[I]); D1 = EX(SRC[I + 1]); D2 = EX(SRC[I + 2]); D3 = EX(SRC[I + 3])
    GRP(QK1(snx, km, qmx), px.p1[2], px.p1[3], px.e[0], px.e[1], px.e[2], px.e[3], a0, a1, a2, a3, scx, 0);
    GRP(QK1(sny, km, qmy); LK(), py.p1[2], py.p1[3], py.e[0], py.e[1], py.e[2], py.e[3], b0, b1, b2, b3, scy, 0);
    GRP(QK2(snx, kr, qrx), p0x[0], p0x[1], a0, a1, a2, a3, c0, c1, c2, c3, scx, 4);
    GRP(QK2(sny, kr, qry); LR(), p0y[0], p0y[1], b0, b1, b2, b3, d0, d1, d2, d3, scy, 4);
    GRP(PV(ox, v1, px.p1), p0x[2], p0x[3], c0, c1, c2, c3, a0, a1, a2, a3, scx, 8);
    GRP(PV(oy, v1, py.p1); LV1(), p0y[2], p0y[3], d0, d1, d2, d3, b0, b1, b2, b3, scy, 8);
    GRP(PV(ox, v0, p0x), px.p1[0], px.p1[1], a0, a1, a2, a3, px.e[0], px.e[1], px.e[2], px.e[3], scx, 12);
    GRP(PV(oy, v0, p0y); LV0(), py.p1[0], py.p1[1], b0, b1, b2, b3, py.e[0], py.e[1], py.e[2], py.e[3], scy, 12);
#undef GRP
}
DI void attn_finish(f32x16& o, Pipe& pp, u32x4 v1) {
    asm volatile("s_nop 0");
    pp.p1[2] = CV(pp.e[0], pp.e[1]); pp.p1[3] = CV(pp.e[2], pp.e[3]);
    asm volatile("s_nop 1");
    PV(o, v1, pp.p1);
    asm volatile("s_nop 7\n\ts_nop 3" : "+v"(o));
}
DI void attn_finish2(f32x16& ox, Pipe& px, f32x16& oy, Pipe& py, u32x4 v1) {
    asm volatile("s_nop 0");
    px.p1[2] = CV(px.e[0], px.e[1]); px.p1[3] = CV(px.e[2], px.e[3]);
    py.p1[2] = CV(py.e[0], py.e[1]); py.p1[3] = CV(py.e[2], py.e[3]);
    PV(ox, v1, px.p1);
    asm volatile("s_nop 0");
    PV(oy, v1, py.p1);
    asm volatile("s_nop 7\n\ts_nop 3" : "+v"(ox), "+v"(oy));
}
DI void pexp(const f32x16& s, u32x4& p0, u32x4& p1) {
    float e[16];
#pragma unroll
    for (int r = 0; r < 16; ++r) e[r] = __builtin_amdgcn_exp2f(s[r]);
    p0 = u32x4{pk(e[0], e[1]), pk(e[2], e[3]), pk(e[4], e[5]), pk(e[6], e[7])};
    p1 = u32x4{pk(e[8], e[9]), pk(e[10], e[11]), pk(e[12], e[13]), pk(e[14], e[15])};
}
DI float rowmax(const f32x16& s) {
    float a = fmaxf(fmaxf(s[0], s[1]), s[2]), b = fmaxf(fmaxf(s[3], s[4]), s[5]);
#pragma unroll
    for (int r = 6; r < 16; r += 2) { if (r & 2) a = fmaxf(fmaxf(a, s[r]), s[r + 1]); else b = fmaxf(fmaxf(b, s[r]), s[r + 1]); }
    return max_h(fmaxf(a, b));
}
DI f32x16 mask_c(int lane, bool diag) {
    const int r = lane & 31, h = lane >> 5; f32x16 m;
#pragma unroll
    for (int e = 0; e < 16; ++e) { const bool ok = diag ? (crow(e, h) <= r) : (crow(e, h) > r); m[e] = ok ? 0.f : NEG_BIG; }
    return m;
}
DI f32x16 attn_slow(const Ctx& c, u32 tile0, int n0, int n1, bool masked, u32x4 qm, u32x2 qr) {
    const int h = c.lane >> 5;
    qr[1] = 0u;
    f32x16 o = {}; float m = NEG_BIG;
    for (int n = n0; n <= n1; ++n) {
        const KV t = load_tile_ws(c, tile0 + (u32)n * TILE_B, !masked);
        f32x16 z = {};
        f32x16 s = qk(t, qm, qr, z);
        if (masked && (n == n1 || n == n1 - 8)) {
            const int r = c.lane & 31;
#pragma unroll
            for (int e = 0; e < 16; ++e) { const bool ok = (n == n1) ? (crow(e, h) <= r) : (crow(e, h) > r); s[e] = ok ? s[e] : NEG_BIG; }
        }
        const float mn = fmaxf(m, rowmax(s)), al = __builtin_amdgcn_exp2f(m - mn);
        m = mn;
#pragma unroll
        for (int e = 0; e < 16; ++e) { o[e] *= al; s[e] = s[e] <= NEG_BIG ? NEG_BIG : s[e] - mn; }
        u32x4 p0, p1; pexp(s, p0, p1);
        o = mfma32(t.v0, p0, o); o = mfma32(t.v1, p1, o);
    }
    return o;
}
DI bool attn_unsafe(const f32x16& o) {
    float z = 0.f;
#pragma unroll
    for (int e = 0; e < 10; ++e) z = __builtin_fmaf(o[e], 0.f, z);
    const float l = o[10];
    return __any(!(l > 0x1p-40f && l < 0x1p30f && z == 0.f));
}
DI void store_o(const Ctx& c, u32 obuf, const f32x16& o, int a, int t) {
    const int r = c.lane & 31, h = c.lane >> 5;
    const float inv = __builtin_amdgcn_rcpf(o[10]);
    const u32x4 mainp = {pk(o[0] * inv, o[1] * inv), pk(o[2] * inv, o[3] * inv), pk(o[4] * inv, o[5] * inv), pk(o[6] * inv, o[7] * inv)};
    const int m = 2 * t + (r >> 4), tok = r & 15;
    const u32 so = obuf + (u32)(c.i * 18 * 4) * 1024;
    st128(c.rs, mainp, (u32)((2 * c.j + (a >> 1)) * 4 + m) * 1024 + ((2 * (a & 1) + h) * 16 + tok) * 16, so);
    if (h == 0) st32(c.rs, pk(o[8] * inv, o[9] * inv), (u32)((16 + (c.j >> 2)) * 4 + m) * 1024 + ((c.j & 3) * 16 + tok) * 16 + 4 * a, so);
}

template <int rep>
DI void a_proj(Ctx& c0, const Params& p) {
    Ctx c = fresh(c0);
    const int lane = c.lane, m = c.wave & 3, kh = c.wave >> 2, tok = lane & 15, g = lane >> 4;
    u32x4 xf[9]; f32x4 acc[5] = {};
    f32x4 s1 = {}, s2 = {}, ca = {}, cb = {};
    Fmt fm; u64 e0 = 0; u32x4 stv;
    if (rep == 0) {
        e0 = __hip_atomic_load((GAS u64*)(p.ws + WS_EPOCH), __ATOMIC_RELAXED, __HIP_MEMORY_SCOPE_AGENT);
        const char* xt = (const char*)p.x + (size_t)(c.i * 64) * (DM * 2);
#pragma unroll
        for (int n = 0; n < 8; ++n) {
            const int row = c.wave * 8 + n;
            glds_in(xt + row * (DM * 2) + (lane ^ (row & 15)) * 16, c.Lb + (row < 60 ? L_XIN + row * 1024 : L_XIN2 + (row - 60) * 1024));
        }
        xf[8] = *(const u32x4*)(xt + (m * 16 + tok) * (DM * 2) + (512 + 32 * kh + 8 * g) * 2);
        load_wimage_a(c, p);
        fmt_issue(c, p, fm);
        STAMP(13);
        if (c.wave < 2) { if (fm.kind[0] == 3) asm volatile("s_waitcnt vmcnt(19)" ::: "memory"); else asm volatile("s_waitcnt vmcnt(12)" ::: "memory"); }
        else { if (fm.kind[0] == 3) asm volatile("s_waitcnt vmcnt(14)" ::: "memory"); else asm volatile("s_waitcnt vmcnt(7)" ::: "memory"); }
        BAR();
        STAMP(18);
        asm volatile("" : "+v"(xf[8]), "+v"(e0));
        { const int row = m * 16 + tok, t2 = tok ^ (g >> 1);
          LAS char* xb = c.L + (row < 60 ? L_XIN + row * 1024 : L_XIN2 + (row - 60) * 1024) + kh * 512 + 8 * (g & 1);
#pragma unroll
          for (int sl = 0; sl < 8; ++sl) {
              const int K = 4 * (sl & 3);
              const u32x2 lo = lld<u32x2>(xb + (sl >> 2) * 256 + ((K ^ t2) * 16)), hi = lld<u32x2>(xb + (sl >> 2) * 256 + (((K + 2) ^ t2) * 16));
              xf[sl] = u32x4{lo[0], lo[1], hi[0], hi[1]};
          } }
        asm volatile("s_nop 1" : "+v"(xf[0]), "+v"(xf[1]), "+v"(xf[2]), "+v"(xf[3]), "+v"(xf[4]), "+v"(xf[5]), "+v"(xf[6]), "+v"(xf[7]), "+v"(xf[8]));
        STAMP(2);
    } else {
        wait_group(c, 5);
        load_xfrags(c, WS_XF + 1 * XF_BYTES, xf);
        stv = stats_issue(c, WS_ST + 1 * ST_BYTES);
        asm volatile("s_waitcnt vmcnt(11)" ::: "memory"); BAR();
        STAMP(32);
    }
    if (rep != 0) { g1_mma<true>(c, xf, acc, [](int, const u32x4 (&)[5]) {}, [](int) {}); stats_finish(c, stv); }
    else {
        u32x4 ones = ones4();
        const int tlc = m < 2 ? 2 * kh + m : 2 * (1 - kh) + (m - 2), q = (lane & 15) >> 2, pp = lane & 3;
        LAS char* bN = c.L + L_R1 + (kh * 256 + 4 * g + q) * WP + 8 * pp + 32 * tlc;
        LAS char* bR = c.L + L_R1 + (512 + 32 * kh + 8 * g + q) * WP + 8 * pp + 32 * tlc;
        asm volatile("" : "+v"(s1), "+v"(s2), "+v"(ca), "+v"(cb), "+v"(ones));
        u32x4 wx[2];
        auto rdx = [&](int sl) {
            const int r0 = sl < 8 ? 32 * sl : 0, hs = sl < 8 ? 16 : 4; LAS char* b = sl < 8 ? bN : bR;
            const u32x2 lo = trr(b + r0 * WP), hi = trr(b + (r0 + hs) * WP);
            wx[sl & 1] = u32x4{lo[0], lo[1], hi[0], hi[1]}; };
        rdx(0);
        g1_mma<true, true>(c, xf, acc, [&](int sl, const u32x4 (&w)[5]) {
            if (sl + 1 < 9) rdx(sl + 1);
            MF16(s1, ones, xf[sl]); MF16(s2, xf[sl], xf[sl]); MF16(cb, w[2], ones); MF16(ca, wx[sl & 1], ones);
        }, [&](int sl) {
            if (sl == 3) {
                if (fm.kind[0] == 3) asm volatile("s_waitcnt vmcnt(9)" ::: "memory"); else asm volatile("s_waitcnt vmcnt(2)" ::: "memory");
                BAR();
            }
        });
        if (c.tid < 180) st128(c.rs, lld<u32x4>(c.L + L_R1 + c.i * 2880 + c.tid * 16), c.tid * 16, WS_WIMGA + (u32)c.j * WIMG_BYTES + (u32)c.i * 2880);
        asm volatile("s_nop 7" : "+v"(s1), "+v"(s2), "+v"(ca), "+v"(cb));
        const int rg = tok & 3;
        float d = rg == 0 ? s2[0] : (rg == 1 ? s2[1] : (rg == 2 ? s2[2] : s2[3]));
        d = sum_g(g == (tok >> 2) ? d : 0.f);
        if (g == 0) lst(c.L + L_STATP + kh * 512 + (m * 16 + tok) * 8, f32x2{s1[0], d});
        const int tl = m < 2 ? 2 * kh + m : 2 * (1 - kh) + (m - 2);
        if (tok == 0) { lst(c.L + L_CSA + (kh * 5 + tl) * 64 + g * 16, ca); if (m == 0) lst(c.L + L_CSA + (kh * 5 + 4) * 64 + g * 16, cb); }
    }
    STAMP(3 + rep * 30);
    g1_exchange(c, acc);
    const f32x2 sa = lld<f32x2>(c.L + L_STATP + (m * 16 + tok) * 8), sb = lld<f32x2>(c.L + L_STATP + 512 + (m * 16 + tok) * 8);
    const float mean = (sa[0] + sb[0]) * (1.f / DM), var = (sa[1] + sb[1]) * (1.f / DM) - mean * mean;
    const float rstd = __builtin_amdgcn_rsqf(fmaxf(var, 0.f) + 1e-5f);
    LAS char* stg = c.L + L_STG + (m * 16 + tok) * STG_P + g * 8;
    auto fin = [&](int u, int t, float sc) {
        const f32x4 cs = lld<f32x4>(c.L + L_CSA + t * 64 + g * 16) + lld<f32x4>(c.L + L_CSA + (5 + t) * 64 + g * 16);
        f32x4 v;
#pragma unroll
        for (int e = 0; e < 4; ++e) v[e] = (acc[u][e] - mean * cs[e]) * sc;
        return u32x2{pk(v[0], v[1]), pk(v[2], v[3])};
    };
    if (kh == 1) {
        const u32x2 d4 = fin(2, 4, g < 2 ? rstd * QSCALE : rstd);
        lst(stg + 128, d4);
        const u32 kva = WS_KVA + rep * KVA_BYTES + (u32)(2 * c.i + (m >> 1)) * TILE_B;
        const int j = c.j, key = (m & 1) * 16 + tok;
        if (j < 2) { if (g >= 2) st64(c.rs, d4, j * 512 + key * 16 + (g - 2) * 8, kva); }
        else if (j == 2) { if (g == 3) st64(c.rs, u32x2{d4[1], 0x00003f80u}, KA_KREM + key * 8, kva); }
        else if (j < 6) {
            asm volatile("s_waitcnt lgkmcnt(0)" ::: "memory");
            const int q = (lane & 15) >> 2, pp = lane & 3;
            LAS char* s = c.L + L_STG + (16 * m + q + 4 * (pp >> 1)) * STG_P + 144 + 8 * (pp & 1);
            const u32x2 lo = trr(s), hi = trr(s + 8 * STG_P);
            const int dl = lane & 7, blk = (m & 1) * 2 + ((lane >> 3) & 1);
            if (j < 5) { if (lane < 16) st128(c.rs, u32x4{lo[0], lo[1], hi[0], hi[1]}, (j == 3 ? KA_V0 : KA_V8) + blk * 128 + dl * 16, kva); }
            else {
                const u32x4 on = ones4();
                const u32x4 v = dl >= 6 ? u32x4{lo[0], lo[1], hi[0], hi[1]} : on;
                if (lane < 16 && (dl >= 6 || dl < 2)) st128(c.rs, v, KA_VX + blk * 64 + (dl >= 6 ? dl - 6 : 2 + dl) * 16, kva);
            }
        }
    }
    lst(stg + (2 * kh) * 32, fin(0, 2 * kh, rstd * QSCALE));
    lst(stg + (2 * kh + 1) * 32, fin(1, 2 * kh + 1, rstd * QSCALE));
    if (rep == 0) {
        fmt_finish(c, fm);
        const u64 e = e0 + 1; c0.elo = c.elo = __builtin_amdgcn_readfirstlane((u32)e); c0.ehi = c.ehi = __builtin_amdgcn_readfirstlane((u32)(e >> 32));
    }
    STAMP(4 + rep * 30);
    publish(c, rep * 6);
}

DI void load_wring(const Ctx& c, u32x4 (&w0)[WRING], u32x4 (&w1)[WRING]) {
    const u32 wso = WS_WF + (u32)((c.j * 8 + c.wave) * 2) * 18 * 1024;
#pragma unroll
    for (int s = 0; s < WRING; ++s) { w0[s] = ld128(c.rs, c.lane * 16, wso + s * 1024); w1[s] = ld128(c.rs, c.lane * 16, wso + (18 + s) * 1024); }
}
DI void colsums_b(const Ctx& c) {
    const int tok = c.lane & 15, g = c.lane >> 4;
    if (c.wave >= 1) {
        const int extra = c.wave == 1 ? 0 : c.wave;
        u32 v[4][5];
#pragma unroll
        for (int q = 0; q < 4; ++q) {
            const int unit = (q < 2 ? c.wave : extra) * 2 + (q & 1);
#pragma unroll
            for (int u = 0; u < 5; ++u) { const int s = g + 4 * u; v[q][u] = (q < 2 || c.wave == 1) ? ld32(c.rs, (s < 18 ? s : 0) * 64 + tok * 4, WS_CSP + (u32)((c.j * 16 + unit) * 18) * 64) : 0u; }
        }
#pragma unroll
        for (int q = 0; q < 4; ++q) {
            const int unit = (q < 2 ? c.wave : extra) * 2 + (q & 1);
            float cs = (asf(v[q][0]) + asf(v[q][1])) + (asf(v[q][2]) + asf(v[q][3])) + (g < 2 ? asf(v[q][4]) : 0.f);
            cs = sum_g(cs);
            if (g == 0 && (q < 2 || c.wave == 1)) lst(c.L + L_CSB + unit * 64 + tok * 4, cs);
        }
    }
}
struct ATail { f32x16 o; u32x4 qm; u32x2 qr; };
DI void a_attn(const Ctx& c0, const Params& p, int rep, ATail& st) {
    const Ctx c = fresh(c0);
    const int lane = c.lane, r = lane & 31, h = lane >> 5, a = c.wave & 3, kh = c.wave >> 2;
    const int ph = rep * 6;
    const u32 kva = WS_KVA + rep * KVA_BYTES;
    u32x4 qmx, qmy; u32x2 qrx, qry;
    { LAS char* s = c.L + L_STG + (32 * kh + r) * STG_P + 36 * a;
      qmx = u32x4{lld<u32>(s + 16 * h), lld<u32>(s + 16 * h + 4), lld<u32>(s + 16 * h + 8), lld<u32>(s + 16 * h + 12)};
      const u32 rx = lld<u32>(s + 32);
      s += (32 - 64 * kh) * STG_P;
      qmy = u32x4{lld<u32>(s + 16 * h), lld<u32>(s + 16 * h + 4), lld<u32>(s + 16 * h + 8), lld<u32>(s + 16 * h + 12)};
      const u32 ry = lld<u32>(s + 32);
      qrx = u32x2{h == 0 ? rx : 0u, 0u}; qry = u32x2{h == 0 ? ry : 0u, 0u}; }
    const int ch0 = c.i >> 2;
    auto chunk_ids = [&](int n) { const int ch = (ch0 + n) & 7; return ch * 32 + (lane & 31); };
    u32 pvo[5];
#pragma unroll
    for (int k = 0; k < 5; ++k) {
        const u32 S = (u32)((c.wave & 3) + 4 * k) * 64 + lane, tile = (S * 52429u) >> 23, ps = S - tile * 160;
        const u32 v = ps - 64, blk = (v * 3450u) >> 16, row = v - blk * 19;
        const u32 src = ps < 64 ? ps * 16 : (ps < 140 ? ka_vrow(blk, row) : (ps < 156 ? KA_KREM + (ps - 140) * 16 : ps * 16));
        pvo[k] = tile * TILE_B + src;
    }
    auto issue_piece = [&](int n, int k) {
        const int ch = (ch0 + n) & 7; const u32 so = kva + (u32)ch * CHUNK_B; const u32 dst = c.Lb + L_RING + (n % 3) * CHUNK_B;
        glds_ws(c.srd, pvo[k], so, dst + ((c.wave & 3) + 4 * k) * 1024);
    };
    auto issue_chunk8 = [&](int n) {
        if (c.wave < 4) { issue_piece(n, 0); issue_piece(n, 2); issue_piece(n, 4); } else { issue_piece(n, 1); issue_piece(n, 3); }
    };
    if (c.wave == 0) poll(c, ph, lane < 32 ? chunk_ids(0) : chunk_ids(1));
    BAR();
    STAMP(6 + rep * 30);
    issue_chunk8(0);
    if (c.wave < 4) { issue_piece(1, 0); issue_piece(1, 1); issue_piece(1, 2); issue_piece(1, 3); issue_piece(1, 4); asm volatile("s_waitcnt vmcnt(5)" ::: "memory"); }
    else DRAIN();
    BAR();
    STAMP(7 + rep * 30); if (rep == 0) CSTAMP(61);
    if (rep == 1) { CSTAMPW(0, 4); CSTAMPW(8, 0); }
    const int vr = vrow_of(r);
    LAS char* kb = c.L + L_RING + lane * 16;
    LAS char* rb = c.L + L_RING + KT_KREM + r * 8;
    LAS char* vb = c.L + L_RING + KT_V + h * KT_VBLK + vr * 16;
    f32x16 sAx, sBx, sAy, sBy, ox, oy;
    const u32x4 k0m = lld<u32x4>(kb); const u32x2 k0r = lld<u32x2>(rb);
    kb += kh * 4 * TILE_B; rb += kh * 4 * TILE_B; vb += kh * 4 * TILE_B;
    const u32x4 kom = lld<u32x4>(kb); const u32x2 kor = lld<u32x2>(rb);
    u32x4 km = lld<u32x4>(kb + TILE_B), v0 = lld<u32x4>(vb), v1 = lld<u32x4>(vb + 2 * KT_VBLK); u32x2 kr = lld<u32x2>(rb + TILE_B);
    Pipe px, py;
    { u32 z = 0u; asm volatile("" : "+v"(z));
      px.e[0] = px.e[1] = px.e[2] = px.e[3] = asf(z); px.p1 = u32x4{z, z, z, z}; py = px; }
    asm volatile("" : "+v"(px.p1), "+v"(px.e[0]), "+v"(px.e[1]), "+v"(px.e[2]), "+v"(px.e[3]), "+v"(qmx), "+v"(qrx));
    asm volatile("s_nop 1" : "+v"(py.p1), "+v"(py.e[0]), "+v"(py.e[1]), "+v"(py.e[2]), "+v"(py.e[3]), "+v"(qmy), "+v"(qry));
    QK1(ox, px.p1, px.p1); QK1(oy, px.p1, px.p1);
    QK1(sBx, k0m, qmx); QK1(sBy, k0m, qmy); QK2(sBx, k0r, qrx); QK2(sBy, k0r, qry);
    QK1(sAx, kom, qmx); QK1(sAy, kom, qmy);
    asm volatile("s_nop 7\n\ts_nop 1" : "+v"(sBx), "+v"(sBy));
    { const float mx = rowmax(sBx), my = rowmax(sBy);
      if (h == 0) { qrx[1] = pk(-mx, 0.f); qry[1] = pk(-my, 0.f); } }
    asm volatile("s_nop 1" : "+v"(qrx), "+v"(qry));
    QK2(sAx, kor, qrx); QK2(sAy, kor, qry);
    asm volatile("s_nop 7\n\ts_nop 3" : "+v"(sAx), "+v"(sAy));
    if (rep == 1) { CSTAMPW(1, 4); CSTAMPW(9, 0); }
    int slot = 0;
    const u64 want = ((u64)c.ehi << 32) | c.elo;
    const u32 fvo = (lane & 31) * 128;
    u64 fl = want;
    if (c.wave == 0) flag_load(fl, ~want, fvo, c.srd, WS_FLAG + (u32)(ph * 256 + ((ch0 + 2) & 7) * 32) * 128);
#pragma unroll 1
    for (int n = 0; n < 8; ++n) {
        const int nslot = slot == 2 ? 0 : slot + 1;
        LAS char* kc = kb + slot * CHUNK_B; LAS char* rc = rb + slot * CHUNK_B; LAS char* vc = vb + slot * CHUNK_B;
        LAS char* kn = kb + nslot * CHUNK_B; LAS char* rn = rb + nslot * CHUNK_B; LAS char* vn = vb + nslot * CHUNK_B;
#define A_TILE(U, SCX, SNX, SCY, SNY, X1, X2) attn_step2(SCX, SNX, ox, px, SCY, SNY, oy, py, km, kr, v0, v1, qmx, qrx, qmy, qry, \
            [&] { km = lld<u32x4>((U) < 2 ? kc + ((U) + 2) * TILE_B : kn + ((U) - 2) * TILE_B); X1; }, \
            [&] { kr = lld<u32x2>((U) < 2 ? rc + ((U) + 2) * TILE_B : rn + ((U) - 2) * TILE_B); }, \
            [&] { v1 = lld<u32x4>(vc + (U) * TILE_B + 2 * KT_VBLK); X2; }, \
            [&] { v0 = lld<u32x4>((U) < 3 ? vc + ((U) + 1) * TILE_B : vn); })
        A_TILE(0, sAx, sBx, sAy, sBy, (void)0, (void)0); A_TILE(1, sBx, sAx, sBy, sAy, (void)0, (void)0);
        if (__builtin_expect(c.wave < 4, 0)) {
            asm volatile("s_waitcnt vmcnt(0)" : "+v"(fl) :: "memory");
            if (__builtin_expect(!__all(fl == want), 0)) poll(c, ph, chunk_ids(n + 2));
        }
        BAR();
        if (__builtin_expect(c.wave < 4, 0)) {
            fl = want;
            if (c.wave == 0 && n + 3 < 8) flag_load(fl, ~want, fvo, c.srd, WS_FLAG + (u32)(ph * 256 + ((ch0 + n + 3) & 7) * 32) * 128);
            if (n + 2 < 8) { issue_piece(n + 2, 0); issue_piece(n + 2, 1); issue_piece(n + 2, 2); issue_piece(n + 2, 3); issue_piece(n + 2, 4); }
            if (n >= 5) load_wimage_o4(c, 0, n - 5);
            if (rep == 0 && n == 0) {
                u32 z0 = 0u; asm volatile("" : "+v"(z0));
                const u32x4 z = {z0, z0, z0, z0};
#pragma unroll
                for (int q = 0; q < 5; ++q)
                    st128(c.rs, z, lane * 16 + q * 1024, WS_KVB + (u32)((4 * c.j + c.wave) * 64 + 2 * c.i) * TILE_B);
                if (c.wave < 2) st128(c.rs, z, (u32)((16 + (c.j >> 2)) * 4 + (lane >> 4)) * 1024 + ((c.j & 3) * 16 + (lane & 15)) * 16, WS_OF + c.wave * XF_BYTES + (u32)(c.i * 72) * 1024);
            }
        }
        A_TILE(2, sAx, sBx, sAy, sBy, (void)0, (void)0); A_TILE(3, sBx, sAx, sBy, sAy, (void)0, (void)0);
#undef A_TILE
        slot = nslot;
    }
    if (rep == 1) { CSTAMPW(2, 4); CSTAMPW(10, 0); }
    attn_finish2(ox, px, oy, py, v1);
    { LAS char* mine = c.L + L_RING + kh * 2 * CHUNK_B + a * 3072 + lane * 16;
#pragma unroll
      for (int e = 0; e < 3; ++e) lst(mine + e * 1024, f32x4{oy[4 * e], oy[4 * e + 1], oy[4 * e + 2], oy[4 * e + 3]}); }
    st.o = ox; st.qm = qmx; st.qr = qrx;
}

template <bool ISB, class HK>
DI void out_proj(const Ctx& c0, const Params& p, int rep, u32 (&xres)[3][2], HK behind_x) {
    const Ctx c = fresh(c0);
    const int lane = c.lane, m = c.wave & 3, kh = c.wave >> 2, tok = lane & 15, g = lane >> 4;
    const int layer = rep * 2 + (ISB ? 1 : 0);
    wait_group(c, rep * 6 + (ISB ? 4 : 1));
    STAMP((ISB ? 25 : 10) + rep * 30);
    u32x4 xf[9]; f32x4 acc[5] = {};
    load_xfrags(c, WS_OF + (rep * RP * 2 + (ISB ? 1 : 0)) * XF_BYTES, xf);
    __builtin_amdgcn_sched_barrier(0);
    behind_x(c);
    if (ISB) {
        asm volatile("s_waitcnt vmcnt(9)" ::: "memory"); BAR();
    }
    g1_mma<false>(c, xf, acc, [](int, const u32x4 (&)[5]) {}, [](int) {});
    STAMP((ISB ? 26 : 11) + rep * 30);
    g1_exchange(c, acc);
    STAMP((ISB ? 27 : 12) + rep * 30);
#pragma unroll
    for (int u = 0; u < 3; ++u) {
        const float v0 = bflo(xres[u][0]) + acc[u][0], v1 = bfhi(xres[u][0]) + acc[u][1], v2 = bflo(xres[u][1]) + acc[u][2], v3 = bfhi(xres[u][1]) + acc[u][3];
        xres[u][0] = pk(v0, v1); xres[u][1] = pk(v2, v3);
    }
    if (ISB && rep == 1) {
        char* o = (char*)p.out + (size_t)(c.i * 64 + m * 16 + tok) * (DM * 2) + (c.j * 72 + 4 * g) * 2;
        *(u32x2*)(o + (2 * kh) * 32) = u32x2{xres[0][0], xres[0][1]};
        *(u32x2*)(o + (2 * kh + 1) * 32) = u32x2{xres[1][0], xres[1][1]};
        if (kh == 1 && g < 2) *(u32x2*)(o + 128) = u32x2{xres[2][0], xres[2][1]};
        return;
    }
    const int xi = layer == 0 ? 0 : (layer == 1 ? 1 : 2 * RP);
    const u32 so = WS_XF + xi * XF_BYTES + (u32)(c.i * 18 * 4 + m) * 1024;
    st128(c.rs, u32x4{xres[0][0], xres[0][1], xres[1][0], xres[1][1]}, lane * 16, so + (u32)(2 * c.j + kh) * 4096);
    if (kh == 1 && g < 2) st64(c.rs, u32x2{xres[2][0], xres[2][1]}, ((c.j & 3) * 16 + tok) * 16 + 8 * g, so + (u32)(16 + (c.j >> 2)) * 4096);
    __builtin_amdgcn_sched_barrier(0);
    float s1 = 0.f, s2 = 0.f;
#pragma unroll
    for (int u = 0; u < 3; ++u) {
        u32 d0 = xres[u][0], d1 = xres[u][1];
        asm volatile("" : "+v"(d0), "+v"(d1));
        if (u == 2 && !(kh == 1 && g < 2)) { d0 = 0u; d1 = 0u; }
        const float a0 = bflo(d0), a1 = bfhi(d0), a2 = bflo(d1), a3 = bfhi(d1);
        s1 += (a0 + a1) + (a2 + a3); s2 += (a0 * a0 + a1 * a1) + (a2 * a2 + a3 * a3);
    }
    s1 = sum_g(s1); s2 = sum_g(s2);
    if (g == 0) st64(c.rs, u32x2{asu(s1), asu(s2)}, (u32)((c.i * 16 + 2 * c.j + kh) * 64 + m * 16 + tok) * 8, WS_ST + xi * ST_BYTES);
    publish(c, rep * 6 + (ISB ? 5 : 2));
    if (!ISB && rep == 0) colsums_b(c);
    if (ISB) load_wimage_part<36, 90>(c, WS_WIMGA, 1, L_A1);
}

DI void a_tail(const Ctx& c0, const Params& p, int rep, ATail& st, u32 (&xres)[3][2], u32x4 (&w0)[WRING], u32x4 (&w1)[WRING]) {
    const Ctx c = fresh(c0);
    const int lane = c.lane, a = c.wave & 3, kh = c.wave >> 2;
    BAR();
    LAS char* theirs = c.L + L_RING + (1 - kh) * 2 * CHUNK_B + a * 3072 + lane * 16;
#pragma unroll
    for (int e = 0; e < 3; ++e) { const f32x4 v = lld<f32x4>(theirs + e * 1024); st.o[4 * e] += v[0]; st.o[4 * e + 1] += v[1]; st.o[4 * e + 2] += v[2]; st.o[4 * e + 3] += v[3]; }
    STAMP(8 + rep * 30); if (rep == 0) CSTAMP(62);
    if (rep == 1) { CSTAMPW(3, 4); CSTAMPW(11, 0); }
    if (attn_unsafe(st.o)) st.o = attn_slow(c, WS_KVA + rep * KVA_BYTES, 0, 63, false, st.qm, st.qr);
    store_o(c, WS_OF + (rep * RP * 2) * XF_BYTES, st.o, a, kh);
    if (rep == 1) { CSTAMPW(4, 4); CSTAMPW(12, 0); }
    publish(c, rep * 6 + 1);
    load_wring(c, w0, w1);
    STAMP(9 + rep * 30);
    out_proj<false>(c, p, rep, xres, [&](const Ctx& cc) {
        if (rep == 0) {
            const int m = cc.wave & 3, k2 = cc.wave >> 2, tok = cc.lane & 15, g = cc.lane >> 4;
            const char* rr = (const char*)p.x + (size_t)(cc.i * 64 + m * 16 + tok) * (DM * 2) + (cc.j * 72 + 4 * g) * 2;
            const u32x2 r0 = *(const u32x2*)(rr + (2 * k2) * 32), r1 = *(const u32x2*)(rr + (2 * k2 + 1) * 32), r2 = *(const u32x2*)(rr + (g < 2 ? 128 : 0));
            xres[0][0] = r0[0]; xres[0][1] = r0[1]; xres[1][0] = r1[0]; xres[1][1] = r1[1]; xres[2][0] = r2[0]; xres[2][1] = r2[1];
        }
    });
}

struct T_ { static constexpr bool value = true; };
struct F_ { static constexpr bool value = false; };
DI void b_proj(const Ctx& c0, const Params& p, int rep, u32x4 (&w0)[WRING], u32x4 (&w1)[WRING], u32x4& qm, u32x2& qr) {
    const Ctx c = fresh(c0);
    const int lane = c.lane, tok = lane & 15, g = lane >> 4, a = c.wave >> 1, odd = c.wave & 1;
    wait_group(c, rep * 6 + 2);
    STAMP(15 + rep * 30);
    const int xi = rep == 0 ? 0 : 2 * RP;
    u32x4 stv[4];
    if (c.wave == 5 || c.wave == 7) stats_issue32(c, WS_ST + xi * ST_BYTES, stv);
    { const u32 so = WS_XF + xi * XF_BYTES + (u32)(c.i * 72) * 1024;
#pragma unroll
      for (int n = 0; n < 9; ++n) glds_ws(c.srd, lane * 16, so + (c.wave + 8 * n) * 1024, c.Lb + L_XB + (c.wave + 8 * n) * 1024); }
    asm volatile("s_waitcnt vmcnt(5)" ::: "memory"); BAR();
    STAMP(16 + rep * 30);
    f32x4 a0[4] = {}, a1[4] = {};
    const u32 wso = WS_WF + (u32)((c.j * 8 + c.wave) * 2) * 18 * 1024;
    LAS char* xb = c.L + L_XB + lane * 16;
    const bool t0 = !odd, t1 = !odd || a == 0, has1 = !odd || a < 2;
    auto body = [&](auto T0, auto T1, auto H1) {
        u32x4 x[2][4];
#pragma unroll
        for (int m = 0; m < 4; ++m) { x[0][m] = lld<u32x4>(xb + m * 1024); asm volatile("" : "+v"(a0[m]), "+v"(a1[m])); }
        if (!H1.value) stats_finish32(c, stv);
#pragma unroll
        for (int s = 0; s < 18; ++s) {
            if (s == 7) {
                if (H1.value) asm volatile("s_waitcnt vmcnt(14)" ::: "memory"); else asm volatile("s_waitcnt vmcnt(7)" ::: "memory");
                static_assert(18 - WRING >= 7, "steps 0..6 each refill the ring");
                BAR();
            }
            if (s + 1 < 18) {
#pragma unroll
                for (int m = 0; m < 4; ++m) x[(s + 1) & 1][m] = lld<u32x4>(xb + ((s + 1) * 4 + m) * 1024);
            }
#pragma unroll
            for (int m = 0; m < 4; ++m) {
                if (T0.value) MF16(a0[m], w0[s % WRING], x[s & 1][m]); else MF16(a0[m], x[s & 1][m], w0[s % WRING]);
                if (H1.value) { if (T1.value) MF16(a1[m], w1[s % WRING], x[s & 1][m]); else MF16(a1[m], x[s & 1][m], w1[s % WRING]); }
            }
            if (s + WRING < 18) { w0[s % WRING] = ld128(c.rs, lane * 16, wso + (WRING + s) * 1024); if (H1.value) w1[s % WRING] = ld128(c.rs, lane * 16, wso + (18 + WRING + s) * 1024); }
        }
        asm volatile("s_nop 7" : "+v"(a0[0]), "+v"(a0[1]), "+v"(a0[2]), "+v"(a0[3]), "+v"(a1[0]), "+v"(a1[1]), "+v"(a1[2]), "+v"(a1[3]));
    };
    if (!odd) body(T_{}, T_{}, T_{}); else if (a == 0) body(F_{}, T_{}, T_{}); else if (a == 1) body(F_{}, F_{}, T_{}); else body(F_{}, F_{}, F_{});
    (void)t0; (void)t1; (void)has1;
    STAMP(17 + rep * 30);
    const u32 kvb = WS_KVB + rep * RP * KVB_BYTES;
    const u32 hb = kvb + (u32)((4 * c.j + a) * 64 + 2 * c.i) * TILE_B;
    auto fin_t = [&](const f32x4 (&acc)[4], int slot, float sc, u32 (&d)[4][2]) {
        const f32x4 cs = lld<f32x4>(c.L + L_CSB + (c.wave * 2 + slot) * 64 + g * 16);
#pragma unroll
        for (int m = 0; m < 4; ++m) {
            const float mean = lld<float>(c.L + L_MEAN + (m * 16 + tok) * 4), rs = lld<float>(c.L + L_MEAN + 256 + (m * 16 + tok) * 4) * sc;
            d[m][0] = pk((acc[m][0] - mean * cs[0]) * rs, (acc[m][1] - mean * cs[1]) * rs);
            d[m][1] = pk((acc[m][2] - mean * cs[2]) * rs, (acc[m][3] - mean * cs[3]) * rs);
        }
    };
    auto fin_p = [&](const f32x4 (&acc)[4], int slot, u32 (&d)[4][2]) {
        const float cs = lld<float>(c.L + L_CSB + (c.wave * 2 + slot) * 64 + tok * 4);
#pragma unroll
        for (int m = 0; m < 4; ++m) {
            const f32x4 mean = lld<f32x4>(c.L + L_MEAN + (m * 16 + 4 * g) * 4), rs = lld<f32x4>(c.L + L_MEAN + 256 + (m * 16 + 4 * g) * 4);
            d[m][0] = pk((acc[m][0] - mean[0] * cs) * rs[0], (acc[m][1] - mean[1] * cs) * rs[1]);
            d[m][1] = pk((acc[m][2] - mean[2] * cs) * rs[2], (acc[m][3] - mean[3] * cs) * rs[3]);
        }
    };
    auto frag16 = [&](const u32 (&d)[4][2], int t) {
        auto x = __builtin_amdgcn_permlane16_swap(d[2 * t][0], d[2 * t + 1][0], false, false);
        auto y = __builtin_amdgcn_permlane16_swap(d[2 * t][1], d[2 * t + 1][1], false, false);
        return u32x4{(u32)x[0], (u32)y[0], (u32)x[1], (u32)y[1]};
    };
    auto frag32 = [&](const u32 (&d)[4][2], int t) {
        auto x = __builtin_amdgcn_permlane32_swap(d[2 * t][0], d[2 * t + 1][0], false, false);
        auto y = __builtin_amdgcn_permlane32_swap(d[2 * t][1], d[2 * t + 1][1], false, false);
        return u32x4{(u32)x[0], (u32)y[0], (u32)x[1], (u32)y[1]};
    };
    u32 d[4][2];
    LAS char* lh = c.L + L_KVB + a * 25600 + 8 * TILE_B; LAS char* l0 = c.L + L_KVB + 8 * TILE_B;
    if (!odd) {
        fin_t(a1, 1, 1.f, d);
        { const u32x4 f0 = frag16(d, 0), f1 = frag16(d, 1);
          st128(c.rs, f0, lane * 16, hb); st128(c.rs, f1, lane * 16, hb + TILE_B);
          lst(lh + lane * 16, f0); lst(lh + TILE_B + lane * 16, f1); }
        fin_t(a0, 0, QSCALE, d);
        qm = frag16(d, 0);
        lst(c.L + L_QT1 + a * 1024 + lane * 16, frag16(d, 1));
    } else {
        fin_p(a0, 0, d);
        { const u32x4 f0 = frag32(d, 0), f1 = frag32(d, 1);
          st128(c.rs, f0, KT_V + g * KT_VBLK + tok * 16, hb); st128(c.rs, f1, KT_V + g * KT_VBLK + tok * 16, hb + TILE_B);
          lst(lh + KT_V + g * KT_VBLK + tok * 16, f0); lst(lh + TILE_B + KT_V + g * KT_VBLK + tok * 16, f1); }
        if (a == 0) {
            fin_t(a1, 1, g < 2 ? QSCALE : 1.f, d);
#pragma unroll
            for (int m = 0; m < 4; ++m) {
                if (g < 2) { lst(c.L + L_QREM + (2 * g) * 256 + (m * 16 + tok) * 4, d[m][0]); lst(c.L + L_QREM + (2 * g + 1) * 256 + (m * 16 + tok) * 4, d[m][1]); }
                else {
                    const u32 hb0 = kvb + (u32)(4 * c.j * 64 + 2 * c.i + (m >> 1)) * TILE_B;
                    st64(c.rs, u32x2{d[m][0], 0x00003f80u}, (u32)(2 * (g - 2)) * 64 * TILE_B + KT_KREM + ((m & 1) * 16 + tok) * 8, hb0);
                    st64(c.rs, u32x2{d[m][1], 0x00003f80u}, (u32)(2 * (g - 2) + 1) * 64 * TILE_B + KT_KREM + ((m & 1) * 16 + tok) * 8, hb0);
                    LAS char* lk = l0 + (2 * (g - 2)) * 25600 + (m >> 1) * TILE_B + KT_KREM + ((m & 1) * 16 + tok) * 8;
                    lst(lk, u32x2{d[m][0], 0x00003f80u}); lst(lk + 25600, u32x2{d[m][1], 0x00003f80u});
                }
            }
        } else if (a == 1) {
            fin_p(a1, 1, d);
            const u32 hb0 = kvb + (u32)(4 * c.j * 64 + 2 * c.i) * TILE_B;
            const u32 vo = (u32)(tok >> 1) * 64 * TILE_B + KT_V + g * KT_VBLK + (16 + (tok & 1)) * 16;
            const u32x4 f0 = frag32(d, 0), f1 = frag32(d, 1);
            if (tok < 8) {
                st128(c.rs, f0, vo, hb0); st128(c.rs, f1, vo, hb0 + TILE_B);
                LAS char* lv = l0 + (tok >> 1) * 25600 + KT_V + g * KT_VBLK + (16 + (tok & 1)) * 16;
                lst(lv, f0); lst(lv + TILE_B, f1);
            }
        } else if (a == 2) {
            const u32 hb0 = kvb + (u32)(4 * c.j * 64 + 2 * c.i) * TILE_B;
            if (lane < 32) {
                const u32x4 on = ones4();
                st128(c.rs, on, (u32)(lane >> 3) * 64 * TILE_B + ((lane >> 2) & 1) * TILE_B + KT_V + (lane & 3) * KT_VBLK + 18 * 16, hb0);
                lst(l0 + (lane >> 3) * 25600 + ((lane >> 2) & 1) * TILE_B + KT_V + (lane & 3) * KT_VBLK + 18 * 16, on);
            }
        }
    }
    publish(c, rep * 6 + 3);
    load_wimage_part<44, 90>(c, WS_WIMG + 8 * WIMG_BYTES, 1);
    if (odd) qm = lld<u32x4>(c.L + L_QT1 + a * 1024 + lane * 16);
    const u32 rem = lld<u32>(c.L + L_QREM + a * 256 + (32 * odd + (lane & 31)) * 4);
    qr = u32x2{lane < 32 ? rem : 0u, 0u};
}

DI void b_attn(const Ctx& c0, const Params& p, int rep, u32x4 qm, u32x2 qr) {
    const Ctx c = fresh(c0);
    const int lane = c.lane, r = lane & 31, h = lane >> 5, a = c.wave >> 1, t = c.wave & 1;
    const int qt = 2 * c.i + t;
    const f32x16 cd = mask_c(lane, true);
    if (c.wave == 1) {
        const f32x16 cm = mask_c(lane, false);
#pragma unroll
        for (int e = 0; e < 4; ++e) lst(c.L + L_MSK + e * 1024 + lane * 16, f32x4{cm[4 * e], cm[4 * e + 1], cm[4 * e + 2], cm[4 * e + 3]});
    }
    LAS char* tb = c.L + L_KVB + a * 25600 + (8 + t) * TILE_B;
    LAS char* kb = tb + lane * 16; LAS char* rb = tb + KT_KREM + r * 8; LAS char* vb = tb + KT_V + h * KT_VBLK + vrow_of(r) * 16;
    const u32x4 k0m = lld<u32x4>(kb); const u32x2 k0r = lld<u32x2>(rb);
    u32x4 km, v0 = lld<u32x4>(vb), v1 = lld<u32x4>(vb + 2 * KT_VBLK); u32x2 kr;
    f32x16 sA, sB, o;
    Pipe pp;
    { u32 z = 0u; asm volatile("" : "+v"(z)); pp.e[0] = pp.e[1] = pp.e[2] = pp.e[3] = asf(z); pp.p1 = u32x4{z, z, z, z}; }
    f32x16 cdp = cd;
    asm volatile("s_nop 1" : "+v"(pp.p1), "+v"(pp.e[0]), "+v"(pp.e[1]), "+v"(pp.e[2]), "+v"(pp.e[3]), "+v"(qm), "+v"(qr), "+v"(cdp));
    QK1(o, pp.p1, pp.p1);
    QK1C(sA, k0m, qm, cdp);
    asm volatile("v_mfma_f32_32x32x8_bf16 %0, %1, %2, %3" : "=&v"(sB) : "v"(k0r), "v"(qr), "v"(sA));
    asm volatile("s_nop 7\n\ts_nop 3" : "+v"(sB), "+v"(sA));
    { const float m0 = rowmax(sB); if (h == 0) qr[1] = pk(-m0, 0.f); }
    asm volatile("s_nop 1" : "+v"(qr));
    QK2(sA, k0r, qr);
    asm volatile("s_nop 7\n\ts_nop 3" : "+v"(sA), "+v"(o), "+v"(v0), "+v"(v1));
    if (c.wave == 0) poll(c, rep * 6 + 3, (lane < 5 && c.i - lane >= 0) ? (c.i - lane) * 8 + c.j : -1);
    BAR();
    STAMP(20 + rep * 30);
    const u32 hb = WS_KVB + rep * RP * KVB_BYTES + (u32)((4 * c.j + a) * 64) * TILE_B;
    { const u32 dst = c.Lb + L_KVB + a * 25600;
#pragma unroll
      for (int n = 0; n < 10; ++n) {
          const int pc = 19 - t - 2 * n;
          u32 vo = lane * 16;
          if (c.i < 4) { const int b = pc * 1024 + lane * 16, tau = b / 2560, kt = 2 * c.i - 8 + tau; vo = (u32)((kt < 0 ? 0 : kt) * 2560 + (b - tau * 2560)); glds_ws(c.srd, vo, hb, dst + pc * 1024); }
          else glds_ws(c.srd, vo, hb + (u32)(2 * c.i - 8) * 2560 + pc * 1024, dst + pc * 1024);
      } }
    asm volatile("s_waitcnt vmcnt(5)" ::: "memory");
    BAR();
    STAMP(21 + rep * 30);
    km = lld<u32x4>(kb - TILE_B); kr = lld<u32x2>(rb - TILE_B);
    auto first = [](f32x16& s, u32x4 k, u32x4 q) { QK1(s, k, q); };
    auto firstm = [&](f32x16& s, u32x4 k, u32x4 q) {
        f32x16 co;
#pragma unroll
        for (int e = 0; e < 4; ++e) { const f32x4 v = lld<f32x4>(c.L + L_MSK + e * 1024 + lane * 16); co[4 * e] = v[0]; co[4 * e + 1] = v[1]; co[4 * e + 2] = v[2]; co[4 * e + 3] = v[3]; }
        QK1C(s, k, q, co); };
    const u32 qoff = h == 0 ? pk(NEG_BIG, 0.f) : 0u;
    kb -= 8 * TILE_B; rb -= 8 * TILE_B; vb -= 8 * TILE_B;
#define B_TILE(U, SC, SN, FIRST) do { u32x2 qs = {qr[0], (U) + 1 <= qt ? qr[1] : qoff}; asm volatile("" : "+v"(qs)); \
        attn_step<((U) < 8)>(SC, SN, o, pp, km, kr, v0, v1, qm, qs, FIRST, \
            [&] { if ((U) + 2 <= 8) km = lld<u32x4>(kb + (8 - ((U) + 2)) * TILE_B); }, [&] { if ((U) + 2 <= 8) kr = lld<u32x2>(rb + (8 - ((U) + 2)) * TILE_B); }, \
            [&] { v1 = lld<u32x4>(vb + (8 - (U)) * TILE_B + 2 * KT_VBLK); }, [&] { if ((U) + 1 <= 8) v0 = lld<u32x4>(vb + (8 - ((U) + 1)) * TILE_B); }); } while (0)
    B_TILE(0, sA, sB, first); B_TILE(1, sB, sA, first);
    DRAIN();
    B_TILE(2, sA, sB, first);
    BAR();
    B_TILE(3, sB, sA, first); B_TILE(4, sA, sB, first); B_TILE(5, sB, sA, first); B_TILE(6, sA, sB, first); B_TILE(7, sB, sA, firstm); B_TILE(8, sA, sB, first);
#undef B_TILE
    attn_finish(o, pp, v1);
    STAMP(22 + rep * 30);
    if (attn_unsafe(o)) o = attn_slow(c, hb, qt - 8 < 0 ? 0 : qt - 8, qt, true, qm, qr);
    store_o(c, WS_OF + (rep * RP * 2 + 1) * XF_BYTES, o, a, t);
    publish(c, rep * 6 + 4);
    load_wimage_part<0, 44>(c, WS_WIMG + 8 * WIMG_BYTES, 1);
    if (rep == 0) load_wimage_part<0, 36>(c, WS_WIMGA, 1, L_A1);
}

__global__ void __launch_bounds__(512, 2) fused_block_kernel(Params p) {
    __shared__ __attribute__((aligned(16))) char lds_[LDS_BYTES];
    Ctx c;
    c.L = (LAS char*)lds_; c.Lb = (u32)(size_t)c.L;
    c.ws = p.ws;
    c.tid = threadIdx.x; c.lane = c.tid & 63; c.wave = __builtin_amdgcn_readfirstlane(c.tid >> 6);
    { const int wg = blockIdx.x, slot = wg >> 3; c.i = (wg & 7) * 4 + (slot >> 3); c.j = slot & 7; }
    c.rs = __builtin_amdgcn_make_buffer_rsrc((void*)p.ws, 0, WS_USED, 0x00020000);
    { const u64 b = (u64)p.ws; c.srd = u32x4{(u32)b, (u32)(b >> 32) & 0xffffu, WS_USED, 0x00020000u}; }
    STAMP(0);
    u32 warm;
    { u64 pc; u32 len;
      asm volatile("s_getpc_b64 %0\nfbk_code_first:\n\ts_mov_b32 %1, fbk_code_last-fbk_code_first" : "=s"(pc), "=s"(len));
      u32 off = ((u32)(blockIdx.x >> 3) * 8 + c.wave) * 256 + c.lane * 4;
      off = off < len ? off : 0u;
      warm = *(const GAS u32*)((pc & ~3ull) + off); }
    c.elo = 0; c.ehi = 0;
    u32 xres[3][2];
    STAMP(1);
    a_proj<0>(c, p);
    asm volatile("" :: "v"(warm));
    STAMP(5);
#pragma unroll 1
    for (int rep = 0; rep < 2; ++rep) {
        u32x4 w0[WRING], w1[WRING]; ATail st;
        a_attn(c, p, rep, st);
        if (rep == 0) {
            if (blockIdx.x == 0 && c.tid == 0) __hip_atomic_store((GAS u64*)(p.ws + WS_EPOCH), ((u64)c.ehi << 32) | c.elo, __ATOMIC_RELAXED, __HIP_MEMORY_SCOPE_AGENT);
        }
        a_tail(c, p, rep, st, xres, w0, w1);
        STAMP(14 + rep * 30);
        u32x4 qm; u32x2 qr;
        b_proj(c, p, rep, w0, w1, qm, qr);
        STAMP(19 + rep * 30);
        b_attn(c, p, rep, qm, qr);
        STAMP(24 + rep * 30);
        out_proj<true>(c, p, rep, xres, [](const Ctx&) {});
        STAMP(29 + rep * 30);
        if (rep == 1) break;
        STAMP(31);
        a_proj<1>(c, p);
        STAMP(35);
    }
    asm volatile("fbk_code_last:");
}

static size_t ws_offset(size_t ws_size) { return (ws_size - WS_USED) & ~(size_t)0x1fffff; }

extern "C" void kernel_launch(void* const* d_in, const int* in_sizes, int n_in, void* d_out, int out_size, void* d_ws, size_t ws_size, hipStream_t stream) {
    Params p;
    p.x = (const u16*)d_in[0]; p.wq = (const u16*)d_in[1]; p.wk = (const u16*)d_in[2]; p.wv = (const u16*)d_in[3]; p.woA = (const u16*)d_in[4];
    p.wqkv = (const u16*)d_in[6]; p.woB = (const u16*)d_in[7]; p.out = (u16*)d_out;
    p.ws = (char*)d_ws + ws_offset(ws_size);
    fused_block_kernel<<<256, 512, 0, stream>>>(p);
}
```

```cpp
#include <hip/hip_runtime.h>
#include <hip/hip_bf16.h>

typedef unsigned short u16;
typedef unsigned u32;
typedef unsigned long long u64;
typedef __bf16 bf16x8 __attribute__((ext_vector_type(8)));
typedef __bf16 bf16x2 __attribute__((ext_vector_type(2)));
typedef short s16x4 __attribute__((ext_vector_type(4)));
typedef float f32x2 __attribute__((ext_vector_type(2)));
typedef float f32x4 __attribute__((ext_vector_type(4)));
typedef float f32x16 __attribute__((ext_vector_type(16)));
typedef u32 u32x2 __attribute__((ext_vector_type(2)));
typedef u32 u32x4 __attribute__((ext_vector_type(4)));
#define LAS __attribute__((address_space(3)))
#define GAS __attribute__((address_space(1)))
#define DI __device__ __forceinline__

constexpr int SEQ = 2048, DM = 576;
constexpr int RP = 0;
constexpr u32 TILE_B = 2560;
constexpr u32 KT_V = 1024, KT_VBLK = 304, KT_KREM = 2240;
constexpr u32 KA_KREM = 1024, KA_V0 = 1280, KA_V8 = 1792, KA_VX = 2304;
constexpr u32 XF_BYTES = 32u * 18 * 4 * 1024;
constexpr u32 WS_EPOCH = 0;
constexpr u32 WS_FLAG = 4096;
constexpr int PH_FMT = 11;
constexpr u32 WS_XF = WS_FLAG + 12u * 256 * 128 + 4096;
constexpr u32 WS_OF = WS_XF + (2 + RP) * XF_BYTES;
constexpr u32 ST_BYTES = 2048u * 16 * 8;
constexpr u32 WS_ST = WS_OF + (2 + 2 * RP) * XF_BYTES;
constexpr u32 KVA_BYTES = 64 * TILE_B;
constexpr u32 WS_KVA = WS_ST + (2 + RP) * ST_BYTES;
constexpr u32 KVB_BYTES = 32 * 64 * TILE_B;
constexpr u32 WS_KVB = WS_KVA + 2 * KVA_BYTES;
constexpr u32 WF_BYTES = 8u * 16 * 18 * 1024;
constexpr u32 WS_WF = WS_KVB + (1 + RP) * KVB_BYTES;
constexpr u32 WS_CSP = WS_WF + WF_BYTES;
constexpr u32 WIMG_BYTES = 90 * 1024;
constexpr u32 WS_WIMG = WS_CSP + 8u * 16 * 18 * 64;
constexpr u32 WS_WIMGA = WS_WIMG + 16 * WIMG_BYTES;
constexpr u32 WS_STAMP = WS_WIMGA + 8 * WIMG_BYTES;
constexpr u32 WS_STAMP2 = WS_STAMP + 256u * 64 * 8 + 256u * 8 * 8 * 8;
constexpr u32 WS_USED = WS_STAMP2 + 256u * 16 * 8;
constexpr int L_XCH = 0;
constexpr int L_RING = 0;
constexpr int CHUNK_B = 8 * TILE_B;
constexpr int L_STG = 40960;
constexpr int STG_P = 168;
constexpr int L_XIN = 0;
constexpr int L_XIN2 = 153600;
constexpr int L_FMT = 24576;
constexpr int L_KVB = 0;
constexpr int L_MSK = 102400;
constexpr int L_R1 = 61440;
constexpr int WP = 160;
constexpr int L_XB = 76800;
constexpr int L_A1 = 24576;
constexpr int L_STATP = 153600;
constexpr int L_MEAN = L_STATP + 1024;
constexpr int L_CSA = L_MEAN + 512;
constexpr int L_CSB = L_CSA + 1024;
constexpr int L_QREM = L_CSB + 1024;
constexpr int L_QT1 = L_QREM + 1024;
constexpr int LDS_BYTES = 163840;
static_assert(L_QT1 + 4096 <= LDS_BYTES, "lds");

#define WRING 4
constexpr float QSCALE = 0.23570226039551584f * 1.4426950408889634f;
constexpr float NEG_BIG = -1e30f;
constexpr u32 SPIN_CAP = 1u << 20;
constexpr int AUX_SC1 = 16;

struct Params { const u16* x; const u16* wq; const u16* wk; const u16* wv; const u16* woA; const u16* wqkv; const u16* woB; u16* out; char* ws; };

DI u32 pk(float a, float b) { f32x2 v = {a, b}; return __builtin_bit_cast(u32, __builtin_convertvector(v, bf16x2)); }
DI float asf(u32 d) { return __builtin_bit_cast(float, d); }
DI u32 asu(float f) { return __builtin_bit_cast(u32, f); }
DI float bflo(u32 d) { return __builtin_bit_cast(float, d << 16); }
DI float bfhi(u32 d) { return __builtin_bit_cast(float, d & 0xffff0000u); }
DI bf16x8 asb(u32x4 v) { return __builtin_bit_cast(bf16x8, v); }
DI f32x4 mfma16(u32x4 a, u32x4 b, f32x4 c) { return __builtin_amdgcn_mfma_f32_16x16x32_bf16(asb(a), asb(b), c, 0, 0, 0); }
DI f32x16 mfma32(u32x4 a, u32x4 b, f32x16 c) { return __builtin_amdgcn_mfma_f32_32x32x16_bf16(asb(a), asb(b), c, 0, 0, 0); }
DI void MF16(f32x4& acc, u32x4 a, u32x4 b) { asm volatile("v_mfma_f32_16x16x32_bf16 %0, %1, %2, %0" : "+v"(acc) : "v"(a), "v"(b)); }
#define SETTLE4(x) asm volatile("s_nop 7" : "+v"(x))
template <class T> DI T lld(LAS char* p) { return *(LAS const T*)p; }
template <class T> DI void lst(LAS char* p, T v) { *(LAS T*)p = v; }
DI u32x2 trr(LAS char* p) { return __builtin_bit_cast(u32x2, __builtin_amdgcn_ds_read_tr16_b64_v4i16((LAS s16x4*)p)); }
#define BAR() asm volatile("s_waitcnt lgkmcnt(0)\n\ts_barrier" ::: "memory")
#define DRAIN() asm volatile("s_waitcnt vmcnt(0)" ::: "memory")
typedef __amdgpu_buffer_rsrc_t rsrc_t;
DI u32x4 ld128(rsrc_t rs, u32 voff, u32 soff) { return __builtin_amdgcn_raw_buffer_load_b128(rs, voff, soff, AUX_SC1); }
DI u32x2 ld64(rsrc_t rs, u32 voff, u32 soff) { return __builtin_amdgcn_raw_buffer_load_b64(rs, voff, soff, AUX_SC1); }
DI u32 ld32(rsrc_t rs, u32 voff, u32 soff) { return __builtin_amdgcn_raw_buffer_load_b32(rs, voff, soff, AUX_SC1); }
DI void st128(rsrc_t rs, u32x4 v, u32 voff, u32 soff) { __builtin_amdgcn_raw_buffer_store_b128(v, rs, voff + soff, 0, AUX_SC1); }
DI void st64(rsrc_t rs, u32x2 v, u32 voff, u32 soff) { __builtin_amdgcn_raw_buffer_store_b64(v, rs, voff, soff, AUX_SC1); }
DI void st32(rsrc_t rs, u32 v, u32 voff, u32 soff) { __builtin_amdgcn_raw_buffer_store_b32(v, rs, voff, soff, AUX_SC1); }
DI void glds_ws(u32x4 srd, u32 voff, u32 soff, u32 ldsdst) {
    soff = __builtin_amdgcn_readfirstlane(soff); ldsdst = __builtin_amdgcn_readfirstlane(ldsdst);
    asm volatile("s_mov_b32 m0, %0\n\ts_nop 3\n\tbuffer_load_dwordx4 %1, %2, %3 offen sc1 lds" :: "s"(ldsdst), "v"(voff), "s"(srd), "s"(soff) : "memory");
}
DI void glds_in(const void* g, u32 ldsdst) {
    ldsdst = __builtin_amdgcn_readfirstlane(ldsdst);
    asm volatile("s_mov_b32 m0, %0\n\ts_nop 0\n\tglobal_load_lds_dwordx4 %1, off" :: "s"(ldsdst), "v"(g) : "memory");
}
DI void flag_load(u64& fl, u64 nomatch, u32 voff, u32x4 srd, u32 soff) {
    soff = __builtin_amdgcn_readfirstlane(soff);
    fl = nomatch;
    asm volatile("s_nop 4\n\tbuffer_load_dwordx2 %0, %1, %2, %3 offen sc1" : "+v"(fl) : "v"(voff), "s"(srd), "s"(soff) : "memory");
}
DI float sum_g(float v) {
    auto a = __builtin_amdgcn_permlane32_swap(__builtin_bit_cast(u32, v), __builtin_bit_cast(u32, v), false, false);
    v = __builtin_bit_cast(float, (u32)a[0]) + __builtin_bit_cast(float, (u32)a[1]);
    auto b = __builtin_amdgcn_permlane16_swap(__builtin_bit_cast(u32, v), __builtin_bit_cast(u32, v), false, false);
    return __builtin_bit_cast(float, (u32)b[0]) + __builtin_bit_cast(float, (u32)b[1]);
}
DI float max_h(float v) {
    auto a = __builtin_amdgcn_permlane32_swap(__builtin_bit_cast(u32, v), __builtin_bit_cast(u32, v), false, false);
    return fmaxf(__builtin_bit_cast(float, (u32)a[0]), __builtin_bit_cast(float, (u32)a[1]));
}
DI u32x4 ones4() { u32 o = 0x3f803f80u; asm volatile("" : "+v"(o)); return u32x4{o, o, o, o}; }
DI int crow(int reg, int h) { return (reg & 3) + 8 * (reg >> 2) + 4 * h; }

#define dg_a (c.dgp[0])
#define dg_b (c.dgp[1])
#define STAMP(n) do { if (c.wave == 0 && c.lane == 0) { if ((n) == 0) dg_a = __builtin_amdgcn_s_memrealtime(); if ((n) == 7) dg_b = __builtin_amdgcn_s_memrealtime(); } } while (0)
#define CSTAMP(n) do {} while (0)
#define CSTAMPW(n, W) do {} while (0)

struct Ctx {
    u64* dgp;
    LAS char* L; u32 Lb;
    char* ws; rsrc_t rs; u32x4 srd;
    int tid, lane, wave, i, j;
    u32 elo, ehi;
};

DI Ctx fresh(const Ctx& c0) {
    Ctx c = c0;
    asm volatile("" : "+v"(c.lane)); asm volatile("" : "+v"(c.tid));
    asm volatile("" : "+s"(c.wave)); asm volatile("" : "+s"(c.i)); asm volatile("" : "+s"(c.j));
    return c;
}
DI GAS u64* flag_ptr(const Ctx& c, int phase, int id) { return (GAS u64*)(c.ws + WS_FLAG + (u32)(phase * 256 + id) * 128); }
DI void signal(const Ctx& c, int phase) {
    if (c.tid == 0) __hip_atomic_store(flag_ptr(c, phase, c.i * 8 + c.j), ((u64)c.ehi << 32) | c.elo, __ATOMIC_RELAXED, __HIP_MEMORY_SCOPE_AGENT);
}
DI void publish(const Ctx& c, int phase) {
    DRAIN();
    BAR();
    signal(c, phase);
}
DI void poll(const Ctx& c, int phase, int id) {
    const u64 want = ((u64)c.ehi << 32) | c.elo;
    GAS u64* f = flag_ptr(c, phase, id < 0 ? 0 : id);
    for (u32 spins = 0; spins < SPIN_CAP; ++spins) {
        const u64 v = __hip_atomic_load(f, __ATOMIC_RELAXED, __HIP_MEMORY_SCOPE_AGENT);
        if (__all(id < 0 || v == want)) break;
        __builtin_amdgcn_s_sleep(1);
    }
    __builtin_amdgcn_fence(__ATOMIC_ACQUIRE, "wavefront");
}
DI void wait_group(const Ctx& c, int phase) {
    if (c.wave == 0) poll(c, phase, c.lane < 8 ? c.i * 8 + c.lane : -1);
    BAR();
}

DI void load_wimage_a(const Ctx& c, const Params& p) {
    const int jj = c.j, w = c.wave;
    const u16 *pk_ = p.wk, *pv_ = p.wv;
    asm volatile("" : "+s"(pk_), "+s"(pv_));
    const char* xsrc = (const char*)((jj >= 3 && jj < 6) ? pv_ : pk_); const int sub = jj < 6 ? jj % 3 : 0;
    xsrc += sub == 0 ? 0 : (sub == 1 ? 16 : 20);
    const char* msrc = (const char*)p.wq + jj * 144;
    const char* a[5]; u32 st[5];
#pragma unroll
    for (int k = 0; k < 5; ++k) {
        const u32 id = 64 * k + c.lane, row = (id * 6554u) >> 16, ch = id - row * 10;
        const bool ex = ch == 9;
        st[k] = ex ? 32 * 36 : 32 * 1152;
        a[k] = (ex ? xsrc : msrc) + (ex ? row * 36 : row * 1152 + ch * 16);
    }
#pragma unroll
    for (int n = 0; n < 3; ++n) {
        const int G = n < 2 ? (w & 3) + 8 * (w >> 2) + 4 * n : 16 + w;
        if (n < 2 || w < 2) {
#pragma unroll
            for (int k = 0; k < 5; ++k) glds_in(a[k] + (u32)G * st[k], c.Lb + L_R1 + (G * 5 + k) * 1024);
        }
    }
}
DI void load_wimage_o(const Ctx& c, u32 base) {
    const u32 so = base + (u32)c.j * WIMG_BYTES;
    if (c.wave >= 1) {
#pragma unroll
        for (int n = 0; n < 13; ++n)
            if (n < 12 || c.wave < 7) glds_ws(c.srd, c.lane * 16, so + (c.wave - 1 + 7 * n) * 1024, c.Lb + L_R1 + (c.wave - 1 + 7 * n) * 1024);
    }
}
template <int P0, int P1>
DI void load_wimage_part(const Ctx& c, u32 base, int w0, int ldsbase = L_R1) {
    const u32 so = base + (u32)c.j * WIMG_BYTES; const int nw = 8 - w0;
    if (c.wave >= w0) {
#pragma unroll
        for (int n = 0; n < (P1 - P0 + 6) / 7; ++n) {
            const int pc = P0 + (c.wave - w0) + nw * n;
            if (pc < P1) glds_ws(c.srd, c.lane * 16, so + pc * 1024, c.Lb + ldsbase + pc * 1024);
        }
    }
}
DI void load_wimage_o4(const Ctx& c, int which, int part) {
    const u32 so = WS_WIMG + (u32)(which * 8 + c.j) * WIMG_BYTES + (u32)part * 30 * 1024; const u32 dst = c.Lb + L_R1 + part * 30 * 1024;
    if (c.wave < 4) {
#pragma unroll
        for (int n = 0; n < 8; ++n)
            if (n < 7 || c.wave < 2) glds_ws(c.srd, c.lane * 16, so + (c.wave + 4 * n) * 1024, dst + (c.wave + 4 * n) * 1024);
    }
}
struct Fmt { u32x4 v[2]; u32 g[8]; u32 dst[2], cdst[2]; int kind[2]; };
DI void fmt_issue(const Ctx& c, const Params& p, Fmt& m) {
    const u16 *pw = p.wqkv, *pa = p.woA, *pb = p.woB;
    asm volatile("" : "+s"(pw), "+s"(pa), "+s"(pb));
    const int lane = c.lane, rho = lane >> 1, g = rho >> 3, e = rho & 7;
#pragma unroll
    for (int r = 0; r < 2; ++r) {
        const int fr = blockIdx.x * 8 + c.wave + 2048 * r;
        int col, A, P, Q, pitch; const GAS char* src;
        if (fr < 2016) {
            const int jj = fr / 252, rem = fr - jj * 252, u = rem / 18, s = rem - u * 18;
            const int wv = u < 8 ? u : (u - 8 < 5 ? u - 8 : 6), slot = u < 8 ? 0 : 1, a = wv >> 1;
            const bool isrem = (wv & 1) && slot == 1;
            const u32 fo = (u32)(((jj * 8 + wv) * 2 + slot) * 18 + s);
            m.dst[r] = WS_WF + fo * 1024; m.cdst[r] = WS_CSP + fo * 64; m.kind[r] = isrem ? 3 : 1;
            P = s < 16 ? 16 : 4; Q = 2; pitch = 1728 * 2; src = (const GAS char*)pw;
            if (!isrem) {
                col = ((wv & 1) ? 1152 : (slot ? 576 : 0)) + 18 * (4 * jj + a) + 8 * (lane & 1);
                A = s < 16 ? 72 * (s >> 1) + 32 * (s & 1) + 4 * g : 72 * (4 * (s - 16) + g) + 64;
            } else {
                const int n = lane & 15, g2 = lane >> 4;
                col = (a == 0 ? (n < 8 ? 0 : 576) : 1152) + 18 * (4 * jj + ((n & 7) >> 1)) + 16 + (n & 1);
                A = s < 16 ? 72 * (s >> 1) + 32 * (s & 1) + 4 * g2 : 72 * (4 * (s - 16) + g2) + 64;
#pragma unroll
                for (int q = 0; q < 8; ++q) m.g[q] = *(const GAS u16*)(src + (A + P * (q >> 2) + (q & 3)) * pitch + col * 2);
            }
        } else {
            const bool on = fr < 3456;
            const int q = on ? fr - 2016 : 0, im = q / 720, rem = q - im * 720, jj = rem / 90, st = rem - jj * 90, s = st / 5, t = st - s * 5;
            m.dst[r] = WS_WIMG + (u32)(im * 8 + jj) * WIMG_BYTES + (u32)st * 1024; m.cdst[r] = 0; m.kind[r] = on ? 2 : 0;
            col = 72 * jj + 16 * t + (t == 4 ? 0 : 8 * (lane & 1));
            if (s < 16) { A = 36 * s + 18 * (g >> 1) + 4 * (g & 1); P = 8; Q = 2; } else { A = 72 * (4 * (s - 16) + g) + 16; P = 36; Q = 18; }
            src = (const GAS char*)(im ? pb : pa); pitch = 576 * 2;
        }
        if (r == 1 || m.kind[r] != 3) m.v[r] = *(const GAS u32x4*)(src + (A + P * (e >> 2) + Q * ((e >> 1) & 1) + (e & 1)) * pitch + col * 2);
    }
}
DI void fmt_finish(const Ctx& c, Fmt& m) {
    asm volatile("" : "+v"(m.v[0]), "+v"(m.v[1]), "+v"(m.g[0]), "+v"(m.g[1]), "+v"(m.g[2]), "+v"(m.g[3]), "+v"(m.g[4]), "+v"(m.g[5]), "+v"(m.g[6]), "+v"(m.g[7]));
    const int lane = c.lane, i16 = lane & 15, g = lane >> 4, q = i16 >> 2, p = i16 & 3;
#pragma unroll
    for (int r = 0; r < 2; ++r) {
        if (m.kind[r] != 0) {
            u32x4 f;
            if (m.kind[r] == 3) { const u32* w = m.g; f = u32x4{w[0] | (w[1] << 16), w[2] | (w[3] << 16), w[4] | (w[5] << 16), w[6] | (w[7] << 16)}; }
            else {
                LAS char* t = c.L + L_FMT + c.wave * 2048 + r * 1024;
                lst(t + lane * 16, m.v[r]);
                asm volatile("s_waitcnt lgkmcnt(0)" ::: "memory");
                const u32x2 lo = trr(t + (8 * g + q) * 32 + 8 * p), hi = trr(t + (8 * g + 4 + q) * 32 + 8 * p);
                f = u32x4{lo[0], lo[1], hi[0], hi[1]};
            }
            st128(c.rs, f, lane * 16, m.dst[r]);
            if (m.kind[r] != 2) {
                float cs = ((bflo(f[0]) + bfhi(f[0])) + (bflo(f[1]) + bfhi(f[1]))) + ((bflo(f[2]) + bfhi(f[2])) + (bflo(f[3]) + bfhi(f[3])));
                cs = sum_g(cs);
                if (lane < 16) st32(c.rs, asu(cs), lane * 4, m.cdst[r]);
            }
        }
    }
}

template <bool PROJ, bool NAT = false, class HK, class PRE>
DI void g1_mma(const Ctx& c, const u32x4 (&xf)[9], f32x4 (&acc)[5], HK hook, PRE pre) {
    const int lane = c.lane, kh = c.wave >> 2, q = (lane & 15) >> 2, p = lane & 3, g = lane >> 4;
    LAS char *bN, *bR, *bNo, *bNt, *bRo, *bRt;
    if (PROJ) {
        if (NAT) { bN = c.L + L_R1 + (kh * 256 + 4 * g + q) * WP + 8 * p; bR = c.L + L_R1 + (512 + 32 * kh + 8 * g + q) * WP + 8 * p; }
        else { bN = c.L + L_A1 + (kh * 288 + 4 * g + q) * WP + 8 * p; bR = c.L + L_A1 + (kh * 288 + 72 * g + q) * WP + 8 * p; }
        bNo = bN + 64 * kh; bNt = bN + 64 - 64 * kh; bRo = bR + 64 * kh; bRt = bR + 64 - 64 * kh;
    } else {
        bN = c.L + L_R1 + kh * 8 * 5120 + lane * 16; bR = c.L + L_R1 + (16 + kh) * 5120 + lane * 16;
        bNo = bN + 2048 * kh; bNt = bN + 2048 - 2048 * kh; bRo = bR + 2048 * kh; bRt = bR + 2048 - 2048 * kh;
    }
    u32x4 w[2][5];
    auto rd = [&](int sl, u32x4 (&d)[5]) {
#pragma unroll
        for (int u = 0; u < 5; ++u) {
            if (PROJ) {
                const int r0 = NAT ? (sl < 8 ? 32 * sl : 0) : (sl < 8 ? 72 * (sl >> 1) + 32 * (sl & 1) : 64), hs = sl < 8 ? 16 : 4;
                LAS char* bb = u < 2 ? (sl < 8 ? bNo : bRo) + 32 * u : (u == 2 ? (sl < 8 ? bN : bR) + 128 : (sl < 8 ? bNt : bRt) + 32 * (u - 3));
                const u32x2 lo = trr(bb + r0 * WP), hi = trr(bb + (r0 + hs) * WP);
                d[u] = u32x4{lo[0], lo[1], hi[0], hi[1]};
            } else {
                LAS char* bb = u < 2 ? (sl < 8 ? bNo : bRo) + 1024 * u : (u == 2 ? (sl < 8 ? bN : bR) + 4096 : (sl < 8 ? bNt : bRt) + 1024 * (u - 3));
                d[u] = lld<u32x4>(bb + (sl < 8 ? sl * 5120 : 0));
            }
        }
    };
    rd(0, w[0]);
#pragma unroll
    for (int u = 0; u < 5; ++u) asm volatile("" : "+v"(acc[u]));
#pragma unroll
    for (int sl = 0; sl < 9; ++sl) {
        pre(sl);
        if (sl + 1 < 9) rd(sl + 1, w[(sl + 1) & 1]);
#pragma unroll
        for (int u = 0; u < 5; ++u) MF16(acc[u], w[sl & 1][u], xf[sl]);
        hook(sl, w[sl & 1]);
    }
    asm volatile("s_nop 7" : "+v"(acc[0]), "+v"(acc[1]), "+v"(acc[2]), "+v"(acc[3]), "+v"(acc[4]));
}
DI void g1_exchange(const Ctx& c, f32x4 (&acc)[5]) {
    LAS char* xw = c.L + L_XCH + c.wave * 3072 + c.lane * 16;
    lst(xw, acc[3]); lst(xw + 1024, acc[4]); lst(xw + 2048, acc[2]);
    BAR();
    LAS char* xr = c.L + L_XCH + (c.wave ^ 4) * 3072 + c.lane * 16;
    acc[0] += lld<f32x4>(xr); acc[1] += lld<f32x4>(xr + 1024); acc[2] += lld<f32x4>(xr + 2048);
}
DI void load_xfrags(const Ctx& c, u32 buf, u32x4 (&xf)[9]) {
    const int m = c.wave & 3, kh = c.wave >> 2;
    const u32 so = buf + (u32)(c.i * 18 * 4 + m) * 1024;
#pragma unroll
    for (int sl = 0; sl < 9; ++sl) { const int s = sl < 8 ? 8 * kh + sl : 16 + kh; xf[sl] = ld128(c.rs, c.lane * 16, so + (u32)s * 4096); }
}
DI u32x4 stats_issue(const Ctx& c, u32 stbuf) {
    const u32 vo = (u32)((c.i * 16 + 2 * (c.lane >> 3)) * 64 + c.wave * 8 + (c.lane & 7)) * 8;
    const u32x2 a = ld64(c.rs, vo, stbuf), b = ld64(c.rs, vo + 512, stbuf);
    return u32x4{a[0], a[1], b[0], b[1]};
}
DI void stats_finish(const Ctx& c, u32x4 v) {
    const int tok = c.wave * 8 + (c.lane & 7);
    float s = asf(v[0]) + asf(v[2]), s2 = asf(v[1]) + asf(v[3]);
    s += __shfl_xor(s, 8); s2 += __shfl_xor(s2, 8);
    s = sum_g(s); s2 = sum_g(s2);
    if (c.lane < 8) {
        lst(c.L + L_STATP + tok * 8, f32x2{s, s2}); lst(c.L + L_STATP + 512 + tok * 8, f32x2{0.f, 0.f});
        const float mean = s * (1.f / DM), var = s2 * (1.f / DM) - mean * mean;
        lst(c.L + L_MEAN + tok * 4, mean); lst(c.L + L_MEAN + 256 + tok * 4, __builtin_amdgcn_rsqf(fmaxf(var, 0.f) + 1e-5f));
    }
}
DI void stats_issue32(const Ctx& c, u32 stbuf, u32x4 (&v)[4]) {
    const u32 vo = (u32)((c.i * 16 + (c.lane >> 4)) * 64 + (c.wave == 7 ? 32 : 0) + 2 * (c.lane & 15)) * 8;
#pragma unroll
    for (int n = 0; n < 4; ++n) v[n] = ld128(c.rs, vo + n * 4 * 512, stbuf);
}
DI void stats_finish32(const Ctx& c, const u32x4 (&v)[4]) {
    float s[4];
#pragma unroll
    for (int e = 0; e < 4; ++e) s[e] = sum_g((asf(v[0][e]) + asf(v[1][e])) + (asf(v[2][e]) + asf(v[3][e])));
    if (c.lane < 16) {
        const int tok = (c.wave == 7 ? 32 : 0) + 2 * c.lane;
        const float m0 = s[0] * (1.f / DM), m1 = s[2] * (1.f / DM);
        lst(c.L + L_MEAN + tok * 4, f32x2{m0, m1});
        lst(c.L + L_MEAN + 256 + tok * 4, f32x2{__builtin_amdgcn_rsqf(fmaxf(s[1] * (1.f / DM) - m0 * m0, 0.f) + 1e-5f), __builtin_amdgcn_rsqf(fmaxf(s[3] * (1.f / DM) - m1 * m1, 0.f) + 1e-5f)});
    }
}

struct KV { u32x4 km; u32x2 kr; u32x4 v0, v1; };
DI int vrow_of(int r) { return r < 18 ? r : 18; }
DI u32 ka_vrow(int blk, int row) { return row < 8 ? KA_V0 + blk * 128 + row * 16 : (row < 16 ? KA_V8 + blk * 128 + (row - 8) * 16 : KA_VX + blk * 64 + (row - 16) * 16); }
DI KV load_tile_ws(const Ctx& c, u32 tile, bool shared) {
    const int r = c.lane & 31, h = c.lane >> 5;
    KV t;
    if (shared) {
        t.km = ld128(c.rs, c.lane * 16, tile);
        t.kr = ld64(c.rs, KA_KREM + r * 8, tile);
        t.v0 = ld128(c.rs, ka_vrow(h, vrow_of(r)), tile);
        t.v1 = ld128(c.rs, ka_vrow(2 + h, vrow_of(r)), tile);
        return t;
    }
    t.km = ld128(c.rs, c.lane * 16, tile);
    t.kr = ld64(c.rs, KT_KREM + r * 8, tile);
    t.v0 = ld128(c.rs, KT_V + h * KT_VBLK + vrow_of(r) * 16, tile);
    t.v1 = ld128(c.rs, KT_V + (2 + h) * KT_VBLK + vrow_of(r) * 16, tile);
    return t;
}
DI f32x16 mfma32k8(u32x2 a, u32x2 b, f32x16 c) { return __builtin_amdgcn_mfma_f32_32x32x8bf16_1k(__builtin_bit_cast(s16x4, a), __builtin_bit_cast(s16x4, b), c, 0, 0, 0); }
DI f32x16 qk(const KV& t, u32x4 qm, u32x2 qr, f32x16 c0) {
    return mfma32k8(t.kr, qr, mfma32(t.km, qm, c0));
}
DI void QK1(f32x16& s, u32x4 km, u32x4 qm) { asm volatile("v_mfma_f32_32x32x16_bf16 %0, %1, %2, 0" : "=&v"(s) : "v"(km), "v"(qm)); }
DI void QK1C(f32x16& s, u32x4 km, u32x4 qm, const f32x16& c0) { asm volatile("v_mfma_f32_32x32x16_bf16 %0, %1, %2, %3" : "=&v"(s) : "v"(km), "v"(qm), "v"(c0)); }
DI void QK2(f32x16& s, u32x2 kr, u32x2 qr) { asm volatile("v_mfma_f32_32x32x8_bf16 %0, %1, %2, %0" : "+v"(s) : "v"(kr), "v"(qr)); }
DI void PV(f32x16& o, u32x4 v, u32x4 p) { asm volatile("v_mfma_f32_32x32x16_bf16 %0, %1, %2, %0" : "+v"(o) : "v"(v), "v"(p)); }
DI float EX(float x) { float r; asm volatile("v_exp_f32 %0, %1" : "=v"(r) : "v"(x)); return r; }
DI u32 CV(float a, float b) { u32 r; asm volatile("v_cvt_pk_bf16_f32 %0, %1, %2" : "=v"(r) : "v"(a), "v"(b)); return r; }
struct Pipe { float e[4]; u32x4 p1; };
template <bool NEXT = true, class F0, class F1, class F2, class F3, class F4>
DI void attn_step(const f32x16& sc, f32x16& sn, f32x16& o, Pipe& pp, const u32x4& km, const u32x2& kr, const u32x4& v0, const u32x4& v1, u32x4 qm, u32x2 qr, F0 first, F1 LK, F2 LR, F3 LV1, F4 LV0) {
    u32x4 p0; float a0, a1, a2, a3, b0, b1, b2, b3;
    if (NEXT) first(sn, km, qm);
    LK();
    pp.p1[2] = CV(pp.e[0], pp.e[1]); pp.p1[3] = CV(pp.e[2], pp.e[3]);
    a0 = EX(sc[0]); a1 = EX(sc[1]); a2 = EX(sc[2]); a3 = EX(sc[3]);
    if (NEXT) QK2(sn, kr, qr); else asm volatile("s_nop 0");
    LR();
    p0[0] = CV(a0, a1); p0[1] = CV(a2, a3);
    b0 = EX(sc[4]); b1 = EX(sc[5]); b2 = EX(sc[6]); b3 = EX(sc[7]);
    PV(o, v1, pp.p1); LV1();
    p0[2] = CV(b0, b1); p0[3] = CV(b2, b3);
    a0 = EX(sc[8]); a1 = EX(sc[9]); a2 = EX(sc[10]); a3 = EX(sc[11]);
    PV(o, v0, p0); LV0();
    pp.p1[0] = CV(a0, a1); pp.p1[1] = CV(a2, a3);
    pp.e[0] = EX(sc[12]); pp.e[1] = EX(sc[13]); pp.e[2] = EX(sc[14]); pp.e[3] = EX(sc[15]);
}
template <class F1, class F2, class F3, class F4>
DI void attn_step2(const f32x16& scx, f32x16& snx, f32x16& ox, Pipe& px, const f32x16& scy, f32x16& sny, f32x16& oy, Pipe& py,
                   const u32x4& km, const u32x2& kr, const u32x4& v0, const u32x4& v1, u32x4 qmx, u32x2 qrx, u32x4 qmy, u32x2 qry, F1 LK, F2 LR, F3 LV1, F4 LV0) {
    u32x4 p0x, p0y; float a0, a1, a2, a3, b0, b1, b2, b3, c0, c1, c2, c3, d0, d1, d2, d3;
#define GRP(MF, PD0, PD1, E0, E1, E2, E3, D0, D1, D2, D3, SRC, I) MF; PD0 = CV(E0, E1); PD1 = CV(E2, E3); D0 = EX(SRC[I]); D1 = EX(SRC[I + 1]); D2 = EX(SRC[I + 2]); D3 = EX(SRC[I + 3])
    GRP(QK1(snx, km, qmx), px.p1[2], px.p1[3], px.e[0], px.e[1], px.e[2], px.e[3], a0, a1, a2, a3, scx, 0);
    GRP(QK1(sny, km, qmy); LK(), py.p1[2], py.p1[3], py.e[0], py.e[1], py.e[2], py.e[3], b0, b1, b2, b3, scy, 0);
    GRP(QK2(snx, kr, qrx), p0x[0], p0x[1], a0, a1, a2, a3, c0, c1, c2, c3, scx, 4);
    GRP(QK2(sny, kr, qry); LR(), p0y[0], p0y[1], b0, b1, b2, b3, d0, d1, d2, d3, scy, 4);
    GRP(PV(ox, v1, px.p1), p0x[2], p0x[3], c0, c1, c2, c3, a0, a1, a2, a3, scx, 8);
    GRP(PV(oy, v1, py.p1); LV1(), p0y[2], p0y[3], d0, d1, d2, d3, b0, b1, b2, b3, scy, 8);
    GRP(PV(ox, v0, p0x), px.p1[0], px.p1[1], a0, a1, a2, a3, px.e[0], px.e[1], px.e[2], px.e[3], scx, 12);
    GRP(PV(oy, v0, p0y); LV0(), py.p1[0], py.p1[1], b0, b1, b2, b3, py.e[0], py.e[1], py.e[2], py.e[3], scy, 12);
#undef GRP
}
DI void attn_finish(f32x16& o, Pipe& pp, u32x4 v1) {
    asm volatile("s_nop 0");
    pp.p1[2] = CV(pp.e[0], pp.e[1]); pp.p1[3] = CV(pp.e[2], pp.e[3]);
    asm volatile("s_nop 1");
    PV(o, v1, pp.p1);
    asm volatile("s_nop 7\n\ts_nop 3" : "+v"(o));
}
DI void attn_finish2(f32x16& ox, Pipe& px, f32x16& oy, Pipe& py, u32x4 v1) {
    asm volatile("s_nop 0");
    px.p1[2] = CV(px.e[0], px.e[1]); px.p1[3] = CV(px.e[2], px.e[3]);
    py.p1[2] = CV(py.e[0], py.e[1]); py.p1[3] = CV(py.e[2], py.e[3]);
    PV(ox, v1, px.p1);
    asm volatile("s_nop 0");
    PV(oy, v1, py.p1);
    asm volatile("s_nop 7\n\ts_nop 3" : "+v"(ox), "+v"(oy));
}
DI void pexp(const f32x16& s, u32x4& p0, u32x4& p1) {
    float e[16];
#pragma unroll
    for (int r = 0; r < 16; ++r) e[r] = __builtin_amdgcn_exp2f(s[r]);
    p0 = u32x4{pk(e[0], e[1]), pk(e[2], e[3]), pk(e[4], e[5]), pk(e[6], e[7])};
    p1 = u32x4{pk(e[8], e[9]), pk(e[10], e[11]), pk(e[12], e[13]), pk(e[14], e[15])};
}
DI float rowmax(const f32x16& s) {
    float a = fmaxf(fmaxf(s[0], s[1]), s[2]), b = fmaxf(fmaxf(s[3], s[4]), s[5]);
#pragma unroll
    for (int r = 6; r < 16; r += 2) { if (r & 2) a = fmaxf(fmaxf(a, s[r]), s[r + 1]); else b = fmaxf(fmaxf(b, s[r]), s[r + 1]); }
    return max_h(fmaxf(a, b));
}
DI f32x16 mask_c(int lane, bool diag) {
    const int r = lane & 31, h = lane >> 5; f32x16 m;
#pragma unroll
    for (int e = 0; e < 16; ++e) { const bool ok = diag ? (crow(e, h) <= r) : (crow(e, h) > r); m[e] = ok ? 0.f : NEG_BIG; }
    return m;
}
DI f32x16 attn_slow(const Ctx& c, u32 tile0, int n0, int n1, bool masked, u32x4 qm, u32x2 qr) {
    const int h = c.lane >> 5;
    qr[1] = 0u;
    f32x16 o = {}; float m = NEG_BIG;
    for (int n = n0; n <= n1; ++n) {
        const KV t = load_tile_ws(c, tile0 + (u32)n * TILE_B, !masked);
        f32x16 z = {};
        f32x16 s = qk(t, qm, qr, z);
        if (masked && (n == n1 || n == n1 - 8)) {
            const int r = c.lane & 31;
#pragma unroll
            for (int e = 0; e < 16; ++e) { const bool ok = (n == n1) ? (crow(e, h) <= r) : (crow(e, h) > r); s[e] = ok ? s[e] : NEG_BIG; }
        }
        const float mn = fmaxf(m, rowmax(s)), al = __builtin_amdgcn_exp2f(m - mn);
        m = mn;
#pragma unroll
        for (int e = 0; e < 16; ++e) { o[e] *= al; s[e] = s[e] <= NEG_BIG ? NEG_BIG : s[e] - mn; }
        u32x4 p0, p1; pexp(s, p0, p1);
        o = mfma32(t.v0, p0, o); o = mfma32(t.v1, p1, o);
    }
    return o;
}
DI bool attn_unsafe(const f32x16& o) {
    float z = 0.f;
#pragma unroll
    for (int e = 0; e < 10; ++e) z = __builtin_fmaf(o[e], 0.f, z);
    const float l = o[10];
    return __any(!(l > 0x1p-40f && l < 0x1p30f && z == 0.f));
}
DI void store_o(const Ctx& c, u32 obuf, const f32x16& o, int a, int t) {
    const int r = c.lane & 31, h = c.lane >> 5;
    const float inv = __builtin_amdgcn_rcpf(o[10]);
    const u32x4 mainp = {pk(o[0] * inv, o[1] * inv), pk(o[2] * inv, o[3] * inv), pk(o[4] * inv, o[5] * inv), pk(o[6] * inv, o[7] * inv)};
    const int m = 2 * t + (r >> 4), tok = r & 15;
    const u32 so = obuf + (u32)(c.i * 18 * 4) * 1024;
    st128(c.rs, mainp, (u32)((2 * c.j + (a >> 1)) * 4 + m) * 1024 + ((2 * (a & 1) + h) * 16 + tok) * 16, so);
    if (h == 0) st32(c.rs, pk(o[8] * inv, o[9] * inv), (u32)((16 + (c.j >> 2)) * 4 + m) * 1024 + ((c.j & 3) * 16 + tok) * 16 + 4 * a, so);
}

template <int rep>
DI void a_proj(Ctx& c0, const Params& p) {
    Ctx c = fresh(c0);
    const int lane = c.lane, m = c.wave & 3, kh = c.wave >> 2, tok = lane & 15, g = lane >> 4;
    u32x4 xf[9]; f32x4 acc[5] = {};
    f32x4 s1 = {}, s2 = {}, ca = {}, cb = {};
    Fmt fm; u64 e0 = 0; u32x4 stv;
    if (rep == 0) {
        e0 = __hip_atomic_load((GAS u64*)(p.ws + WS_EPOCH), __ATOMIC_RELAXED, __HIP_MEMORY_SCOPE_AGENT);
        const char* xt = (const char*)p.x + (size_t)(c.i * 64) * (DM * 2);
#pragma unroll
        for (int n = 0; n < 8; ++n) {
            const int row = c.wave * 8 + n;
            glds_in(xt + row * (DM * 2) + (lane ^ (row & 15)) * 16, c.Lb + (row < 60 ? L_XIN + row * 1024 : L_XIN2 + (row - 60) * 1024));
        }
        xf[8] = *(const u32x4*)(xt + (m * 16 + tok) * (DM * 2) + (512 + 32 * kh + 8 * g) * 2);
        load_wimage_a(c, p);
        fmt_issue(c, p, fm);
        STAMP(13);
        if (c.wave < 2) { if (fm.kind[0] == 3) asm volatile("s_waitcnt vmcnt(19)" ::: "memory"); else asm volatile("s_waitcnt vmcnt(12)" ::: "memory"); }
        else { if (fm.kind[0] == 3) asm volatile("s_waitcnt vmcnt(14)" ::: "memory"); else asm volatile("s_waitcnt vmcnt(7)" ::: "memory"); }
        BAR();
        STAMP(18);
        asm volatile("" : "+v"(xf[8]), "+v"(e0));
        { const int row = m * 16 + tok, t2 = tok ^ (g >> 1);
          LAS char* xb = c.L + (row < 60 ? L_XIN + row * 1024 : L_XIN2 + (row - 60) * 1024) + kh * 512 + 8 * (g & 1);
#pragma unroll
          for (int sl = 0; sl < 8; ++sl) {
              const int K = 4 * (sl & 3);
              const u32x2 lo = lld<u32x2>(xb + (sl >> 2) * 256 + ((K ^ t2) * 16)), hi = lld<u32x2>(xb + (sl >> 2) * 256 + (((K + 2) ^ t2) * 16));
              xf[sl] = u32x4{lo[0], lo[1], hi[0], hi[1]};
          } }
        asm volatile("s_nop 1" : "+v"(xf[0]), "+v"(xf[1]), "+v"(xf[2]), "+v"(xf[3]), "+v"(xf[4]), "+v"(xf[5]), "+v"(xf[6]), "+v"(xf[7]), "+v"(xf[8]));
        STAMP(2);
    } else {
        wait_group(c, 5);
        load_xfrags(c, WS_XF + 1 * XF_BYTES, xf);
        stv = stats_issue(c, WS_ST + 1 * ST_BYTES);
        asm volatile("s_waitcnt vmcnt(11)" ::: "memory"); BAR();
        STAMP(32);
    }
    if (rep != 0) { g1_mma<true>(c, xf, acc, [](int, const u32x4 (&)[5]) {}, [](int) {}); stats_finish(c, stv); }
    else {
        u32x4 ones = ones4();
        const int tlc = m < 2 ? 2 * kh + m : 2 * (1 - kh) + (m - 2), q = (lane & 15) >> 2, pp = lane & 3;
        LAS char* bN = c.L + L_R1 + (kh * 256 + 4 * g + q) * WP + 8 * pp + 32 * tlc;
        LAS char* bR = c.L + L_R1 + (512 + 32 * kh + 8 * g + q) * WP + 8 * pp + 32 * tlc;
        asm volatile("" : "+v"(s1), "+v"(s2), "+v"(ca), "+v"(cb), "+v"(ones));
        u32x4 wx[2];
        auto rdx = [&](int sl) {
            const int r0 = sl < 8 ? 32 * sl : 0, hs = sl < 8 ? 16 : 4; LAS char* b = sl < 8 ? bN : bR;
            const u32x2 lo = trr(b + r0 * WP), hi = trr(b + (r0 + hs) * WP);
            wx[sl & 1] = u32x4{lo[0], lo[1], hi[0], hi[1]}; };
        rdx(0);
        g1_mma<true, true>(c, xf, acc, [&](int sl, const u32x4 (&w)[5]) {
            if (sl + 1 < 9) rdx(sl + 1);
            MF16(s1, ones, xf[sl]); MF16(s2, xf[sl], xf[sl]); MF16(cb, w[2], ones); MF16(ca, wx[sl & 1], ones);
        }, [&](int sl) {
            if (sl == 3) {
                if (fm.kind[0] == 3) asm volatile("s_waitcnt vmcnt(9)" ::: "memory"); else asm volatile("s_waitcnt vmcnt(2)" ::: "memory");
                BAR();
            }
        });
        if (c.tid < 180) st128(c.rs, lld<u32x4>(c.L + L_R1 + c.i * 2880 + c.tid * 16), c.tid * 16, WS_WIMGA + (u32)c.j * WIMG_BYTES + (u32)c.i * 2880);
        asm volatile("s_nop 7" : "+v"(s1), "+v"(s2), "+v"(ca), "+v"(cb));
        const int rg = tok & 3;
        float d = rg == 0 ? s2[0] : (rg == 1 ? s2[1] : (rg == 2 ? s2[2] : s2[3]));
        d = sum_g(g == (tok >> 2) ? d : 0.f);
        if (g == 0) lst(c.L + L_STATP + kh * 512 + (m * 16 + tok) * 8, f32x2{s1[0], d});
        const int tl = m < 2 ? 2 * kh + m : 2 * (1 - kh) + (m - 2);
        if (tok == 0) { lst(c.L + L_CSA + (kh * 5 + tl) * 64 + g * 16, ca); if (m == 0) lst(c.L + L_CSA + (kh * 5 + 4) * 64 + g * 16, cb); }
    }
    STAMP(3 + rep * 30);
    g1_exchange(c, acc);
    const f32x2 sa = lld<f32x2>(c.L + L_STATP + (m * 16 + tok) * 8), sb = lld<f32x2>(c.L + L_STATP + 512 + (m * 16 + tok) * 8);
    const float mean = (sa[0] + sb[0]) * (1.f / DM), var = (sa[1] + sb[1]) * (1.f / DM) - mean * mean;
    const float rstd = __builtin_amdgcn_rsqf(fmaxf(var, 0.f) + 1e-5f);
    LAS char* stg = c.L + L_STG + (m * 16 + tok) * STG_P + g * 8;
    auto fin = [&](int u, int t, float sc) {
        const f32x4 cs = lld<f32x4>(c.L + L_CSA + t * 64 + g * 16) + lld<f32x4>(c.L + L_CSA + (5 + t) * 64 + g * 16);
        f32x4 v;
#pragma unroll
        for (int e = 0; e < 4; ++e) v[e] = (acc[u][e] - mean * cs[e]) * sc;
        return u32x2{pk(v[0], v[1]), pk(v[2], v[3])};
    };
    if (kh == 1) {
        const u32x2 d4 = fin(2, 4, g < 2 ? rstd * QSCALE : rstd);
        lst(stg + 128, d4);
        const u32 kva = WS_KVA + rep * KVA_BYTES + (u32)(2 * c.i + (m >> 1)) * TILE_B;
        const int j = c.j, key = (m & 1) * 16 + tok;
        if (j < 2) { if (g >= 2) st64(c.rs, d4, j * 512 + key * 16 + (g - 2) * 8, kva); }
        else if (j == 2) { if (g == 3) st64(c.rs, u32x2{d4[1], 0x00003f80u}, KA_KREM + key * 8, kva); }
        else if (j < 6) {
            asm volatile("s_waitcnt lgkmcnt(0)" ::: "memory");
            const int q = (lane & 15) >> 2, pp = lane & 3;
            LAS char* s = c.L + L_STG + (16 * m + q + 4 * (pp >> 1)) * STG_P + 144 + 8 * (pp & 1);
            const u32x2 lo = trr(s), hi = trr(s + 8 * STG_P);
            const int dl = lane & 7, blk = (m & 1) * 2 + ((lane >> 3) & 1);
            if (j < 5) { if (lane < 16) st128(c.rs, u32x4{lo[0], lo[1], hi[0], hi[1]}, (j == 3 ? KA_V0 : KA_V8) + blk * 128 + dl * 16, kva); }
            else {
                const u32x4 on = ones4();
                const u32x4 v = dl >= 6 ? u32x4{lo[0], lo[1], hi[0], hi[1]} : on;
                if (lane < 16 && (dl >= 6 || dl < 2)) st128(c.rs, v, KA_VX + blk * 64 + (dl >= 6 ? dl - 6 : 2 + dl) * 16, kva);
            }
        }
    }
    lst(stg + (2 * kh) * 32, fin(0, 2 * kh, rstd * QSCALE));
    lst(stg + (2 * kh + 1) * 32, fin(1, 2 * kh + 1, rstd * QSCALE));
    if (rep == 0) {
        fmt_finish(c, fm);
        const u64 e = e0 + 1; c0.elo = c.elo = __builtin_amdgcn_readfirstlane((u32)e); c0.ehi = c.ehi = __builtin_amdgcn_readfirstlane((u32)(e >> 32));
    }
    STAMP(4 + rep * 30);
    publish(c, rep * 6);
}

DI void load_wring(const Ctx& c, u32x4 (&w0)[WRING], u32x4 (&w1)[WRING]) {
    const u32 wso = WS_WF + (u32)((c.j * 8 + c.wave) * 2) * 18 * 1024;
#pragma unroll
    for (int s = 0; s < WRING; ++s) { w0[s] = ld128(c.rs, c.lane * 16, wso + s * 1024); w1[s] = ld128(c.rs, c.lane * 16, wso + (18 + s) * 1024); }
}
DI void colsums_b(const Ctx& c) {
    const int tok = c.lane & 15, g = c.lane >> 4;
    if (c.wave >= 1) {
        const int extra = c.wave == 1 ? 0 : c.wave;
        u32 v[4][5];
#pragma unroll
        for (int q = 0; q < 4; ++q) {
            const int unit = (q < 2 ? c.wave : extra) * 2 + (q & 1);
#pragma unroll
            for (int u = 0; u < 5; ++u) { const int s = g + 4 * u; v[q][u] = (q < 2 || c.wave == 1) ? ld32(c.rs, (s < 18 ? s : 0) * 64 + tok * 4, WS_CSP + (u32)((c.j * 16 + unit) * 18) * 64) : 0u; }
        }
#pragma unroll
        for (int q = 0; q < 4; ++q) {
            const int unit = (q < 2 ? c.wave : extra) * 2 + (q & 1);
            float cs = (asf(v[q][0]) + asf(v[q][1])) + (asf(v[q][2]) + asf(v[q][3])) + (g < 2 ? asf(v[q][4]) : 0.f);
            cs = sum_g(cs);
            if (g == 0 && (q < 2 || c.wave == 1)) lst(c.L + L_CSB + unit * 64 + tok * 4, cs);
        }
    }
}
struct ATail { f32x16 o; u32x4 qm; u32x2 qr; };
DI void a_attn(const Ctx& c0, const Params& p, int rep, ATail& st) {
    const Ctx c = fresh(c0);
    const int lane = c.lane, r = lane & 31, h = lane >> 5, a = c.wave & 3, kh = c.wave >> 2;
    const int ph = rep * 6;
    const u32 kva = WS_KVA + rep * KVA_BYTES;
    u32x4 qmx, qmy; u32x2 qrx, qry;
    { LAS char* s = c.L + L_STG + (32 * kh + r) * STG_P + 36 * a;
      qmx = u32x4{lld<u32>(s + 16 * h), lld<u32>(s + 16 * h + 4), lld<u32>(s + 16 * h + 8), lld<u32>(s + 16 * h + 12)};
      const u32 rx = lld<u32>(s + 32);
      s += (32 - 64 * kh) * STG_P;
      qmy = u32x4{lld<u32>(s + 16 * h), lld<u32>(s + 16 * h + 4), lld<u32>(s + 16 * h + 8), lld<u32>(s + 16 * h + 12)};
      const u32 ry = lld<u32>(s + 32);
      qrx = u32x2{h == 0 ? rx : 0u, 0u}; qry = u32x2{h == 0 ? ry : 0u, 0u}; }
    const int ch0 = c.i >> 2;
    auto chunk_ids = [&](int n) { const int ch = (ch0 + n) & 7; return ch * 32 + (lane & 31); };
    u32 pvo[5];
#pragma unroll
    for (int k = 0; k < 5; ++k) {
        const u32 S = (u32)((c.wave & 3) + 4 * k) * 64 + lane, tile = (S * 52429u) >> 23, ps = S - tile * 160;
        const u32 v = ps - 64, blk = (v * 3450u) >> 16, row = v - blk * 19;
        const u32 src = ps < 64 ? ps * 16 : (ps < 140 ? ka_vrow(blk, row) : (ps < 156 ? KA_KREM + (ps - 140) * 16 : ps * 16));
        pvo[k] = tile * TILE_B + src;
    }
    auto issue_piece = [&](int n, int k) {
        const int ch = (ch0 + n) & 7; const u32 so = kva + (u32)ch * CHUNK_B; const u32 dst = c.Lb + L_RING + (n % 3) * CHUNK_B;
        glds_ws(c.srd, pvo[k], so, dst + ((c.wave & 3) + 4 * k) * 1024);
    };
    auto issue_chunk8 = [&](int n) {
        if (c.wave < 4) { issue_piece(n, 0); issue_piece(n, 2); issue_piece(n, 4); } else { issue_piece(n, 1); issue_piece(n, 3); }
    };
    if (c.wave == 0) poll(c, ph, lane < 32 ? chunk_ids(0) : chunk_ids(1));
    BAR();
    STAMP(6 + rep * 30);
    issue_chunk8(0);
    if (c.wave < 4) { issue_piece(1, 0); issue_piece(1, 1); issue_piece(1, 2); issue_piece(1, 3); issue_piece(1, 4); asm volatile("s_waitcnt vmcnt(5)" ::: "memory"); }
    else DRAIN();
    BAR();
    STAMP(7 + rep * 30); if (rep == 0) CSTAMP(61);
    if (rep == 1) { CSTAMPW(0, 4); CSTAMPW(8, 0); }
    const int vr = vrow_of(r);
    LAS char* kb = c.L + L_RING + lane * 16;
    LAS char* rb = c.L + L_RING + KT_KREM + r * 8;
    LAS char* vb = c.L + L_RING + KT_V + h * KT_VBLK + vr * 16;
    f32x16 sAx, sBx, sAy, sBy, ox, oy;
    const u32x4 k0m = lld<u32x4>(kb); const u32x2 k0r = lld<u32x2>(rb);
    kb += kh * 4 * TILE_B; rb += kh * 4 * TILE_B; vb += kh * 4 * TILE_B;
    const u32x4 kom = lld<u32x4>(kb); const u32x2 kor = lld<u32x2>(rb);
    u32x4 km = lld<u32x4>(kb + TILE_B), v0 = lld<u32x4>(vb), v1 = lld<u32x4>(vb + 2 * KT_VBLK); u32x2 kr = lld<u32x2>(rb + TILE_B);
    Pipe px, py;
    { u32 z = 0u; asm volatile("" : "+v"(z));
      px.e[0] = px.e[1] = px.e[2] = px.e[3] = asf(z); px.p1 = u32x4{z, z, z, z}; py = px; }
    asm volatile("" : "+v"(px.p1), "+v"(px.e[0]), "+v"(px.e[1]), "+v"(px.e[2]), "+v"(px.e[3]), "+v"(qmx), "+v"(qrx));
    asm volatile("s_nop 1" : "+v"(py.p1), "+v"(py.e[0]), "+v"(py.e[1]), "+v"(py.e[2]), "+v"(py.e[3]), "+v"(qmy), "+v"(qry));
    QK1(ox, px.p1, px.p1); QK1(oy, px.p1, px.p1);
    QK1(sBx, k0m, qmx); QK1(sBy, k0m, qmy); QK2(sBx, k0r, qrx); QK2(sBy, k0r, qry);
    QK1(sAx, kom, qmx); QK1(sAy, kom, qmy);
    asm volatile("s_nop 7\n\ts_nop 1" : "+v"(sBx), "+v"(sBy));
    { const float mx = rowmax(sBx), my = rowmax(sBy);
      if (h == 0) { qrx[1] = pk(-mx, 0.f); qry[1] = pk(-my, 0.f); } }
    asm volatile("s_nop 1" : "+v"(qrx), "+v"(qry));
    QK2(sAx, kor, qrx); QK2(sAy, kor, qry);
    asm volatile("s_nop 7\n\ts_nop 3" : "+v"(sAx), "+v"(sAy));
    if (rep == 1) { CSTAMPW(1, 4); CSTAMPW(9, 0); }
    int slot = 0;
    const u64 want = ((u64)c.ehi << 32) | c.elo;
    const u32 fvo = (lane & 31) * 128;
    u64 fl = want;
    if (c.wave == 0) flag_load(fl, ~want, fvo, c.srd, WS_FLAG + (u32)(ph * 256 + ((ch0 + 2) & 7) * 32) * 128);
#pragma unroll 1
    for (int n = 0; n < 8; ++n) {
        const int nslot = slot == 2 ? 0 : slot + 1;
        LAS char* kc = kb + slot * CHUNK_B; LAS char* rc = rb + slot * CHUNK_B; LAS char* vc = vb + slot * CHUNK_B;
        LAS char* kn = kb + nslot * CHUNK_B; LAS char* rn = rb + nslot * CHUNK_B; LAS char* vn = vb + nslot * CHUNK_B;
#define A_TILE(U, SCX, SNX, SCY, SNY, X1, X2) attn_step2(SCX, SNX, ox, px, SCY, SNY, oy, py, km, kr, v0, v1, qmx, qrx, qmy, qry, \
            [&] { km = lld<u32x4>((U) < 2 ? kc + ((U) + 2) * TILE_B : kn + ((U) - 2) * TILE_B); X1; }, \
            [&] { kr = lld<u32x2>((U) < 2 ? rc + ((U) + 2) * TILE_B : rn + ((U) - 2) * TILE_B); }, \
            [&] { v1 = lld<u32x4>(vc + (U) * TILE_B + 2 * KT_VBLK); X2; }, \
            [&] { v0 = lld<u32x4>((U) < 3 ? vc + ((U) + 1) * TILE_B : vn); })
        A_TILE(0, sAx, sBx, sAy, sBy, (void)0, (void)0); A_TILE(1, sBx, sAx, sBy, sAy, (void)0, (void)0);
        if (__builtin_expect(c.wave < 4, 0)) {
            asm volatile("s_waitcnt vmcnt(0)" : "+v"(fl) :: "memory");
            if (__builtin_expect(!__all(fl == want), 0)) poll(c, ph, chunk_ids(n + 2));
        }
        BAR();
        if (__builtin_expect(c.wave < 4, 0)) {
            fl = want;
            if (c.wave == 0 && n + 3 < 8) flag_load(fl, ~want, fvo, c.srd, WS_FLAG + (u32)(ph * 256 + ((ch0 + n + 3) & 7) * 32) * 128);
            if (n + 2 < 8) { issue_piece(n + 2, 0); issue_piece(n + 2, 1); issue_piece(n + 2, 2); issue_piece(n + 2, 3); issue_piece(n + 2, 4); }
            if (n >= 5) load_wimage_o4(c, 0, n - 5);
            if (rep == 0 && n == 0) {
                u32 z0 = 0u; asm volatile("" : "+v"(z0));
                const u32x4 z = {z0, z0, z0, z0};
#pragma unroll
                for (int q = 0; q < 5; ++q)
                    st128(c.rs, z, lane * 16 + q * 1024, WS_KVB + (u32)((4 * c.j + c.wave) * 64 + 2 * c.i) * TILE_B);
                if (c.wave < 2) st128(c.rs, z, (u32)((16 + (c.j >> 2)) * 4 + (lane >> 4)) * 1024 + ((c.j & 3) * 16 + (lane & 15)) * 16, WS_OF + c.wave * XF_BYTES + (u32)(c.i * 72) * 1024);
            }
        }
        A_TILE(2, sAx, sBx, sAy, sBy, (void)0, (void)0); A_TILE(3, sBx, sAx, sBy, sAy, (void)0, (void)0);
#undef A_TILE
        slot = nslot;
    }
    if (rep == 1) { CSTAMPW(2, 4); CSTAMPW(10, 0); }
    attn_finish2(ox, px, oy, py, v1);
    { LAS char* mine = c.L + L_RING + kh * 2 * CHUNK_B + a * 3072 + lane * 16;
#pragma unroll
      for (int e = 0; e < 3; ++e) lst(mine + e * 1024, f32x4{oy[4 * e], oy[4 * e + 1], oy[4 * e + 2], oy[4 * e + 3]}); }
    st.o = ox; st.qm = qmx; st.qr = qrx;
}

template <bool ISB, class HK>
DI void out_proj(const Ctx& c0, const Params& p, int rep, u32 (&xres)[3][2], HK behind_x) {
    const Ctx c = fresh(c0);
    const int lane = c.lane, m = c.wave & 3, kh = c.wave >> 2, tok = lane & 15, g = lane >> 4;
    const int layer = rep * 2 + (ISB ? 1 : 0);
    wait_group(c, rep * 6 + (ISB ? 4 : 1));
    STAMP((ISB ? 25 : 10) + rep * 30);
    u32x4 xf[9]; f32x4 acc[5] = {};
    load_xfrags(c, WS_OF + (rep * RP * 2 + (ISB ? 1 : 0)) * XF_BYTES, xf);
    __builtin_amdgcn_sched_barrier(0);
    behind_x(c);
    if (ISB) {
        asm volatile("s_waitcnt vmcnt(9)" ::: "memory"); BAR();
    }
    g1_mma<false>(c, xf, acc, [](int, const u32x4 (&)[5]) {}, [](int) {});
    STAMP((ISB ? 26 : 11) + rep * 30);
    g1_exchange(c, acc);
    STAMP((ISB ? 27 : 12) + rep * 30);
#pragma unroll
    for (int u = 0; u < 3; ++u) {
        const float v0 = bflo(xres[u][0]) + acc[u][0], v1 = bfhi(xres[u][0]) + acc[u][1], v2 = bflo(xres[u][1]) + acc[u][2], v3 = bfhi(xres[u][1]) + acc[u][3];
        xres[u][0] = pk(v0, v1); xres[u][1] = pk(v2, v3);
    }
    if (ISB && rep == 1) {
        char* o = (char*)p.out + (size_t)(c.i * 64 + m * 16 + tok) * (DM * 2) + (c.j * 72 + 4 * g) * 2;
        *(u32x2*)(o + (2 * kh) * 32) = u32x2{xres[0][0], xres[0][1]};
        *(u32x2*)(o + (2 * kh + 1) * 32) = u32x2{xres[1][0], xres[1][1]};
        if (kh == 1 && g < 2) *(u32x2*)(o + 128) = u32x2{xres[2][0], xres[2][1]};
        return;
    }
    const int xi = layer == 0 ? 0 : (layer == 1 ? 1 : 2 * RP);
    const u32 so = WS_XF + xi * XF_BYTES + (u32)(c.i * 18 * 4 + m) * 1024;
    st128(c.rs, u32x4{xres[0][0], xres[0][1], xres[1][0], xres[1][1]}, lane * 16, so + (u32)(2 * c.j + kh) * 4096);
    if (kh == 1 && g < 2) st64(c.rs, u32x2{xres[2][0], xres[2][1]}, ((c.j & 3) * 16 + tok) * 16 + 8 * g, so + (u32)(16 + (c.j >> 2)) * 4096);
    __builtin_amdgcn_sched_barrier(0);
    float s1 = 0.f, s2 = 0.f;
#pragma unroll
    for (int u = 0; u < 3; ++u) {
        u32 d0 = xres[u][0], d1 = xres[u][1];
        asm volatile("" : "+v"(d0), "+v"(d1));
        if (u == 2 && !(kh == 1 && g < 2)) { d0 = 0u; d1 = 0u; }
        const float a0 = bflo(d0), a1 = bfhi(d0), a2 = bflo(d1), a3 = bfhi(d1);
        s1 += (a0 + a1) + (a2 + a3); s2 += (a0 * a0 + a1 * a1) + (a2 * a2 + a3 * a3);
    }
    s1 = sum_g(s1); s2 = sum_g(s2);
    if (g == 0) st64(c.rs, u32x2{asu(s1), asu(s2)}, (u32)((c.i * 16 + 2 * c.j + kh) * 64 + m * 16 + tok) * 8, WS_ST + xi * ST_BYTES);
    publish(c, rep * 6 + (ISB ? 5 : 2));
    if (!ISB && rep == 0) colsums_b(c);
    if (ISB) load_wimage_part<36, 90>(c, WS_WIMGA, 1, L_A1);
}

DI void a_tail(const Ctx& c0, const Params& p, int rep, ATail& st, u32 (&xres)[3][2], u32x4 (&w0)[WRING], u32x4 (&w1)[WRING]) {
    const Ctx c = fresh(c0);
    const int lane = c.lane, a = c.wave & 3, kh = c.wave >> 2;
    BAR();
    LAS char* theirs = c.L + L_RING + (1 - kh) * 2 * CHUNK_B + a * 3072 + lane * 16;
#pragma unroll
    for (int e = 0; e < 3; ++e) { const f32x4 v = lld<f32x4>(theirs + e * 1024); st.o[4 * e] += v[0]; st.o[4 * e + 1] += v[1]; st.o[4 * e + 2] += v[2]; st.o[4 * e + 3] += v[3]; }
    STAMP(8 + rep * 30); if (rep == 0) CSTAMP(62);
    if (rep == 1) { CSTAMPW(3, 4); CSTAMPW(11, 0); }
    if (attn_unsafe(st.o)) st.o = attn_slow(c, WS_KVA + rep * KVA_BYTES, 0, 63, false, st.qm, st.qr);
    store_o(c, WS_OF + (rep * RP * 2) * XF_BYTES, st.o, a, kh);
    if (rep == 1) { CSTAMPW(4, 4); CSTAMPW(12, 0); }
    publish(c, rep * 6 + 1);
    load_wring(c, w0, w1);
    STAMP(9 + rep * 30);
    out_proj<false>(c, p, rep, xres, [&](const Ctx& cc) {
        if (rep == 0) {
            const int m = cc.wave & 3, k2 = cc.wave >> 2, tok = cc.lane & 15, g = cc.lane >> 4;
            const char* rr = (const char*)p.x + (size_t)(cc.i * 64 + m * 16 + tok) * (DM * 2) + (cc.j * 72 + 4 * g) * 2;
            const u32x2 r0 = *(const u32x2*)(rr + (2 * k2) * 32), r1 = *(const u32x2*)(rr + (2 * k2 + 1) * 32), r2 = *(const u32x2*)(rr + (g < 2 ? 128 : 0));
            xres[0][0] = r0[0]; xres[0][1] = r0[1]; xres[1][0] = r1[0]; xres[1][1] = r1[1]; xres[2][0] = r2[0]; xres[2][1] = r2[1];
        }
    });
}

struct T_ { static constexpr bool value = true; };
struct F_ { static constexpr bool value = false; };
DI void b_proj(const Ctx& c0, const Params& p, int rep, u32x4 (&w0)[WRING], u32x4 (&w1)[WRING], u32x4& qm, u32x2& qr) {
    const Ctx c = fresh(c0);
    const int lane = c.lane, tok = lane & 15, g = lane >> 4, a = c.wave >> 1, odd = c.wave & 1;
    wait_group(c, rep * 6 + 2);
    STAMP(15 + rep * 30);
    const int xi = rep == 0 ? 0 : 2 * RP;
    u32x4 stv[4];
    if (c.wave == 5 || c.wave == 7) stats_issue32(c, WS_ST + xi * ST_BYTES, stv);
    { const u32 so = WS_XF + xi * XF_BYTES + (u32)(c.i * 72) * 1024;
#pragma unroll
      for (int n = 0; n < 9; ++n) glds_ws(c.srd, lane * 16, so + (c.wave + 8 * n) * 1024, c.Lb + L_XB + (c.wave + 8 * n) * 1024); }
    asm volatile("s_waitcnt vmcnt(5)" ::: "memory"); BAR();
    STAMP(16 + rep * 30);
    f32x4 a0[4] = {}, a1[4] = {};
    const u32 wso = WS_WF + (u32)((c.j * 8 + c.wave) * 2) * 18 * 1024;
    LAS char* xb = c.L + L_XB + lane * 16;
    const bool t0 = !odd, t1 = !odd || a == 0, has1 = !odd || a < 2;
    auto body = [&](auto T0, auto T1, auto H1) {
        u32x4 x[2][4];
#pragma unroll
        for (int m = 0; m < 4; ++m) { x[0][m] = lld<u32x4>(xb + m * 1024); asm volatile("" : "+v"(a0[m]), "+v"(a1[m])); }
        if (!H1.value) stats_finish32(c, stv);
#pragma unroll
        for (int s = 0; s < 18; ++s) {
            if (s == 7) {
                if (H1.value) asm volatile("s_waitcnt vmcnt(14)" ::: "memory"); else asm volatile("s_waitcnt vmcnt(7)" ::: "memory");
                static_assert(18 - WRING >= 7, "steps 0..6 each refill the ring");
                BAR();
            }
            if (s + 1 < 18) {
#pragma unroll
                for (int m = 0; m < 4; ++m) x[(s + 1) & 1][m] = lld<u32x4>(xb + ((s + 1) * 4 + m) * 1024);
            }
#pragma unroll
            for (int m = 0; m < 4; ++m) {
                if (T0.value) MF16(a0[m], w0[s % WRING], x[s & 1][m]); else MF16(a0[m], x[s & 1][m], w0[s % WRING]);
                if (H1.value) { if (T1.value) MF16(a1[m], w1[s % WRING], x[s & 1][m]); else MF16(a1[m], x[s & 1][m], w1[s % WRING]); }
            }
            if (s + WRING < 18) { w0[s % WRING] = ld128(c.rs, lane * 16, wso + (WRING + s) * 1024); if (H1.value) w1[s % WRING] = ld128(c.rs, lane * 16, wso + (18 + WRING + s) * 1024); }
        }
        asm volatile("s_nop 7" : "+v"(a0[0]), "+v"(a0[1]), "+v"(a0[2]), "+v"(a0[3]), "+v"(a1[0]), "+v"(a1[1]), "+v"(a1[2]), "+v"(a1[3]));
    };
    if (!odd) body(T_{}, T_{}, T_{}); else if (a == 0) body(F_{}, T_{}, T_{}); else if (a == 1) body(F_{}, F_{}, T_{}); else body(F_{}, F_{}, F_{});
    (void)t0; (void)t1; (void)has1;
    STAMP(17 + rep * 30);
    const u32 kvb = WS_KVB + rep * RP * KVB_BYTES;
    const u32 hb = kvb + (u32)((4 * c.j + a) * 64 + 2 * c.i) * TILE_B;
    auto fin_t = [&](const f32x4 (&acc)[4], int slot, float sc, u32 (&d)[4][2]) {
        const f32x4 cs = lld<f32x4>(c.L + L_CSB + (c.wave * 2 + slot) * 64 + g * 16);
#pragma unroll
        for (int m = 0; m < 4; ++m) {
            const float mean = lld<float>(c.L + L_MEAN + (m * 16 + tok) * 4), rs = lld<float>(c.L + L_MEAN + 256 + (m * 16 + tok) * 4) * sc;
            d[m][0] = pk((acc[m][0] - mean * cs[0]) * rs, (acc[m][1] - mean * cs[1]) * rs);
            d[m][1] = pk((acc[m][2] - mean * cs[2]) * rs, (acc[m][3] - mean * cs[3]) * rs);
        }
    };
    auto fin_p = [&](const f32x4 (&acc)[4], int slot, u32 (&d)[4][2]) {
        const float cs = lld<float>(c.L + L_CSB + (c.wave * 2 + slot) * 64 + tok * 4);
#pragma unroll
        for (int m = 0; m < 4; ++m) {
            const f32x4 mean = lld<f32x4>(c.L + L_MEAN + (m * 16 + 4 * g) * 4), rs = lld<f32x4>(c.L + L_MEAN + 256 + (m * 16 + 4 * g) * 4);
            d[m][0] = pk((acc[m][0] - mean[0] * cs) * rs[0], (acc[m][1] - mean[1] * cs) * rs[1]);
            d[m][1] = pk((acc[m][2] - mean[2] * cs) * rs[2], (acc[m][3] - mean[3] * cs) * rs[3]);
        }
    };
    auto frag16 = [&](const u32 (&d)[4][2], int t) {
        auto x = __builtin_amdgcn_permlane16_swap(d[2 * t][0], d[2 * t + 1][0], false, false);
        auto y = __builtin_amdgcn_permlane16_swap(d[2 * t][1], d[2 * t + 1][1], false, false);
        return u32x4{(u32)x[0], (u32)y[0], (u32)x[1], (u32)y[1]};
    };
    auto frag32 = [&](const u32 (&d)[4][2], int t) {
        auto x = __builtin_amdgcn_permlane32_swap(d[2 * t][0], d[2 * t + 1][0], false, false);
        auto y = __builtin_amdgcn_permlane32_swap(d[2 * t][1], d[2 * t + 1][1], false, false);
        return u32x4{(u32)x[0], (u32)y[0], (u32)x[1], (u32)y[1]};
    };
    u32 d[4][2];
    LAS char* lh = c.L + L_KVB + a * 25600 + 8 * TILE_B; LAS char* l0 = c.L + L_KVB + 8 * TILE_B;
    if (!odd) {
        fin_t(a1, 1, 1.f, d);
        { const u32x4 f0 = frag16(d, 0), f1 = frag16(d, 1);
          st128(c.rs, f0, lane * 16, hb); st128(c.rs, f1, lane * 16, hb + TILE_B);
          lst(lh + lane * 16, f0); lst(lh + TILE_B + lane * 16, f1); }
        fin_t(a0, 0, QSCALE, d);
        qm = frag16(d, 0);
        lst(c.L + L_QT1 + a * 1024 + lane * 16, frag16(d, 1));
    } else {
        fin_p(a0, 0, d);
        { const u32x4 f0 = frag32(d, 0), f1 = frag32(d, 1);
          st128(c.rs, f0, KT_V + g * KT_VBLK + tok * 16, hb); st128(c.rs, f1, KT_V + g * KT_VBLK + tok * 16, hb + TILE_B);
          lst(lh + KT_V + g * KT_VBLK + tok * 16, f0); lst(lh + TILE_B + KT_V + g * KT_VBLK + tok * 16, f1); }
        if (a == 0) {
            fin_t(a1, 1, g < 2 ? QSCALE : 1.f, d);
#pragma unroll
            for (int m = 0; m < 4; ++m) {
                if (g < 2) { lst(c.L + L_QREM + (2 * g) * 256 + (m * 16 + tok) * 4, d[m][0]); lst(c.L + L_QREM + (2 * g + 1) * 256 + (m * 16 + tok) * 4, d[m][1]); }
                else {
                    const u32 hb0 = kvb + (u32)(4 * c.j * 64 + 2 * c.i + (m >> 1)) * TILE_B;
                    st64(c.rs, u32x2{d[m][0], 0x00003f80u}, (u32)(2 * (g - 2)) * 64 * TILE_B + KT_KREM + ((m & 1) * 16 + tok) * 8, hb0);
                    st64(c.rs, u32x2{d[m][1], 0x00003f80u}, (u32)(2 * (g - 2) + 1) * 64 * TILE_B + KT_KREM + ((m & 1) * 16 + tok) * 8, hb0);
                    LAS char* lk = l0 + (2 * (g - 2)) * 25600 + (m >> 1) * TILE_B + KT_KREM + ((m & 1) * 16 + tok) * 8;
                    lst(lk, u32x2{d[m][0], 0x00003f80u}); lst(lk + 25600, u32x2{d[m][1], 0x00003f80u});
                }
            }
        } else if (a == 1) {
            fin_p(a1, 1, d);
            const u32 hb0 = kvb + (u32)(4 * c.j * 64 + 2 * c.i) * TILE_B;
            const u32 vo = (u32)(tok >> 1) * 64 * TILE_B + KT_V + g * KT_VBLK + (16 + (tok & 1)) * 16;
            const u32x4 f0 = frag32(d, 0), f1 = frag32(d, 1);
            if (tok < 8) {
                st128(c.rs, f0, vo, hb0); st128(c.rs, f1, vo, hb0 + TILE_B);
                LAS char* lv = l0 + (tok >> 1) * 25600 + KT_V + g * KT_VBLK + (16 + (tok & 1)) * 16;
                lst(lv, f0); lst(lv + TILE_B, f1);
            }
        } else if (a == 2) {
            const u32 hb0 = kvb + (u32)(4 * c.j * 64 + 2 * c.i) * TILE_B;
            if (lane < 32) {
                const u32x4 on = ones4();
                st128(c.rs, on, (u32)(lane >> 3) * 64 * TILE_B + ((lane >> 2) & 1) * TILE_B + KT_V + (lane & 3) * KT_VBLK + 18 * 16, hb0);
                lst(l0 + (lane >> 3) * 25600 + ((lane >> 2) & 1) * TILE_B + KT_V + (lane & 3) * KT_VBLK + 18 * 16, on);
            }
        }
    }
    publish(c, rep * 6 + 3);
    load_wimage_part<44, 90>(c, WS_WIMG + 8 * WIMG_BYTES, 1);
    if (odd) qm = lld<u32x4>(c.L + L_QT1 + a * 1024 + lane * 16);
    const u32 rem = lld<u32>(c.L + L_QREM + a * 256 + (32 * odd + (lane & 31)) * 4);
    qr = u32x2{lane < 32 ? rem : 0u, 0u};
}

DI void b_attn(const Ctx& c0, const Params& p, int rep, u32x4 qm, u32x2 qr) {
    const Ctx c = fresh(c0);
    const int lane = c.lane, r = lane & 31, h = lane >> 5, a = c.wave >> 1, t = c.wave & 1;
    const int qt = 2 * c.i + t;
    const f32x16 cd = mask_c(lane, true);
    if (c.wave == 1) {
        const f32x16 cm = mask_c(lane, false);
#pragma unroll
        for (int e = 0; e < 4; ++e) lst(c.L + L_MSK + e * 1024 + lane * 16, f32x4{cm[4 * e], cm[4 * e + 1], cm[4 * e + 2], cm[4 * e + 3]});
    }
    LAS char* tb = c.L + L_KVB + a * 25600 + (8 + t) * TILE_B;
    LAS char* kb = tb + lane * 16; LAS char* rb = tb + KT_KREM + r * 8; LAS char* vb = tb + KT_V + h * KT_VBLK + vrow_of(r) * 16;
    const u32x4 k0m = lld<u32x4>(kb); const u32x2 k0r = lld<u32x2>(rb);
    u32x4 km, v0 = lld<u32x4>(vb), v1 = lld<u32x4>(vb + 2 * KT_VBLK); u32x2 kr;
    f32x16 sA, sB, o;
    Pipe pp;
    { u32 z = 0u; asm volatile("" : "+v"(z)); pp.e[0] = pp.e[1] = pp.e[2] = pp.e[3] = asf(z); pp.p1 = u32x4{z, z, z, z}; }
    f32x16 cdp = cd;
    asm volatile("s_nop 1" : "+v"(pp.p1), "+v"(pp.e[0]), "+v"(pp.e[1]), "+v"(pp.e[2]), "+v"(pp.e[3]), "+v"(qm), "+v"(qr), "+v"(cdp));
    QK1(o, pp.p1, pp.p1);
    QK1C(sA, k0m, qm, cdp);
    asm volatile("v_mfma_f32_32x32x8_bf16 %0, %1, %2, %3" : "=&v"(sB) : "v"(k0r), "v"(qr), "v"(sA));
    asm volatile("s_nop 7\n\ts_nop 3" : "+v"(sB), "+v"(sA));
    { const float m0 = rowmax(sB); if (h == 0) qr[1] = pk(-m0, 0.f); }
    asm volatile("s_nop 1" : "+v"(qr));
    QK2(sA, k0r, qr);
    asm volatile("s_nop 7\n\ts_nop 3" : "+v"(sA), "+v"(o), "+v"(v0), "+v"(v1));
    if (c.wave == 0) poll(c, rep * 6 + 3, (lane < 5 && c.i - lane >= 0) ? (c.i - lane) * 8 + c.j : -1);
    BAR();
    STAMP(20 + rep * 30);
    const u32 hb = WS_KVB + rep * RP * KVB_BYTES + (u32)((4 * c.j + a) * 64) * TILE_B;
    { const u32 dst = c.Lb + L_KVB + a * 25600;
#pragma unroll
      for (int n = 0; n < 10; ++n) {
          const int pc = 19 - t - 2 * n;
          u32 vo = lane * 16;
          if (c.i < 4) { const int b = pc * 1024 + lane * 16, tau = b / 2560, kt = 2 * c.i - 8 + tau; vo = (u32)((kt < 0 ? 0 : kt) * 2560 + (b - tau * 2560)); glds_ws(c.srd, vo, hb, dst + pc * 1024); }
          else glds_ws(c.srd, vo, hb + (u32)(2 * c.i - 8) * 2560 + pc * 1024, dst + pc * 1024);
      } }
    asm volatile("s_waitcnt vmcnt(5)" ::: "memory");
    BAR();
    STAMP(21 + rep * 30);
    km = lld<u32x4>(kb - TILE_B); kr = lld<u32x2>(rb - TILE_B);
    auto first = [](f32x16& s, u32x4 k, u32x4 q) { QK1(s, k, q); };
    auto firstm = [&](f32x16& s, u32x4 k, u32x4 q) {
        f32x16 co;
#pragma unroll
        for (int e = 0; e < 4; ++e) { const f32x4 v = lld<f32x4>(c.L + L_MSK + e * 1024 + lane * 16); co[4 * e] = v[0]; co[4 * e + 1] = v[1]; co[4 * e + 2] = v[2]; co[4 * e + 3] = v[3]; }
        QK1C(s, k, q, co); };
    const u32 qoff = h == 0 ? pk(NEG_BIG, 0.f) : 0u;
    kb -= 8 * TILE_B; rb -= 8 * TILE_B; vb -= 8 * TILE_B;
#define B_TILE(U, SC, SN, FIRST) do { u32x2 qs = {qr[0], (U) + 1 <= qt ? qr[1] : qoff}; asm volatile("" : "+v"(qs)); \
        attn_step<((U) < 8)>(SC, SN, o, pp, km, kr, v0, v1, qm, qs, FIRST, \
            [&] { if ((U) + 2 <= 8) km = lld<u32x4>(kb + (8 - ((U) + 2)) * TILE_B); }, [&] { if ((U) + 2 <= 8) kr = lld<u32x2>(rb + (8 - ((U) + 2)) * TILE_B); }, \
            [&] { v1 = lld<u32x4>(vb + (8 - (U)) * TILE_B + 2 * KT_VBLK); }, [&] { if ((U) + 1 <= 8) v0 = lld<u32x4>(vb + (8 - ((U) + 1)) * TILE_B); }); } while (0)
    B_TILE(0, sA, sB, first); B_TILE(1, sB, sA, first);
    DRAIN();
    B_TILE(2, sA, sB, first);
    BAR();
    B_TILE(3, sB, sA, first); B_TILE(4, sA, sB, first); B_TILE(5, sB, sA, first); B_TILE(6, sA, sB, first); B_TILE(7, sB, sA, firstm); B_TILE(8, sA, sB, first);
#undef B_TILE
    attn_finish(o, pp, v1);
    STAMP(22 + rep * 30);
    if (attn_unsafe(o)) o = attn_slow(c, hb, qt - 8 < 0 ? 0 : qt - 8, qt, true, qm, qr);
    store_o(c, WS_OF + (rep * RP * 2 + 1) * XF_BYTES, o, a, t);
    publish(c, rep * 6 + 4);
    load_wimage_part<0, 44>(c, WS_WIMG + 8 * WIMG_BYTES, 1);
    if (rep == 0) load_wimage_part<0, 36>(c, WS_WIMGA, 1, L_A1);
}

__global__ void __launch_bounds__(512, 2) fused_block_kernel(Params p) {
    __shared__ __attribute__((aligned(16))) char lds_[LDS_BYTES];
    Ctx c;
    c.L = (LAS char*)lds_; c.Lb = (u32)(size_t)c.L;
    c.ws = p.ws;
    c.dgp = (u64*)(p.ws + WS_STAMP + (u32)blockIdx.x * 64);
    c.tid = threadIdx.x; c.lane = c.tid & 63; c.wave = __builtin_amdgcn_readfirstlane(c.tid >> 6);
    { const int wg = blockIdx.x, slot = wg >> 3; c.i = (wg & 7) * 4 + (slot >> 3); c.j = slot & 7; }
    c.rs = __builtin_amdgcn_make_buffer_rsrc((void*)p.ws, 0, WS_USED, 0x00020000);
    { const u64 b = (u64)p.ws; c.srd = u32x4{(u32)b, (u32)(b >> 32) & 0xffffu, WS_USED, 0x00020000u}; }
    STAMP(0);
    u32 warm;
    { u64 pc; u32 len;
      asm volatile("s_getpc_b64 %0\nfbk_code_first:\n\ts_mov_b32 %1, fbk_code_last-fbk_code_first" : "=s"(pc), "=s"(len));
      u32 off = ((u32)(blockIdx.x >> 3) * 8 + c.wave) * 256 + c.lane * 4;
      off = off < len ? off : 0u;
      warm = *(const GAS u32*)((pc & ~3ull) + off); }
    c.elo = 0; c.ehi = 0;
    u32 xres[3][2];
    STAMP(1);
    a_proj<0>(c, p);
    asm volatile("" :: "v"(warm));
    STAMP(5);
#pragma unroll 1
    for (int rep = 0; rep < 2; ++rep) {
        u32x4 w0[WRING], w1[WRING]; ATail st;
        a_attn(c, p, rep, st);
        if (rep == 0) {
            if (blockIdx.x == 0 && c.tid == 0) __hip_atomic_store((GAS u64*)(p.ws + WS_EPOCH), ((u64)c.ehi << 32) | c.elo, __ATOMIC_RELAXED, __HIP_MEMORY_SCOPE_AGENT);
        }
        a_tail(c, p, rep, st, xres, w0, w1);
        STAMP(14 + rep * 30);
        u32x4 qm; u32x2 qr;
        b_proj(c, p, rep, w0, w1, qm, qr);
        STAMP(19 + rep * 30);
        b_attn(c, p, rep, qm, qr);
        STAMP(24 + rep * 30);
        out_proj<true>(c, p, rep, xres, [](const Ctx&) {});
        STAMP(29 + rep * 30);
        if (rep == 1) break;
        STAMP(31);
        a_proj<1>(c, p);
        STAMP(35);
    }
    {
        DRAIN(); BAR();
        const u64 ta = __hip_atomic_load((GAS u64*)&c.dgp[0], __ATOMIC_RELAXED, __HIP_MEMORY_SCOPE_AGENT), tb = __hip_atomic_load((GAS u64*)&c.dgp[1], __ATOMIC_RELAXED, __HIP_MEMORY_SCOPE_AGENT);
        u64 span = tb - ta; if (span > 10000) span = 10000;
        const u64 t0 = __builtin_amdgcn_s_memrealtime();
        while (__builtin_amdgcn_s_memrealtime() - t0 < span * 0) { __builtin_amdgcn_s_sleep(1); }
    }
    asm volatile("fbk_code_last:");
}

static size_t ws_offset(size_t ws_size) { return (ws_size - WS_USED) & ~(size_t)0x1fffff; }

extern "C" void kernel_launch(void* const* d_in, const int* in_sizes, int n_in, void* d_out, int out_size, void* d_ws, size_t ws_size, hipStream_t stream) {
    Params p;
    p.x = (const u16*)d_in[0]; p.wq = (const u16*)d_in[1]; p.wk = (const u16*)d_in[2]; p.wv = (const u16*)d_in[3]; p.woA = (const u16*)d_in[4];
    p.wqkv = (const u16*)d_in[6]; p.woB = (const u16*)d_in[7]; p.out = (u16*)d_out;
    p.ws = (char*)d_ws + ws_offset(ws_size);
    fused_block_kernel<<<256, 512, 0, stream>>>(p);
}
```

```cpp
#include <hip/hip_runtime.h>
#include <hip/hip_bf16.h>

typedef unsigned short u16;
typedef unsigned u32;
typedef unsigned long long u64;
typedef __bf16 bf16x8 __attribute__((ext_vector_type(8)));
typedef __bf16 bf16x2 __attribute__((ext_vector_type(2)));
typedef short s16x4 __attribute__((ext_vector_type(4)));
typedef float f32x2 __attribute__((ext_vector_type(2)));
typedef float f32x4 __attribute__((ext_vector_type(4)));
typedef float f32x16 __attribute__((ext_vector_type(16)));
typedef u32 u32x2 __attribute__((ext_vector_type(2)));
typedef u32 u32x4 __attribute__((ext_vector_type(4)));
#define LAS __attribute__((address_space(3)))
#define GAS __attribute__((address_space(1)))
#define DI __device__ __forceinline__

constexpr int SEQ = 2048, DM = 576;
constexpr int RP = 0;
constexpr u32 TILE_B = 2560;
constexpr u32 KT_V = 1024, KT_VBLK = 304, KT_KREM = 2240;
constexpr u32 KA_KREM = 1024, KA_V0 = 1280, KA_V8 = 1792, KA_VX = 2304;
constexpr u32 XF_BYTES = 32u * 18 * 4 * 1024;
constexpr u32 WS_EPOCH = 0;
constexpr u32 WS_FLAG = 4096;
constexpr int PH_FMT = 11;
constexpr u32 WS_XF = WS_FLAG + 12u * 256 * 128 + 4096;
constexpr u32 WS_OF = WS_XF + (2 + RP) * XF_BYTES;
constexpr u32 ST_BYTES = 2048u * 16 * 8;
constexpr u32 WS_ST = WS_OF + (2 + 2 * RP) * XF_BYTES;
constexpr u32 KVA_BYTES = 64 * TILE_B;
constexpr u32 WS_KVA = WS_ST + (2 + RP) * ST_BYTES;
constexpr u32 KVB_BYTES = 32 * 64 * TILE_B;
constexpr u32 WS_KVB = WS_KVA + 2 * KVA_BYTES;
constexpr u32 WF_BYTES = 8u * 16 * 18 * 1024;
constexpr u32 WS_WF = WS_KVB + (1 + RP) * KVB_BYTES;
constexpr u32 WS_CSP = WS_WF + WF_BYTES;
constexpr u32 WIMG_BYTES = 90 * 1024;
constexpr u32 WS_WIMG = WS_CSP + 8u * 16 * 18 * 64;
constexpr u32 WS_WIMGA = WS_WIMG + 16 * WIMG_BYTES;
constexpr u32 WS_STAMP = WS_WIMGA + 8 * WIMG_BYTES;
constexpr u32 WS_STAMP2 = WS_STAMP + 256u * 64 * 8 + 256u * 8 * 8 * 8;
constexpr u32 WS_USED = WS_STAMP2 + 256u * 16 * 8;
constexpr int L_XCH = 0;
constexpr int L_RING = 0;
constexpr int CHUNK_B = 8 * TILE_B;
constexpr int L_STG = 40960;
constexpr int STG_P = 168;
constexpr int L_XIN = 0;
constexpr int L_XIN2 = 153600;
constexpr int L_FMT = 24576;
constexpr int L_KVB = 0;
constexpr int L_MSK = 102400;
constexpr int L_R1 = 61440;
constexpr int WP = 160;
constexpr int L_XB = 76800;
constexpr int L_A1 = 24576;
constexpr int L_STATP = 153600;
constexpr int L_MEAN = L_STATP + 1024;
constexpr int L_CSA = L_MEAN + 512;
constexpr int L_CSB = L_CSA + 1024;
constexpr int L_QREM = L_CSB + 1024;
constexpr int L_QT1 = L_QREM + 1024;
constexpr int LDS_BYTES = 163840;
static_assert(L_QT1 + 4096 <= LDS_BYTES, "lds");

#define WRING 4
constexpr float QSCALE = 0.23570226039551584f * 1.4426950408889634f;
constexpr float NEG_BIG = -1e30f;
constexpr u32 SPIN_CAP = 1u << 20;
constexpr int AUX_SC1 = 16;

struct Params { const u16* x; const u16* wq; const u16* wk; const u16* wv; const u16* woA; const u16* wqkv; const u16* woB; u16* out; char* ws; };

DI u32 pk(float a, float b) { f32x2 v = {a, b}; return __builtin_bit_cast(u32, __builtin_convertvector(v, bf16x2)); }
DI float asf(u32 d) { return __builtin_bit_cast(float, d); }
DI u32 asu(float f) { return __builtin_bit_cast(u32, f); }
DI float bflo(u32 d) { return __builtin_bit_cast(float, d << 16); }
DI float bfhi(u32 d) { return __builtin_bit_cast(float, d & 0xffff0000u); }
DI bf16x8 asb(u32x4 v) { return __builtin_bit_cast(bf16x8, v); }
DI f32x4 mfma16(u32x4 a, u32x4 b, f32x4 c) { return __builtin_amdgcn_mfma_f32_16x16x32_bf16(asb(a), asb(b), c, 0, 0, 0); }
DI f32x16 mfma32(u32x4 a, u32x4 b, f32x16 c) { return __builtin_amdgcn_mfma_f32_32x32x16_bf16(asb(a), asb(b), c, 0, 0, 0); }
DI void MF16(f32x4& acc, u32x4 a, u32x4 b) { asm volatile("v_mfma_f32_16x16x32_bf16 %0, %1, %2, %0" : "+v"(acc) : "v"(a), "v"(b)); }
#define SETTLE4(x) asm volatile("s_nop 7" : "+v"(x))
template <class T> DI T lld(LAS char* p) { return *(LAS const T*)p; }
template <class T> DI void lst(LAS char* p, T v) { *(LAS T*)p = v; }
DI u32x2 trr(LAS char* p) { return __builtin_bit_cast(u32x2, __builtin_amdgcn_ds_read_tr16_b64_v4i16((LAS s16x4*)p)); }
#define BAR() asm volatile("s_waitcnt lgkmcnt(0)\n\ts_barrier" ::: "memory")
#define DRAIN() asm volatile("s_waitcnt vmcnt(0)" ::: "memory")
typedef __amdgpu_buffer_rsrc_t rsrc_t;
DI u32x4 ld128(rsrc_t rs, u32 voff, u32 soff) { return __builtin_amdgcn_raw_buffer_load_b128(rs, voff, soff, AUX_SC1); }
DI u32x2 ld64(rsrc_t rs, u32 voff, u32 soff) { return __builtin_amdgcn_raw_buffer_load_b64(rs, voff, soff, AUX_SC1); }
DI u32 ld32(rsrc_t rs, u32 voff, u32 soff) { return __builtin_amdgcn_raw_buffer_load_b32(rs, voff, soff, AUX_SC1); }
DI void st128(rsrc_t rs, u32x4 v, u32 voff, u32 soff) { __builtin_amdgcn_raw_buffer_store_b128(v, rs, voff + soff, 0, AUX_SC1); }
DI void st64(rsrc_t rs, u32x2 v, u32 voff, u32 soff) { __builtin_amdgcn_raw_buffer_store_b64(v, rs, voff, soff, AUX_SC1); }
DI void st32(rsrc_t rs, u32 v, u32 voff, u32 soff) { __builtin_amdgcn_raw_buffer_store_b32(v, rs, voff, soff, AUX_SC1); }
DI void glds_ws(u32x4 srd, u32 voff, u32 soff, u32 ldsdst) {
    soff = __builtin_amdgcn_readfirstlane(soff); ldsdst = __builtin_amdgcn_readfirstlane(ldsdst);
    asm volatile("s_mov_b32 m0, %0\n\ts_nop 3\n\tbuffer_load_dwordx4 %1, %2, %3 offen sc1 lds" :: "s"(ldsdst), "v"(voff), "s"(srd), "s"(soff) : "memory");
}
DI void glds_in(const void* g, u32 ldsdst) {
    ldsdst = __builtin_amdgcn_readfirstlane(ldsdst);
    asm volatile("s_mov_b32 m0, %0\n\ts_nop 0\n\tglobal_load_lds_dwordx4 %1, off" :: "s"(ldsdst), "v"(g) : "memory");
}
DI void flag_load(u64& fl, u64 nomatch, u32 voff, u32x4 srd, u32 soff) {
    soff = __builtin_amdgcn_readfirstlane(soff);
    fl = nomatch;
    asm volatile("s_nop 4\n\tbuffer_load_dwordx2 %0, %1, %2, %3 offen sc1" : "+v"(fl) : "v"(voff), "s"(srd), "s"(soff) : "memory");
}
DI float sum_g(float v) {
    auto a = __builtin_amdgcn_permlane32_swap(__builtin_bit_cast(u32, v), __builtin_bit_cast(u32, v), false, false);
    v = __builtin_bit_cast(float, (u32)a[0]) + __builtin_bit_cast(float, (u32)a[1]);
    auto b = __builtin_amdgcn_permlane16_swap(__builtin_bit_cast(u32, v), __builtin_bit_cast(u32, v), false, false);
    return __builtin_bit_cast(float, (u32)b[0]) + __builtin_bit_cast(float, (u32)b[1]);
}
DI float max_h(float v) {
    auto a = __builtin_amdgcn_permlane32_swap(__builtin_bit_cast(u32, v), __builtin_bit_cast(u32, v), false, false);
    return fmaxf(__builtin_bit_cast(float, (u32)a[0]), __builtin_bit_cast(float, (u32)a[1]));
}
DI u32x4 ones4() { u32 o = 0x3f803f80u; asm volatile("" : "+v"(o)); return u32x4{o, o, o, o}; }
DI int crow(int reg, int h) { return (reg & 3) + 8 * (reg >> 2) + 4 * h; }

#define STAMP(n) do {} while (0)
#define CSTAMP(n) do {} while (0)
#define CSTAMPW(n, W) do {} while (0)

struct Ctx {
    LAS char* L; u32 Lb;
    char* ws; rsrc_t rs; u32x4 srd;
    int tid, lane, wave, i, j;
    u32 elo, ehi;
};

DI Ctx fresh(const Ctx& c0) {
    Ctx c = c0;
    asm volatile("" : "+v"(c.lane)); asm volatile("" : "+v"(c.tid));
    asm volatile("" : "+s"(c.wave)); asm volatile("" : "+s"(c.i)); asm volatile("" : "+s"(c.j));
    return c;
}
DI GAS u64* flag_ptr(const Ctx& c, int phase, int id) { return (GAS u64*)(c.ws + WS_FLAG + (u32)(phase * 256 + id) * 128); }
DI void signal(const Ctx& c, int phase) {
    if (c.tid == 0) __hip_atomic_store(flag_ptr(c, phase, c.i * 8 + c.j), ((u64)c.ehi << 32) | c.elo, __ATOMIC_RELAXED, __HIP_MEMORY_SCOPE_AGENT);
}
DI void publish(const Ctx& c, int phase) {
    DRAIN();
    BAR();
    signal(c, phase);
}
DI void poll(const Ctx& c, int phase, int id) {
    const u64 want = ((u64)c.ehi << 32) | c.elo;
    GAS u64* f = flag_ptr(c, phase, id < 0 ? 0 : id);
    for (u32 spins = 0; spins < SPIN_CAP; ++spins) {
        const u64 v = __hip_atomic_load(f, __ATOMIC_RELAXED, __HIP_MEMORY_SCOPE_AGENT);
        if (__all(id < 0 || v == want)) break;
        __builtin_amdgcn_s_sleep(1);
    }
    __builtin_amdgcn_fence(__ATOMIC_ACQUIRE, "wavefront");
}
DI void wait_group(const Ctx& c, int phase) {
    if (c.wave == 0) poll(c, phase, c.lane < 8 ? c.i * 8 + c.lane : -1);
    BAR();
}

DI void load_wimage_a(const Ctx& c, const Params& p) {
    const int jj = c.j, w = c.wave;
    const u16 *pk_ = p.wk, *pv_ = p.wv;
    asm volatile("" : "+s"(pk_), "+s"(pv_));
    const char* xsrc = (const char*)((jj >= 3 && jj < 6) ? pv_ : pk_); const int sub = jj < 6 ? jj % 3 : 0;
    xsrc += sub == 0 ? 0 : (sub == 1 ? 16 : 20);
    const char* msrc = (const char*)p.wq + jj * 144;
    const char* a[5]; u32 st[5];
#pragma unroll
    for (int k = 0; k < 5; ++k) {
        const u32 id = 64 * k + c.lane, row = (id * 6554u) >> 16, ch = id - row * 10;
        const bool ex = ch == 9;
        st[k] = ex ? 32 * 36 : 32 * 1152;
        a[k] = (ex ? xsrc : msrc) + (ex ? row * 36 : row * 1152 + ch * 16);
    }
#pragma unroll
    for (int n = 0; n < 3; ++n) {
        const int G = n < 2 ? (w & 3) + 8 * (w >> 2) + 4 * n : 16 + w;
        if (n < 2 || w < 2) {
#pragma unroll
            for (int k = 0; k < 5; ++k) glds_in(a[k] + (u32)G * st[k], c.Lb + L_R1 + (G * 5 + k) * 1024);
        }
    }
}
DI void load_wimage_o(const Ctx& c, u32 base) {
    const u32 so = base + (u32)c.j * WIMG_BYTES;
    if (c.wave >= 1) {
#pragma unroll
        for (int n = 0; n < 13; ++n)
            if (n < 12 || c.wave < 7) glds_ws(c.srd, c.lane * 16, so + (c.wave - 1 + 7 * n) * 1024, c.Lb + L_R1 + (c.wave - 1 + 7 * n) * 1024);
    }
}
template <int P0, int P1>
DI void load_wimage_part(const Ctx& c, u32 base, int w0, int ldsbase = L_R1) {
    const u32 so = base + (u32)c.j * WIMG_BYTES; const int nw = 8 - w0;
    if (c.wave >= w0) {
#pragma unroll
        for (int n = 0; n < (P1 - P0 + 6) / 7; ++n) {
            const int pc = P0 + (c.wave - w0) + nw * n;
            if (pc < P1) glds_ws(c.srd, c.lane * 16, so + pc * 1024, c.Lb + ldsbase + pc * 1024);
        }
    }
}
DI void load_wimage_o4(const Ctx& c, int which, int part) {
    const u32 so = WS_WIMG + (u32)(which * 8 + c.j) * WIMG_BYTES + (u32)part * 30 * 1024; const u32 dst = c.Lb + L_R1 + part * 30 * 1024;
    if (c.wave < 4) {
#pragma unroll
        for (int n = 0; n < 8; ++n)
            if (n < 7 || c.wave < 2) glds_ws(c.srd, c.lane * 16, so + (c.wave + 4 * n) * 1024, dst + (c.wave + 4 * n) * 1024);
    }
}
struct Fmt { u32x4 v[2]; u32 g[8]; u32 dst[2], cdst[2]; int kind[2]; };
DI void fmt_issue(const Ctx& c, const Params& p, Fmt& m) {
    const u16 *pw = p.wqkv, *pa = p.woA, *pb = p.woB;
    asm volatile("" : "+s"(pw), "+s"(pa), "+s"(pb));
    const int lane = c.lane, rho = lane >> 1, g = rho >> 3, e = rho & 7;
#pragma unroll
    for (int r = 0; r < 2; ++r) {
        const int fr = blockIdx.x * 8 + c.wave + 2048 * r;
        int col, A, P, Q, pitch; const GAS char* src;
        if (fr < 2016) {
            const int jj = fr / 252, rem = fr - jj * 252, u = rem / 18, s = rem - u * 18;
            const int wv = u < 8 ? u : (u - 8 < 5 ? u - 8 : 6), slot = u < 8 ? 0 : 1, a = wv >> 1;
            const bool isrem = (wv & 1) && slot == 1;
            const u32 fo = (u32)(((jj * 8 + wv) * 2 + slot) * 18 + s);
            m.dst[r] = WS_WF + fo * 1024; m.cdst[r] = WS_CSP + fo * 64; m.kind[r] = isrem ? 3 : 1;
            P = s < 16 ? 16 : 4; Q = 2; pitch = 1728 * 2; src = (const GAS char*)pw;
            if (!isrem) {
                col = ((wv & 1) ? 1152 : (slot ? 576 : 0)) + 18 * (4 * jj + a) + 8 * (lane & 1);
                A = s < 16 ? 72 * (s >> 1) + 32 * (s & 1) + 4 * g : 72 * (4 * (s - 16) + g) + 64;
            } else {
                const int n = lane & 15, g2 = lane >> 4;
                col = (a == 0 ? (n < 8 ? 0 : 576) : 1152) + 18 * (4 * jj + ((n & 7) >> 1)) + 16 + (n & 1);
                A = s < 16 ? 72 * (s >> 1) + 32 * (s & 1) + 4 * g2 : 72 * (4 * (s - 16) + g2) + 64;
#pragma unroll
                for (int q = 0; q < 8; ++q) m.g[q] = *(const GAS u16*)(src + (A + P * (q >> 2) + (q & 3)) * pitch + col * 2);
            }
        } else {
            const bool on = fr < 3456;
            const int q = on ? fr - 2016 : 0, im = q / 720, rem = q - im * 720, jj = rem / 90, st = rem - jj * 90, s = st / 5, t = st - s * 5;
            m.dst[r] = WS_WIMG + (u32)(im * 8 + jj) * WIMG_BYTES + (u32)st * 1024; m.cdst[r] = 0; m.kind[r] = on ? 2 : 0;
            col = 72 * jj + 16 * t + (t == 4 ? 0 : 8 * (lane & 1));
            if (s < 16) { A = 36 * s + 18 * (g >> 1) + 4 * (g & 1); P = 8; Q = 2; } else { A = 72 * (4 * (s - 16) + g) + 16; P = 36; Q = 18; }
            src = (const GAS char*)(im ? pb : pa); pitch = 576 * 2;
        }
        if (r == 1 || m.kind[r] != 3) m.v[r] = *(const GAS u32x4*)(src + (A + P * (e >> 2) + Q * ((e >> 1) & 1) + (e & 1)) * pitch + col * 2);
    }
}
DI void fmt_finish(const Ctx& c, Fmt& m) {
    asm volatile("" : "+v"(m.v[0]), "+v"(m.v[1]), "+v"(m.g[0]), "+v"(m.g[1]), "+v"(m.g[2]), "+v"(m.g[3]), "+v"(m.g[4]), "+v"(m.g[5]), "+v"(m.g[6]), "+v"(m.g[7]));
    const int lane = c.lane, i16 = lane & 15, g = lane >> 4, q = i16 >> 2, p = i16 & 3;
#pragma unroll
    for (int r = 0; r < 2; ++r) {
        if (m.kind[r] != 0) {
            u32x4 f;
            if (m.kind[r] == 3) { const u32* w = m.g; f = u32x4{w[0] | (w[1] << 16), w[2] | (w[3] << 16), w[4] | (w[5] << 16), w[6] | (w[7] << 16)}; }
            else {
                LAS char* t = c.L + L_FMT + c.wave * 2048 + r * 1024;
                lst(t + lane * 16, m.v[r]);
                asm volatile("s_waitcnt lgkmcnt(0)" ::: "memory");
                const u32x2 lo = trr(t + (8 * g + q) * 32 + 8 * p), hi = trr(t + (8 * g + 4 + q) * 32 + 8 * p);
                f = u32x4{lo[0], lo[1], hi[0], hi[1]};
            }
            st128(c.rs, f, lane * 16, m.dst[r]);
            if (m.kind[r] != 2) {
                float cs = ((bflo(f[0]) + bfhi(f[0])) + (bflo(f[1]) + bfhi(f[1]))) + ((bflo(f[2]) + bfhi(f[2])) + (bflo(f[3]) + bfhi(f[3])));
                cs = sum_g(cs);
                if (lane < 16) st32(c.rs, asu(cs), lane * 4, m.cdst[r]);
            }
        }
    }
}

template <bool PROJ, bool NAT = false, class HK, class PRE>
DI void g1_mma(const Ctx& c, const u32x4 (&xf)[9], f32x4 (&acc)[5], HK hook, PRE pre) {
    const int lane = c.lane, kh = c.wave >> 2, q = (lane & 15) >> 2, p = lane & 3, g = lane >> 4;
    LAS char *bN, *bR, *bNo, *bNt, *bRo, *bRt;
    if (PROJ) {
        if (NAT) { bN = c.L + L_R1 + (kh * 256 + 4 * g + q) * WP + 8 * p; bR = c.L + L_R1 + (512 + 32 * kh + 8 * g + q) * WP + 8 * p; }
        else { bN = c.L + L_A1 + (kh * 288 + 4 * g + q) * WP + 8 * p; bR = c.L + L_A1 + (kh * 288 + 72 * g + q) * WP + 8 * p; }
        bNo = bN + 64 * kh; bNt = bN + 64 - 64 * kh; bRo = bR + 64 * kh; bRt = bR + 64 - 64 * kh;
    } else {
        bN = c.L + L_R1 + kh * 8 * 5120 + lane * 16; bR = c.L + L_R1 + (16 + kh) * 5120 + lane * 16;
        bNo = bN + 2048 * kh; bNt = bN + 2048 - 2048 * kh; bRo = bR + 2048 * kh; bRt = bR + 2048 - 2048 * kh;
    }
    u32x4 w[2][5];
    auto rd = [&](int sl, u32x4 (&d)[5]) {
#pragma unroll
        for (int u = 0; u < 5; ++u) {
            if (PROJ) {
                const int r0 = NAT ? (sl < 8 ? 32 * sl : 0) : (sl < 8 ? 72 * (sl >> 1) + 32 * (sl & 1) : 64), hs = sl < 8 ? 16 : 4;
                LAS char* bb = u < 2 ? (sl < 8 ? bNo : bRo) + 32 * u : (u == 2 ? (sl < 8 ? bN : bR) + 128 : (sl < 8 ? bNt : bRt) + 32 * (u - 3));
                const u32x2 lo = trr(bb + r0 * WP), hi = trr(bb + (r0 + hs) * WP);
                d[u] = u32x4{lo[0], lo[1], hi[0], hi[1]};
            } else {
                LAS char* bb = u < 2 ? (sl < 8 ? bNo : bRo) + 1024 * u : (u == 2 ? (sl < 8 ? bN : bR) + 4096 : (sl < 8 ? bNt : bRt) + 1024 * (u - 3));
                d[u] = lld<u32x4>(bb + (sl < 8 ? sl * 5120 : 0));
            }
        }
    };
    rd(0, w[0]);
#pragma unroll
    for (int u = 0; u < 5; ++u) asm volatile("" : "+v"(acc[u]));
#pragma unroll
    for (int sl = 0; sl < 9; ++sl) {
        pre(sl);
        if (sl + 1 < 9) rd(sl + 1, w[(sl + 1) & 1]);
#pragma unroll
        for (int u = 0; u < 5; ++u) MF16(acc[u], w[sl & 1][u], xf[sl]);
        hook(sl, w[sl & 1]);
    }
    asm volatile("s_nop 7" : "+v"(acc[0]), "+v"(acc[1]), "+v"(acc[2]), "+v"(acc[3]), "+v"(acc[4]));
}
DI void g1_exchange(const Ctx& c, f32x4 (&acc)[5]) {
    LAS char* xw = c.L + L_XCH + c.wave * 3072 + c.lane * 16;
    lst(xw, acc[3]); lst(xw + 1024, acc[4]); lst(xw + 2048, acc[2]);
    BAR();
    LAS char* xr = c.L + L_XCH + (c.wave ^ 4) * 3072 + c.lane * 16;
    acc[0] += lld<f32x4>(xr); acc[1] += lld<f32x4>(xr + 1024); acc[2] += lld<f32x4>(xr + 2048);
}
DI void load_xfrags(const Ctx& c, u32 buf, u32x4 (&xf)[9]) {
    const int m = c.wave & 3, kh = c.wave >> 2;
    const u32 so = buf + (u32)(c.i * 18 * 4 + m) * 1024;
#pragma unroll
    for (int sl = 0; sl < 9; ++sl) { const int s = sl < 8 ? 8 * kh + sl : 16 + kh; xf[sl] = ld128(c.rs, c.lane * 16, so + (u32)s * 4096); }
}
DI u32x4 stats_issue(const Ctx& c, u32 stbuf) {
    const u32 vo = (u32)((c.i * 16 + 2 * (c.lane >> 3)) * 64 + c.wave * 8 + (c.lane & 7)) * 8;
    const u32x2 a = ld64(c.rs, vo, stbuf), b = ld64(c.rs, vo + 512, stbuf);
    return u32x4{a[0], a[1], b[0], b[1]};
}
DI void stats_finish(const Ctx& c, u32x4 v) {
    const int tok = c.wave * 8 + (c.lane & 7);
    float s = asf(v[0]) + asf(v[2]), s2 = asf(v[1]) + asf(v[3]);
    s += __shfl_xor(s, 8); s2 += __shfl_xor(s2, 8);
    s = sum_g(s); s2 = sum_g(s2);
    if (c.lane < 8) {
        lst(c.L + L_STATP + tok * 8, f32x2{s, s2}); lst(c.L + L_STATP + 512 + tok * 8, f32x2{0.f, 0.f});
        const float mean = s * (1.f / DM), var = s2 * (1.f / DM) - mean * mean;
        lst(c.L + L_MEAN + tok * 4, mean); lst(c.L + L_MEAN + 256 + tok * 4, __builtin_amdgcn_rsqf(fmaxf(var, 0.f) + 1e-5f));
    }
}
DI void stats_issue32(const Ctx& c, u32 stbuf, u32x4 (&v)[4]) {
    const u32 vo = (u32)((c.i * 16 + (c.lane >> 4)) * 64 + (c.wave == 7 ? 32 : 0) + 2 * (c.lane & 15)) * 8;
#pragma unroll
    for (int n = 0; n < 4; ++n) v[n] = ld128(c.rs, vo + n * 4 * 512, stbuf);
}
DI void stats_finish32(const Ctx& c, const u32x4 (&v)[4]) {
    float s[4];
#pragma unroll
    for (int e = 0; e < 4; ++e) s[e] = sum_g((asf(v[0][e]) + asf(v[1][e])) + (asf(v[2][e]) + asf(v[3][e])));
    if (c.lane < 16) {
        const int tok = (c.wave == 7 ? 32 : 0) + 2 * c.lane;
        const float m0 = s[0] * (1.f / DM), m1 = s[2] * (1.f / DM);
        lst(c.L + L_MEAN + tok * 4, f32x2{m0, m1});
        lst(c.L + L_MEAN + 256 + tok * 4, f32x2{__builtin_amdgcn_rsqf(fmaxf(s[1] * (1.f / DM) - m0 * m0, 0.f) + 1e-5f), __builtin_amdgcn_rsqf(fmaxf(s[3] * (1.f / DM) - m1 * m1, 0.f) + 1e-5f)});
    }
}

struct KV { u32x4 km; u32x2 kr; u32x4 v0, v1; };
DI int vrow_of(int r) { return r < 18 ? r : 18; }
DI u32 ka_vrow(int blk, int row) { return row < 8 ? KA_V0 + blk * 128 + row * 16 : (row < 16 ? KA_V8 + blk * 128 + (row - 8) * 16 : KA_VX + blk * 64 + (row - 16) * 16); }
DI KV load_tile_ws(const Ctx& c, u32 tile, bool shared) {
    const int r = c.lane & 31, h = c.lane >> 5;
    KV t;
    if (shared) {
        t.km = ld128(c.rs, c.lane * 16, tile);
        t.kr = ld64(c.rs, KA_KREM + r * 8, tile);
        t.v0 = ld128(c.rs, ka_vrow(h, vrow_of(r)), tile);
        t.v1 = ld128(c.rs, ka_vrow(2 + h, vrow_of(r)), tile);
        return t;
    }
    t.km = ld128(c.rs, c.lane * 16, tile);
    t.kr = ld64(c.rs, KT_KREM + r * 8, tile);
    t.v0 = ld128(c.rs, KT_V + h * KT_VBLK + vrow_of(r) * 16, tile);
    t.v1 = ld128(c.rs, KT_V + (2 + h) * KT_VBLK + vrow_of(r) * 16, tile);
    return t;
}
DI f32x16 mfma32k8(u32x2 a, u32x2 b, f32x16 c) { return __builtin_amdgcn_mfma_f32_32x32x8bf16_1k(__builtin_bit_cast(s16x4, a), __builtin_bit_cast(s16x4, b), c, 0, 0, 0); }
DI f32x16 qk(const KV& t, u32x4 qm, u32x2 qr, f32x16 c0) {
    return mfma32k8(t.kr, qr, mfma32(t.km, qm, c0));
}
DI void QK1(f32x16& s, u32x4 km, u32x4 qm) { asm volatile("v_mfma_f32_32x32x16_bf16 %0, %1, %2, 0" : "=&v"(s) : "v"(km), "v"(qm)); }
DI void QK1C(f32x16& s, u32x4 km, u32x4 qm, const f32x16& c0) { asm volatile("v_mfma_f32_32x32x16_bf16 %0, %1, %2, %3" : "=&v"(s) : "v"(km), "v"(qm), "v"(c0)); }
DI void QK2(f32x16& s, u32x2 kr, u32x2 qr) { asm volatile("v_mfma_f32_32x32x8_bf16 %0, %1, %2, %0" : "+v"(s) : "v"(kr), "v"(qr)); }
DI void PV(f32x16& o, u32x4 v, u32x4 p) { asm volatile("v_mfma_f32_32x32x16_bf16 %0, %1, %2, %0" : "+v"(o) : "v"(v), "v"(p)); }
DI float EX(float x) { float r; asm volatile("v_exp_f32 %0, %1" : "=v"(r) : "v"(x)); return r; }
DI u32 CV(float a, float b) { u32 r; asm volatile("v_cvt_pk_bf16_f32 %0, %1, %2" : "=v"(r) : "v"(a), "v"(b)); return r; }
struct Pipe { float e[4]; u32x4 p1; };
template <bool NEXT = true, class F0, class F1, class F2, class F3, class F4>
DI void attn_step(const f32x16& sc, f32x16& sn, f32x16& o, Pipe& pp, const u32x4& km, const u32x2& kr, const u32x4& v0, const u32x4& v1, u32x4 qm, u32x2 qr, F0 first, F1 LK, F2 LR, F3 LV1, F4 LV0) {
    u32x4 p0; float a0, a1, a2, a3, b0, b1, b2, b3;
    if (NEXT) first(sn, km, qm);
    LK();
    pp.p1[2] = CV(pp.e[0], pp.e[1]); pp.p1[3] = CV(pp.e[2], pp.e[3]);
    a0 = EX(sc[0]); a1 = EX(sc[1]); a2 = EX(sc[2]); a3 = EX(sc[3]);
    if (NEXT) QK2(sn, kr, qr); else asm volatile("s_nop 0");
    LR();
    p0[0] = CV(a0, a1); p0[1] = CV(a2, a3);
    b0 = EX(sc[4]); b1 = EX(sc[5]); b2 = EX(sc[6]); b3 = EX(sc[7]);
    PV(o, v1, pp.p1); LV1();
    p0[2] = CV(b0, b1); p0[3] = CV(b2, b3);
    a0 = EX(sc[8]); a1 = EX(sc[9]); a2 = EX(sc[10]); a3 = EX(sc[11]);
    PV(o, v0, p0); LV0();
    pp.p1[0] = CV(a0, a1); pp.p1[1] = CV(a2, a3);
    pp.e[0] = EX(sc[12]); pp.e[1] = EX(sc[13]); pp.e[2] = EX(sc[14]); pp.e[3] = EX(sc[15]);
}
template <class F1, class F2, class F3, class F4>
DI void attn_step2(const f32x16& scx, f32x16& snx, f32x16& ox, Pipe& px, const f32x16& scy, f32x16& sny, f32x16& oy, Pipe& py,
                   const u32x4& km, const u32x2& kr, const u32x4& v0, const u32x4& v1, u32x4 qmx, u32x2 qrx, u32x4 qmy, u32x2 qry, F1 LK, F2 LR, F3 LV1, F4 LV0) {
    u32x4 p0x, p0y; float a0, a1, a2, a3, b0, b1, b2, b3, c0, c1, c2, c3, d0, d1, d2, d3;
#define GRP(MF, PD0, PD1, E0, E1, E2, E3, D0, D1, D2, D3, SRC, I) MF; PD0 = CV(E0, E1); PD1 = CV(E2, E3); D0 = EX(SRC[I]); D1 = EX(SRC[I + 1]); D2 = EX(SRC[I + 2]); D3 = EX(SRC[I + 3])
    GRP(QK1(snx, km, qmx), px.p1[2], px.p1[3], px.e[0], px.e[1], px.e[2], px.e[3], a0, a1, a2, a3, scx, 0);
    GRP(QK1(sny, km, qmy); LK(), py.p1[2], py.p1[3], py.e[0], py.e[1], py.e[2], py.e[3], b0, b1, b2, b3, scy, 0);
    GRP(QK2(snx, kr, qrx), p0x[0], p0x[1], a0, a1, a2, a3, c0, c1, c2, c3, scx, 4);
    GRP(QK2(sny, kr, qry); LR(), p0y[0], p0y[1], b0, b1, b2, b3, d0, d1, d2, d3, scy, 4);
    GRP(PV(ox, v1, px.p1), p0x[2], p0x[3], c0, c1, c2, c3, a0, a1, a2, a3, scx, 8);
    GRP(PV(oy, v1, py.p1); LV1(), p0y[2], p0y[3], d0, d1, d2, d3, b0, b1, b2, b3, scy, 8);
    GRP(PV(ox, v0, p0x), px.p1[0], px.p1[1], a0, a1, a2, a3, px.e[0], px.e[1], px.e[2], px.e[3], scx, 12);
    GRP(PV(oy, v0, p0y); LV0(), py.p1[0], py.p1[1], b0, b1, b2, b3, py.e[0], py.e[1], py.e[2], py.e[3], scy, 12);
#undef GRP
}
DI void attn_finish(f32x16& o, Pipe& pp, u32x4 v1) {
    asm volatile("s_nop 0");
    pp.p1[2] = CV(pp.e[0], pp.e[1]); pp.p1[3] = CV(pp.e[2], pp.e[3]);
    asm volatile("s_nop 1");
    PV(o, v1, pp.p1);
    asm volatile("s_nop 7\n\ts_nop 3" : "+v"(o));
}
DI void attn_finish2(f32x16& ox, Pipe& px, f32x16& oy, Pipe& py, u32x4 v1) {
    asm volatile("s_nop 0");
    px.p1[2] = CV(px.e[0], px.e[1]); px.p1[3] = CV(px.e[2], px.e[3]);
    py.p1[2] = CV(py.e[0], py.e[1]); py.p1[3] = CV(py.e[2], py.e[3]);
    PV(ox, v1, px.p1);
    asm volatile("s_nop 0");
    PV(oy, v1, py.p1);
    asm volatile("s_nop 7\n\ts_nop 3" : "+v"(ox), "+v"(oy));
}
DI void pexp(const f32x16& s, u32x4& p0, u32x4& p1) {
    float e[16];
#pragma unroll
    for (int r = 0; r < 16; ++r) e[r] = __builtin_amdgcn_exp2f(s[r]);
    p0 = u32x4{pk(e[0], e[1]), pk(e[2], e[3]), pk(e[4], e[5]), pk(e[6], e[7])};
    p1 = u32x4{pk(e[8], e[9]), pk(e[10], e[11]), pk(e[12], e[13]), pk(e[14], e[15])};
}
DI float rowmax(const f32x16& s) {
    float a = fmaxf(fmaxf(s[0], s[1]), s[2]), b = fmaxf(fmaxf(s[3], s[4]), s[5]);
#pragma unroll
    for (int r = 6; r < 16; r += 2) { if (r & 2) a = fmaxf(fmaxf(a, s[r]), s[r + 1]); else b = fmaxf(fmaxf(b, s[r]), s[r + 1]); }
    return max_h(fmaxf(a, b));
}
DI f32x16 mask_c(int lane, bool diag) {
    const int r = lane & 31, h = lane >> 5; f32x16 m;
#pragma unroll
    for (int e = 0; e < 16; ++e) { const bool ok = diag ? (crow(e, h) <= r) : (crow(e, h) > r); m[e] = ok ? 0.f : NEG_BIG; }
    return m;
}
DI f32x16 attn_slow(const Ctx& c, u32 tile0, int n0, int n1, bool masked, u32x4 qm, u32x2 qr) {
    const int h = c.lane >> 5;
    qr[1] = 0u;
    f32x16 o = {}; float m = NEG_BIG;
    for (int n = n0; n <= n1; ++n) {
        const KV t = load_tile_ws(c, tile0 + (u32)n * TILE_B, !masked);
        f32x16 z = {};
        f32x16 s = qk(t, qm, qr, z);
        if (masked && (n == n1 || n == n1 - 8)) {
            const int r = c.lane & 31;
#pragma unroll
            for (int e = 0; e < 16; ++e) { const bool ok = (n == n1) ? (crow(e, h) <= r) : (crow(e, h) > r); s[e] = ok ? s[e] : NEG_BIG; }
        }
        const float mn = fmaxf(m, rowmax(s)), al = __builtin_amdgcn_exp2f(m - mn);
        m = mn;
#pragma unroll
        for (int e = 0; e < 16; ++e) { o[e] *= al; s[e] = s[e] <= NEG_BIG ? NEG_BIG : s[e] - mn; }
        u32x4 p0, p1; pexp(s, p0, p1);
        o = mfma32(t.v0, p0, o); o = mfma32(t.v1, p1, o);
    }
    return o;
}
DI bool attn_unsafe(const f32x16& o) {
    float z = 0.f;
#pragma unroll
    for (int e = 0; e < 10; ++e) z = __builtin_fmaf(o[e], 0.f, z);
    const float l = o[10];
    return __any(!(l > 0x1p-40f && l < 0x1p30f && z == 0.f));
}
DI void store_o(const Ctx& c, u32 obuf, const f32x16& o, int a, int t) {
    const int r = c.lane & 31, h = c.lane >> 5;
    const float inv = __builtin_amdgcn_rcpf(o[10]);
    const u32x4 mainp = {pk(o[0] * inv, o[1] * inv), pk(o[2] * inv, o[3] * inv), pk(o[4] * inv, o[5] * inv), pk(o[6] * inv, o[7] * inv)};
    const int m = 2 * t + (r >> 4), tok = r & 15;
    const u32 so = obuf + (u32)(c.i * 18 * 4) * 1024;
    st128(c.rs, mainp, (u32)((2 * c.j + (a >> 1)) * 4 + m) * 1024 + ((2 * (a & 1) + h) * 16 + tok) * 16, so);
    if (h == 0) st32(c.rs, pk(o[8] * inv, o[9] * inv), (u32)((16 + (c.j >> 2)) * 4 + m) * 1024 + ((c.j & 3) * 16 + tok) * 16 + 4 * a, so);
}

template <int rep>
DI void a_proj(Ctx& c0, const Params& p) {
    Ctx c = fresh(c0);
    const int lane = c.lane, m = c.wave & 3, kh = c.wave >> 2, tok = lane & 15, g = lane >> 4;
    u32x4 xf[9]; f32x4 acc[5] = {};
    f32x4 s1 = {}, s2 = {}, ca = {}, cb = {};
    Fmt fm; u64 e0 = 0; u32x4 stv;
    if (rep == 0) {
        e0 = __hip_atomic_load((GAS u64*)(p.ws + WS_EPOCH), __ATOMIC_RELAXED, __HIP_MEMORY_SCOPE_AGENT);
        const char* xt = (const char*)p.x + (size_t)(c.i * 64) * (DM * 2);
#pragma unroll
        for (int n = 0; n < 8; ++n) {
            const int row = c.wave * 8 + n;
            glds_in(xt + row * (DM * 2) + (lane ^ (row & 15)) * 16, c.Lb + (row < 60 ? L_XIN + row * 1024 : L_XIN2 + (row - 60) * 1024));
        }
        xf[8] = *(const u32x4*)(xt + (m * 16 + tok) * (DM * 2) + (512 + 32 * kh + 8 * g) * 2);
        load_wimage_a(c, p);
        fmt_issue(c, p, fm);
        STAMP(13);
        if (c.wave < 2) { if (fm.kind[0] == 3) asm volatile("s_waitcnt vmcnt(19)" ::: "memory"); else asm volatile("s_waitcnt vmcnt(12)" ::: "memory"); }
        else { if (fm.kind[0] == 3) asm volatile("s_waitcnt vmcnt(14)" ::: "memory"); else asm volatile("s_waitcnt vmcnt(7)" ::: "memory"); }
        BAR();
        STAMP(18);
        asm volatile("" : "+v"(xf[8]), "+v"(e0));
        { const int row = m * 16 + tok, t2 = tok ^ (g >> 1);
          LAS char* xb = c.L + (row < 60 ? L_XIN + row * 1024 : L_XIN2 + (row - 60) * 1024) + kh * 512 + 8 * (g & 1);
#pragma unroll
          for (int sl = 0; sl < 8; ++sl) {
              const int K = 4 * (sl & 3);
              const u32x2 lo = lld<u32x2>(xb + (sl >> 2) * 256 + ((K ^ t2) * 16)), hi = lld<u32x2>(xb + (sl >> 2) * 256 + (((K + 2) ^ t2) * 16));
              xf[sl] = u32x4{lo[0], lo[1], hi[0], hi[1]};
          } }
        asm volatile("s_nop 1" : "+v"(xf[0]), "+v"(xf[1]), "+v"(xf[2]), "+v"(xf[3]), "+v"(xf[4]), "+v"(xf[5]), "+v"(xf[6]), "+v"(xf[7]), "+v"(xf[8]));
        STAMP(2);
    } else {
        wait_group(c, 5);
        load_xfrags(c, WS_XF + 1 * XF_BYTES, xf);
        stv = stats_issue(c, WS_ST + 1 * ST_BYTES);
        asm volatile("s_waitcnt vmcnt(11)" ::: "memory"); BAR();
        STAMP(32);
    }
    if (rep != 0) { g1_mma<true>(c, xf, acc, [](int, const u32x4 (&)[5]) {}, [](int) {}); stats_finish(c, stv); }
    else {
        u32x4 ones = ones4();
        const int tlc = m < 2 ? 2 * kh + m : 2 * (1 - kh) + (m - 2), q = (lane & 15) >> 2, pp = lane & 3;
        LAS char* bN = c.L + L_R1 + (kh * 256 + 4 * g + q) * WP + 8 * pp + 32 * tlc;
        LAS char* bR = c.L + L_R1 + (512 + 32 * kh + 8 * g + q) * WP + 8 * pp + 32 * tlc;
        asm volatile("" : "+v"(s1), "+v"(s2), "+v"(ca), "+v"(cb), "+v"(ones));
        u32x4 wx[2];
        auto rdx = [&](int sl) {
            const int r0 = sl < 8 ? 32 * sl : 0, hs = sl < 8 ? 16 : 4; LAS char* b = sl < 8 ? bN : bR;
            const u32x2 lo = trr(b + r0 * WP), hi = trr(b + (r0 + hs) * WP);
            wx[sl & 1] = u32x4{lo[0], lo[1], hi[0], hi[1]}; };
        rdx(0);
        g1_mma<true, true>(c, xf, acc, [&](int sl, const u32x4 (&w)[5]) {
            if (sl + 1 < 9) rdx(sl + 1);
            MF16(s1, ones, xf[sl]); MF16(s2, xf[sl], xf[sl]); MF16(cb, w[2], ones); MF16(ca, wx[sl & 1], ones);
        }, [&](int sl) {
            if (sl == 3) {
                if (fm.kind[0] == 3) asm volatile("s_waitcnt vmcnt(9)" ::: "memory"); else asm volatile("s_waitcnt vmcnt(2)" ::: "memory");
                BAR();
            }
        });
        if (c.tid < 180) st128(c.rs, lld<u32x4>(c.L + L_R1 + c.i * 2880 + c.tid * 16), c.tid * 16, WS_WIMGA + (u32)c.j * WIMG_BYTES + (u32)c.i * 2880);
        asm volatile("s_nop 7" : "+v"(s1), "+v"(s2), "+v"(ca), "+v"(cb));
        const int rg = tok & 3;
        float d = rg == 0 ? s2[0] : (rg == 1 ? s2[1] : (rg == 2 ? s2[2] : s2[3]));
        d = sum_g(g == (tok >> 2) ? d : 0.f);
        if (g == 0) lst(c.L + L_STATP + kh * 512 + (m * 16 + tok) * 8, f32x2{s1[0], d});
        const int tl = m < 2 ? 2 * kh + m : 2 * (1 - kh) + (m - 2);
        if (tok == 0) { lst(c.L + L_CSA + (kh * 5 + tl) * 64 + g * 16, ca); if (m == 0) lst(c.L + L_CSA + (kh * 5 + 4) * 64 + g * 16, cb); }
    }
    STAMP(3 + rep * 30);
    g1_exchange(c, acc);
    const f32x2 sa = lld<f32x2>(c.L + L_STATP + (m * 16 + tok) * 8), sb = lld<f32x2>(c.L + L_STATP + 512 + (m * 16 + tok) * 8);
    const float mean = (sa[0] + sb[0]) * (1.f / DM), var = (sa[1] + sb[1]) * (1.f / DM) - mean * mean;
    const float rstd = __builtin_amdgcn_rsqf(fmaxf(var, 0.f) + 1e-5f);
    LAS char* stg = c.L + L_STG + (m * 16 + tok) * STG_P + g * 8;
    auto fin = [&](int u, int t, float sc) {
        const f32x4 cs = lld<f32x4>(c.L + L_CSA + t * 64 + g * 16) + lld<f32x4>(c.L + L_CSA + (5 + t) * 64 + g * 16);
        f32x4 v;
#pragma unroll
        for (int e = 0; e < 4; ++e) v[e] = (acc[u][e] - mean * cs[e]) * sc;
        return u32x2{pk(v[0], v[1]), pk(v[2], v[3])};
    };
    if (kh == 1) {
        const u32x2 d4 = fin(2, 4, g < 2 ? rstd * QSCALE : rstd);
        lst(stg + 128, d4);
        const u32 kva = WS_KVA + rep * KVA_BYTES + (u32)(2 * c.i + (m >> 1)) * TILE_B;
        const int j = c.j, key = (m & 1) * 16 + tok;
        if (j < 2) { if (g >= 2) st64(c.rs, d4, j * 512 + key * 16 + (g - 2) * 8, kva); }
        else if (j == 2) { if (g == 3) st64(c.rs, u32x2{d4[1], 0x00003f80u}, KA_KREM + key * 8, kva); }
        else if (j < 6) {
            asm volatile("s_waitcnt lgkmcnt(0)" ::: "memory");
            const int q = (lane & 15) >> 2, pp = lane & 3;
            LAS char* s = c.L + L_STG + (16 * m + q + 4 * (pp >> 1)) * STG_P + 144 + 8 * (pp & 1);
            const u32x2 lo = trr(s), hi = trr(s + 8 * STG_P);
            const int dl = lane & 7, blk = (m & 1) * 2 + ((lane >> 3) & 1);
            if (j < 5) { if (lane < 16) st128(c.rs, u32x4{lo[0], lo[1], hi[0], hi[1]}, (j == 3 ? KA_V0 : KA_V8) + blk * 128 + dl * 16, kva); }
            else {
                const u32x4 on = ones4();
                const u32x4 v = dl >= 6 ? u32x4{lo[0], lo[1], hi[0], hi[1]} : on;
                if (lane < 16 && (dl >= 6 || dl < 2)) st128(c.rs, v, KA_VX + blk * 64 + (dl >= 6 ? dl - 6 : 2 + dl) * 16, kva);
            }
        }
    }
    lst(stg + (2 * kh) * 32, fin(0, 2 * kh, rstd * QSCALE));
    lst(stg + (2 * kh + 1) * 32, fin(1, 2 * kh + 1, rstd * QSCALE));
    if (rep == 0) {
        fmt_finish(c, fm);
        const u64 e = e0 + 1; c0.elo = c.elo = __builtin_amdgcn_readfirstlane((u32)e); c0.ehi = c.ehi = __builtin_amdgcn_readfirstlane((u32)(e >> 32));
    }
    STAMP(4 + rep * 30);
    publish(c, rep * 6);
}

DI void load_wring(const Ctx& c, u32x4 (&w0)[WRING], u32x4 (&w1)[WRING]) {
    const u32 wso = WS_WF + (u32)((c.j * 8 + c.wave) * 2) * 18 * 1024;
#pragma unroll
    for (int s = 0; s < WRING; ++s) { w0[s] = ld128(c.rs, c.lane * 16, wso + s * 1024); w1[s] = ld128(c.rs, c.lane * 16, wso + (18 + s) * 1024); }
}
DI void colsums_b(const Ctx& c) {
    const int tok = c.lane & 15, g = c.lane >> 4;
    if (c.wave >= 1) {
        const int extra = c.wave == 1 ? 0 : c.wave;
        u32 v[4][5];
#pragma unroll
        for (int q = 0; q < 4; ++q) {
            const int unit = (q < 2 ? c.wave : extra) * 2 + (q & 1);
#pragma unroll
            for (int u = 0; u < 5; ++u) { const int s = g + 4 * u; v[q][u] = (q < 2 || c.wave == 1) ? ld32(c.rs, (s < 18 ? s : 0) * 64 + tok * 4, WS_CSP + (u32)((c.j * 16 + unit) * 18) * 64) : 0u; }
        }
#pragma unroll
        for (int q = 0; q < 4; ++q) {
            const int unit = (q < 2 ? c.wave : extra) * 2 + (q & 1);
            float cs = (asf(v[q][0]) + asf(v[q][1])) + (asf(v[q][2]) + asf(v[q][3])) + (g < 2 ? asf(v[q][4]) : 0.f);
            cs = sum_g(cs);
            if (g == 0 && (q < 2 || c.wave == 1)) lst(c.L + L_CSB + unit * 64 + tok * 4, cs);
        }
    }
}
struct ATail { f32x16 o; u32x4 qm; u32x2 qr; };
DI void a_attn(const Ctx& c0, const Params& p, int rep, ATail& st) {
    const Ctx c = fresh(c0);
    const int lane = c.lane, r = lane & 31, h = lane >> 5, a = c.wave & 3, kh = c.wave >> 2;
    const int ph = rep * 6;
    const u32 kva = WS_KVA + rep * KVA_BYTES;
    u32x4 qmx, qmy; u32x2 qrx, qry;
    { LAS char* s = c.L + L_STG + (32 * kh + r) * STG_P + 36 * a;
      qmx = u32x4{lld<u32>(s + 16 * h), lld<u32>(s + 16 * h + 4), lld<u32>(s + 16 * h + 8), lld<u32>(s + 16 * h + 12)};
      const u32 rx = lld<u32>(s + 32);
      s += (32 - 64 * kh) * STG_P;
      qmy = u32x4{lld<u32>(s + 16 * h), lld<u32>(s + 16 * h + 4), lld<u32>(s + 16 * h + 8), lld<u32>(s + 16 * h + 12)};
      const u32 ry = lld<u32>(s + 32);
      qrx = u32x2{h == 0 ? rx : 0u, 0u}; qry = u32x2{h == 0 ? ry : 0u, 0u}; }
    const int ch0 = c.i >> 2;
    auto chunk_ids = [&](int n) { const int ch = (ch0 + n) & 7; return ch * 32 + (lane & 31); };
    u32 pvo[5];
#pragma unroll
    for (int k = 0; k < 5; ++k) {
        const u32 S = (u32)((c.wave & 3) + 4 * k) * 64 + lane, tile = (S * 52429u) >> 23, ps = S - tile * 160;
        const u32 v = ps - 64, blk = (v * 3450u) >> 16, row = v - blk * 19;
        const u32 src = ps < 64 ? ps * 16 : (ps < 140 ? ka_vrow(blk, row) : (ps < 156 ? KA_KREM + (ps - 140) * 16 : ps * 16));
        pvo[k] = tile * TILE_B + src;
    }
    auto issue_piece = [&](int n, int k) {
        const int ch = (ch0 + n) & 7; const u32 so = kva + (u32)ch * CHUNK_B; const u32 dst = c.Lb + L_RING + (n % 3) * CHUNK_B;
        glds_ws(c.srd, pvo[k], so, dst + ((c.wave & 3) + 4 * k) * 1024);
    };
    auto issue_chunk8 = [&](int n) {
        if (c.wave < 4) { issue_piece(n, 0); issue_piece(n, 2); issue_piece(n, 4); } else { issue_piece(n, 1); issue_piece(n, 3); }
    };
    if (c.wave == 0) poll(c, ph, lane < 32 ? chunk_ids(0) : chunk_ids(1));
    BAR();
    STAMP(6 + rep * 30);
    issue_chunk8(0);
    if (c.wave < 4) { issue_piece(1, 0); issue_piece(1, 1); issue_piece(1, 2); issue_piece(1, 3); issue_piece(1, 4); asm volatile("s_waitcnt vmcnt(5)" ::: "memory"); }
    else DRAIN();
    BAR();
    STAMP(7 + rep * 30); if (rep == 0) CSTAMP(61);
    if (rep == 1) { CSTAMPW(0, 4); CSTAMPW(8, 0); }
    const int vr = vrow_of(r);
    LAS char* kb = c.L + L_RING + lane * 16;
    LAS char* rb = c.L + L_RING + KT_KREM + r * 8;
    LAS char* vb = c.L + L_RING + KT_V + h * KT_VBLK + vr * 16;
    f32x16 sAx, sBx, sAy, sBy, ox, oy;
    const u32x4 k0m = lld<u32x4>(kb); const u32x2 k0r = lld<u32x2>(rb);
    kb += kh * 4 * TILE_B; rb += kh * 4 * TILE_B; vb += kh * 4 * TILE_B;
    const u32x4 kom = lld<u32x4>(kb); const u32x2 kor = lld<u32x2>(rb);
    u32x4 km = lld<u32x4>(kb + TILE_B), v0 = lld<u32x4>(vb), v1 = lld<u32x4>(vb + 2 * KT_VBLK); u32x2 kr = lld<u32x2>(rb + TILE_B);
    Pipe px, py;
    { u32 z = 0u; asm volatile("" : "+v"(z));
      px.e[0] = px.e[1] = px.e[2] = px.e[3] = asf(z); px.p1 = u32x4{z, z, z, z}; py = px; }
    asm volatile("" : "+v"(px.p1), "+v"(px.e[0]), "+v"(px.e[1]), "+v"(px.e[2]), "+v"(px.e[3]), "+v"(qmx), "+v"(qrx));
    asm volatile("s_nop 1" : "+v"(py.p1), "+v"(py.e[0]), "+v"(py.e[1]), "+v"(py.e[2]), "+v"(py.e[3]), "+v"(qmy), "+v"(qry));
    QK1(ox, px.p1, px.p1); QK1(oy, px.p1, px.p1);
    QK1(sBx, k0m, qmx); QK1(sBy, k0m, qmy); QK2(sBx, k0r, qrx); QK2(sBy, k0r, qry);
    QK1(sAx, kom, qmx); QK1(sAy, kom, qmy);
    asm volatile("s_nop 7\n\ts_nop 1" : "+v"(sBx), "+v"(sBy));
    { const float mx = rowmax(sBx), my = rowmax(sBy);
      if (h == 0) { qrx[1] = pk(-mx, 0.f); qry[1] = pk(-my, 0.f); } }
    asm volatile("s_nop 1" : "+v"(qrx), "+v"(qry));
    QK2(sAx, kor, qrx); QK2(sAy, kor, qry);
    asm volatile("s_nop 7\n\ts_nop 3" : "+v"(sAx), "+v"(sAy));
    if (rep == 1) { CSTAMPW(1, 4); CSTAMPW(9, 0); }
    int slot = 0;
    const u64 want = ((u64)c.ehi << 32) | c.elo;
    const u32 fvo = (lane & 31) * 128;
    u64 fl = want;
    if (c.wave == 0) flag_load(fl, ~want, fvo, c.srd, WS_FLAG + (u32)(ph * 256 + ((ch0 + 2) & 7) * 32) * 128);
#pragma unroll 1
    for (int n = 0; n < 8; ++n) {
        const int nslot = slot == 2 ? 0 : slot + 1;
        LAS char* kc = kb + slot * CHUNK_B; LAS char* rc = rb + slot * CHUNK_B; LAS char* vc = vb + slot * CHUNK_B;
        LAS char* kn = kb + nslot * CHUNK_B; LAS char* rn = rb + nslot * CHUNK_B; LAS char* vn = vb + nslot * CHUNK_B;
#define A_TILE(U, SCX, SNX, SCY, SNY, X1, X2) attn_step2(SCX, SNX, ox, px, SCY, SNY, oy, py, km, kr, v0, v1, qmx, qrx, qmy, qry, \
            [&] { km = lld<u32x4>((U) < 2 ? kc + ((U) + 2) * TILE_B : kn + ((U) - 2) * TILE_B); X1; }, \
            [&] { kr = lld<u32x2>((U) < 2 ? rc + ((U) + 2) * TILE_B : rn + ((U) - 2) * TILE_B); }, \
            [&] { v1 = lld<u32x4>(vc + (U) * TILE_B + 2 * KT_VBLK); X2; }, \
            [&] { v0 = lld<u32x4>((U) < 3 ? vc + ((U) + 1) * TILE_B : vn); })
        A_TILE(0, sAx, sBx, sAy, sBy, (void)0, (void)0); A_TILE(1, sBx, sAx, sBy, sAy, (void)0, (void)0);
        if (__builtin_expect(c.wave < 4, 0)) {
            asm volatile("s_waitcnt vmcnt(0)" : "+v"(fl) :: "memory");
            if (__builtin_expect(!__all(fl == want), 0)) poll(c, ph, chunk_ids(n + 2));
        }
        BAR();
        if (__builtin_expect(c.wave < 4, 0)) {
            fl = want;
            if (c.wave == 0 && n + 3 < 8) flag_load(fl, ~want, fvo, c.srd, WS_FLAG + (u32)(ph * 256 + ((ch0 + n + 3) & 7) * 32) * 128);
            if (n + 2 < 8) { issue_piece(n + 2, 0); issue_piece(n + 2, 1); issue_piece(n + 2, 2); issue_piece(n + 2, 3); issue_piece(n + 2, 4); }
            if (n >= 5) load_wimage_o4(c, 0, n - 5);
            if (rep == 0 && n == 0) {
                u32 z0 = 0u; asm volatile("" : "+v"(z0));
                const u32x4 z = {z0, z0, z0, z0};
#pragma unroll
                for (int q = 0; q < 5; ++q)
                    st128(c.rs, z, lane * 16 + q * 1024, WS_KVB + (u32)((4 * c.j + c.wave) * 64 + 2 * c.i) * TILE_B);
                if (c.wave < 2) st128(c.rs, z, (u32)((16 + (c.j >> 2)) * 4 + (lane >> 4)) * 1024 + ((c.j & 3) * 16 + (lane & 15)) * 16, WS_OF + c.wave * XF_BYTES + (u32)(c.i * 72) * 1024);
            }
        }
        A_TILE(2, sAx, sBx, sAy, sBy, (void)0, (void)0); A_TILE(3, sBx, sAx, sBy, sAy, (void)0, (void)0);
#undef A_TILE
        slot = nslot;
    }
    if (rep == 1) { CSTAMPW(2, 4); CSTAMPW(10, 0); }
    attn_finish2(ox, px, oy, py, v1);
    { LAS char* mine = c.L + L_RING + kh * 2 * CHUNK_B + a * 3072 + lane * 16;
#pragma unroll
      for (int e = 0; e < 3; ++e) lst(mine + e * 1024, f32x4{oy[4 * e], oy[4 * e + 1], oy[4 * e + 2], oy[4 * e + 3]}); }
    st.o = ox; st.qm = qmx; st.qr = qrx;
}

template <bool ISB, class HK>
DI void out_proj(const Ctx& c0, const Params& p, int rep, u32 (&xres)[3][2], HK behind_x) {
    const Ctx c = fresh(c0);
    const int lane = c.lane, m = c.wave & 3, kh = c.wave >> 2, tok = lane & 15, g = lane >> 4;
    const int layer = rep * 2 + (ISB ? 1 : 0);
    wait_group(c, rep * 6 + (ISB ? 4 : 1));
    STAMP((ISB ? 25 : 10) + rep * 30);
    u32x4 xf[9]; f32x4 acc[5] = {};
    load_xfrags(c, WS_OF + (rep * RP * 2 + (ISB ? 1 : 0)) * XF_BYTES, xf);
    __builtin_amdgcn_sched_barrier(0);
    behind_x(c);
    if (ISB) {
        asm volatile("s_waitcnt vmcnt(9)" ::: "memory"); BAR();
    }
    g1_mma<false>(c, xf, acc, [](int, const u32x4 (&)[5]) {}, [](int) {});
    STAMP((ISB ? 26 : 11) + rep * 30);
    g1_exchange(c, acc);
    STAMP((ISB ? 27 : 12) + rep * 30);
#pragma unroll
    for (int u = 0; u < 3; ++u) {
        const float v0 = bflo(xres[u][0]) + acc[u][0], v1 = bfhi(xres[u][0]) + acc[u][1], v2 = bflo(xres[u][1]) + acc[u][2], v3 = bfhi(xres[u][1]) + acc[u][3];
        xres[u][0] = pk(v0, v1); xres[u][1] = pk(v2, v3);
    }
    if (ISB && rep == 1) {
        const rsrc_t ro = __builtin_amdgcn_make_buffer_rsrc((void*)p.out, 0, SEQ * DM * 2, 0x00020000);
        const u32 oo = (u32)(c.i * 64 + m * 16 + tok) * (DM * 2) + (c.j * 72 + 4 * g) * 2;
        __builtin_amdgcn_raw_buffer_store_b64(u32x2{xres[0][0], xres[0][1]}, ro, oo + (2 * kh) * 32, 0, 16);
        __builtin_amdgcn_raw_buffer_store_b64(u32x2{xres[1][0], xres[1][1]}, ro, oo + (2 * kh + 1) * 32, 0, 16);
        if (kh == 1 && g < 2) __builtin_amdgcn_raw_buffer_store_b64(u32x2{xres[2][0], xres[2][1]}, ro, oo + 128, 0, 16);
        return;
    }
    const int xi = layer == 0 ? 0 : (layer == 1 ? 1 : 2 * RP);
    const u32 so = WS_XF + xi * XF_BYTES + (u32)(c.i * 18 * 4 + m) * 1024;
    st128(c.rs, u32x4{xres[0][0], xres[0][1], xres[1][0], xres[1][1]}, lane * 16, so + (u32)(2 * c.j + kh) * 4096);
    if (kh == 1 && g < 2) st64(c.rs, u32x2{xres[2][0], xres[2][1]}, ((c.j & 3) * 16 + tok) * 16 + 8 * g, so + (u32)(16 + (c.j >> 2)) * 4096);
    __builtin_amdgcn_sched_barrier(0);
    float s1 = 0.f, s2 = 0.f;
#pragma unroll
    for (int u = 0; u < 3; ++u) {
        u32 d0 = xres[u][0], d1 = xres[u][1];
        asm volatile("" : "+v"(d0), "+v"(d1));
        if (u == 2 && !(kh == 1 && g < 2)) { d0 = 0u; d1 = 0u; }
        const float a0 = bflo(d0), a1 = bfhi(d0), a2 = bflo(d1), a3 = bfhi(d1);
        s1 += (a0 + a1) + (a2 + a3); s2 += (a0 * a0 + a1 * a1) + (a2 * a2 + a3 * a3);
    }
    s1 = sum_g(s1); s2 = sum_g(s2);
    if (g == 0) st64(c.rs, u32x2{asu(s1), asu(s2)}, (u32)((c.i * 16 + 2 * c.j + kh) * 64 + m * 16 + tok) * 8, WS_ST + xi * ST_BYTES);
    publish(c, rep * 6 + (ISB ? 5 : 2));
    if (!ISB && rep == 0) colsums_b(c);
    if (ISB) load_wimage_part<36, 90>(c, WS_WIMGA, 1, L_A1);
}

DI void a_tail(const Ctx& c0, const Params& p, int rep, ATail& st, u32 (&xres)[3][2], u32x4 (&w0)[WRING], u32x4 (&w1)[WRING]) {
    const Ctx c = fresh(c0);
    const int lane = c.lane, a = c.wave & 3, kh = c.wave >> 2;
    BAR();
    LAS char* theirs = c.L + L_RING + (1 - kh) * 2 * CHUNK_B + a * 3072 + lane * 16;
#pragma unroll
    for (int e = 0; e < 3; ++e) { const f32x4 v = lld<f32x4>(theirs + e * 1024); st.o[4 * e] += v[0]; st.o[4 * e + 1] += v[1]; st.o[4 * e + 2] += v[2]; st.o[4 * e + 3] += v[3]; }
    STAMP(8 + rep * 30); if (rep == 0) CSTAMP(62);
    if (rep == 1) { CSTAMPW(3, 4); CSTAMPW(11, 0); }
    if (attn_unsafe(st.o)) st.o = attn_slow(c, WS_KVA + rep * KVA_BYTES, 0, 63, false, st.qm, st.qr);
    store_o(c, WS_OF + (rep * RP * 2) * XF_BYTES, st.o, a, kh);
    if (rep == 1) { CSTAMPW(4, 4); CSTAMPW(12, 0); }
    publish(c, rep * 6 + 1);
    load_wring(c, w0, w1);
    STAMP(9 + rep * 30);
    out_proj<false>(c, p, rep, xres, [&](const Ctx& cc) {
        if (rep == 0) {
            const int m = cc.wave & 3, k2 = cc.wave >> 2, tok = cc.lane & 15, g = cc.lane >> 4;
            const char* rr = (const char*)p.x + (size_t)(cc.i * 64 + m * 16 + tok) * (DM * 2) + (cc.j * 72 + 4 * g) * 2;
            const u32x2 r0 = *(const u32x2*)(rr + (2 * k2) * 32), r1 = *(const u32x2*)(rr + (2 * k2 + 1) * 32), r2 = *(const u32x2*)(rr + (g < 2 ? 128 : 0));
            xres[0][0] = r0[0]; xres[0][1] = r0[1]; xres[1][0] = r1[0]; xres[1][1] = r1[1]; xres[2][0] = r2[0]; xres[2][1] = r2[1];
        }
    });
}

struct T_ { static constexpr bool value = true; };
struct F_ { static constexpr bool value = false; };
DI void b_proj(const Ctx& c0, const Params& p, int rep, u32x4 (&w0)[WRING], u32x4 (&w1)[WRING], u32x4& qm, u32x2& qr) {
    const Ctx c = fresh(c0);
    const int lane = c.lane, tok = lane & 15, g = lane >> 4, a = c.wave >> 1, odd = c.wave & 1;
    wait_group(c, rep * 6 + 2);
    STAMP(15 + rep * 30);
    const int xi = rep == 0 ? 0 : 2 * RP;
    u32x4 stv[4];
    if (c.wave == 5 || c.wave == 7) stats_issue32(c, WS_ST + xi * ST_BYTES, stv);
    { const u32 so = WS_XF + xi * XF_BYTES + (u32)(c.i * 72) * 1024;
#pragma unroll
      for (int n = 0; n < 9; ++n) glds_ws(c.srd, lane * 16, so + (c.wave + 8 * n) * 1024, c.Lb + L_XB + (c.wave + 8 * n) * 1024); }
    asm volatile("s_waitcnt vmcnt(5)" ::: "memory"); BAR();
    STAMP(16 + rep * 30);
    f32x4 a0[4] = {}, a1[4] = {};
    const u32 wso = WS_WF + (u32)((c.j * 8 + c.wave) * 2) * 18 * 1024;
    LAS char* xb = c.L + L_XB + lane * 16;
    const bool t0 = !odd, t1 = !odd || a == 0, has1 = !odd || a < 2;
    auto body = [&](auto T0, auto T1, auto H1) {
        u32x4 x[2][4];
#pragma unroll
        for (int m = 0; m < 4; ++m) { x[0][m] = lld<u32x4>(xb + m * 1024); asm volatile("" : "+v"(a0[m]), "+v"(a1[m])); }
        if (!H1.value) stats_finish32(c, stv);
#pragma unroll
        for (int s = 0; s < 18; ++s) {
            if (s == 7) {
                if (H1.value) asm volatile("s_waitcnt vmcnt(14)" ::: "memory"); else asm volatile("s_waitcnt vmcnt(7)" ::: "memory");
                static_assert(18 - WRING >= 7, "steps 0..6 each refill the ring");
                BAR();
            }
            if (s + 1 < 18) {
#pragma unroll
                for (int m = 0; m < 4; ++m) x[(s + 1) & 1][m] = lld<u32x4>(xb + ((s + 1) * 4 + m) * 1024);
            }
#pragma unroll
            for (int m = 0; m < 4; ++m) {
                if (T0.value) MF16(a0[m], w0[s % WRING], x[s & 1][m]); else MF16(a0[m], x[s & 1][m], w0[s % WRING]);
                if (H1.value) { if (T1.value) MF16(a1[m], w1[s % WRING], x[s & 1][m]); else MF16(a1[m], x[s & 1][m], w1[s % WRING]); }
            }
            if (s + WRING < 18) { w0[s % WRING] = ld128(c.rs, lane * 16, wso + (WRING + s) * 1024); if (H1.value) w1[s % WRING] = ld128(c.rs, lane * 16, wso + (18 + WRING + s) * 1024); }
        }
        asm volatile("s_nop 7" : "+v"(a0[0]), "+v"(a0[1]), "+v"(a0[2]), "+v"(a0[3]), "+v"(a1[0]), "+v"(a1[1]), "+v"(a1[2]), "+v"(a1[3]));
    };
    if (!odd) body(T_{}, T_{}, T_{}); else if (a == 0) body(F_{}, T_{}, T_{}); else if (a == 1) body(F_{}, F_{}, T_{}); else body(F_{}, F_{}, F_{});
    (void)t0; (void)t1; (void)has1;
    STAMP(17 + rep * 30);
    const u32 kvb = WS_KVB + rep * RP * KVB_BYTES;
    const u32 hb = kvb + (u32)((4 * c.j + a) * 64 + 2 * c.i) * TILE_B;
    auto fin_t = [&](const f32x4 (&acc)[4], int slot, float sc, u32 (&d)[4][2]) {
        const f32x4 cs = lld<f32x4>(c.L + L_CSB + (c.wave * 2 + slot) * 64 + g * 16);
#pragma unroll
        for (int m = 0; m < 4; ++m) {
            const float mean = lld<float>(c.L + L_MEAN + (m * 16 + tok) * 4), rs = lld<float>(c.L + L_MEAN + 256 + (m * 16 + tok) * 4) * sc;
            d[m][0] = pk((acc[m][0] - mean * cs[0]) * rs, (acc[m][1] - mean * cs[1]) * rs);
            d[m][1] = pk((acc[m][2] - mean * cs[2]) * rs, (acc[m][3] - mean * cs[3]) * rs);
        }
    };
    auto fin_p = [&](const f32x4 (&acc)[4], int slot, u32 (&d)[4][2]) {
        const float cs = lld<float>(c.L + L_CSB + (c.wave * 2 + slot) * 64 + tok * 4);
#pragma unroll
        for (int m = 0; m < 4; ++m) {
            const f32x4 mean = lld<f32x4>(c.L + L_MEAN + (m * 16 + 4 * g) * 4), rs = lld<f32x4>(c.L + L_MEAN + 256 + (m * 16 + 4 * g) * 4);
            d[m][0] = pk((acc[m][0] - mean[0] * cs) * rs[0], (acc[m][1] - mean[1] * cs) * rs[1]);
            d[m][1] = pk((acc[m][2] - mean[2] * cs) * rs[2], (acc[m][3] - mean[3] * cs) * rs[3]);
        }
    };
    auto frag16 = [&](const u32 (&d)[4][2], int t) {
        auto x = __builtin_amdgcn_permlane16_swap(d[2 * t][0], d[2 * t + 1][0], false, false);
        auto y = __builtin_amdgcn_permlane16_swap(d[2 * t][1], d[2 * t + 1][1], false, false);
        return u32x4{(u32)x[0], (u32)y[0], (u32)x[1], (u32)y[1]};
    };
    auto frag32 = [&](const u32 (&d)[4][2], int t) {
        auto x = __builtin_amdgcn_permlane32_swap(d[2 * t][0], d[2 * t + 1][0], false, false);
        auto y = __builtin_amdgcn_permlane32_swap(d[2 * t][1], d[2 * t + 1][1], false, false);
        return u32x4{(u32)x[0], (u32)y[0], (u32)x[1], (u32)y[1]};
    };
    u32 d[4][2];
    LAS char* lh = c.L + L_KVB + a * 25600 + 8 * TILE_B; LAS char* l0 = c.L + L_KVB + 8 * TILE_B;
    if (!odd) {
        fin_t(a1, 1, 1.f, d);
        { const u32x4 f0 = frag16(d, 0), f1 = frag16(d, 1);
          st128(c.rs, f0, lane * 16, hb); st128(c.rs, f1, lane * 16, hb + TILE_B);
          lst(lh + lane * 16, f0); lst(lh + TILE_B + lane * 16, f1); }
        fin_t(a0, 0, QSCALE, d);
        qm = frag16(d, 0);
        lst(c.L + L_QT1 + a * 1024 + lane * 16, frag16(d, 1));
    } else {
        fin_p(a0, 0, d);
        { const u32x4 f0 = frag32(d, 0), f1 = frag32(d, 1);
          st128(c.rs, f0, KT_V + g * KT_VBLK + tok * 16, hb); st128(c.rs, f1, KT_V + g * KT_VBLK + tok * 16, hb + TILE_B);
          lst(lh + KT_V + g * KT_VBLK + tok * 16, f0); lst(lh + TILE_B + KT_V + g * KT_VBLK + tok * 16, f1); }
        if (a == 0) {
            fin_t(a1, 1, g < 2 ? QSCALE : 1.f, d);
#pragma unroll
            for (int m = 0; m < 4; ++m) {
                if (g < 2) { lst(c.L + L_QREM + (2 * g) * 256 + (m * 16 + tok) * 4, d[m][0]); lst(c.L + L_QREM + (2 * g + 1) * 256 + (m * 16 + tok) * 4, d[m][1]); }
                else {
                    const u32 hb0 = kvb + (u32)(4 * c.j * 64 + 2 * c.i + (m >> 1)) * TILE_B;
                    st64(c.rs, u32x2{d[m][0], 0x00003f80u}, (u32)(2 * (g - 2)) * 64 * TILE_B + KT_KREM + ((m & 1) * 16 + tok) * 8, hb0);
                    st64(c.rs, u32x2{d[m][1], 0x00003f80u}, (u32)(2 * (g - 2) + 1) * 64 * TILE_B + KT_KREM + ((m & 1) * 16 + tok) * 8, hb0);
                    LAS char* lk = l0 + (2 * (g - 2)) * 25600 + (m >> 1) * TILE_B + KT_KREM + ((m & 1) * 16 + tok) * 8;
                    lst(lk, u32x2{d[m][0], 0x00003f80u}); lst(lk + 25600, u32x2{d[m][1], 0x00003f80u});
                }
            }
        } else if (a == 1) {
            fin_p(a1, 1, d);
            const u32 hb0 = kvb + (u32)(4 * c.j * 64 + 2 * c.i) * TILE_B;
            const u32 vo = (u32)(tok >> 1) * 64 * TILE_B + KT_V + g * KT_VBLK + (16 + (tok & 1)) * 16;
            const u32x4 f0 = frag32(d, 0), f1 = frag32(d, 1);
            if (tok < 8) {
                st128(c.rs, f0, vo, hb0); st128(c.rs, f1, vo, hb0 + TILE_B);
                LAS char* lv = l0 + (tok >> 1) * 25600 + KT_V + g * KT_VBLK + (16 + (tok & 1)) * 16;
                lst(lv, f0); lst(lv + TILE_B, f1);
            }
        } else if (a == 2) {
            const u32 hb0 = kvb + (u32)(4 * c.j * 64 + 2 * c.i) * TILE_B;
            if (lane < 32) {
                const u32x4 on = ones4();
                st128(c.rs, on, (u32)(lane >> 3) * 64 * TILE_B + ((lane >> 2) & 1) * TILE_B + KT_V + (lane & 3) * KT_VBLK + 18 * 16, hb0);
                lst(l0 + (lane >> 3) * 25600 + ((lane >> 2) & 1) * TILE_B + KT_V + (lane & 3) * KT_VBLK + 18 * 16, on);
            }
        }
    }
    publish(c, rep * 6 + 3);
    load_wimage_part<44, 90>(c, WS_WIMG + 8 * WIMG_BYTES, 1);
    if (odd) qm = lld<u32x4>(c.L + L_QT1 + a * 1024 + lane * 16);
    const u32 rem = lld<u32>(c.L + L_QREM + a * 256 + (32 * odd + (lane & 31)) * 4);
    qr = u32x2{lane < 32 ? rem : 0u, 0u};
}

DI void b_attn(const Ctx& c0, const Params& p, int rep, u32x4 qm, u32x2 qr) {
    const Ctx c = fresh(c0);
    const int lane = c.lane, r = lane & 31, h = lane >> 5, a = c.wave >> 1, t = c.wave & 1;
    const int qt = 2 * c.i + t;
    const f32x16 cd = mask_c(lane, true);
    if (c.wave == 1) {
        const f32x16 cm = mask_c(lane, false);
#pragma unroll
        for (int e = 0; e < 4; ++e) lst(c.L + L_MSK + e * 1024 + lane * 16, f32x4{cm[4 * e], cm[4 * e + 1], cm[4 * e + 2], cm[4 * e + 3]});
    }
    LAS char* tb = c.L + L_KVB + a * 25600 + (8 + t) * TILE_B;
    LAS char* kb = tb + lane * 16; LAS char* rb = tb + KT_KREM + r * 8; LAS char* vb = tb + KT_V + h * KT_VBLK + vrow_of(r) * 16;
    const u32x4 k0m = lld<u32x4>(kb); const u32x2 k0r = lld<u32x2>(rb);
    u32x4 km, v0 = lld<u32x4>(vb), v1 = lld<u32x4>(vb + 2 * KT_VBLK); u32x2 kr;
    f32x16 sA, sB, o;
    Pipe pp;
    { u32 z = 0u; asm volatile("" : "+v"(z)); pp.e[0] = pp.e[1] = pp.e[2] = pp.e[3] = asf(z); pp.p1 = u32x4{z, z, z, z}; }
    f32x16 cdp = cd;
    asm volatile("s_nop 1" : "+v"(pp.p1), "+v"(pp.e[0]), "+v"(pp.e[1]), "+v"(pp.e[2]), "+v"(pp.e[3]), "+v"(qm), "+v"(qr), "+v"(cdp));
    QK1(o, pp.p1, pp.p1);
    QK1C(sA, k0m, qm, cdp);
    asm volatile("v_mfma_f32_32x32x8_bf16 %0, %1, %2, %3" : "=&v"(sB) : "v"(k0r), "v"(qr), "v"(sA));
    asm volatile("s_nop 7\n\ts_nop 3" : "+v"(sB), "+v"(sA));
    { const float m0 = rowmax(sB); if (h == 0) qr[1] = pk(-m0, 0.f); }
    asm volatile("s_nop 1" : "+v"(qr));
    QK2(sA, k0r, qr);
    asm volatile("s_nop 7\n\ts_nop 3" : "+v"(sA), "+v"(o), "+v"(v0), "+v"(v1));
    if (c.wave == 0) poll(c, rep * 6 + 3, (lane < 5 && c.i - lane >= 0) ? (c.i - lane) * 8 + c.j : -1);
    BAR();
    STAMP(20 + rep * 30);
    const u32 hb = WS_KVB + rep * RP * KVB_BYTES + (u32)((4 * c.j + a) * 64) * TILE_B;
    { const u32 dst = c.Lb + L_KVB + a * 25600;
#pragma unroll
      for (int n = 0; n < 10; ++n) {
          const int pc = 19 - t - 2 * n;
          u32 vo = lane * 16;
          if (c.i < 4) { const int b = pc * 1024 + lane * 16, tau = b / 2560, kt = 2 * c.i - 8 + tau; vo = (u32)((kt < 0 ? 0 : kt) * 2560 + (b - tau * 2560)); glds_ws(c.srd, vo, hb, dst + pc * 1024); }
          else glds_ws(c.srd, vo, hb + (u32)(2 * c.i - 8) * 2560 + pc * 1024, dst + pc * 1024);
      } }
    asm volatile("s_waitcnt vmcnt(5)" ::: "memory");
    BAR();
    STAMP(21 + rep * 30);
    km = lld<u32x4>(kb - TILE_B); kr = lld<u32x2>(rb - TILE_B);
    auto first = [](f32x16& s, u32x4 k, u32x4 q) { QK1(s, k, q); };
    auto firstm = [&](f32x16& s, u32x4 k, u32x4 q) {
        f32x16 co;
#pragma unroll
        for (int e = 0; e < 4; ++e) { const f32x4 v = lld<f32x4>(c.L + L_MSK + e * 1024 + lane * 16); co[4 * e] = v[0]; co[4 * e + 1] = v[1]; co[4 * e + 2] = v[2]; co[4 * e + 3] = v[3]; }
        QK1C(s, k, q, co); };
    const u32 qoff = h == 0 ? pk(NEG_BIG, 0.f) : 0u;
    kb -= 8 * TILE_B; rb -= 8 * TILE_B; vb -= 8 * TILE_B;
#define B_TILE(U, SC, SN, FIRST) do { u32x2 qs = {qr[0], (U) + 1 <= qt ? qr[1] : qoff}; asm volatile("" : "+v"(qs)); \
        attn_step<((U) < 8)>(SC, SN, o, pp, km, kr, v0, v1, qm, qs, FIRST, \
            [&] { if ((U) + 2 <= 8) km = lld<u32x4>(kb + (8 - ((U) + 2)) * TILE_B); }, [&] { if ((U) + 2 <= 8) kr = lld<u32x2>(rb + (8 - ((U) + 2)) * TILE_B); }, \
            [&] { v1 = lld<u32x4>(vb + (8 - (U)) * TILE_B + 2 * KT_VBLK); }, [&] { if ((U) + 1 <= 8) v0 = lld<u32x4>(vb + (8 - ((U) + 1)) * TILE_B); }); } while (0)
    B_TILE(0, sA, sB, first); B_TILE(1, sB, sA, first);
    DRAIN();
    B_TILE(2, sA, sB, first);
    BAR();
    B_TILE(3, sB, sA, first); B_TILE(4, sA, sB, first); B_TILE(5, sB, sA, first); B_TILE(6, sA, sB, first); B_TILE(7, sB, sA, firstm); B_TILE(8, sA, sB, first);
#undef B_TILE
    attn_finish(o, pp, v1);
    STAMP(22 + rep * 30);
    if (attn_unsafe(o)) o = attn_slow(c, hb, qt - 8 < 0 ? 0 : qt - 8, qt, true, qm, qr);
    store_o(c, WS_OF + (rep * RP * 2 + 1) * XF_BYTES, o, a, t);
    publish(c, rep * 6 + 4);
    load_wimage_part<0, 44>(c, WS_WIMG + 8 * WIMG_BYTES, 1);
    if (rep == 0) load_wimage_part<0, 36>(c, WS_WIMGA, 1, L_A1);
}

__global__ void __launch_bounds__(512, 2) fused_block_kernel(Params p) {
    __shared__ __attribute__((aligned(16))) char lds_[LDS_BYTES];
    Ctx c;
    c.L = (LAS char*)lds_; c.Lb = (u32)(size_t)c.L;
    c.ws = p.ws;
    c.tid = threadIdx.x; c.lane = c.tid & 63; c.wave = __builtin_amdgcn_readfirstlane(c.tid >> 6);
    { const int wg = blockIdx.x, slot = wg >> 3; c.i = (wg & 7) * 4 + (slot >> 3); c.j = slot & 7; }
    c.rs = __builtin_amdgcn_make_buffer_rsrc((void*)p.ws, 0, WS_USED, 0x00020000);
    { const u64 b = (u64)p.ws; c.srd = u32x4{(u32)b, (u32)(b >> 32) & 0xffffu, WS_USED, 0x00020000u}; }
    STAMP(0);
    u32 warm;
    { u64 pc; u32 len;
      asm volatile("s_getpc_b64 %0\nfbk_code_first:\n\ts_mov_b32 %1, fbk_code_last-fbk_code_first" : "=s"(pc), "=s"(len));
      u32 off = ((u32)(blockIdx.x >> 3) * 8 + c.wave) * 256 + c.lane * 4;
      off = off < len ? off : 0u;
      warm = *(const GAS u32*)((pc & ~3ull) + off); }
    c.elo = 0; c.ehi = 0;
    u32 xres[3][2];
    STAMP(1);
    a_proj<0>(c, p);
    asm volatile("" :: "v"(warm));
    STAMP(5);
#pragma unroll 1
    for (int rep = 0; rep < 2; ++rep) {
        u32x4 w0[WRING], w1[WRING]; ATail st;
        a_attn(c, p, rep, st);
        if (rep == 0) {
            if (blockIdx.x == 0 && c.tid == 0) __hip_atomic_store((GAS u64*)(p.ws + WS_EPOCH), ((u64)c.ehi << 32) | c.elo, __ATOMIC_RELAXED, __HIP_MEMORY_SCOPE_AGENT);
        }
        a_tail(c, p, rep, st, xres, w0, w1);
        STAMP(14 + rep * 30);
        u32x4 qm; u32x2 qr;
        b_proj(c, p, rep, w0, w1, qm, qr);
        STAMP(19 + rep * 30);
        b_attn(c, p, rep, qm, qr);
        STAMP(24 + rep * 30);
        out_proj<true>(c, p, rep, xres, [](const Ctx&) {});
        STAMP(29 + rep * 30);
        if (rep == 1) break;
        STAMP(31);
        a_proj<1>(c, p);
        STAMP(35);
    }
    asm volatile("fbk_code_last:");
}

static size_t ws_offset(size_t ws_size) { return (ws_size - WS_USED) & ~(size_t)0x1fffff; }

extern "C" void kernel_launch(void* const* d_in, const int* in_sizes, int n_in, void* d_out, int out_size, void* d_ws, size_t ws_size, hipStream_t stream) {
    Params p;
    p.x = (const u16*)d_in[0]; p.wq = (const u16*)d_in[1]; p.wk = (const u16*)d_in[2]; p.wv = (const u16*)d_in[3]; p.woA = (const u16*)d_in[4];
    p.wqkv = (const u16*)d_in[6]; p.woB = (const u16*)d_in[7]; p.out = (u16*)d_out;
    p.ws = (char*)d_ws + ws_offset(ws_size);
    fused_block_kernel<<<256, 512, 0, stream>>>(p);
}
```

```cpp
#include <hip/hip_runtime.h>
#include <hip/hip_bf16.h>

typedef unsigned short u16;
typedef unsigned u32;
typedef unsigned long long u64;
typedef __bf16 bf16x8 __attribute__((ext_vector_type(8)));
typedef __bf16 bf16x2 __attribute__((ext_vector_type(2)));
typedef short s16x4 __attribute__((ext_vector_type(4)));
typedef float f32x2 __attribute__((ext_vector_type(2)));
typedef float f32x4 __attribute__((ext_vector_type(4)));
typedef float f32x16 __attribute__((ext_vector_type(16)));
typedef u32 u32x2 __attribute__((ext_vector_type(2)));
typedef u32 u32x4 __attribute__((ext_vector_type(4)));
#define LAS __attribute__((address_space(3)))
#define GAS __attribute__((address_space(1)))
#define DI __device__ __forceinline__

constexpr int SEQ = 2048, DM = 576;
constexpr int RP = 0;
constexpr u32 TILE_B = 2560;
constexpr u32 KT_V = 1024, KT_VBLK = 304, KT_KREM = 2240;
constexpr u32 KA_KREM = 1024, KA_V0 = 1280, KA_V8 = 1792, KA_VX = 2304;
constexpr u32 XF_BYTES = 32u * 18 * 4 * 1024;
constexpr u32 WS_EPOCH = 0;
constexpr u32 WS_FLAG = 4096;
constexpr int PH_FMT = 11;
constexpr u32 WS_XF = WS_FLAG + 12u * 256 * 128 + 4096;
constexpr u32 WS_OF = WS_XF + (2 + RP) * XF_BYTES;
constexpr u32 ST_BYTES = 2048u * 16 * 8;
constexpr u32 WS_ST = WS_OF + (2 + 2 * RP) * XF_BYTES;
constexpr u32 KVA_BYTES = 64 * TILE_B;
constexpr u32 WS_KVA = WS_ST + (2 + RP) * ST_BYTES;
constexpr u32 KVB_BYTES = 32 * 64 * TILE_B;
constexpr u32 WS_KVB = WS_KVA + 2 * KVA_BYTES;
constexpr u32 WF_BYTES = 8u * 16 * 18 * 1024;
constexpr u32 WS_WF = WS_KVB + (1 + RP) * KVB_BYTES;
constexpr u32 WS_CSP = WS_WF + WF_BYTES;
constexpr u32 WIMG_BYTES = 90 * 1024;
constexpr u32 WS_WIMG = WS_CSP + 8u * 16 * 18 * 64;
constexpr u32 WS_WIMGA = WS_WIMG + 16 * WIMG_BYTES;
constexpr u32 WS_STAMP = WS_WIMGA + 8 * WIMG_BYTES;
constexpr u32 WS_STAMP2 = WS_STAMP + 256u * 64 * 8 + 256u * 8 * 8 * 8;
constexpr u32 WS_USED = WS_STAMP2 + 256u * 16 * 8;
constexpr int L_XCH = 0;
constexpr int L_RING = 0;
constexpr int CHUNK_B = 8 * TILE_B;
constexpr int L_STG = 40960;
constexpr int STG_P = 168;
constexpr int L_XIN = 0;
constexpr int L_XIN2 = 153600;
constexpr int L_FMT = 24576;
constexpr int L_KVB = 0;
constexpr int L_MSK = 102400;
constexpr int L_R1 = 61440;
constexpr int WP = 160;
constexpr int L_XB = 76800;
constexpr int L_A1 = 24576;
constexpr int L_STATP = 153600;
constexpr int L_MEAN = L_STATP + 1024;
constexpr int L_CSA = L_MEAN + 512;
constexpr int L_CSB = L_CSA + 1024;
constexpr int L_QREM = L_CSB + 1024;
constexpr int L_QT1 = L_QREM + 1024;
constexpr int LDS_BYTES = 163840;
static_assert(L_QT1 + 4096 <= LDS_BYTES, "lds");

#define WRING 4
constexpr float QSCALE = 0.23570226039551584f * 1.4426950408889634f;
constexpr float NEG_BIG = -1e30f;
constexpr u32 SPIN_CAP = 1u << 20;
constexpr int AUX_SC1 = 16;

struct Params { const u16* x; const u16* wq; const u16* wk; const u16* wv; const u16* woA; const u16* wqkv; const u16* woB; u16* out; char* ws; };

DI u32 pk(float a, float b) { f32x2 v = {a, b}; return __builtin_bit_cast(u32, __builtin_convertvector(v, bf16x2)); }
DI float asf(u32 d) { return __builtin_bit_cast(float, d); }
DI u32 asu(float f) { return __builtin_bit_cast(u32, f); }
DI float bflo(u32 d) { return __builtin_bit_cast(float, d << 16); }
DI float bfhi(u32 d) { return __builtin_bit_cast(float, d & 0xffff0000u); }
DI bf16x8 asb(u32x4 v) { return __builtin_bit_cast(bf16x8, v); }
DI f32x4 mfma16(u32x4 a, u32x4 b, f32x4 c) { return __builtin_amdgcn_mfma_f32_16x16x32_bf16(asb(a), asb(b), c, 0, 0, 0); }
DI f32x16 mfma32(u32x4 a, u32x4 b, f32x16 c) { return __builtin_amdgcn_mfma_f32_32x32x16_bf16(asb(a), asb(b), c, 0, 0, 0); }
DI void MF16(f32x4& acc, u32x4 a, u32x4 b) { asm volatile("v_mfma_f32_16x16x32_bf16 %0, %1, %2, %0" : "+v"(acc) : "v"(a), "v"(b)); }
#define SETTLE4(x) asm volatile("s_nop 7" : "+v"(x))
template <class T> DI T lld(LAS char* p) { return *(LAS const T*)p; }
template <class T> DI void lst(LAS char* p, T v) { *(LAS T*)p = v; }
DI u32x2 trr(LAS char* p) { return __builtin_bit_cast(u32x2, __builtin_amdgcn_ds_read_tr16_b64_v4i16((LAS s16x4*)p)); }
#define BAR() asm volatile("s_waitcnt lgkmcnt(0)\n\ts_barrier" ::: "memory")
#define DRAIN() asm volatile("s_waitcnt vmcnt(0)" ::: "memory")
typedef __amdgpu_buffer_rsrc_t rsrc_t;
DI u32x4 ld128(rsrc_t rs, u32 voff, u32 soff) { return __builtin_amdgcn_raw_buffer_load_b128(rs, voff, soff, AUX_SC1); }
DI u32x2 ld64(rsrc_t rs, u32 voff, u32 soff) { return __builtin_amdgcn_raw_buffer_load_b64(rs, voff, soff, AUX_SC1); }
DI u32 ld32(rsrc_t rs, u32 voff, u32 soff) { return __builtin_amdgcn_raw_buffer_load_b32(rs, voff, soff, AUX_SC1); }
DI void st128(rsrc_t rs, u32x4 v, u32 voff, u32 soff) { __builtin_amdgcn_raw_buffer_store_b128(v, rs, voff + soff, 0, AUX_SC1); }
DI void st64(rsrc_t rs, u32x2 v, u32 voff, u32 soff) { __builtin_amdgcn_raw_buffer_store_b64(v, rs, voff, soff, AUX_SC1); }
DI void st32(rsrc_t rs, u32 v, u32 voff, u32 soff) { __builtin_amdgcn_raw_buffer_store_b32(v, rs, voff, soff, AUX_SC1); }
DI void glds_ws(u32x4 srd, u32 voff, u32 soff, u32 ldsdst) {
    soff = __builtin_amdgcn_readfirstlane(soff); ldsdst = __builtin_amdgcn_readfirstlane(ldsdst);
    asm volatile("s_mov_b32 m0, %0\n\ts_nop 3\n\tbuffer_load_dwordx4 %1, %2, %3 offen sc1 lds" :: "s"(ldsdst), "v"(voff), "s"(srd), "s"(soff) : "memory");
}
DI void glds_in(const void* g, u32 ldsdst) {
    ldsdst = __builtin_amdgcn_readfirstlane(ldsdst);
    asm volatile("s_mov_b32 m0, %0\n\ts_nop 0\n\tglobal_load_lds_dwordx4 %1, off" :: "s"(ldsdst), "v"(g) : "memory");
}
DI void flag_load(u64& fl, u64 nomatch, u32 voff, u32x4 srd, u32 soff) {
    soff = __builtin_amdgcn_readfirstlane(soff);
    fl = nomatch;
    asm volatile("s_nop 4\n\tbuffer_load_dwordx2 %0, %1, %2, %3 offen sc1" : "+v"(fl) : "v"(voff), "s"(srd), "s"(soff) : "memory");
}
DI float sum_g(float v) {
    auto a = __builtin_amdgcn_permlane32_swap(__builtin_bit_cast(u32, v), __builtin_bit_cast(u32, v), false, false);
    v = __builtin_bit_cast(float, (u32)a[0]) + __builtin_bit_cast(float, (u32)a[1]);
    auto b = __builtin_amdgcn_permlane16_swap(__builtin_bit_cast(u32, v), __builtin_bit_cast(u32, v), false, false);
    return __builtin_bit_cast(float, (u32)b[0]) + __builtin_bit_cast(float, (u32)b[1]);
}
DI float max_h(float v) {
    auto a = __builtin_amdgcn_permlane32_swap(__builtin_bit_cast(u32, v), __builtin_bit_cast(u32, v), false, false);
    return fmaxf(__builtin_bit_cast(float, (u32)a[0]), __builtin_bit_cast(float, (u32)a[1]));
}
DI u32x4 ones4() { u32 o = 0x3f803f80u; asm volatile("" : "+v"(o)); return u32x4{o, o, o, o}; }
DI int crow(int reg, int h) { return (reg & 3) + 8 * (reg >> 2) + 4 * h; }

#define STAMP(n) do {} while (0)
#define CSTAMP(n) do {} while (0)
#define CSTAMPW(n, W) do {} while (0)

struct Ctx {
    LAS char* L; u32 Lb;
    char* ws; rsrc_t rs; u32x4 srd;
    int tid, lane, wave, i, j;
    u32 elo, ehi;
};

DI Ctx fresh(const Ctx& c0) {
    Ctx c = c0;
    asm volatile("" : "+v"(c.lane)); asm volatile("" : "+v"(c.tid));
    asm volatile("" : "+s"(c.wave)); asm volatile("" : "+s"(c.i)); asm volatile("" : "+s"(c.j));
    return c;
}
DI GAS u64* flag_ptr(const Ctx& c, int phase, int id) { return (GAS u64*)(c.ws + WS_FLAG + (u32)(phase * 256 + id) * 128); }
DI void signal(const Ctx& c, int phase) {
    if (c.tid == 0) __hip_atomic_store(flag_ptr(c, phase, c.i * 8 + c.j), ((u64)c.ehi << 32) | c.elo, __ATOMIC_RELAXED, __HIP_MEMORY_SCOPE_AGENT);
}
DI void publish(const Ctx& c, int phase) {
    DRAIN();
    BAR();
    signal(c, phase);
}
DI void poll(const Ctx& c, int phase, int id) {
    const u64 want = ((u64)c.ehi << 32) | c.elo;
    GAS u64* f = flag_ptr(c, phase, id < 0 ? 0 : id);
    for (u32 spins = 0; spins < SPIN_CAP; ++spins) {
        const u64 v = __hip_atomic_load(f, __ATOMIC_RELAXED, __HIP_MEMORY_SCOPE_AGENT);
        if (__all(id < 0 || v == want)) break;
        __builtin_amdgcn_s_sleep(1);
    }
    __builtin_amdgcn_fence(__ATOMIC_ACQUIRE, "wavefront");
}
DI void wait_group(const Ctx& c, int phase) {
    if (c.wave == 0) poll(c, phase, c.lane < 8 ? c.i * 8 + c.lane : -1);
    BAR();
}

DI void load_wimage_a(const Ctx& c, const Params& p) {
    const int jj = c.j, w = c.wave;
    const u16 *pk_ = p.wk, *pv_ = p.wv;
    asm volatile("" : "+s"(pk_), "+s"(pv_));
    const char* xsrc = (const char*)((jj >= 3 && jj < 6) ? pv_ : pk_); const int sub = jj < 6 ? jj % 3 : 0;
    xsrc += sub == 0 ? 0 : (sub == 1 ? 16 : 20);
    const char* msrc = (const char*)p.wq + jj * 144;
    const char* a[5]; u32 st[5];
#pragma unroll
    for (int k = 0; k < 5; ++k) {
        const u32 id = 64 * k + c.lane, row = (id * 6554u) >> 16, ch = id - row * 10;
        const bool ex = ch == 9;
        st[k] = ex ? 32 * 36 : 32 * 1152;
        a[k] = (ex ? xsrc : msrc) + (ex ? row * 36 : row * 1152 + ch * 16);
    }
#pragma unroll
    for (int n = 0; n < 3; ++n) {
        const int G = n < 2 ? (w & 3) + 8 * (w >> 2) + 4 * n : 16 + w;
        if (n < 2 || w < 2) {
#pragma unroll
            for (int k = 0; k < 5; ++k) glds_in(a[k] + (u32)G * st[k], c.Lb + L_R1 + (G * 5 + k) * 1024);
        }
    }
}
DI void load_wimage_o(const Ctx& c, u32 base) {
    const u32 so = base + (u32)c.j * WIMG_BYTES;
    if (c.wave >= 1) {
#pragma unroll
        for (int n = 0; n < 13; ++n)
            if (n < 12 || c.wave < 7) glds_ws(c.srd, c.lane * 16, so + (c.wave - 1 + 7 * n) * 1024, c.Lb + L_R1 + (c.wave - 1 + 7 * n) * 1024);
    }
}
template <int P0, int P1>
DI void load_wimage_part(const Ctx& c, u32 base, int w0, int ldsbase = L_R1) {
    const u32 so = base + (u32)c.j * WIMG_BYTES; const int nw = 8 - w0;
    if (c.wave >= w0) {
#pragma unroll
        for (int n = 0; n < (P1 - P0 + 6) / 7; ++n) {
            const int pc = P0 + (c.wave - w0) + nw * n;
            if (pc < P1) glds_ws(c.srd, c.lane * 16, so + pc * 1024, c.Lb + ldsbase + pc * 1024);
        }
    }
}
DI void load_wimage_o4(const Ctx& c, int which, int part) {
    const u32 so = WS_WIMG + (u32)(which * 8 + c.j) * WIMG_BYTES + (u32)part * 30 * 1024; const u32 dst = c.Lb + L_R1 + part * 30 * 1024;
    if (c.wave < 4) {
#pragma unroll
        for (int n = 0; n < 8; ++n)
            if (n < 7 || c.wave < 2) glds_ws(c.srd, c.lane * 16, so + (c.wave + 4 * n) * 1024, dst + (c.wave + 4 * n) * 1024);
    }
}
struct Fmt { u32x4 v[2]; u32 g[8]; u32 dst[2], cdst[2]; int kind[2]; };
DI void fmt_issue(const Ctx& c, const Params& p, Fmt& m) {
    const u16 *pw = p.wqkv, *pa = p.woA, *pb = p.woB;
    asm volatile("" : "+s"(pw), "+s"(pa), "+s"(pb));
    const int lane = c.lane, rho = lane >> 1, g = rho >> 3, e = rho & 7;
#pragma unroll
    for (int r = 0; r < 2; ++r) {
        const int fr = blockIdx.x * 8 + c.wave + 2048 * r;
        int col, A, P, Q, pitch; const GAS char* src;
        if (fr < 2016) {
            const int jj = fr / 252, rem = fr - jj * 252, u = rem / 18, s = rem - u * 18;
            const int wv = u < 8 ? u : (u - 8 < 5 ? u - 8 : 6), slot = u < 8 ? 0 : 1, a = wv >> 1;
            const bool isrem = (wv & 1) && slot == 1;
            const u32 fo = (u32)(((jj * 8 + wv) * 2 + slot) * 18 + s);
            m.dst[r] = WS_WF + fo * 1024; m.cdst[r] = WS_CSP + fo * 64; m.kind[r] = isrem ? 3 : 1;
            P = s < 16 ? 16 : 4; Q = 2; pitch = 1728 * 2; src = (const GAS char*)pw;
            if (!isrem) {
                col = ((wv & 1) ? 1152 : (slot ? 576 : 0)) + 18 * (4 * jj + a) + 8 * (lane & 1);
                A = s < 16 ? 72 * (s >> 1) + 32 * (s & 1) + 4 * g : 72 * (4 * (s - 16) + g) + 64;
            } else {
                const int n = lane & 15, g2 = lane >> 4;
                col = (a == 0 ? (n < 8 ? 0 : 576) : 1152) + 18 * (4 * jj + ((n & 7) >> 1)) + 16 + (n & 1);
                A = s < 16 ? 72 * (s >> 1) + 32 * (s & 1) + 4 * g2 : 72 * (4 * (s - 16) + g2) + 64;
#pragma unroll
                for (int q = 0; q < 8; ++q) m.g[q] = *(const GAS u16*)(src + (A + P * (q >> 2) + (q & 3)) * pitch + col * 2);
            }
        } else {
            const bool on = fr < 3456;
            const int q = on ? fr - 2016 : 0, im = q / 720, rem = q - im * 720, jj = rem / 90, st = rem - jj * 90, s = st / 5, t = st - s * 5;
            m.dst[r] = WS_WIMG + (u32)(im * 8 + jj) * WIMG_BYTES + (u32)st * 1024; m.cdst[r] = 0; m.kind[r] = on ? 2 : 0;
            col = 72 * jj + 16 * t + (t == 4 ? 0 : 8 * (lane & 1));
            if (s < 16) { A = 36 * s + 18 * (g >> 1) + 4 * (g & 1); P = 8; Q = 2; } else { A = 72 * (4 * (s - 16) + g) + 16; P = 36; Q = 18; }
            src = (const GAS char*)(im ? pb : pa); pitch = 576 * 2;
        }
        if (r == 1 || m.kind[r] != 3) m.v[r] = *(const GAS u32x4*)(src + (A + P * (e >> 2) + Q * ((e >> 1) & 1) + (e & 1)) * pitch + col * 2);
    }
}
DI void fmt_finish(const Ctx& c, Fmt& m) {
    asm volatile("" : "+v"(m.v[0]), "+v"(m.v[1]), "+v"(m.g[0]), "+v"(m.g[1]), "+v"(m.g[2]), "+v"(m.g[3]), "+v"(m.g[4]), "+v"(m.g[5]), "+v"(m.g[6]), "+v"(m.g[7]));
    const int lane = c.lane, i16 = lane & 15, g = lane >> 4, q = i16 >> 2, p = i16 & 3;
#pragma unroll
    for (int r = 0; r < 2; ++r) {
        if (m.kind[r] != 0) {
            u32x4 f;
            if (m.kind[r] == 3) { const u32* w = m.g; f = u32x4{w[0] | (w[1] << 16), w[2] | (w[3] << 16), w[4] | (w[5] << 16), w[6] | (w[7] << 16)}; }
            else {
                LAS char* t = c.L + L_FMT + c.wave * 2048 + r * 1024;
                lst(t + lane * 16, m.v[r]);
                asm volatile("s_waitcnt lgkmcnt(0)" ::: "memory");
                const u32x2 lo = trr(t + (8 * g + q) * 32 + 8 * p), hi = trr(t + (8 * g + 4 + q) * 32 + 8 * p);
                f = u32x4{lo[0], lo[1], hi[0], hi[1]};
            }
            st128(c.rs, f, lane * 16, m.dst[r]);
            if (m.kind[r] != 2) {
                float cs = ((bflo(f[0]) + bfhi(f[0])) + (bflo(f[1]) + bfhi(f[1]))) + ((bflo(f[2]) + bfhi(f[2])) + (bflo(f[3]) + bfhi(f[3])));
                cs = sum_g(cs);
                if (lane < 16) st32(c.rs, asu(cs), lane * 4, m.cdst[r]);
            }
        }
    }
}

template <bool PROJ, bool NAT = false, class HK, class PRE>
DI void g1_mma(const Ctx& c, const u32x4 (&xf)[9], f32x4 (&acc)[5], HK hook, PRE pre) {
    const int lane = c.lane, kh = c.wave >> 2, q = (lane & 15) >> 2, p = lane & 3, g = lane >> 4;
    LAS char *bN, *bR, *bNo, *bNt, *bRo, *bRt;
    if (PROJ) {
        if (NAT) { bN = c.L + L_R1 + (kh * 256 + 4 * g + q) * WP + 8 * p; bR = c.L + L_R1 + (512 + 32 * kh + 8 * g + q) * WP + 8 * p; }
        else { bN = c.L + L_A1 + (kh * 288 + 4 * g + q) * WP + 8 * p; bR = c.L + L_A1 + (kh * 288 + 72 * g + q) * WP + 8 * p; }
        bNo = bN + 64 * kh; bNt = bN + 64 - 64 * kh; bRo = bR + 64 * kh; bRt = bR + 64 - 64 * kh;
    } else {
        bN = c.L + L_R1 + kh * 8 * 5120 + lane * 16; bR = c.L + L_R1 + (16 + kh) * 5120 + lane * 16;
        bNo = bN + 2048 * kh; bNt = bN + 2048 - 2048 * kh; bRo = bR + 2048 * kh; bRt = bR + 2048 - 2048 * kh;
    }
    u32x4 w[2][5];
    auto rd = [&](int sl, u32x4 (&d)[5]) {
#pragma unroll
        for (int u = 0; u < 5; ++u) {
            if (PROJ) {
                const int r0 = NAT ? (sl < 8 ? 32 * sl : 0) : (sl < 8 ? 72 * (sl >> 1) + 32 * (sl & 1) : 64), hs = sl < 8 ? 16 : 4;
                LAS char* bb = u < 2 ? (sl < 8 ? bNo : bRo) + 32 * u : (u == 2 ? (sl < 8 ? bN : bR) + 128 : (sl < 8 ? bNt : bRt) + 32 * (u - 3));
                const u32x2 lo = trr(bb + r0 * WP), hi = trr(bb + (r0 + hs) * WP);
                d[u] = u32x4{lo[0], lo[1], hi[0], hi[1]};
            } else {
                LAS char* bb = u < 2 ? (sl < 8 ? bNo : bRo) + 1024 * u : (u == 2 ? (sl < 8 ? bN : bR) + 4096 : (sl < 8 ? bNt : bRt) + 1024 * (u - 3));
                d[u] = lld<u32x4>(bb + (sl < 8 ? sl * 5120 : 0));
            }
        }
    };
    rd(0, w[0]);
#pragma unroll
    for (int u = 0; u < 5; ++u) asm volatile("" : "+v"(acc[u]));
#pragma unroll
    for (int sl = 0; sl < 9; ++sl) {
        pre(sl);
        if (sl + 1 < 9) rd(sl + 1, w[(sl + 1) & 1]);
#pragma unroll
        for (int u = 0; u < 5; ++u) MF16(acc[u], w[sl & 1][u], xf[sl]);
        hook(sl, w[sl & 1]);
    }
    asm volatile("s_nop 7" : "+v"(acc[0]), "+v"(acc[1]), "+v"(acc[2]), "+v"(acc[3]), "+v"(acc[4]));
}
DI void g1_exchange(const Ctx& c, f32x4 (&acc)[5]) {
    LAS char* xw = c.L + L_XCH + c.wave * 3072 + c.lane * 16;
    lst(xw, acc[3]); lst(xw + 1024, acc[4]); lst(xw + 2048, acc[2]);
    BAR();
    LAS char* xr = c.L + L_XCH + (c.wave ^ 4) * 3072 + c.lane * 16;
    acc[0] += lld<f32x4>(xr); acc[1] += lld<f32x4>(xr + 1024); acc[2] += lld<f32x4>(xr + 2048);
}
DI void load_xfrags(const Ctx& c, u32 buf, u32x4 (&xf)[9]) {
    const int m = c.wave & 3, kh = c.wave >> 2;
    const u32 so = buf + (u32)(c.i * 18 * 4 + m) * 1024;
#pragma unroll
    for (int sl = 0; sl < 9; ++sl) { const int s = sl < 8 ? 8 * kh + sl : 16 + kh; xf[sl] = ld128(c.rs, c.lane * 16, so + (u32)s * 4096); }
}
DI u32x4 stats_issue(const Ctx& c, u32 stbuf) {
    const u32 vo = (u32)((c.i * 16 + 2 * (c.lane >> 3)) * 64 + c.wave * 8 + (c.lane & 7)) * 8;
    const u32x2 a = ld64(c.rs, vo, stbuf), b = ld64(c.rs, vo + 512, stbuf);
    return u32x4{a[0], a[1], b[0], b[1]};
}
DI void stats_finish(const Ctx& c, u32x4 v) {
    const int tok = c.wave * 8 + (c.lane & 7);
    float s = asf(v[0]) + asf(v[2]), s2 = asf(v[1]) + asf(v[3]);
    s += __shfl_xor(s, 8); s2 += __shfl_xor(s2, 8);
    s = sum_g(s); s2 = sum_g(s2);
    if (c.lane < 8) {
        lst(c.L + L_STATP + tok * 8, f32x2{s, s2}); lst(c.L + L_STATP + 512 + tok * 8, f32x2{0.f, 0.f});
        const float mean = s * (1.f / DM), var = s2 * (1.f / DM) - mean * mean;
        lst(c.L + L_MEAN + tok * 4, mean); lst(c.L + L_MEAN + 256 + tok * 4, __builtin_amdgcn_rsqf(fmaxf(var, 0.f) + 1e-5f));
    }
}
DI void stats_issue32(const Ctx& c, u32 stbuf, u32x4 (&v)[4]) {
    const u32 vo = (u32)((c.i * 16 + (c.lane >> 4)) * 64 + (c.wave == 7 ? 32 : 0) + 2 * (c.lane & 15)) * 8;
#pragma unroll
    for (int n = 0; n < 4; ++n) v[n] = ld128(c.rs, vo + n * 4 * 512, stbuf);
}
DI void stats_finish32(const Ctx& c, const u32x4 (&v)[4]) {
    float s[4];
#pragma unroll
    for (int e = 0; e < 4; ++e) s[e] = sum_g((asf(v[0][e]) + asf(v[1][e])) + (asf(v[2][e]) + asf(v[3][e])));
    if (c.lane < 16) {
        const int tok = (c.wave == 7 ? 32 : 0) + 2 * c.lane;
        const float m0 = s[0] * (1.f / DM), m1 = s[2] * (1.f / DM);
        lst(c.L + L_MEAN + tok * 4, f32x2{m0, m1});
        lst(c.L + L_MEAN + 256 + tok * 4, f32x2{__builtin_amdgcn_rsqf(fmaxf(s[1] * (1.f / DM) - m0 * m0, 0.f) + 1e-5f), __builtin_amdgcn_rsqf(fmaxf(s[3] * (1.f / DM) - m1 * m1, 0.f) + 1e-5f)});
    }
}

struct KV { u32x4 km; u32x2 kr; u32x4 v0, v1; };
DI int vrow_of(int r) { return r < 18 ? r : 18; }
DI u32 ka_vrow(int blk, int row) { return row < 8 ? KA_V0 + blk * 128 + row * 16 : (row < 16 ? KA_V8 + blk * 128 + (row - 8) * 16 : KA_VX + blk * 64 + (row - 16) * 16); }
DI KV load_tile_ws(const Ctx& c, u32 tile, bool shared) {
    const int r = c.lane & 31, h = c.lane >> 5;
    KV t;
    if (shared) {
        t.km = ld128(c.rs, c.lane * 16, tile);
        t.kr = ld64(c.rs, KA_KREM + r * 8, tile);
        t.v0 = ld128(c.rs, ka_vrow(h, vrow_of(r)), tile);
        t.v1 = ld128(c.rs, ka_vrow(2 + h, vrow_of(r)), tile);
        return t;
    }
    t.km = ld128(c.rs, c.lane * 16, tile);
    t.kr = ld64(c.rs, KT_KREM + r * 8, tile);
    t.v0 = ld128(c.rs, KT_V + h * KT_VBLK + vrow_of(r) * 16, tile);
    t.v1 = ld128(c.rs, KT_V + (2 + h) * KT_VBLK + vrow_of(r) * 16, tile);
    return t;
}
DI f32x16 mfma32k8(u32x2 a, u32x2 b, f32x16 c) { return __builtin_amdgcn_mfma_f32_32x32x8bf16_1k(__builtin_bit_cast(s16x4, a), __builtin_bit_cast(s16x4, b), c, 0, 0, 0); }
DI f32x16 qk(const KV& t, u32x4 qm, u32x2 qr, f32x16 c0) {
    return mfma32k8(t.kr, qr, mfma32(t.km, qm, c0));
}
DI void QK1(f32x16& s, u32x4 km, u32x4 qm) { asm volatile("v_mfma_f32_32x32x16_bf16 %0, %1, %2, 0" : "=&v"(s) : "v"(km), "v"(qm)); }
DI void QK1C(f32x16& s, u32x4 km, u32x4 qm, const f32x16& c0) { asm volatile("v_mfma_f32_32x32x16_bf16 %0, %1, %2, %3" : "=&v"(s) : "v"(km), "v"(qm), "v"(c0)); }
DI void QK2(f32x16& s, u32x2 kr, u32x2 qr) { asm volatile("v_mfma_f32_32x32x8_bf16 %0, %1, %2, %0" : "+v"(s) : "v"(kr), "v"(qr)); }
DI void PV(f32x16& o, u32x4 v, u32x4 p) { asm volatile("v_mfma_f32_32x32x16_bf16 %0, %1, %2, %0" : "+v"(o) : "v"(v), "v"(p)); }
DI float EX(float x) { float r; asm volatile("v_exp_f32 %0, %1" : "=v"(r) : "v"(x)); return r; }
DI u32 CV(float a, float b) { u32 r; asm volatile("v_cvt_pk_bf16_f32 %0, %1, %2" : "=v"(r) : "v"(a), "v"(b)); return r; }
struct Pipe { float e[4]; u32x4 p1; };
template <bool NEXT = true, class F0, class F1, class F2, class F3, class F4>
DI void attn_step(const f32x16& sc, f32x16& sn, f32x16& o, Pipe& pp, const u32x4& km, const u32x2& kr, const u32x4& v0, const u32x4& v1, u32x4 qm, u32x2 qr, F0 first, F1 LK, F2 LR, F3 LV1, F4 LV0) {
    u32x4 p0; float a0, a1, a2, a3, b0, b1, b2, b3;
    if (NEXT) first(sn, km, qm);
    LK();
    pp.p1[2] = CV(pp.e[0], pp.e[1]); pp.p1[3] = CV(pp.e[2], pp.e[3]);
    a0 = EX(sc[0]); a1 = EX(sc[1]); a2 = EX(sc[2]); a3 = EX(sc[3]);
    if (NEXT) QK2(sn, kr, qr); else asm volatile("s_nop 0");
    LR();
    p0[0] = CV(a0, a1); p0[1] = CV(a2, a3);
    b0 = EX(sc[4]); b1 = EX(sc[5]); b2 = EX(sc[6]); b3 = EX(sc[7]);
    PV(o, v1, pp.p1); LV1();
    p0[2] = CV(b0, b1); p0[3] = CV(b2, b3);
    a0 = EX(sc[8]); a1 = EX(sc[9]); a2 = EX(sc[10]); a3 = EX(sc[11]);
    PV(o, v0, p0); LV0();
    pp.p1[0] = CV(a0, a1); pp.p1[1] = CV(a2, a3);
    pp.e[0] = EX(sc[12]); pp.e[1] = EX(sc[13]); pp.e[2] = EX(sc[14]); pp.e[3] = EX(sc[15]);
}
template <class F1, class F2, class F3, class F4>
DI void attn_step2(const f32x16& scx, f32x16& snx, f32x16& ox, Pipe& px, const f32x16& scy, f32x16& sny, f32x16& oy, Pipe& py,
                   const u32x4& km, const u32x2& kr, const u32x4& v0, const u32x4& v1, u32x4 qmx, u32x2 qrx, u32x4 qmy, u32x2 qry, F1 LK, F2 LR, F3 LV1, F4 LV0) {
    u32x4 p0x, p0y; float a0, a1, a2, a3, b0, b1, b2, b3, c0, c1, c2, c3, d0, d1, d2, d3;
#define GRP(MF, PD0, PD1, E0, E1, E2, E3, D0, D1, D2, D3, SRC, I) MF; PD0 = CV(E0, E1); PD1 = CV(E2, E3); D0 = EX(SRC[I]); D1 = EX(SRC[I + 1]); D2 = EX(SRC[I + 2]); D3 = EX(SRC[I + 3])
    GRP(QK1(snx, km, qmx), px.p1[2], px.p1[3], px.e[0], px.e[1], px.e[2], px.e[3], a0, a1, a2, a3, scx, 0);
    GRP(QK1(sny, km, qmy); LK(), py.p1[2], py.p1[3], py.e[0], py.e[1], py.e[2], py.e[3], b0, b1, b2, b3, scy, 0);
    GRP(QK2(snx, kr, qrx), p0x[0], p0x[1], a0, a1, a2, a3, c0, c1, c2, c3, scx, 4);
    GRP(QK2(sny, kr, qry); LR(), p0y[0], p0y[1], b0, b1, b2, b3, d0, d1, d2, d3, scy, 4);
    GRP(PV(ox, v1, px.p1), p0x[2], p0x[3], c0, c1, c2, c3, a0, a1, a2, a3, scx, 8);
    GRP(PV(oy, v1, py.p1); LV1(), p0y[2], p0y[3], d0, d1, d2, d3, b0, b1, b2, b3, scy, 8);
    GRP(PV(ox, v0, p0x), px.p1[0], px.p1[1], a0, a1, a2, a3, px.e[0], px.e[1], px.e[2], px.e[3], scx, 12);
    GRP(PV(oy, v0, p0y); LV0(), py.p1[0], py.p1[1], b0, b1, b2, b3, py.e[0], py.e[1], py.e[2], py.e[3], scy, 12);
#undef GRP
}
DI void attn_finish(f32x16& o, Pipe& pp, u32x4 v1) {
    asm volatile("s_nop 0");
    pp.p1[2] = CV(pp.e[0], pp.e[1]); pp.p1[3] = CV(pp.e[2], pp.e[3]);
    asm volatile("s_nop 1");
    PV(o, v1, pp.p1);
    asm volatile("s_nop 7\n\ts_nop 3" : "+v"(o));
}
DI void attn_finish2(f32x16& ox, Pipe& px, f32x16& oy, Pipe& py, u32x4 v1) {
    asm volatile("s_nop 0");
    px.p1[2] = CV(px.e[0], px.e[1]); px.p1[3] = CV(px.e[2], px.e[3]);
    py.p1[2] = CV(py.e[0], py.e[1]); py.p1[3] = CV(py.e[2], py.e[3]);
    PV(ox, v1, px.p1);
    asm volatile("s_nop 0");
    PV(oy, v1, py.p1);
    asm volatile("s_nop 7\n\ts_nop 3" : "+v"(ox), "+v"(oy));
}
DI void pexp(const f32x16& s, u32x4& p0, u32x4& p1) {
    float e[16];
#pragma unroll
    for (int r = 0; r < 16; ++r) e[r] = __builtin_amdgcn_exp2f(s[r]);
    p0 = u32x4{pk(e[0], e[1]), pk(e[2], e[3]), pk(e[4], e[5]), pk(e[6], e[7])};
    p1 = u32x4{pk(e[8], e[9]), pk(e[10], e[11]), pk(e[12], e[13]), pk(e[14], e[15])};
}
DI float rowmax(const f32x16& s) {
    float a = fmaxf(fmaxf(s[0], s[1]), s[2]), b = fmaxf(fmaxf(s[3], s[4]), s[5]);
#pragma unroll
    for (int r = 6; r < 16; r += 2) { if (r & 2) a = fmaxf(fmaxf(a, s[r]), s[r + 1]); else b = fmaxf(fmaxf(b, s[r]), s[r + 1]); }
    return max_h(fmaxf(a, b));
}
DI f32x16 mask_c(int lane, bool diag) {
    const int r = lane & 31, h = lane >> 5; f32x16 m;
#pragma unroll
    for (int e = 0; e < 16; ++e) { const bool ok = diag ? (crow(e, h) <= r) : (crow(e, h) > r); m[e] = ok ? 0.f : NEG_BIG; }
    return m;
}
DI f32x16 attn_slow(const Ctx& c, u32 tile0, int n0, int n1, bool masked, u32x4 qm, u32x2 qr) {
    const int h = c.lane >> 5;
    qr[1] = 0u;
    f32x16 o = {}; float m = NEG_BIG;
    for (int n = n0; n <= n1; ++n) {
        const KV t = load_tile_ws(c, tile0 + (u32)n * TILE_B, !masked);
        f32x16 z = {};
        f32x16 s = qk(t, qm, qr, z);
        if (masked && (n == n1 || n == n1 - 8)) {
            const int r = c.lane & 31;
#pragma unroll
            for (int e = 0; e < 16; ++e) { const bool ok = (n == n1) ? (crow(e, h) <= r) : (crow(e, h) > r); s[e] = ok ? s[e] : NEG_BIG; }
        }
        const float mn = fmaxf(m, rowmax(s)), al = __builtin_amdgcn_exp2f(m - mn);
        m = mn;
#pragma unroll
        for (int e = 0; e < 16; ++e) { o[e] *= al; s[e] = s[e] <= NEG_BIG ? NEG_BIG : s[e] - mn; }
        u32x4 p0, p1; pexp(s, p0, p1);
        o = mfma32(t.v0, p0, o); o = mfma32(t.v1, p1, o);
    }
    return o;
}
DI bool attn_unsafe(const f32x16& o) {
    float z = 0.f;
#pragma unroll
    for (int e = 0; e < 10; ++e) z = __builtin_fmaf(o[e], 0.f, z);
    const float l = o[10];
    return __any(!(l > 0x1p-40f && l < 0x1p30f && z == 0.f));
}
DI void store_o(const Ctx& c, u32 obuf, const f32x16& o, int a, int t) {
    const int r = c.lane & 31, h = c.lane >> 5;
    const float inv = __builtin_amdgcn_rcpf(o[10]);
    const u32x4 mainp = {pk(o[0] * inv, o[1] * inv), pk(o[2] * inv, o[3] * inv), pk(o[4] * inv, o[5] * inv), pk(o[6] * inv, o[7] * inv)};
    const int m = 2 * t + (r >> 4), tok = r & 15;
    const u32 so = obuf + (u32)(c.i * 18 * 4) * 1024;
    st128(c.rs, mainp, (u32)((2 * c.j + (a >> 1)) * 4 + m) * 1024 + ((2 * (a & 1) + h) * 16 + tok) * 16, so);
    if (h == 0) st32(c.rs, pk(o[8] * inv, o[9] * inv), (u32)((16 + (c.j >> 2)) * 4 + m) * 1024 + ((c.j & 3) * 16 + tok) * 16 + 4 * a, so);
}

template <int rep>
DI void a_proj(Ctx& c0, const Params& p) {
    Ctx c = fresh(c0);
    const int lane = c.lane, m = c.wave & 3, kh = c.wave >> 2, tok = lane & 15, g = lane >> 4;
    u32x4 xf[9]; f32x4 acc[5] = {};
    f32x4 s1 = {}, s2 = {}, ca = {}, cb = {};
    Fmt fm; u64 e0 = 0; u32x4 stv;
    if (rep == 0) {
        e0 = __hip_atomic_load((GAS u64*)(p.ws + WS_EPOCH), __ATOMIC_RELAXED, __HIP_MEMORY_SCOPE_AGENT);
        const char* xt = (const char*)p.x + (size_t)(c.i * 64) * (DM * 2);
#pragma unroll
        for (int n = 0; n < 8; ++n) {
            const int row = c.wave * 8 + n;
            glds_in(xt + row * (DM * 2) + (lane ^ (row & 15)) * 16, c.Lb + (row < 60 ? L_XIN + row * 1024 : L_XIN2 + (row - 60) * 1024));
        }
        xf[8] = *(const u32x4*)(xt + (m * 16 + tok) * (DM * 2) + (512 + 32 * kh + 8 * g) * 2);
        load_wimage_a(c, p);
        fmt_issue(c, p, fm);
        STAMP(13);
        if (c.wave < 2) { if (fm.kind[0] == 3) asm volatile("s_waitcnt vmcnt(19)" ::: "memory"); else asm volatile("s_waitcnt vmcnt(12)" ::: "memory"); }
        else { if (fm.kind[0] == 3) asm volatile("s_waitcnt vmcnt(14)" ::: "memory"); else asm volatile("s_waitcnt vmcnt(7)" ::: "memory"); }
        BAR();
        STAMP(18);
        asm volatile("" : "+v"(xf[8]), "+v"(e0));
        { const int row = m * 16 + tok, t2 = tok ^ (g >> 1);
          LAS char* xb = c.L + (row < 60 ? L_XIN + row * 1024 : L_XIN2 + (row - 60) * 1024) + kh * 512 + 8 * (g & 1);
#pragma unroll
          for (int sl = 0; sl < 8; ++sl) {
              const int K = 4 * (sl & 3);
              const u32x2 lo = lld<u32x2>(xb + (sl >> 2) * 256 + ((K ^ t2) * 16)), hi = lld<u32x2>(xb + (sl >> 2) * 256 + (((K + 2) ^ t2) * 16));
              xf[sl] = u32x4{lo[0], lo[1], hi[0], hi[1]};
          } }
        asm volatile("s_nop 1" : "+v"(xf[0]), "+v"(xf[1]), "+v"(xf[2]), "+v"(xf[3]), "+v"(xf[4]), "+v"(xf[5]), "+v"(xf[6]), "+v"(xf[7]), "+v"(xf[8]));
        STAMP(2);
    } else {
        wait_group(c, 5);
        load_xfrags(c, WS_XF + 1 * XF_BYTES, xf);
        stv = stats_issue(c, WS_ST + 1 * ST_BYTES);
        asm volatile("s_waitcnt vmcnt(11)" ::: "memory"); BAR();
        STAMP(32);
    }
    if (rep != 0) { g1_mma<true>(c, xf, acc, [](int, const u32x4 (&)[5]) {}, [](int) {}); stats_finish(c, stv); }
    else {
        u32x4 ones = ones4();
        const int tlc = m < 2 ? 2 * kh + m : 2 * (1 - kh) + (m - 2), q = (lane & 15) >> 2, pp = lane & 3;
        LAS char* bN = c.L + L_R1 + (kh * 256 + 4 * g + q) * WP + 8 * pp + 32 * tlc;
        LAS char* bR = c.L + L_R1 + (512 + 32 * kh + 8 * g + q) * WP + 8 * pp + 32 * tlc;
        asm volatile("" : "+v"(s1), "+v"(s2), "+v"(ca), "+v"(cb), "+v"(ones));
        u32x4 wx[2];
        auto rdx = [&](int sl) {
            const int r0 = sl < 8 ? 32 * sl : 0, hs = sl < 8 ? 16 : 4; LAS char* b = sl < 8 ? bN : bR;
            const u32x2 lo = trr(b + r0 * WP), hi = trr(b + (r0 + hs) * WP);
            wx[sl & 1] = u32x4{lo[0], lo[1], hi[0], hi[1]}; };
        rdx(0);
        g1_mma<true, true>(c, xf, acc, [&](int sl, const u32x4 (&w)[5]) {
            if (sl + 1 < 9) rdx(sl + 1);
            MF16(s1, ones, xf[sl]); MF16(s2, xf[sl], xf[sl]); MF16(cb, w[2], ones); MF16(ca, wx[sl & 1], ones);
        }, [&](int sl) {
            if (sl == 3) {
                if (fm.kind[0] == 3) asm volatile("s_waitcnt vmcnt(9)" ::: "memory"); else asm volatile("s_waitcnt vmcnt(2)" ::: "memory");
                BAR();
            }
        });
        if (c.tid < 180) st128(c.rs, lld<u32x4>(c.L + L_R1 + c.i * 2880 + c.tid * 16), c.tid * 16, WS_WIMGA + (u32)c.j * WIMG_BYTES + (u32)c.i * 2880);
        asm volatile("s_nop 7" : "+v"(s1), "+v"(s2), "+v"(ca), "+v"(cb));
        const int rg = tok & 3;
        float d = rg == 0 ? s2[0] : (rg == 1 ? s2[1] : (rg == 2 ? s2[2] : s2[3]));
        d = sum_g(g == (tok >> 2) ? d : 0.f);
        if (g == 0) lst(c.L + L_STATP + kh * 512 + (m * 16 + tok) * 8, f32x2{s1[0], d});
        const int tl = m < 2 ? 2 * kh + m : 2 * (1 - kh) + (m - 2);
        if (tok == 0) { lst(c.L + L_CSA + (kh * 5 + tl) * 64 + g * 16, ca); if (m == 0) lst(c.L + L_CSA + (kh * 5 + 4) * 64 + g * 16, cb); }
    }
    STAMP(3 + rep * 30);
    g1_exchange(c, acc);
    const f32x2 sa = lld<f32x2>(c.L + L_STATP + (m * 16 + tok) * 8), sb = lld<f32x2>(c.L + L_STATP + 512 + (m * 16 + tok) * 8);
    const float mean = (sa[0] + sb[0]) * (1.f / DM), var = (sa[1] + sb[1]) * (1.f / DM) - mean * mean;
    const float rstd = __builtin_amdgcn_rsqf(fmaxf(var, 0.f) + 1e-5f);
    LAS char* stg = c.L + L_STG + (m * 16 + tok) * STG_P + g * 8;
    auto fin = [&](int u, int t, float sc) {
        const f32x4 cs = lld<f32x4>(c.L + L_CSA + t * 64 + g * 16) + lld<f32x4>(c.L + L_CSA + (5 + t) * 64 + g * 16);
        f32x4 v;
#pragma unroll
        for (int e = 0; e < 4; ++e) v[e] = (acc[u][e] - mean * cs[e]) * sc;
        return u32x2{pk(v[0], v[1]), pk(v[2], v[3])};
    };
    if (kh == 1) {
        const u32x2 d4 = fin(2, 4, g < 2 ? rstd * QSCALE : rstd);
        lst(stg + 128, d4);
        const u32 kva = WS_KVA + rep * KVA_BYTES + (u32)(2 * c.i + (m >> 1)) * TILE_B;
        const int j = c.j, key = (m & 1) * 16 + tok;
        if (j < 2) { if (g >= 2) st64(c.rs, d4, j * 512 + key * 16 + (g - 2) * 8, kva); }
        else if (j == 2) { if (g == 3) st64(c.rs, u32x2{d4[1], 0x00003f80u}, KA_KREM + key * 8, kva); }
        else if (j < 6) {
            asm volatile("s_waitcnt lgkmcnt(0)" ::: "memory");
            const int q = (lane & 15) >> 2, pp = lane & 3;
            LAS char* s = c.L + L_STG + (16 * m + q + 4 * (pp >> 1)) * STG_P + 144 + 8 * (pp & 1);
            const u32x2 lo = trr(s), hi = trr(s + 8 * STG_P);
            const int dl = lane & 7, blk = (m & 1) * 2 + ((lane >> 3) & 1);
            if (j < 5) { if (lane < 16) st128(c.rs, u32x4{lo[0], lo[1], hi[0], hi[1]}, (j == 3 ? KA_V0 : KA_V8) + blk * 128 + dl * 16, kva); }
            else {
                const u32x4 on = ones4();
                const u32x4 v = dl >= 6 ? u32x4{lo[0], lo[1], hi[0], hi[1]} : on;
                if (lane < 16 && (dl >= 6 || dl < 2)) st128(c.rs, v, KA_VX + blk * 64 + (dl >= 6 ? dl - 6 : 2 + dl) * 16, kva);
            }
        }
    }
    lst(stg + (2 * kh) * 32, fin(0, 2 * kh, rstd * QSCALE));
    lst(stg + (2 * kh + 1) * 32, fin(1, 2 * kh + 1, rstd * QSCALE));
    if (rep == 0) {
        fmt_finish(c, fm);
        const u64 e = e0 + 1; c0.elo = c.elo = __builtin_amdgcn_readfirstlane((u32)e); c0.ehi = c.ehi = __builtin_amdgcn_readfirstlane((u32)(e >> 32));
    }
    STAMP(4 + rep * 30);
    publish(c, rep * 6);
}

DI int unit1_of(int wave) { return (wave == 5 || wave == 7) ? (wave - 1) * 2 + 1 : wave * 2 + 1; }
DI void load_wring(const Ctx& c, u32x4 (&w0)[WRING], u32x4 (&w1)[WRING]) {
    const u32 ws0 = WS_WF + (u32)(c.j * 16 + c.wave * 2) * 18 * 1024, ws1 = WS_WF + (u32)(c.j * 16 + unit1_of(c.wave)) * 18 * 1024;
#pragma unroll
    for (int s = 0; s < WRING; ++s) { w0[s] = ld128(c.rs, c.lane * 16, ws0 + s * 1024); w1[s] = ld128(c.rs, c.lane * 16, ws1 + s * 1024); }
}
DI void colsums_b(const Ctx& c) {
    const int tok = c.lane & 15, g = c.lane >> 4;
    if (c.wave >= 1) {
        const int extra = c.wave == 1 ? 0 : c.wave;
        u32 v[4][5];
#pragma unroll
        for (int q = 0; q < 4; ++q) {
            const int unit = (q < 2 ? c.wave : extra) * 2 + (q & 1);
#pragma unroll
            for (int u = 0; u < 5; ++u) { const int s = g + 4 * u; v[q][u] = (q < 2 || c.wave == 1) ? ld32(c.rs, (s < 18 ? s : 0) * 64 + tok * 4, WS_CSP + (u32)((c.j * 16 + unit) * 18) * 64) : 0u; }
        }
#pragma unroll
        for (int q = 0; q < 4; ++q) {
            const int unit = (q < 2 ? c.wave : extra) * 2 + (q & 1);
            float cs = (asf(v[q][0]) + asf(v[q][1])) + (asf(v[q][2]) + asf(v[q][3])) + (g < 2 ? asf(v[q][4]) : 0.f);
            cs = sum_g(cs);
            if (g == 0 && (q < 2 || c.wave == 1)) lst(c.L + L_CSB + unit * 64 + tok * 4, cs);
        }
    }
}
struct ATail { f32x16 o; u32x4 qm; u32x2 qr; };
DI void a_attn(const Ctx& c0, const Params& p, int rep, ATail& st) {
    const Ctx c = fresh(c0);
    const int lane = c.lane, r = lane & 31, h = lane >> 5, a = c.wave & 3, kh = c.wave >> 2;
    const int ph = rep * 6;
    const u32 kva = WS_KVA + rep * KVA_BYTES;
    u32x4 qmx, qmy; u32x2 qrx, qry;
    { LAS char* s = c.L + L_STG + (32 * kh + r) * STG_P + 36 * a;
      qmx = u32x4{lld<u32>(s + 16 * h), lld<u32>(s + 16 * h + 4), lld<u32>(s + 16 * h + 8), lld<u32>(s + 16 * h + 12)};
      const u32 rx = lld<u32>(s + 32);
      s += (32 - 64 * kh) * STG_P;
      qmy = u32x4{lld<u32>(s + 16 * h), lld<u32>(s + 16 * h + 4), lld<u32>(s + 16 * h + 8), lld<u32>(s + 16 * h + 12)};
      const u32 ry = lld<u32>(s + 32);
      qrx = u32x2{h == 0 ? rx : 0u, 0u}; qry = u32x2{h == 0 ? ry : 0u, 0u}; }
    const int ch0 = c.i >> 2;
    auto chunk_ids = [&](int n) { const int ch = (ch0 + n) & 7; return ch * 32 + (lane & 31); };
    u32 pvo[5];
#pragma unroll
    for (int k = 0; k < 5; ++k) {
        const u32 S = (u32)((c.wave & 3) + 4 * k) * 64 + lane, tile = (S * 52429u) >> 23, ps = S - tile * 160;
        const u32 v = ps - 64, blk = (v * 3450u) >> 16, row = v - blk * 19;
        const u32 src = ps < 64 ? ps * 16 : (ps < 140 ? ka_vrow(blk, row) : (ps < 156 ? KA_KREM + (ps - 140) * 16 : ps * 16));
        pvo[k] = tile * TILE_B + src;
    }
    auto issue_piece = [&](int n, int k) {
        const int ch = (ch0 + n) & 7; const u32 so = kva + (u32)ch * CHUNK_B; const u32 dst = c.Lb + L_RING + (n % 3) * CHUNK_B;
        glds_ws(c.srd, pvo[k], so, dst + ((c.wave & 3) + 4 * k) * 1024);
    };
    auto issue_chunk8 = [&](int n) {
        if (c.wave < 4) { issue_piece(n, 0); issue_piece(n, 2); issue_piece(n, 4); } else { issue_piece(n, 1); issue_piece(n, 3); }
    };
    if (c.wave == 0) poll(c, ph, lane < 32 ? chunk_ids(0) : chunk_ids(1));
    BAR();
    STAMP(6 + rep * 30);
    issue_chunk8(0);
    if (c.wave < 4) { issue_piece(1, 0); issue_piece(1, 1); issue_piece(1, 2); issue_piece(1, 3); issue_piece(1, 4); asm volatile("s_waitcnt vmcnt(5)" ::: "memory"); }
    else DRAIN();
    BAR();
    STAMP(7 + rep * 30); if (rep == 0) CSTAMP(61);
    if (rep == 1) { CSTAMPW(0, 4); CSTAMPW(8, 0); }
    const int vr = vrow_of(r);
    LAS char* kb = c.L + L_RING + lane * 16;
    LAS char* rb = c.L + L_RING + KT_KREM + r * 8;
    LAS char* vb = c.L + L_RING + KT_V + h * KT_VBLK + vr * 16;
    f32x16 sAx, sBx, sAy, sBy, ox, oy;
    const u32x4 k0m = lld<u32x4>(kb); const u32x2 k0r = lld<u32x2>(rb);
    kb += kh * 4 * TILE_B; rb += kh * 4 * TILE_B; vb += kh * 4 * TILE_B;
    const u32x4 kom = lld<u32x4>(kb); const u32x2 kor = lld<u32x2>(rb);
    u32x4 km = lld<u32x4>(kb + TILE_B), v0 = lld<u32x4>(vb), v1 = lld<u32x4>(vb + 2 * KT_VBLK); u32x2 kr = lld<u32x2>(rb + TILE_B);
    Pipe px, py;
    { u32 z = 0u; asm volatile("" : "+v"(z));
      px.e[0] = px.e[1] = px.e[2] = px.e[3] = asf(z); px.p1 = u32x4{z, z, z, z}; py = px; }
    asm volatile("" : "+v"(px.p1), "+v"(px.e[0]), "+v"(px.e[1]), "+v"(px.e[2]), "+v"(px.e[3]), "+v"(qmx), "+v"(qrx));
    asm volatile("s_nop 1" : "+v"(py.p1), "+v"(py.e[0]), "+v"(py.e[1]), "+v"(py.e[2]), "+v"(py.e[3]), "+v"(qmy), "+v"(qry));
    QK1(ox, px.p1, px.p1); QK1(oy, px.p1, px.p1);
    QK1(sBx, k0m, qmx); QK1(sBy, k0m, qmy); QK2(sBx, k0r, qrx); QK2(sBy, k0r, qry);
    QK1(sAx, kom, qmx); QK1(sAy, kom, qmy);
    asm volatile("s_nop 7\n\ts_nop 1" : "+v"(sBx), "+v"(sBy));
    { const float mx = rowmax(sBx), my = rowmax(sBy);
      if (h == 0) { qrx[1] = pk(-mx, 0.f); qry[1] = pk(-my, 0.f); } }
    asm volatile("s_nop 1" : "+v"(qrx), "+v"(qry));
    QK2(sAx, kor, qrx); QK2(sAy, kor, qry);
    asm volatile("s_nop 7\n\ts_nop 3" : "+v"(sAx), "+v"(sAy));
    if (rep == 1) { CSTAMPW(1, 4); CSTAMPW(9, 0); }
    int slot = 0;
    const u64 want = ((u64)c.ehi << 32) | c.elo;
    const u32 fvo = (lane & 31) * 128;
    u64 fl = want;
    if (c.wave == 0) flag_load(fl, ~want, fvo, c.srd, WS_FLAG + (u32)(ph * 256 + ((ch0 + 2) & 7) * 32) * 128);
#pragma unroll 1
    for (int n = 0; n < 8; ++n) {
        const int nslot = slot == 2 ? 0 : slot + 1;
        LAS char* kc = kb + slot * CHUNK_B; LAS char* rc = rb + slot * CHUNK_B; LAS char* vc = vb + slot * CHUNK_B;
        LAS char* kn = kb + nslot * CHUNK_B; LAS char* rn = rb + nslot * CHUNK_B; LAS char* vn = vb + nslot * CHUNK_B;
#define A_TILE(U, SCX, SNX, SCY, SNY, X1, X2) asm volatile("" :: "v"(km), "v"(kr), "v"(v1));        \
        attn_step2(SCX, SNX, ox, px, SCY, SNY, oy, py, km, kr, v0, v1, qmx, qrx, qmy, qry, \
            [&] { km = lld<u32x4>((U) < 2 ? kc + ((U) + 2) * TILE_B : kn + ((U) - 2) * TILE_B); X1; }, \
            [&] { kr = lld<u32x2>((U) < 2 ? rc + ((U) + 2) * TILE_B : rn + ((U) - 2) * TILE_B); }, \
            [&] { v1 = lld<u32x4>(vc + (U) * TILE_B + 2 * KT_VBLK); X2; }, \
            [&] { v0 = lld<u32x4>((U) < 3 ? vc + ((U) + 1) * TILE_B : vn); })
        A_TILE(0, sAx, sBx, sAy, sBy, (void)0, (void)0); A_TILE(1, sBx, sAx, sBy, sAy, (void)0, (void)0);
        if (__builtin_expect(c.wave < 4, 0)) {
            asm volatile("s_waitcnt vmcnt(0)" : "+v"(fl) :: "memory");
            if (__builtin_expect(!__all(fl == want), 0)) poll(c, ph, chunk_ids(n + 2));
        }
        BAR();
        if (__builtin_expect(c.wave < 4, 0)) {
            fl = want;
            if (c.wave == 0 && n + 3 < 8) flag_load(fl, ~want, fvo, c.srd, WS_FLAG + (u32)(ph * 256 + ((ch0 + n + 3) & 7) * 32) * 128);
            if (n + 2 < 8) { issue_piece(n + 2, 0); issue_piece(n + 2, 1); issue_piece(n + 2, 2); issue_piece(n + 2, 3); issue_piece(n + 2, 4); }
            if (n >= 5) load_wimage_o4(c, 0, n - 5);
            if (rep == 0 && n == 0) {
                u32 z0 = 0u; asm volatile("" : "+v"(z0));
                const u32x4 z = {z0, z0, z0, z0};
#pragma unroll
                for (int q = 0; q < 5; ++q)
                    st128(c.rs, z, lane * 16 + q * 1024, WS_KVB + (u32)((4 * c.j + c.wave) * 64 + 2 * c.i) * TILE_B);
                if (c.wave < 2) st128(c.rs, z, (u32)((16 + (c.j >> 2)) * 4 + (lane >> 4)) * 1024 + ((c.j & 3) * 16 + (lane & 15)) * 16, WS_OF + c.wave * XF_BYTES + (u32)(c.i * 72) * 1024);
            }
        }
        A_TILE(2, sAx, sBx, sAy, sBy, (void)0, (void)0); A_TILE(3, sBx, sAx, sBy, sAy, (void)0, (void)0);
#undef A_TILE
        slot = nslot;
    }
    if (rep == 1) { CSTAMPW(2, 4); CSTAMPW(10, 0); }
    attn_finish2(ox, px, oy, py, v1);
    { LAS char* mine = c.L + L_RING + kh * 2 * CHUNK_B + a * 3072 + lane * 16;
#pragma unroll
      for (int e = 0; e < 3; ++e) lst(mine + e * 1024, f32x4{oy[4 * e], oy[4 * e + 1], oy[4 * e + 2], oy[4 * e + 3]}); }
    st.o = ox; st.qm = qmx; st.qr = qrx;
}

template <bool ISB, class HK>
DI void out_proj(const Ctx& c0, const Params& p, int rep, u32 (&xres)[3][2], HK behind_x) {
    const Ctx c = fresh(c0);
    const int lane = c.lane, m = c.wave & 3, kh = c.wave >> 2, tok = lane & 15, g = lane >> 4;
    const int layer = rep * 2 + (ISB ? 1 : 0);
    wait_group(c, rep * 6 + (ISB ? 4 : 1));
    STAMP((ISB ? 25 : 10) + rep * 30);
    u32x4 xf[9]; f32x4 acc[5] = {};
    load_xfrags(c, WS_OF + (rep * RP * 2 + (ISB ? 1 : 0)) * XF_BYTES, xf);
    __builtin_amdgcn_sched_barrier(0);
    behind_x(c);
    if (ISB) {
        asm volatile("s_waitcnt vmcnt(9)" ::: "memory"); BAR();
    }
    g1_mma<false>(c, xf, acc, [](int, const u32x4 (&)[5]) {}, [](int) {});
    STAMP((ISB ? 26 : 11) + rep * 30);
    g1_exchange(c, acc);
    STAMP((ISB ? 27 : 12) + rep * 30);
#pragma unroll
    for (int u = 0; u < 3; ++u) {
        const float v0 = bflo(xres[u][0]) + acc[u][0], v1 = bfhi(xres[u][0]) + acc[u][1], v2 = bflo(xres[u][1]) + acc[u][2], v3 = bfhi(xres[u][1]) + acc[u][3];
        xres[u][0] = pk(v0, v1); xres[u][1] = pk(v2, v3);
    }
    if (ISB && rep == 1) {
        char* o = (char*)p.out + (size_t)(c.i * 64 + m * 16 + tok) * (DM * 2) + (c.j * 72 + 4 * g) * 2;
        *(u32x2*)(o + (2 * kh) * 32) = u32x2{xres[0][0], xres[0][1]};
        *(u32x2*)(o + (2 * kh + 1) * 32) = u32x2{xres[1][0], xres[1][1]};
        if (kh == 1 && g < 2) *(u32x2*)(o + 128) = u32x2{xres[2][0], xres[2][1]};
        return;
    }
    const int xi = layer == 0 ? 0 : (layer == 1 ? 1 : 2 * RP);
    const u32 so = WS_XF + xi * XF_BYTES + (u32)(c.i * 18 * 4 + m) * 1024;
    st128(c.rs, u32x4{xres[0][0], xres[0][1], xres[1][0], xres[1][1]}, lane * 16, so + (u32)(2 * c.j + kh) * 4096);
    if (kh == 1 && g < 2) st64(c.rs, u32x2{xres[2][0], xres[2][1]}, ((c.j & 3) * 16 + tok) * 16 + 8 * g, so + (u32)(16 + (c.j >> 2)) * 4096);
    __builtin_amdgcn_sched_barrier(0);
    float s1 = 0.f, s2 = 0.f;
#pragma unroll
    for (int u = 0; u < 3; ++u) {
        u32 d0 = xres[u][0], d1 = xres[u][1];
        asm volatile("" : "+v"(d0), "+v"(d1));
        if (u == 2 && !(kh == 1 && g < 2)) { d0 = 0u; d1 = 0u; }
        const float a0 = bflo(d0), a1 = bfhi(d0), a2 = bflo(d1), a3 = bfhi(d1);
        s1 += (a0 + a1) + (a2 + a3); s2 += (a0 * a0 + a1 * a1) + (a2 * a2 + a3 * a3);
    }
    s1 = sum_g(s1); s2 = sum_g(s2);
    if (g == 0) st64(c.rs, u32x2{asu(s1), asu(s2)}, (u32)((c.i * 16 + 2 * c.j + kh) * 64 + m * 16 + tok) * 8, WS_ST + xi * ST_BYTES);
    publish(c, rep * 6 + (ISB ? 5 : 2));
    if (!ISB && rep == 0) colsums_b(c);
    if (ISB) load_wimage_part<36, 90>(c, WS_WIMGA, 1, L_A1);
}

DI void a_tail(const Ctx& c0, const Params& p, int rep, ATail& st, u32 (&xres)[3][2], u32x4 (&w0)[WRING], u32x4 (&w1)[WRING]) {
    const Ctx c = fresh(c0);
    const int lane = c.lane, a = c.wave & 3, kh = c.wave >> 2;
    BAR();
    LAS char* theirs = c.L + L_RING + (1 - kh) * 2 * CHUNK_B + a * 3072 + lane * 16;
#pragma unroll
    for (int e = 0; e < 3; ++e) { const f32x4 v = lld<f32x4>(theirs + e * 1024); st.o[4 * e] += v[0]; st.o[4 * e + 1] += v[1]; st.o[4 * e + 2] += v[2]; st.o[4 * e + 3] += v[3]; }
    STAMP(8 + rep * 30); if (rep == 0) CSTAMP(62);
    if (rep == 1) { CSTAMPW(3, 4); CSTAMPW(11, 0); }
    if (attn_unsafe(st.o)) st.o = attn_slow(c, WS_KVA + rep * KVA_BYTES, 0, 63, false, st.qm, st.qr);
    store_o(c, WS_OF + (rep * RP * 2) * XF_BYTES, st.o, a, kh);
    if (rep == 1) { CSTAMPW(4, 4); CSTAMPW(12, 0); }
    publish(c, rep * 6 + 1);
    load_wring(c, w0, w1);
    STAMP(9 + rep * 30);
    out_proj<false>(c, p, rep, xres, [&](const Ctx& cc) {
        if (rep == 0) {
            const int m = cc.wave & 3, k2 = cc.wave >> 2, tok = cc.lane & 15, g = cc.lane >> 4;
            const char* rr = (const char*)p.x + (size_t)(cc.i * 64 + m * 16 + tok) * (DM * 2) + (cc.j * 72 + 4 * g) * 2;
            const u32x2 r0 = *(const u32x2*)(rr + (2 * k2) * 32), r1 = *(const u32x2*)(rr + (2 * k2 + 1) * 32), r2 = *(const u32x2*)(rr + (g < 2 ? 128 : 0));
            xres[0][0] = r0[0]; xres[0][1] = r0[1]; xres[1][0] = r1[0]; xres[1][1] = r1[1]; xres[2][0] = r2[0]; xres[2][1] = r2[1];
        }
    });
}

struct T_ { static constexpr bool value = true; };
struct F_ { static constexpr bool value = false; };
template <int V> struct I_ { static constexpr int value = V; };
DI void b_proj(const Ctx& c0, const Params& p, int rep, u32x4 (&w0)[WRING], u32x4 (&w1)[WRING], u32x4& qm, u32x2& qr) {
    const Ctx c = fresh(c0);
    const int lane = c.lane, tok = lane & 15, g = lane >> 4, a = c.wave >> 1, odd = c.wave & 1;
    wait_group(c, rep * 6 + 2);
    STAMP(15 + rep * 30);
    const int xi = rep == 0 ? 0 : 2 * RP;
    u32x4 stv[4];
    if (c.wave == 5 || c.wave == 7) stats_issue32(c, WS_ST + xi * ST_BYTES, stv);
    { const u32 so = WS_XF + xi * XF_BYTES + (u32)(c.i * 72) * 1024;
#pragma unroll
      for (int n = 0; n < 9; ++n) glds_ws(c.srd, lane * 16, so + (c.wave + 8 * n) * 1024, c.Lb + L_XB + (c.wave + 8 * n) * 1024); }
    asm volatile("s_waitcnt vmcnt(5)" ::: "memory"); BAR();
    STAMP(16 + rep * 30);
    f32x4 a0[4] = {}, a1[4] = {};
    const u32 wso = WS_WF + (u32)(c.j * 16 + c.wave * 2) * 18 * 1024, wso1 = WS_WF + (u32)(c.j * 16 + unit1_of(c.wave)) * 18 * 1024;
    LAS char* xb = c.L + L_XB + lane * 16;
    const bool t0 = !odd, t1 = !odd || a == 0, has1 = !odd || a < 2;
    auto body = [&](auto T0, auto T1, auto M0, auto M1, auto ST) {
        u32x4 x[2][4];
#pragma unroll
        for (int m = 0; m < 4; ++m) { x[0][m] = lld<u32x4>(xb + m * 1024); asm volatile("" : "+v"(a0[m]), "+v"(a1[m])); }
        if (ST.value) stats_finish32(c, stv);
#pragma unroll
        for (int s = 0; s < 18; ++s) {
            if (s == 7) {
                asm volatile("s_waitcnt vmcnt(14)" ::: "memory");
                static_assert(18 - WRING >= 7, "steps 0..6 each refill the ring");
                BAR();
            }
            if (s + 1 < 18) {
#pragma unroll
                for (int m = 0; m < 4; ++m) x[(s + 1) & 1][m] = lld<u32x4>(xb + ((s + 1) * 4 + m) * 1024);
            }
#pragma unroll
            for (int m = 0; m < 4; ++m) {
                if (T0.value) MF16(a0[m], w0[s % WRING], x[s & 1][m]); else MF16(a0[m], x[s & 1][m], w0[s % WRING]);
                if (m >= M0.value && m < M1.value) { if (T1.value) MF16(a1[m], w1[s % WRING], x[s & 1][m]); else MF16(a1[m], x[s & 1][m], w1[s % WRING]); }
            }
            if (s + WRING < 18) { w0[s % WRING] = ld128(c.rs, lane * 16, wso + (WRING + s) * 1024); w1[s % WRING] = ld128(c.rs, lane * 16, wso1 + (WRING + s) * 1024); }
        }
        asm volatile("s_nop 7" : "+v"(a0[0]), "+v"(a0[1]), "+v"(a0[2]), "+v"(a0[3]), "+v"(a1[0]), "+v"(a1[1]), "+v"(a1[2]), "+v"(a1[3]));
    };
    if (!odd) { if (a < 2) body(T_{}, T_{}, I_<0>{}, I_<4>{}, F_{}); else body(T_{}, T_{}, I_<0>{}, I_<2>{}, F_{}); }
    else if (a == 0) body(F_{}, T_{}, I_<0>{}, I_<4>{}, F_{});
    else if (a == 1) body(F_{}, F_{}, I_<0>{}, I_<4>{}, F_{});
    else body(F_{}, T_{}, I_<2>{}, I_<4>{}, T_{});
    (void)t0; (void)t1; (void)has1;
    STAMP(17 + rep * 30);
    const u32 kvb = WS_KVB + rep * RP * KVB_BYTES;
    const u32 hb = kvb + (u32)((4 * c.j + a) * 64 + 2 * c.i) * TILE_B;
    auto fin_t = [&](const f32x4 (&acc)[4], int slot, float sc, u32 (&d)[4][2]) {
        const f32x4 cs = lld<f32x4>(c.L + L_CSB + (slot ? unit1_of(c.wave) : c.wave * 2) * 64 + g * 16);
#pragma unroll
        for (int m = 0; m < 4; ++m) {
            const float mean = lld<float>(c.L + L_MEAN + (m * 16 + tok) * 4), rs = lld<float>(c.L + L_MEAN + 256 + (m * 16 + tok) * 4) * sc;
            d[m][0] = pk((acc[m][0] - mean * cs[0]) * rs, (acc[m][1] - mean * cs[1]) * rs);
            d[m][1] = pk((acc[m][2] - mean * cs[2]) * rs, (acc[m][3] - mean * cs[3]) * rs);
        }
    };
    auto fin_p = [&](const f32x4 (&acc)[4], int slot, u32 (&d)[4][2]) {
        const float cs = lld<float>(c.L + L_CSB + (c.wave * 2 + slot) * 64 + tok * 4);
#pragma unroll
        for (int m = 0; m < 4; ++m) {
            const f32x4 mean = lld<f32x4>(c.L + L_MEAN + (m * 16 + 4 * g) * 4), rs = lld<f32x4>(c.L + L_MEAN + 256 + (m * 16 + 4 * g) * 4);
            d[m][0] = pk((acc[m][0] - mean[0] * cs) * rs[0], (acc[m][1] - mean[1] * cs) * rs[1]);
            d[m][1] = pk((acc[m][2] - mean[2] * cs) * rs[2], (acc[m][3] - mean[3] * cs) * rs[3]);
        }
    };
    auto frag16 = [&](const u32 (&d)[4][2], int t) {
        auto x = __builtin_amdgcn_permlane16_swap(d[2 * t][0], d[2 * t + 1][0], false, false);
        auto y = __builtin_amdgcn_permlane16_swap(d[2 * t][1], d[2 * t + 1][1], false, false);
        return u32x4{(u32)x[0], (u32)y[0], (u32)x[1], (u32)y[1]};
    };
    auto frag32 = [&](const u32 (&d)[4][2], int t) {
        auto x = __builtin_amdgcn_permlane32_swap(d[2 * t][0], d[2 * t + 1][0], false, false);
        auto y = __builtin_amdgcn_permlane32_swap(d[2 * t][1], d[2 * t + 1][1], false, false);
        return u32x4{(u32)x[0], (u32)y[0], (u32)x[1], (u32)y[1]};
    };
    u32 d[4][2];
    LAS char* lh = c.L + L_KVB + a * 25600 + 8 * TILE_B; LAS char* l0 = c.L + L_KVB + 8 * TILE_B;
    if (!odd) {
        fin_t(a1, 1, 1.f, d);
        { const u32x4 f0 = frag16(d, 0);
          st128(c.rs, f0, lane * 16, hb); lst(lh + lane * 16, f0);
          if (a < 2) { const u32x4 f1 = frag16(d, 1); st128(c.rs, f1, lane * 16, hb + TILE_B); lst(lh + TILE_B + lane * 16, f1); } }
        fin_t(a0, 0, QSCALE, d);
        qm = frag16(d, 0);
        lst(c.L + L_QT1 + a * 1024 + lane * 16, frag16(d, 1));
    } else {
        fin_p(a0, 0, d);
        { const u32x4 f0 = frag32(d, 0), f1 = frag32(d, 1);
          st128(c.rs, f0, KT_V + g * KT_VBLK + tok * 16, hb); st128(c.rs, f1, KT_V + g * KT_VBLK + tok * 16, hb + TILE_B);
          lst(lh + KT_V + g * KT_VBLK + tok * 16, f0); lst(lh + TILE_B + KT_V + g * KT_VBLK + tok * 16, f1); }
        if (a == 0) {
            fin_t(a1, 1, g < 2 ? QSCALE : 1.f, d);
#pragma unroll
            for (int m = 0; m < 4; ++m) {
                if (g < 2) { lst(c.L + L_QREM + (2 * g) * 256 + (m * 16 + tok) * 4, d[m][0]); lst(c.L + L_QREM + (2 * g + 1) * 256 + (m * 16 + tok) * 4, d[m][1]); }
                else {
                    const u32 hb0 = kvb + (u32)(4 * c.j * 64 + 2 * c.i + (m >> 1)) * TILE_B;
                    st64(c.rs, u32x2{d[m][0], 0x00003f80u}, (u32)(2 * (g - 2)) * 64 * TILE_B + KT_KREM + ((m & 1) * 16 + tok) * 8, hb0);
                    st64(c.rs, u32x2{d[m][1], 0x00003f80u}, (u32)(2 * (g - 2) + 1) * 64 * TILE_B + KT_KREM + ((m & 1) * 16 + tok) * 8, hb0);
                    LAS char* lk = l0 + (2 * (g - 2)) * 25600 + (m >> 1) * TILE_B + KT_KREM + ((m & 1) * 16 + tok) * 8;
                    lst(lk, u32x2{d[m][0], 0x00003f80u}); lst(lk + 25600, u32x2{d[m][1], 0x00003f80u});
                }
            }
        } else if (a == 1) {
            fin_p(a1, 1, d);
            const u32 hb0 = kvb + (u32)(4 * c.j * 64 + 2 * c.i) * TILE_B;
            const u32 vo = (u32)(tok >> 1) * 64 * TILE_B + KT_V + g * KT_VBLK + (16 + (tok & 1)) * 16;
            const u32x4 f0 = frag32(d, 0), f1 = frag32(d, 1);
            if (tok < 8) {
                st128(c.rs, f0, vo, hb0); st128(c.rs, f1, vo, hb0 + TILE_B);
                LAS char* lv = l0 + (tok >> 1) * 25600 + KT_V + g * KT_VBLK + (16 + (tok & 1)) * 16;
                lst(lv, f0); lst(lv + TILE_B, f1);
            }
        } else {
            fin_t(a1, 1, 1.f, d);
            const u32x4 f1 = frag16(d, 1);
            st128(c.rs, f1, lane * 16, hb + TILE_B); lst(lh + TILE_B + lane * 16, f1);
        }
        if (a == 2) {
            const u32 hb0 = kvb + (u32)(4 * c.j * 64 + 2 * c.i) * TILE_B;
            if (lane < 32) {
                const u32x4 on = ones4();
                st128(c.rs, on, (u32)(lane >> 3) * 64 * TILE_B + ((lane >> 2) & 1) * TILE_B + KT_V + (lane & 3) * KT_VBLK + 18 * 16, hb0);
                lst(l0 + (lane >> 3) * 25600 + ((lane >> 2) & 1) * TILE_B + KT_V + (lane & 3) * KT_VBLK + 18 * 16, on);
            }
        }
    }
    publish(c, rep * 6 + 3);
    load_wimage_part<44, 90>(c, WS_WIMG + 8 * WIMG_BYTES, 1);
    if (odd) qm = lld<u32x4>(c.L + L_QT1 + a * 1024 + lane * 16);
    const u32 rem = lld<u32>(c.L + L_QREM + a * 256 + (32 * odd + (lane & 31)) * 4);
    qr = u32x2{lane < 32 ? rem : 0u, 0u};
}

DI void b_attn(const Ctx& c0, const Params& p, int rep, u32x4 qm, u32x2 qr) {
    const Ctx c = fresh(c0);
    const int lane = c.lane, r = lane & 31, h = lane >> 5, a = c.wave >> 1, t = c.wave & 1;
    const int qt = 2 * c.i + t;
    const f32x16 cd = mask_c(lane, true);
    if (c.wave == 1) {
        const f32x16 cm = mask_c(lane, false);
#pragma unroll
        for (int e = 0; e < 4; ++e) lst(c.L + L_MSK + e * 1024 + lane * 16, f32x4{cm[4 * e], cm[4 * e + 1], cm[4 * e + 2], cm[4 * e + 3]});
    }
    LAS char* tb = c.L + L_KVB + a * 25600 + (8 + t) * TILE_B;
    LAS char* kb = tb + lane * 16; LAS char* rb = tb + KT_KREM + r * 8; LAS char* vb = tb + KT_V + h * KT_VBLK + vrow_of(r) * 16;
    const u32x4 k0m = lld<u32x4>(kb); const u32x2 k0r = lld<u32x2>(rb);
    u32x4 km, v0 = lld<u32x4>(vb), v1 = lld<u32x4>(vb + 2 * KT_VBLK); u32x2 kr;
    f32x16 sA, sB, o;
    Pipe pp;
    { u32 z = 0u; asm volatile("" : "+v"(z)); pp.e[0] = pp.e[1] = pp.e[2] = pp.e[3] = asf(z); pp.p1 = u32x4{z, z, z, z}; }
    f32x16 cdp = cd;
    asm volatile("s_nop 1" : "+v"(pp.p1), "+v"(pp.e[0]), "+v"(pp.e[1]), "+v"(pp.e[2]), "+v"(pp.e[3]), "+v"(qm), "+v"(qr), "+v"(cdp));
    QK1(o, pp.p1, pp.p1);
    QK1C(sA, k0m, qm, cdp);
    asm volatile("v_mfma_f32_32x32x8_bf16 %0, %1, %2, %3" : "=&v"(sB) : "v"(k0r), "v"(qr), "v"(sA));
    asm volatile("s_nop 7\n\ts_nop 3" : "+v"(sB), "+v"(sA));
    { const float m0 = rowmax(sB); if (h == 0) qr[1] = pk(-m0, 0.f); }
    asm volatile("s_nop 1" : "+v"(qr));
    QK2(sA, k0r, qr);
    asm volatile("s_nop 7\n\ts_nop 3" : "+v"(sA), "+v"(o), "+v"(v0), "+v"(v1));
    if (c.wave == 0) poll(c, rep * 6 + 3, (lane < 5 && c.i - lane >= 0) ? (c.i - lane) * 8 + c.j : -1);
    BAR();
    STAMP(20 + rep * 30);
    const u32 hb = WS_KVB + rep * RP * KVB_BYTES + (u32)((4 * c.j + a) * 64) * TILE_B;
    { const u32 dst = c.Lb + L_KVB + a * 25600;
#pragma unroll
      for (int n = 0; n < 10; ++n) {
          const int pc = 19 - t - 2 * n;
          u32 vo = lane * 16;
          if (c.i < 4) { const int b = pc * 1024 + lane * 16, tau = b / 2560, kt = 2 * c.i - 8 + tau; vo = (u32)((kt < 0 ? 0 : kt) * 2560 + (b - tau * 2560)); glds_ws(c.srd, vo, hb, dst + pc * 1024); }
          else glds_ws(c.srd, vo, hb + (u32)(2 * c.i - 8) * 2560 + pc * 1024, dst + pc * 1024);
      } }
    asm volatile("s_waitcnt vmcnt(5)" ::: "memory");
    BAR();
    STAMP(21 + rep * 30);
    km = lld<u32x4>(kb - TILE_B); kr = lld<u32x2>(rb - TILE_B);
    auto first = [](f32x16& s, u32x4 k, u32x4 q) { QK1(s, k, q); };
    auto firstm = [&](f32x16& s, u32x4 k, u32x4 q) {
        f32x16 co;
#pragma unroll
        for (int e = 0; e < 4; ++e) { const f32x4 v = lld<f32x4>(c.L + L_MSK + e * 1024 + lane * 16); co[4 * e] = v[0]; co[4 * e + 1] = v[1]; co[4 * e + 2] = v[2]; co[4 * e + 3] = v[3]; }
        QK1C(s, k, q, co); };
    const u32 qoff = h == 0 ? pk(NEG_BIG, 0.f) : 0u;
    kb -= 8 * TILE_B; rb -= 8 * TILE_B; vb -= 8 * TILE_B;
#define B_TILE(U, SC, SN, FIRST) do { u32x2 qs = {qr[0], (U) + 1 <= qt ? qr[1] : qoff}; asm volatile("" : "+v"(qs)); \
        attn_step<((U) < 8)>(SC, SN, o, pp, km, kr, v0, v1, qm, qs, FIRST, \
            [&] { if ((U) + 2 <= 8) km = lld<u32x4>(kb + (8 - ((U) + 2)) * TILE_B); }, [&] { if ((U) + 2 <= 8) kr = lld<u32x2>(rb + (8 - ((U) + 2)) * TILE_B); }, \
            [&] { v1 = lld<u32x4>(vb + (8 - (U)) * TILE_B + 2 * KT_VBLK); }, [&] { if ((U) + 1 <= 8) v0 = lld<u32x4>(vb + (8 - ((U) + 1)) * TILE_B); }); } while (0)
    B_TILE(0, sA, sB, first); B_TILE(1, sB, sA, first);
    DRAIN();
    B_TILE(2, sA, sB, first);
    BAR();
    B_TILE(3, sB, sA, first); B_TILE(4, sA, sB, first); B_TILE(5, sB, sA, first); B_TILE(6, sA, sB, first); B_TILE(7, sB, sA, firstm); B_TILE(8, sA, sB, first);
#undef B_TILE
    attn_finish(o, pp, v1);
    STAMP(22 + rep * 30);
    if (attn_unsafe(o)) o = attn_slow(c, hb, qt - 8 < 0 ? 0 : qt - 8, qt, true, qm, qr);
    store_o(c, WS_OF + (rep * RP * 2 + 1) * XF_BYTES, o, a, t);
    publish(c, rep * 6 + 4);
    load_wimage_part<0, 44>(c, WS_WIMG + 8 * WIMG_BYTES, 1);
    if (rep == 0) load_wimage_part<0, 36>(c, WS_WIMGA, 1, L_A1);
}

__global__ void __launch_bounds__(512, 2) fused_block_kernel(Params p) {
    __shared__ __attribute__((aligned(16))) char lds_[LDS_BYTES];
    Ctx c;
    c.L = (LAS char*)lds_; c.Lb = (u32)(size_t)c.L;
    c.ws = p.ws;
    c.tid = threadIdx.x; c.lane = c.tid & 63; c.wave = __builtin_amdgcn_readfirstlane(c.tid >> 6);
    { const int wg = blockIdx.x, slot = wg >> 3; c.i = (wg & 7) * 4 + (slot >> 3); c.j = slot & 7; }
    c.rs = __builtin_amdgcn_make_buffer_rsrc((void*)p.ws, 0, WS_USED, 0x00020000);
    { const u64 b = (u64)p.ws; c.srd = u32x4{(u32)b, (u32)(b >> 32) & 0xffffu, WS_USED, 0x00020000u}; }
    STAMP(0);
    u32 warm;
    { u64 pc; u32 len;
      asm volatile("s_getpc_b64 %0\nfbk_code_first:\n\ts_mov_b32 %1, fbk_code_last-fbk_code_first" : "=s"(pc), "=s"(len));
      u32 off = ((u32)(blockIdx.x >> 3) * 8 + c.wave) * 256 + c.lane * 4;
      off = off < len ? off : 0u;
      warm = *(const GAS u32*)((pc & ~3ull) + off); }
    c.elo = 0; c.ehi = 0;
    u32 xres[3][2];
    STAMP(1);
    a_proj<0>(c, p);
    asm volatile("" :: "v"(warm));
    STAMP(5);
#pragma unroll 1
    for (int rep = 0; rep < 2; ++rep) {
        u32x4 w0[WRING], w1[WRING]; ATail st;
        a_attn(c, p, rep, st);
        if (rep == 0) {
            if (blockIdx.x == 0 && c.tid == 0) __hip_atomic_store((GAS u64*)(p.ws + WS_EPOCH), ((u64)c.ehi << 32) | c.elo, __ATOMIC_RELAXED, __HIP_MEMORY_SCOPE_AGENT);
        }
        a_tail(c, p, rep, st, xres, w0, w1);
        STAMP(14 + rep * 30);
        u32x4 qm; u32x2 qr;
        b_proj(c, p, rep, w0, w1, qm, qr);
        STAMP(19 + rep * 30);
        b_attn(c, p, rep, qm, qr);
        STAMP(24 + rep * 30);
        out_proj<true>(c, p, rep, xres, [](const Ctx&) {});
        STAMP(29 + rep * 30);
        if (rep == 1) break;
        STAMP(31);
        a_proj<1>(c, p);
        STAMP(35);
    }
    asm volatile("fbk_code_last:");
}

static size_t ws_offset(size_t ws_size) { return (ws_size - WS_USED) & ~(size_t)0x1fffff; }

extern "C" void kernel_launch(void* const* d_in, const int* in_sizes, int n_in, void* d_out, int out_size, void* d_ws, size_t ws_size, hipStream_t stream) {
    Params p;
    p.x = (const u16*)d_in[0]; p.wq = (const u16*)d_in[1]; p.wk = (const u16*)d_in[2]; p.wv = (const u16*)d_in[3]; p.woA = (const u16*)d_in[4];
    p.wqkv = (const u16*)d_in[6]; p.woB = (const u16*)d_in[7]; p.out = (u16*)d_out;
    p.ws = (char*)d_ws + ws_offset(ws_size);
    fused_block_kernel<<<256, 512, 0, stream>>>(p);
}
```

```cpp
#include <hip/hip_runtime.h>
#include <hip/hip_bf16.h>

typedef unsigned short u16;
typedef unsigned u32;
typedef unsigned long long u64;
typedef __bf16 bf16x8 __attribute__((ext_vector_type(8)));
typedef __bf16 bf16x2 __attribute__((ext_vector_type(2)));
typedef short s16x4 __attribute__((ext_vector_type(4)));
typedef float f32x2 __attribute__((ext_vector_type(2)));
typedef float f32x4 __attribute__((ext_vector_type(4)));
typedef float f32x16 __attribute__((ext_vector_type(16)));
typedef u32 u32x2 __attribute__((ext_vector_type(2)));
typedef u32 u32x4 __attribute__((ext_vector_type(4)));
#define LAS __attribute__((address_space(3)))
#define GAS __attribute__((address_space(1)))
#define DI __device__ __forceinline__

constexpr int SEQ = 2048, DM = 576;
constexpr int RP = 0;
constexpr u32 TILE_B = 2560;
constexpr u32 KT_V = 1024, KT_VBLK = 304, KT_KREM = 2240;
constexpr u32 KA_KREM = 1024, KA_V0 = 1280, KA_V8 = 1792, KA_VX = 2304;
constexpr u32 XF_BYTES = 32u * 18 * 4 * 1024;
constexpr u32 WS_EPOCH = 0;
constexpr u32 WS_FLAG = 4096;
constexpr int PH_FMT = 11;
constexpr u32 WS_XF = WS_FLAG + 12u * 256 * 128 + 4096;
constexpr u32 WS_OF = WS_XF + (2 + RP) * XF_BYTES;
constexpr u32 ST_BYTES = 2048u * 16 * 8;
constexpr u32 WS_ST = WS_OF + (2 + 2 * RP) * XF_BYTES;
constexpr u32 KVA_BYTES = 64 * TILE_B;
constexpr u32 WS_KVA = WS_ST + (2 + RP) * ST_BYTES;
constexpr u32 KVB_BYTES = 32 * 64 * TILE_B;
constexpr u32 WS_KVB = WS_KVA + 2 * KVA_BYTES;
constexpr u32 WF_BYTES = 8u * 16 * 18 * 1024;
constexpr u32 WS_WF = WS_KVB + (1 + RP) * KVB_BYTES;
constexpr u32 WS_CSP = WS_WF + WF_BYTES;
constexpr u32 WIMG_BYTES = 90 * 1024;
constexpr u32 WS_WIMG = WS_CSP + 8u * 16 * 18 * 64;
constexpr u32 WS_WIMGA = WS_WIMG + 16 * WIMG_BYTES;
constexpr u32 WS_STAMP = WS_WIMGA + 8 * WIMG_BYTES;
constexpr u32 WS_STAMP2 = WS_STAMP + 256u * 64 * 8 + 256u * 8 * 8 * 8;
constexpr u32 WS_USED = WS_STAMP2 + 256u * 16 * 8;
constexpr int L_XCH = 0;
constexpr int L_RING = 0;
constexpr int CHUNK_B = 8 * TILE_B;
constexpr int L_STG = 40960;
constexpr int STG_P = 168;
constexpr int L_XIN = 0;
constexpr int L_XIN2 = 153600;
constexpr int L_FMT = 24576;
constexpr int L_KVB = 0;
constexpr int L_MSK = 102400;
constexpr int L_R1 = 61440;
constexpr int WP = 160;
constexpr int L_XB = 76800;
constexpr int L_A1 = 24576;
constexpr int L_STATP = 153600;
constexpr int L_MEAN = L_STATP + 1024;
constexpr int L_CSA = L_MEAN + 512;
constexpr int L_CSB = L_CSA + 1024;
constexpr int L_QREM = L_CSB + 1024;
constexpr int L_QT1 = L_QREM + 1024;
constexpr int LDS_BYTES = 163840;
static_assert(L_QT1 + 4096 <= LDS_BYTES, "lds");

#define WRING 4
constexpr float QSCALE = 0.23570226039551584f * 1.4426950408889634f;
constexpr float NEG_BIG = -1e30f;
constexpr u32 SPIN_CAP = 1u << 20;
constexpr int AUX_SC1 = 16;

struct Params { const u16* x; const u16* wq; const u16* wk; const u16* wv; const u16* woA; const u16* wqkv; const u16* woB; u16* out; char* ws; };

DI u32 pk(float a, float b) { f32x2 v = {a, b}; return __builtin_bit_cast(u32, __builtin_convertvector(v, bf16x2)); }
DI float asf(u32 d) { return __builtin_bit_cast(float, d); }
DI u32 asu(float f) { return __builtin_bit_cast(u32, f); }
DI float bflo(u32 d) { return __builtin_bit_cast(float, d << 16); }
DI float bfhi(u32 d) { return __builtin_bit_cast(float, d & 0xffff0000u); }
DI bf16x8 asb(u32x4 v) { return __builtin_bit_cast(bf16x8, v); }
DI f32x4 mfma16(u32x4 a, u32x4 b, f32x4 c) { return __builtin_amdgcn_mfma_f32_16x16x32_bf16(asb(a), asb(b), c, 0, 0, 0); }
DI f32x16 mfma32(u32x4 a, u32x4 b, f32x16 c) { return __builtin_amdgcn_mfma_f32_32x32x16_bf16(asb(a), asb(b), c, 0, 0, 0); }
DI void MF16(f32x4& acc, u32x4 a, u32x4 b) { asm volatile("v_mfma_f32_16x16x32_bf16 %0, %1, %2, %0" : "+v"(acc) : "v"(a), "v"(b)); }
#define SETTLE4(x) asm volatile("s_nop 7" : "+v"(x))
template <class T> DI T lld(LAS char* p) { return *(LAS const T*)p; }
template <class T> DI void lst(LAS char* p, T v) { *(LAS T*)p = v; }
DI u32x2 trr(LAS char* p) { return __builtin_bit_cast(u32x2, __builtin_amdgcn_ds_read_tr16_b64_v4i16((LAS s16x4*)p)); }
#define BAR() asm volatile("s_waitcnt lgkmcnt(0)\n\ts_barrier" ::: "memory")
#define DRAIN() asm volatile("s_waitcnt vmcnt(0)" ::: "memory")
typedef __amdgpu_buffer_rsrc_t rsrc_t;
DI u32x4 ld128(rsrc_t rs, u32 voff, u32 soff) { return __builtin_amdgcn_raw_buffer_load_b128(rs, voff, soff, AUX_SC1); }
DI u32x2 ld64(rsrc_t rs, u32 voff, u32 soff) { return __builtin_amdgcn_raw_buffer_load_b64(rs, voff, soff, AUX_SC1); }
DI u32 ld32(rsrc_t rs, u32 voff, u32 soff) { return __builtin_amdgcn_raw_buffer_load_b32(rs, voff, soff, AUX_SC1); }
DI void st128(rsrc_t rs, u32x4 v, u32 voff, u32 soff) { __builtin_amdgcn_raw_buffer_store_b128(v, rs, voff + soff, 0, AUX_SC1); }
DI void st64(rsrc_t rs, u32x2 v, u32 voff, u32 soff) { __builtin_amdgcn_raw_buffer_store_b64(v, rs, voff, soff, AUX_SC1); }
DI void st32(rsrc_t rs, u32 v, u32 voff, u32 soff) { __builtin_amdgcn_raw_buffer_store_b32(v, rs, voff, soff, AUX_SC1); }
DI void glds_ws(u32x4 srd, u32 voff, u32 soff, u32 ldsdst) {
    soff = __builtin_amdgcn_readfirstlane(soff); ldsdst = __builtin_amdgcn_readfirstlane(ldsdst);
    asm volatile("s_mov_b32 m0, %0\n\ts_nop 3\n\tbuffer_load_dwordx4 %1, %2, %3 offen sc1 lds" :: "s"(ldsdst), "v"(voff), "s"(srd), "s"(soff) : "memory");
}
DI void glds_in(const void* g, u32 ldsdst) {
    ldsdst = __builtin_amdgcn_readfirstlane(ldsdst);
    asm volatile("s_mov_b32 m0, %0\n\ts_nop 0\n\tglobal_load_lds_dwordx4 %1, off" :: "s"(ldsdst), "v"(g) : "memory");
}
DI void flag_load(u64& fl, u64 nomatch, u32 voff, u32x4 srd, u32 soff) {
    soff = __builtin_amdgcn_readfirstlane(soff);
    fl = nomatch;
    asm volatile("s_nop 4\n\tbuffer_load_dwordx2 %0, %1, %2, %3 offen sc1" : "+v"(fl) : "v"(voff), "s"(srd), "s"(soff) : "memory");
}
DI float sum_g(float v) {
    auto a = __builtin_amdgcn_permlane32_swap(__builtin_bit_cast(u32, v), __builtin_bit_cast(u32, v), false, false);
    v = __builtin_bit_cast(float, (u32)a[0]) + __builtin_bit_cast(float, (u32)a[1]);
    auto b = __builtin_amdgcn_permlane16_swap(__builtin_bit_cast(u32, v), __builtin_bit_cast(u32, v), false, false);
    return __builtin_bit_cast(float, (u32)b[0]) + __builtin_bit_cast(float, (u32)b[1]);
}
DI float max_h(float v) {
    auto a = __builtin_amdgcn_permlane32_swap(__builtin_bit_cast(u32, v), __builtin_bit_cast(u32, v), false, false);
    return fmaxf(__builtin_bit_cast(float, (u32)a[0]), __builtin_bit_cast(float, (u32)a[1]));
}
DI u32x4 ones4() { u32 o = 0x3f803f80u; asm volatile("" : "+v"(o)); return u32x4{o, o, o, o}; }
DI int crow(int reg, int h) { return (reg & 3) + 8 * (reg >> 2) + 4 * h; }

#define dg_a (c.dgp[0])
#define dg_b (c.dgp[1])
#define STAMP(n) do { if (c.wave == 0 && c.lane == 0) { if ((n) == 8) dg_a = __builtin_amdgcn_s_memrealtime(); if ((n) == 14) dg_b = __builtin_amdgcn_s_memrealtime(); } } while (0)
#define CSTAMP(n) do {} while (0)
#define CSTAMPW(n, W) do {} while (0)

struct Ctx {
    u64* dgp;
    LAS char* L; u32 Lb;
    char* ws; rsrc_t rs; u32x4 srd;
    int tid, lane, wave, i, j;
    u32 elo, ehi;
};

DI Ctx fresh(const Ctx& c0) {
    Ctx c = c0;
    asm volatile("" : "+v"(c.lane)); asm volatile("" : "+v"(c.tid));
    asm volatile("" : "+s"(c.wave)); asm volatile("" : "+s"(c.i)); asm volatile("" : "+s"(c.j));
    return c;
}
DI GAS u64* flag_ptr(const Ctx& c, int phase, int id) { return (GAS u64*)(c.ws + WS_FLAG + (u32)(phase * 256 + id) * 128); }
DI void signal(const Ctx& c, int phase) {
    if (c.tid == 0) __hip_atomic_store(flag_ptr(c, phase, c.i * 8 + c.j), ((u64)c.ehi << 32) | c.elo, __ATOMIC_RELAXED, __HIP_MEMORY_SCOPE_AGENT);
}
DI void publish(const Ctx& c, int phase) {
    DRAIN();
    BAR();
    signal(c, phase);
}
DI void poll(const Ctx& c, int phase, int id) {
    const u64 want = ((u64)c.ehi << 32) | c.elo;
    GAS u64* f = flag_ptr(c, phase, id < 0 ? 0 : id);
    for (u32 spins = 0; spins < SPIN_CAP; ++spins) {
        const u64 v = __hip_atomic_load(f, __ATOMIC_RELAXED, __HIP_MEMORY_SCOPE_AGENT);
        if (__all(id < 0 || v == want)) break;
        __builtin_amdgcn_s_sleep(1);
    }
    __builtin_amdgcn_fence(__ATOMIC_ACQUIRE, "wavefront");
}
DI void wait_group(const Ctx& c, int phase) {
    if (c.wave == 0) poll(c, phase, c.lane < 8 ? c.i * 8 + c.lane : -1);
    BAR();
}

DI void load_wimage_a(const Ctx& c, const Params& p) {
    const int jj = c.j, w = c.wave;
    const u16 *pk_ = p.wk, *pv_ = p.wv;
    asm volatile("" : "+s"(pk_), "+s"(pv_));
    const char* xsrc = (const char*)((jj >= 3 && jj < 6) ? pv_ : pk_); const int sub = jj < 6 ? jj % 3 : 0;
    xsrc += sub == 0 ? 0 : (sub == 1 ? 16 : 20);
    const char* msrc = (const char*)p.wq + jj * 144;
    const char* a[5]; u32 st[5];
#pragma unroll
    for (int k = 0; k < 5; ++k) {
        const u32 id = 64 * k + c.lane, row = (id * 6554u) >> 16, ch = id - row * 10;
        const bool ex = ch == 9;
        st[k] = ex ? 32 * 36 : 32 * 1152;
        a[k] = (ex ? xsrc : msrc) + (ex ? row * 36 : row * 1152 + ch * 16);
    }
#pragma unroll
    for (int n = 0; n < 3; ++n) {
        const int G = n < 2 ? (w & 3) + 8 * (w >> 2) + 4 * n : 16 + w;
        if (n < 2 || w < 2) {
#pragma unroll
            for (int k = 0; k < 5; ++k) glds_in(a[k] + (u32)G * st[k], c.Lb + L_R1 + (G * 5 + k) * 1024);
        }
    }
}
DI void load_wimage_o(const Ctx& c, u32 base) {
    const u32 so = base + (u32)c.j * WIMG_BYTES;
    if (c.wave >= 1) {
#pragma unroll
        for (int n = 0; n < 13; ++n)
            if (n < 12 || c.wave < 7) glds_ws(c.srd, c.lane * 16, so + (c.wave - 1 + 7 * n) * 1024, c.Lb + L_R1 + (c.wave - 1 + 7 * n) * 1024);
    }
}
template <int P0, int P1>
DI void load_wimage_part(const Ctx& c, u32 base, int w0, int ldsbase = L_R1) {
    const u32 so = base + (u32)c.j * WIMG_BYTES; const int nw = 8 - w0;
    if (c.wave >= w0) {
#pragma unroll
        for (int n = 0; n < (P1 - P0 + 6) / 7; ++n) {
            const int pc = P0 + (c.wave - w0) + nw * n;
            if (pc < P1) glds_ws(c.srd, c.lane * 16, so + pc * 1024, c.Lb + ldsbase + pc * 1024);
        }
    }
}
DI void load_wimage_o4(const Ctx& c, int which, int part) {
    const u32 so = WS_WIMG + (u32)(which * 8 + c.j) * WIMG_BYTES + (u32)part * 30 * 1024; const u32 dst = c.Lb + L_R1 + part * 30 * 1024;
    if (c.wave < 4) {
#pragma unroll
        for (int n = 0; n < 8; ++n)
            if (n < 7 || c.wave < 2) glds_ws(c.srd, c.lane * 16, so + (c.wave + 4 * n) * 1024, dst + (c.wave + 4 * n) * 1024);
    }
}
struct Fmt { u32x4 v[2]; u32 g[8]; u32 dst[2], cdst[2]; int kind[2]; };
DI void fmt_issue(const Ctx& c, const Params& p, Fmt& m) {
    const u16 *pw = p.wqkv, *pa = p.woA, *pb = p.woB;
    asm volatile("" : "+s"(pw), "+s"(pa), "+s"(pb));
    const int lane = c.lane, rho = lane >> 1, g = rho >> 3, e = rho & 7;
#pragma unroll
    for (int r = 0; r < 2; ++r) {
        const int fr = blockIdx.x * 8 + c.wave + 2048 * r;
        int col, A, P, Q, pitch; const GAS char* src;
        if (fr < 2016) {
            const int jj = fr / 252, rem = fr - jj * 252, u = rem / 18, s = rem - u * 18;
            const int wv = u < 8 ? u : (u - 8 < 5 ? u - 8 : 6), slot = u < 8 ? 0 : 1, a = wv >> 1;
            const bool isrem = (wv & 1) && slot == 1;
            const u32 fo = (u32)(((jj * 8 + wv) * 2 + slot) * 18 + s);
            m.dst[r] = WS_WF + fo * 1024; m.cdst[r] = WS_CSP + fo * 64; m.kind[r] = isrem ? 3 : 1;
            P = s < 16 ? 16 : 4; Q = 2; pitch = 1728 * 2; src = (const GAS char*)pw;
            if (!isrem) {
                col = ((wv & 1) ? 1152 : (slot ? 576 : 0)) + 18 * (4 * jj + a) + 8 * (lane & 1);
                A = s < 16 ? 72 * (s >> 1) + 32 * (s & 1) + 4 * g : 72 * (4 * (s - 16) + g) + 64;
            } else {
                const int n = lane & 15, g2 = lane >> 4;
                col = (a == 0 ? (n < 8 ? 0 : 576) : 1152) + 18 * (4 * jj + ((n & 7) >> 1)) + 16 + (n & 1);
                A = s < 16 ? 72 * (s >> 1) + 32 * (s & 1) + 4 * g2 : 72 * (4 * (s - 16) + g2) + 64;
#pragma unroll
                for (int q = 0; q < 8; ++q) m.g[q] = *(const GAS u16*)(src + (A + P * (q >> 2) + (q & 3)) * pitch + col * 2);
            }
        } else {
            const bool on = fr < 3456;
            const int q = on ? fr - 2016 : 0, im = q / 720, rem = q - im * 720, jj = rem / 90, st = rem - jj * 90, s = st / 5, t = st - s * 5;
            m.dst[r] = WS_WIMG + (u32)(im * 8 + jj) * WIMG_BYTES + (u32)st * 1024; m.cdst[r] = 0; m.kind[r] = on ? 2 : 0;
            col = 72 * jj + 16 * t + (t == 4 ? 0 : 8 * (lane & 1));
            if (s < 16) { A = 36 * s + 18 * (g >> 1) + 4 * (g & 1); P = 8; Q = 2; } else { A = 72 * (4 * (s - 16) + g) + 16; P = 36; Q = 18; }
            src = (const GAS char*)(im ? pb : pa); pitch = 576 * 2;
        }
        if (r == 1 || m.kind[r] != 3) m.v[r] = *(const GAS u32x4*)(src + (A + P * (e >> 2) + Q * ((e >> 1) & 1) + (e & 1)) * pitch + col * 2);
    }
}
DI void fmt_finish(const Ctx& c, Fmt& m) {
    asm volatile("" : "+v"(m.v[0]), "+v"(m.v[1]), "+v"(m.g[0]), "+v"(m.g[1]), "+v"(m.g[2]), "+v"(m.g[3]), "+v"(m.g[4]), "+v"(m.g[5]), "+v"(m.g[6]), "+v"(m.g[7]));
    const int lane = c.lane, i16 = lane & 15, g = lane >> 4, q = i16 >> 2, p = i16 & 3;
#pragma unroll
    for (int r = 0; r < 2; ++r) {
        if (m.kind[r] != 0) {
            u32x4 f;
            if (m.kind[r] == 3) { const u32* w = m.g; f = u32x4{w[0] | (w[1] << 16), w[2] | (w[3] << 16), w[4] | (w[5] << 16), w[6] | (w[7] << 16)}; }
            else {
                LAS char* t = c.L + L_FMT + c.wave * 2048 + r * 1024;
                lst(t + lane * 16, m.v[r]);
                asm volatile("s_waitcnt lgkmcnt(0)" ::: "memory");
                const u32x2 lo = trr(t + (8 * g + q) * 32 + 8 * p), hi = trr(t + (8 * g + 4 + q) * 32 + 8 * p);
                f = u32x4{lo[0], lo[1], hi[0], hi[1]};
            }
            st128(c.rs, f, lane * 16, m.dst[r]);
            if (m.kind[r] != 2) {
                float cs = ((bflo(f[0]) + bfhi(f[0])) + (bflo(f[1]) + bfhi(f[1]))) + ((bflo(f[2]) + bfhi(f[2])) + (bflo(f[3]) + bfhi(f[3])));
                cs = sum_g(cs);
                if (lane < 16) st32(c.rs, asu(cs), lane * 4, m.cdst[r]);
            }
        }
    }
}

template <bool PROJ, bool NAT = false, class HK, class PRE>
DI void g1_mma(const Ctx& c, const u32x4 (&xf)[9], f32x4 (&acc)[5], HK hook, PRE pre) {
    const int lane = c.lane, kh = c.wave >> 2, q = (lane & 15) >> 2, p = lane & 3, g = lane >> 4;
    LAS char *bN, *bR, *bNo, *bNt, *bRo, *bRt;
    if (PROJ) {
        if (NAT) { bN = c.L + L_R1 + (kh * 256 + 4 * g + q) * WP + 8 * p; bR = c.L + L_R1 + (512 + 32 * kh + 8 * g + q) * WP + 8 * p; }
        else { bN = c.L + L_A1 + (kh * 288 + 4 * g + q) * WP + 8 * p; bR = c.L + L_A1 + (kh * 288 + 72 * g + q) * WP + 8 * p; }
        bNo = bN + 64 * kh; bNt = bN + 64 - 64 * kh; bRo = bR + 64 * kh; bRt = bR + 64 - 64 * kh;
    } else {
        bN = c.L + L_R1 + kh * 8 * 5120 + lane * 16; bR = c.L + L_R1 + (16 + kh) * 5120 + lane * 16;
        bNo = bN + 2048 * kh; bNt = bN + 2048 - 2048 * kh; bRo = bR + 2048 * kh; bRt = bR + 2048 - 2048 * kh;
    }
    u32x4 w[2][5];
    auto rd = [&](int sl, u32x4 (&d)[5]) {
#pragma unroll
        for (int u = 0; u < 5; ++u) {
            if (PROJ) {
                const int r0 = NAT ? (sl < 8 ? 32 * sl : 0) : (sl < 8 ? 72 * (sl >> 1) + 32 * (sl & 1) : 64), hs = sl < 8 ? 16 : 4;
                LAS char* bb = u < 2 ? (sl < 8 ? bNo : bRo) + 32 * u : (u == 2 ? (sl < 8 ? bN : bR) + 128 : (sl < 8 ? bNt : bRt) + 32 * (u - 3));
                const u32x2 lo = trr(bb + r0 * WP), hi = trr(bb + (r0 + hs) * WP);
                d[u] = u32x4{lo[0], lo[1], hi[0], hi[1]};
            } else {
                LAS char* bb = u < 2 ? (sl < 8 ? bNo : bRo) + 1024 * u : (u == 2 ? (sl < 8 ? bN : bR) + 4096 : (sl < 8 ? bNt : bRt) + 1024 * (u - 3));
                d[u] = lld<u32x4>(bb + (sl < 8 ? sl * 5120 : 0));
            }
        }
    };
    rd(0, w[0]);
#pragma unroll
    for (int u = 0; u < 5; ++u) asm volatile("" : "+v"(acc[u]));
#pragma unroll
    for (int sl = 0; sl < 9; ++sl) {
        pre(sl);
        if (sl + 1 < 9) rd(sl + 1, w[(sl + 1) & 1]);
#pragma unroll
        for (int u = 0; u < 5; ++u) MF16(acc[u], w[sl & 1][u], xf[sl]);
        hook(sl, w[sl & 1]);
    }
    asm volatile("s_nop 7" : "+v"(acc[0]), "+v"(acc[1]), "+v"(acc[2]), "+v"(acc[3]), "+v"(acc[4]));
}
DI void g1_exchange(const Ctx& c, f32x4 (&acc)[5]) {
    LAS char* xw = c.L + L_XCH + c.wave * 3072 + c.lane * 16;
    lst(xw, acc[3]); lst(xw + 1024, acc[4]); lst(xw + 2048, acc[2]);
    BAR();
    LAS char* xr = c.L + L_XCH + (c.wave ^ 4) * 3072 + c.lane * 16;
    acc[0] += lld<f32x4>(xr); acc[1] += lld<f32x4>(xr + 1024); acc[2] += lld<f32x4>(xr + 2048);
}
DI void load_xfrags(const Ctx& c, u32 buf, u32x4 (&xf)[9]) {
    const int m = c.wave & 3, kh = c.wave >> 2;
    const u32 so = buf + (u32)(c.i * 18 * 4 + m) * 1024;
#pragma unroll
    for (int sl = 0; sl < 9; ++sl) { const int s = sl < 8 ? 8 * kh + sl : 16 + kh; xf[sl] = ld128(c.rs, c.lane * 16, so + (u32)s * 4096); }
}
DI u32x4 stats_issue(const Ctx& c, u32 stbuf) {
    const u32 vo = (u32)((c.i * 16 + 2 * (c.lane >> 3)) * 64 + c.wave * 8 + (c.lane & 7)) * 8;
    const u32x2 a = ld64(c.rs, vo, stbuf), b = ld64(c.rs, vo + 512, stbuf);
    return u32x4{a[0], a[1], b[0], b[1]};
}
DI void stats_finish(const Ctx& c, u32x4 v) {
    const int tok = c.wave * 8 + (c.lane & 7);
    float s = asf(v[0]) + asf(v[2]), s2 = asf(v[1]) + asf(v[3]);
    s += __shfl_xor(s, 8); s2 += __shfl_xor(s2, 8);
    s = sum_g(s); s2 = sum_g(s2);
    if (c.lane < 8) {
        lst(c.L + L_STATP + tok * 8, f32x2{s, s2}); lst(c.L + L_STATP + 512 + tok * 8, f32x2{0.f, 0.f});
        const float mean = s * (1.f / DM), var = s2 * (1.f / DM) - mean * mean;
        lst(c.L + L_MEAN + tok * 4, mean); lst(c.L + L_MEAN + 256 + tok * 4, __builtin_amdgcn_rsqf(fmaxf(var, 0.f) + 1e-5f));
    }
}
DI void stats_issue32(const Ctx& c, u32 stbuf, u32x4 (&v)[4]) {
    const u32 vo = (u32)((c.i * 16 + (c.lane >> 4)) * 64 + (c.wave == 7 ? 32 : 0) + 2 * (c.lane & 15)) * 8;
#pragma unroll
    for (int n = 0; n < 4; ++n) v[n] = ld128(c.rs, vo + n * 4 * 512, stbuf);
}
DI void stats_finish32(const Ctx& c, const u32x4 (&v)[4]) {
    float s[4];
#pragma unroll
    for (int e = 0; e < 4; ++e) s[e] = sum_g((asf(v[0][e]) + asf(v[1][e])) + (asf(v[2][e]) + asf(v[3][e])));
    if (c.lane < 16) {
        const int tok = (c.wave == 7 ? 32 : 0) + 2 * c.lane;
        const float m0 = s[0] * (1.f / DM), m1 = s[2] * (1.f / DM);
        lst(c.L + L_MEAN + tok * 4, f32x2{m0, m1});
        lst(c.L + L_MEAN + 256 + tok * 4, f32x2{__builtin_amdgcn_rsqf(fmaxf(s[1] * (1.f / DM) - m0 * m0, 0.f) + 1e-5f), __builtin_amdgcn_rsqf(fmaxf(s[3] * (1.f / DM) - m1 * m1, 0.f) + 1e-5f)});
    }
}

struct KV { u32x4 km; u32x2 kr; u32x4 v0, v1; };
DI int vrow_of(int r) { return r < 18 ? r : 18; }
DI u32 ka_vrow(int blk, int row) { return row < 8 ? KA_V0 + blk * 128 + row * 16 : (row < 16 ? KA_V8 + blk * 128 + (row - 8) * 16 : KA_VX + blk * 64 + (row - 16) * 16); }
DI KV load_tile_ws(const Ctx& c, u32 tile, bool shared) {
    const int r = c.lane & 31, h = c.lane >> 5;
    KV t;
    if (shared) {
        t.km = ld128(c.rs, c.lane * 16, tile);
        t.kr = ld64(c.rs, KA_KREM + r * 8, tile);
        t.v0 = ld128(c.rs, ka_vrow(h, vrow_of(r)), tile);
        t.v1 = ld128(c.rs, ka_vrow(2 + h, vrow_of(r)), tile);
        return t;
    }
    t.km = ld128(c.rs, c.lane * 16, tile);
    t.kr = ld64(c.rs, KT_KREM + r * 8, tile);
    t.v0 = ld128(c.rs, KT_V + h * KT_VBLK + vrow_of(r) * 16, tile);
    t.v1 = ld128(c.rs, KT_V + (2 + h) * KT_VBLK + vrow_of(r) * 16, tile);
    return t;
}
DI f32x16 mfma32k8(u32x2 a, u32x2 b, f32x16 c) { return __builtin_amdgcn_mfma_f32_32x32x8bf16_1k(__builtin_bit_cast(s16x4, a), __builtin_bit_cast(s16x4, b), c, 0, 0, 0); }
DI f32x16 qk(const KV& t, u32x4 qm, u32x2 qr, f32x16 c0) {
    return mfma32k8(t.kr, qr, mfma32(t.km, qm, c0));
}
DI void QK1(f32x16& s, u32x4 km, u32x4 qm) { asm volatile("v_mfma_f32_32x32x16_bf16 %0, %1, %2, 0" : "=&v"(s) : "v"(km), "v"(qm)); }
DI void QK1C(f32x16& s, u32x4 km, u32x4 qm, const f32x16& c0) { asm volatile("v_mfma_f32_32x32x16_bf16 %0, %1, %2, %3" : "=&v"(s) : "v"(km), "v"(qm), "v"(c0)); }
DI void QK2(f32x16& s, u32x2 kr, u32x2 qr) { asm volatile("v_mfma_f32_32x32x8_bf16 %0, %1, %2, %0" : "+v"(s) : "v"(kr), "v"(qr)); }
DI void PV(f32x16& o, u32x4 v, u32x4 p) { asm volatile("v_mfma_f32_32x32x16_bf16 %0, %1, %2, %0" : "+v"(o) : "v"(v), "v"(p)); }
DI float EX(float x) { float r; asm volatile("v_exp_f32 %0, %1" : "=v"(r) : "v"(x)); return r; }
DI u32 CV(float a, float b) { u32 r; asm volatile("v_cvt_pk_bf16_f32 %0, %1, %2" : "=v"(r) : "v"(a), "v"(b)); return r; }
struct Pipe { float e[4]; u32x4 p1; };
template <bool NEXT = true, class F0, class F1, class F2, class F3, class F4>
DI void attn_step(const f32x16& sc, f32x16& sn, f32x16& o, Pipe& pp, const u32x4& km, const u32x2& kr, const u32x4& v0, const u32x4& v1, u32x4 qm, u32x2 qr, F0 first, F1 LK, F2 LR, F3 LV1, F4 LV0) {
    u32x4 p0; float a0, a1, a2, a3, b0, b1, b2, b3;
    if (NEXT) first(sn, km, qm);
    LK();
    pp.p1[2] = CV(pp.e[0], pp.e[1]); pp.p1[3] = CV(pp.e[2], pp.e[3]);
    a0 = EX(sc[0]); a1 = EX(sc[1]); a2 = EX(sc[2]); a3 = EX(sc[3]);
    if (NEXT) QK2(sn, kr, qr); else asm volatile("s_nop 0");
    LR();
    p0[0] = CV(a0, a1); p0[1] = CV(a2, a3);
    b0 = EX(sc[4]); b1 = EX(sc[5]); b2 = EX(sc[6]); b3 = EX(sc[7]);
    PV(o, v1, pp.p1); LV1();
    p0[2] = CV(b0, b1); p0[3] = CV(b2, b3);
    a0 = EX(sc[8]); a1 = EX(sc[9]); a2 = EX(sc[10]); a3 = EX(sc[11]);
    PV(o, v0, p0); LV0();
    pp.p1[0] = CV(a0, a1); pp.p1[1] = CV(a2, a3);
    pp.e[0] = EX(sc[12]); pp.e[1] = EX(sc[13]); pp.e[2] = EX(sc[14]); pp.e[3] = EX(sc[15]);
}
template <class F1, class F2, class F3, class F4>
DI void attn_step2(const f32x16& scx, f32x16& snx, f32x16& ox, Pipe& px, const f32x16& scy, f32x16& sny, f32x16& oy, Pipe& py,
                   const u32x4& km, const u32x2& kr, const u32x4& v0, const u32x4& v1, u32x4 qmx, u32x2 qrx, u32x4 qmy, u32x2 qry, F1 LK, F2 LR, F3 LV1, F4 LV0) {
    u32x4 p0x, p0y; float a0, a1, a2, a3, b0, b1, b2, b3, c0, c1, c2, c3, d0, d1, d2, d3;
#define GRP(MF, PD0, PD1, E0, E1, E2, E3, D0, D1, D2, D3, SRC, I) MF; PD0 = CV(E0, E1); PD1 = CV(E2, E3); D0 = EX(SRC[I]); D1 = EX(SRC[I + 1]); D2 = EX(SRC[I + 2]); D3 = EX(SRC[I + 3])
    GRP(QK1(snx, km, qmx), px.p1[2], px.p1[3], px.e[0], px.e[1], px.e[2], px.e[3], a0, a1, a2, a3, scx, 0);
    GRP(QK1(sny, km, qmy); LK(), py.p1[2], py.p1[3], py.e[0], py.e[1], py.e[2], py.e[3], b0, b1, b2, b3, scy, 0);
    GRP(QK2(snx, kr, qrx), p0x[0], p0x[1], a0, a1, a2, a3, c0, c1, c2, c3, scx, 4);
    GRP(QK2(sny, kr, qry); LR(), p0y[0], p0y[1], b0, b1, b2, b3, d0, d1, d2, d3, scy, 4);
    GRP(PV(ox, v1, px.p1), p0x[2], p0x[3], c0, c1, c2, c3, a0, a1, a2, a3, scx, 8);
    GRP(PV(oy, v1, py.p1); LV1(), p0y[2], p0y[3], d0, d1, d2, d3, b0, b1, b2, b3, scy, 8);
    GRP(PV(ox, v0, p0x), px.p1[0], px.p1[1], a0, a1, a2, a3, px.e[0], px.e[1], px.e[2], px.e[3], scx, 12);
    GRP(PV(oy, v0, p0y); LV0(), py.p1[0], py.p1[1], b0, b1, b2, b3, py.e[0], py.e[1], py.e[2], py.e[3], scy, 12);
#undef GRP
}
DI void attn_finish(f32x16& o, Pipe& pp, u32x4 v1) {
    asm volatile("s_nop 0");
    pp.p1[2] = CV(pp.e[0], pp.e[1]); pp.p1[3] = CV(pp.e[2], pp.e[3]);
    asm volatile("s_nop 1");
    PV(o, v1, pp.p1);
    asm volatile("s_nop 7\n\ts_nop 3" : "+v"(o));
}
DI void attn_finish2(f32x16& ox, Pipe& px, f32x16& oy, Pipe& py, u32x4 v1) {
    asm volatile("s_nop 0");
    px.p1[2] = CV(px.e[0], px.e[1]); px.p1[3] = CV(px.e[2], px.e[3]);
    py.p1[2] = CV(py.e[0], py.e[1]); py.p1[3] = CV(py.e[2], py.e[3]);
    PV(ox, v1, px.p1);
    asm volatile("s_nop 0");
    PV(oy, v1, py.p1);
    asm volatile("s_nop 7\n\ts_nop 3" : "+v"(ox), "+v"(oy));
}
DI void pexp(const f32x16& s, u32x4& p0, u32x4& p1) {
    float e[16];
#pragma unroll
    for (int r = 0; r < 16; ++r) e[r] = __builtin_amdgcn_exp2f(s[r]);
    p0 = u32x4{pk(e[0], e[1]), pk(e[2], e[3]), pk(e[4], e[5]), pk(e[6], e[7])};
    p1 = u32x4{pk(e[8], e[9]), pk(e[10], e[11]), pk(e[12], e[13]), pk(e[14], e[15])};
}
DI float rowmax(const f32x16& s) {
    float a = fmaxf(fmaxf(s[0], s[1]), s[2]), b = fmaxf(fmaxf(s[3], s[4]), s[5]);
#pragma unroll
    for (int r = 6; r < 16; r += 2) { if (r & 2) a = fmaxf(fmaxf(a, s[r]), s[r + 1]); else b = fmaxf(fmaxf(b, s[r]), s[r + 1]); }
    return max_h(fmaxf(a, b));
}
DI f32x16 mask_c(int lane, bool diag) {
    const int r = lane & 31, h = lane >> 5; f32x16 m;
#pragma unroll
    for (int e = 0; e < 16; ++e) { const bool ok = diag ? (crow(e, h) <= r) : (crow(e, h) > r); m[e] = ok ? 0.f : NEG_BIG; }
    return m;
}
DI f32x16 attn_slow(const Ctx& c, u32 tile0, int n0, int n1, bool masked, u32x4 qm, u32x2 qr) {
    const int h = c.lane >> 5;
    qr[1] = 0u;
    f32x16 o = {}; float m = NEG_BIG;
    for (int n = n0; n <= n1; ++n) {
        const KV t = load_tile_ws(c, tile0 + (u32)n * TILE_B, !masked);
        f32x16 z = {};
        f32x16 s = qk(t, qm, qr, z);
        if (masked && (n == n1 || n == n1 - 8)) {
            const int r = c.lane & 31;
#pragma unroll
            for (int e = 0; e < 16; ++e) { const bool ok = (n == n1) ? (crow(e, h) <= r) : (crow(e, h) > r); s[e] = ok ? s[e] : NEG_BIG; }
        }
        const float mn = fmaxf(m, rowmax(s)), al = __builtin_amdgcn_exp2f(m - mn);
        m = mn;
#pragma unroll
        for (int e = 0; e < 16; ++e) { o[e] *= al; s[e] = s[e] <= NEG_BIG ? NEG_BIG : s[e] - mn; }
        u32x4 p0, p1; pexp(s, p0, p1);
        o = mfma32(t.v0, p0, o); o = mfma32(t.v1, p1, o);
    }
    return o;
}
DI bool attn_unsafe(const f32x16& o) {
    float z = 0.f;
#pragma unroll
    for (int e = 0; e < 10; ++e) z = __builtin_fmaf(o[e], 0.f, z);
    const float l = o[10];
    return __any(!(l > 0x1p-40f && l < 0x1p30f && z == 0.f));
}
DI void store_o(const Ctx& c, u32 obuf, const f32x16& o, int a, int t) {
    const int r = c.lane & 31, h = c.lane >> 5;
    const float inv = __builtin_amdgcn_rcpf(o[10]);
    const u32x4 mainp = {pk(o[0] * inv, o[1] * inv), pk(o[2] * inv, o[3] * inv), pk(o[4] * inv, o[5] * inv), pk(o[6] * inv, o[7] * inv)};
    const int m = 2 * t + (r >> 4), tok = r & 15;
    const u32 so = obuf + (u32)(c.i * 18 * 4) * 1024;
    st128(c.rs, mainp, (u32)((2 * c.j + (a >> 1)) * 4 + m) * 1024 + ((2 * (a & 1) + h) * 16 + tok) * 16, so);
    if (h == 0) st32(c.rs, pk(o[8] * inv, o[9] * inv), (u32)((16 + (c.j >> 2)) * 4 + m) * 1024 + ((c.j & 3) * 16 + tok) * 16 + 4 * a, so);
}

template <int rep>
DI void a_proj(Ctx& c0, const Params& p) {
    Ctx c = fresh(c0);
    const int lane = c.lane, m = c.wave & 3, kh = c.wave >> 2, tok = lane & 15, g = lane >> 4;
    u32x4 xf[9]; f32x4 acc[5] = {};
    f32x4 s1 = {}, s2 = {}, ca = {}, cb = {};
    Fmt fm; u64 e0 = 0; u32x4 stv;
    if (rep == 0) {
        e0 = __hip_atomic_load((GAS u64*)(p.ws + WS_EPOCH), __ATOMIC_RELAXED, __HIP_MEMORY_SCOPE_AGENT);
        const char* xt = (const char*)p.x + (size_t)(c.i * 64) * (DM * 2);
#pragma unroll
        for (int n = 0; n < 8; ++n) {
            const int row = c.wave * 8 + n;
            glds_in(xt + row * (DM * 2) + (lane ^ (row & 15)) * 16, c.Lb + (row < 60 ? L_XIN + row * 1024 : L_XIN2 + (row - 60) * 1024));
        }
        xf[8] = *(const u32x4*)(xt + (m * 16 + tok) * (DM * 2) + (512 + 32 * kh + 8 * g) * 2);
        load_wimage_a(c, p);
        fmt_issue(c, p, fm);
        STAMP(13);
        if (c.wave < 2) { if (fm.kind[0] == 3) asm volatile("s_waitcnt vmcnt(19)" ::: "memory"); else asm volatile("s_waitcnt vmcnt(12)" ::: "memory"); }
        else { if (fm.kind[0] == 3) asm volatile("s_waitcnt vmcnt(14)" ::: "memory"); else asm volatile("s_waitcnt vmcnt(7)" ::: "memory"); }
        BAR();
        STAMP(18);
        asm volatile("" : "+v"(xf[8]), "+v"(e0));
        { const int row = m * 16 + tok, t2 = tok ^ (g >> 1);
          LAS char* xb = c.L + (row < 60 ? L_XIN + row * 1024 : L_XIN2 + (row - 60) * 1024) + kh * 512 + 8 * (g & 1);
#pragma unroll
          for (int sl = 0; sl < 8; ++sl) {
              const int K = 4 * (sl & 3);
              const u32x2 lo = lld<u32x2>(xb + (sl >> 2) * 256 + ((K ^ t2) * 16)), hi = lld<u32x2>(xb + (sl >> 2) * 256 + (((K + 2) ^ t2) * 16));
              xf[sl] = u32x4{lo[0], lo[1], hi[0], hi[1]};
          } }
        asm volatile("s_nop 1" : "+v"(xf[0]), "+v"(xf[1]), "+v"(xf[2]), "+v"(xf[3]), "+v"(xf[4]), "+v"(xf[5]), "+v"(xf[6]), "+v"(xf[7]), "+v"(xf[8]));
        STAMP(2);
    } else {
        wait_group(c, 5);
        load_xfrags(c, WS_XF + 1 * XF_BYTES, xf);
        stv = stats_issue(c, WS_ST + 1 * ST_BYTES);
        asm volatile("s_waitcnt vmcnt(11)" ::: "memory"); BAR();
        STAMP(32);
    }
    if (rep != 0) { g1_mma<true>(c, xf, acc, [](int, const u32x4 (&)[5]) {}, [](int) {}); stats_finish(c, stv); }
    else {
        u32x4 ones = ones4();
        const int tlc = m < 2 ? 2 * kh + m : 2 * (1 - kh) + (m - 2), q = (lane & 15) >> 2, pp = lane & 3;
        LAS char* bN = c.L + L_R1 + (kh * 256 + 4 * g + q) * WP + 8 * pp + 32 * tlc;
        LAS char* bR = c.L + L_R1 + (512 + 32 * kh + 8 * g + q) * WP + 8 * pp + 32 * tlc;
        asm volatile("" : "+v"(s1), "+v"(s2), "+v"(ca), "+v"(cb), "+v"(ones));
        u32x4 wx[2];
        auto rdx = [&](int sl) {
            const int r0 = sl < 8 ? 32 * sl : 0, hs = sl < 8 ? 16 : 4; LAS char* b = sl < 8 ? bN : bR;
            const u32x2 lo = trr(b + r0 * WP), hi = trr(b + (r0 + hs) * WP);
            wx[sl & 1] = u32x4{lo[0], lo[1], hi[0], hi[1]}; };
        rdx(0);
        g1_mma<true, true>(c, xf, acc, [&](int sl, const u32x4 (&w)[5]) {
            if (sl + 1 < 9) rdx(sl + 1);
            MF16(s1, ones, xf[sl]); MF16(s2, xf[sl], xf[sl]); MF16(cb, w[2], ones); MF16(ca, wx[sl & 1], ones);
        }, [&](int sl) {
            if (sl == 3) {
                if (fm.kind[0] == 3) asm volatile("s_waitcnt vmcnt(9)" ::: "memory"); else asm volatile("s_waitcnt vmcnt(2)" ::: "memory");
                BAR();
            }
        });
        if (c.tid < 180) st128(c.rs, lld<u32x4>(c.L + L_R1 + c.i * 2880 + c.tid * 16), c.tid * 16, WS_WIMGA + (u32)c.j * WIMG_BYTES + (u32)c.i * 2880);
        asm volatile("s_nop 7" : "+v"(s1), "+v"(s2), "+v"(ca), "+v"(cb));
        const int rg = tok & 3;
        float d = rg == 0 ? s2[0] : (rg == 1 ? s2[1] : (rg == 2 ? s2[2] : s2[3]));
        d = sum_g(g == (tok >> 2) ? d : 0.f);
        if (g == 0) lst(c.L + L_STATP + kh * 512 + (m * 16 + tok) * 8, f32x2{s1[0], d});
        const int tl = m < 2 ? 2 * kh + m : 2 * (1 - kh) + (m - 2);
        if (tok == 0) { lst(c.L + L_CSA + (kh * 5 + tl) * 64 + g * 16, ca); if (m == 0) lst(c.L + L_CSA + (kh * 5 + 4) * 64 + g * 16, cb); }
    }
    STAMP(3 + rep * 30);
    g1_exchange(c, acc);
    const f32x2 sa = lld<f32x2>(c.L + L_STATP + (m * 16 + tok) * 8), sb = lld<f32x2>(c.L + L_STATP + 512 + (m * 16 + tok) * 8);
    const float mean = (sa[0] + sb[0]) * (1.f / DM), var = (sa[1] + sb[1]) * (1.f / DM) - mean * mean;
    const float rstd = __builtin_amdgcn_rsqf(fmaxf(var, 0.f) + 1e-5f);
    LAS char* stg = c.L + L_STG + (m * 16 + tok) * STG_P + g * 8;
    auto fin = [&](int u, int t, float sc) {
        const f32x4 cs = lld<f32x4>(c.L + L_CSA + t * 64 + g * 16) + lld<f32x4>(c.L + L_CSA + (5 + t) * 64 + g * 16);
        f32x4 v;
#pragma unroll
        for (int e = 0; e < 4; ++e) v[e] = (acc[u][e] - mean * cs[e]) * sc;
        return u32x2{pk(v[0], v[1]), pk(v[2], v[3])};
    };
    if (kh == 1) {
        const u32x2 d4 = fin(2, 4, g < 2 ? rstd * QSCALE : rstd);
        lst(stg + 128, d4);
        const u32 kva = WS_KVA + rep * KVA_BYTES + (u32)(2 * c.i + (m >> 1)) * TILE_B;
        const int j = c.j, key = (m & 1) * 16 + tok;
        if (j < 2) { if (g >= 2) st64(c.rs, d4, j * 512 + key * 16 + (g - 2) * 8, kva); }
        else if (j == 2) { if (g == 3) st64(c.rs, u32x2{d4[1], 0x00003f80u}, KA_KREM + key * 8, kva); }
        else if (j < 6) {
            asm volatile("s_waitcnt lgkmcnt(0)" ::: "memory");
            const int q = (lane & 15) >> 2, pp = lane & 3;
            LAS char* s = c.L + L_STG + (16 * m + q + 4 * (pp >> 1)) * STG_P + 144 + 8 * (pp & 1);
            const u32x2 lo = trr(s), hi = trr(s + 8 * STG_P);
            const int dl = lane & 7, blk = (m & 1) * 2 + ((lane >> 3) & 1);
            if (j < 5) { if (lane < 16) st128(c.rs, u32x4{lo[0], lo[1], hi[0], hi[1]}, (j == 3 ? KA_V0 : KA_V8) + blk * 128 + dl * 16, kva); }
            else {
                const u32x4 on = ones4();
                const u32x4 v = dl >= 6 ? u32x4{lo[0], lo[1], hi[0], hi[1]} : on;
                if (lane < 16 && (dl >= 6 || dl < 2)) st128(c.rs, v, KA_VX + blk * 64 + (dl >= 6 ? dl - 6 : 2 + dl) * 16, kva);
            }
        }
    }
    lst(stg + (2 * kh) * 32, fin(0, 2 * kh, rstd * QSCALE));
    lst(stg + (2 * kh + 1) * 32, fin(1, 2 * kh + 1, rstd * QSCALE));
    if (rep == 0) {
        fmt_finish(c, fm);
        const u64 e = e0 + 1; c0.elo = c.elo = __builtin_amdgcn_readfirstlane((u32)e); c0.ehi = c.ehi = __builtin_amdgcn_readfirstlane((u32)(e >> 32));
    }
    STAMP(4 + rep * 30);
    publish(c, rep * 6);
}

DI void load_wring(const Ctx& c, u32x4 (&w0)[WRING], u32x4 (&w1)[WRING]) {
    const u32 wso = WS_WF + (u32)((c.j * 8 + c.wave) * 2) * 18 * 1024;
#pragma unroll
    for (int s = 0; s < WRING; ++s) { w0[s] = ld128(c.rs, c.lane * 16, wso + s * 1024); w1[s] = ld128(c.rs, c.lane * 16, wso + (18 + s) * 1024); }
}
DI void colsums_b(const Ctx& c) {
    const int tok = c.lane & 15, g = c.lane >> 4;
    if (c.wave >= 1) {
        const int extra = c.wave == 1 ? 0 : c.wave;
        u32 v[4][5];
#pragma unroll
        for (int q = 0; q < 4; ++q) {
            const int unit = (q < 2 ? c.wave : extra) * 2 + (q & 1);
#pragma unroll
            for (int u = 0; u < 5; ++u) { const int s = g + 4 * u; v[q][u] = (q < 2 || c.wave == 1) ? ld32(c.rs, (s < 18 ? s : 0) * 64 + tok * 4, WS_CSP + (u32)((c.j * 16 + unit) * 18) * 64) : 0u; }
        }
#pragma unroll
        for (int q = 0; q < 4; ++q) {
            const int unit = (q < 2 ? c.wave : extra) * 2 + (q & 1);
            float cs = (asf(v[q][0]) + asf(v[q][1])) + (asf(v[q][2]) + asf(v[q][3])) + (g < 2 ? asf(v[q][4]) : 0.f);
            cs = sum_g(cs);
            if (g == 0 && (q < 2 || c.wave == 1)) lst(c.L + L_CSB + unit * 64 + tok * 4, cs);
        }
    }
}
struct ATail { f32x16 o; u32x4 qm; u32x2 qr; };
DI void a_attn(const Ctx& c0, const Params& p, int rep, ATail& st) {
    const Ctx c = fresh(c0);
    const int lane = c.lane, r = lane & 31, h = lane >> 5, a = c.wave & 3, kh = c.wave >> 2;
    const int ph = rep * 6;
    const u32 kva = WS_KVA + rep * KVA_BYTES;
    u32x4 qmx, qmy; u32x2 qrx, qry;
    { LAS char* s = c.L + L_STG + (32 * kh + r) * STG_P + 36 * a;
      qmx = u32x4{lld<u32>(s + 16 * h), lld<u32>(s + 16 * h + 4), lld<u32>(s + 16 * h + 8), lld<u32>(s + 16 * h + 12)};
      const u32 rx = lld<u32>(s + 32);
      s += (32 - 64 * kh) * STG_P;
      qmy = u32x4{lld<u32>(s + 16 * h), lld<u32>(s + 16 * h + 4), lld<u32>(s + 16 * h + 8), lld<u32>(s + 16 * h + 12)};
      const u32 ry = lld<u32>(s + 32);
      qrx = u32x2{h == 0 ? rx : 0u, 0u}; qry = u32x2{h == 0 ? ry : 0u, 0u}; }
    const int ch0 = c.i >> 2;
    auto chunk_ids = [&](int n) { const int ch = (ch0 + n) & 7; return ch * 32 + (lane & 31); };
    u32 pvo[5];
#pragma unroll
    for (int k = 0; k < 5; ++k) {
        const u32 S = (u32)((c.wave & 3) + 4 * k) * 64 + lane, tile = (S * 52429u) >> 23, ps = S - tile * 160;
        const u32 v = ps - 64, blk = (v * 3450u) >> 16, row = v - blk * 19;
        const u32 src = ps < 64 ? ps * 16 : (ps < 140 ? ka_vrow(blk, row) : (ps < 156 ? KA_KREM + (ps - 140) * 16 : ps * 16));
        pvo[k] = tile * TILE_B + src;
    }
    auto issue_piece = [&](int n, int k) {
        const int ch = (ch0 + n) & 7; const u32 so = kva + (u32)ch * CHUNK_B; const u32 dst = c.Lb + L_RING + (n % 3) * CHUNK_B;
        glds_ws(c.srd, pvo[k], so, dst + ((c.wave & 3) + 4 * k) * 1024);
    };
    auto issue_chunk8 = [&](int n) {
        if (c.wave < 4) { issue_piece(n, 0); issue_piece(n, 2); issue_piece(n, 4); } else { issue_piece(n, 1); issue_piece(n, 3); }
    };
    if (c.wave == 0) poll(c, ph, lane < 32 ? chunk_ids(0) : chunk_ids(1));
    BAR();
    STAMP(6 + rep * 30);
    issue_chunk8(0);
    if (c.wave < 4) { issue_piece(1, 0); issue_piece(1, 1); issue_piece(1, 2); issue_piece(1, 3); issue_piece(1, 4); asm volatile("s_waitcnt vmcnt(5)" ::: "memory"); }
    else DRAIN();
    BAR();
    STAMP(7 + rep * 30); if (rep == 0) CSTAMP(61);
    if (rep == 1) { CSTAMPW(0, 4); CSTAMPW(8, 0); }
    const int vr = vrow_of(r);
    LAS char* kb = c.L + L_RING + lane * 16;
    LAS char* rb = c.L + L_RING + KT_KREM + r * 8;
    LAS char* vb = c.L + L_RING + KT_V + h * KT_VBLK + vr * 16;
    f32x16 sAx, sBx, sAy, sBy, ox, oy;
    const u32x4 k0m = lld<u32x4>(kb); const u32x2 k0r = lld<u32x2>(rb);
    kb += kh * 4 * TILE_B; rb += kh * 4 * TILE_B; vb += kh * 4 * TILE_B;
    const u32x4 kom = lld<u32x4>(kb); const u32x2 kor = lld<u32x2>(rb);
    u32x4 km = lld<u32x4>(kb + TILE_B), v0 = lld<u32x4>(vb), v1 = lld<u32x4>(vb + 2 * KT_VBLK); u32x2 kr = lld<u32x2>(rb + TILE_B);
    Pipe px, py;
    { u32 z = 0u; asm volatile("" : "+v"(z));
      px.e[0] = px.e[1] = px.e[2] = px.e[3] = asf(z); px.p1 = u32x4{z, z, z, z}; py = px; }
    asm volatile("" : "+v"(px.p1), "+v"(px.e[0]), "+v"(px.e[1]), "+v"(px.e[2]), "+v"(px.e[3]), "+v"(qmx), "+v"(qrx));
    asm volatile("s_nop 1" : "+v"(py.p1), "+v"(py.e[0]), "+v"(py.e[1]), "+v"(py.e[2]), "+v"(py.e[3]), "+v"(qmy), "+v"(qry));
    QK1(ox, px.p1, px.p1); QK1(oy, px.p1, px.p1);
    QK1(sBx, k0m, qmx); QK1(sBy, k0m, qmy); QK2(sBx, k0r, qrx); QK2(sBy, k0r, qry);
    QK1(sAx, kom, qmx); QK1(sAy, kom, qmy);
    asm volatile("s_nop 7\n\ts_nop 1" : "+v"(sBx), "+v"(sBy));
    { const float mx = rowmax(sBx), my = rowmax(sBy);
      if (h == 0) { qrx[1] = pk(-mx, 0.f); qry[1] = pk(-my, 0.f); } }
    asm volatile("s_nop 1" : "+v"(qrx), "+v"(qry));
    QK2(sAx, kor, qrx); QK2(sAy, kor, qry);
    asm volatile("s_nop 7\n\ts_nop 3" : "+v"(sAx), "+v"(sAy));
    if (rep == 1) { CSTAMPW(1, 4); CSTAMPW(9, 0); }
    int slot = 0;
    const u64 want = ((u64)c.ehi << 32) | c.elo;
    const u32 fvo = (lane & 31) * 128;
    u64 fl = want;
    if (c.wave == 0) flag_load(fl, ~want, fvo, c.srd, WS_FLAG + (u32)(ph * 256 + ((ch0 + 2) & 7) * 32) * 128);
#pragma unroll 1
    for (int n = 0; n < 8; ++n) {
        const int nslot = slot == 2 ? 0 : slot + 1;
        LAS char* kc = kb + slot * CHUNK_B; LAS char* rc = rb + slot * CHUNK_B; LAS char* vc = vb + slot * CHUNK_B;
        LAS char* kn = kb + nslot * CHUNK_B; LAS char* rn = rb + nslot * CHUNK_B; LAS char* vn = vb + nslot * CHUNK_B;
#define A_TILE(U, SCX, SNX, SCY, SNY, X1, X2) asm volatile("" :: "v"(km), "v"(kr), "v"(v1));        \
        attn_step2(SCX, SNX, ox, px, SCY, SNY, oy, py, km, kr, v0, v1, qmx, qrx, qmy, qry, \
            [&] { km = lld<u32x4>((U) < 2 ? kc + ((U) + 2) * TILE_B : kn + ((U) - 2) * TILE_B); X1; }, \
            [&] { kr = lld<u32x2>((U) < 2 ? rc + ((U) + 2) * TILE_B : rn + ((U) - 2) * TILE_B); }, \
            [&] { v1 = lld<u32x4>(vc + (U) * TILE_B + 2 * KT_VBLK); X2; }, \
            [&] { v0 = lld<u32x4>((U) < 3 ? vc + ((U) + 1) * TILE_B : vn); })
        A_TILE(0, sAx, sBx, sAy, sBy, (void)0, (void)0); A_TILE(1, sBx, sAx, sBy, sAy, (void)0, (void)0);
        if (__builtin_expect(c.wave < 4, 0)) {
            asm volatile("s_waitcnt vmcnt(0)" : "+v"(fl) :: "memory");
            if (__builtin_expect(!__all(fl == want), 0)) poll(c, ph, chunk_ids(n + 2));
        }
        BAR();
        if (__builtin_expect(c.wave < 4, 0)) {
            fl = want;
            if (c.wave == 0 && n + 3 < 8) flag_load(fl, ~want, fvo, c.srd, WS_FLAG + (u32)(ph * 256 + ((ch0 + n + 3) & 7) * 32) * 128);
            if (n + 2 < 8) { issue_piece(n + 2, 0); issue_piece(n + 2, 1); issue_piece(n + 2, 2); issue_piece(n + 2, 3); issue_piece(n + 2, 4); }
            if (n >= 5) load_wimage_o4(c, 0, n - 5);
            if (rep == 0 && n == 0) {
                u32 z0 = 0u; asm volatile("" : "+v"(z0));
                const u32x4 z = {z0, z0, z0, z0};
#pragma unroll
                for (int q = 0; q < 5; ++q)
                    st128(c.rs, z, lane * 16 + q * 1024, WS_KVB + (u32)((4 * c.j + c.wave) * 64 + 2 * c.i) * TILE_B);
                if (c.wave < 2) st128(c.rs, z, (u32)((16 + (c.j >> 2)) * 4 + (lane >> 4)) * 1024 + ((c.j & 3) * 16 + (lane & 15)) * 16, WS_OF + c.wave * XF_BYTES + (u32)(c.i * 72) * 1024);
            }
        }
        A_TILE(2, sAx, sBx, sAy, sBy, (void)0, (void)0); A_TILE(3, sBx, sAx, sBy, sAy, (void)0, (void)0);
#undef A_TILE
        slot = nslot;
    }
    if (rep == 1) { CSTAMPW(2, 4); CSTAMPW(10, 0); }
    attn_finish2(ox, px, oy, py, v1);
    { LAS char* mine = c.L + L_RING + kh * 2 * CHUNK_B + a * 3072 + lane * 16;
#pragma unroll
      for (int e = 0; e < 3; ++e) lst(mine + e * 1024, f32x4{oy[4 * e], oy[4 * e + 1], oy[4 * e + 2], oy[4 * e + 3]}); }
    st.o = ox; st.qm = qmx; st.qr = qrx;
}

template <bool ISB, class HK>
DI void out_proj(const Ctx& c0, const Params& p, int rep, u32 (&xres)[3][2], HK behind_x) {
    const Ctx c = fresh(c0);
    const int lane = c.lane, m = c.wave & 3, kh = c.wave >> 2, tok = lane & 15, g = lane >> 4;
    const int layer = rep * 2 + (ISB ? 1 : 0);
    wait_group(c, rep * 6 + (ISB ? 4 : 1));
    STAMP((ISB ? 25 : 10) + rep * 30);
    u32x4 xf[9]; f32x4 acc[5] = {};
    load_xfrags(c, WS_OF + (rep * RP * 2 + (ISB ? 1 : 0)) * XF_BYTES, xf);
    __builtin_amdgcn_sched_barrier(0);
    behind_x(c);
    if (ISB) {
        asm volatile("s_waitcnt vmcnt(9)" ::: "memory"); BAR();
    }
    g1_mma<false>(c, xf, acc, [](int, const u32x4 (&)[5]) {}, [](int) {});
    STAMP((ISB ? 26 : 11) + rep * 30);
    g1_exchange(c, acc);
    STAMP((ISB ? 27 : 12) + rep * 30);
#pragma unroll
    for (int u = 0; u < 3; ++u) {
        const float v0 = bflo(xres[u][0]) + acc[u][0], v1 = bfhi(xres[u][0]) + acc[u][1], v2 = bflo(xres[u][1]) + acc[u][2], v3 = bfhi(xres[u][1]) + acc[u][3];
        xres[u][0] = pk(v0, v1); xres[u][1] = pk(v2, v3);
    }
    if (ISB && rep == 1) {
        char* o = (char*)p.out + (size_t)(c.i * 64 + m * 16 + tok) * (DM * 2) + (c.j * 72 + 4 * g) * 2;
        *(u32x2*)(o + (2 * kh) * 32) = u32x2{xres[0][0], xres[0][1]};
        *(u32x2*)(o + (2 * kh + 1) * 32) = u32x2{xres[1][0], xres[1][1]};
        if (kh == 1 && g < 2) *(u32x2*)(o + 128) = u32x2{xres[2][0], xres[2][1]};
        return;
    }
    const int xi = layer == 0 ? 0 : (layer == 1 ? 1 : 2 * RP);
    const u32 so = WS_XF + xi * XF_BYTES + (u32)(c.i * 18 * 4 + m) * 1024;
    st128(c.rs, u32x4{xres[0][0], xres[0][1], xres[1][0], xres[1][1]}, lane * 16, so + (u32)(2 * c.j + kh) * 4096);
    if (kh == 1 && g < 2) st64(c.rs, u32x2{xres[2][0], xres[2][1]}, ((c.j & 3) * 16 + tok) * 16 + 8 * g, so + (u32)(16 + (c.j >> 2)) * 4096);
    __builtin_amdgcn_sched_barrier(0);
    float s1 = 0.f, s2 = 0.f;
#pragma unroll
    for (int u = 0; u < 3; ++u) {
        u32 d0 = xres[u][0], d1 = xres[u][1];
        asm volatile("" : "+v"(d0), "+v"(d1));
        if (u == 2 && !(kh == 1 && g < 2)) { d0 = 0u; d1 = 0u; }
        const float a0 = bflo(d0), a1 = bfhi(d0), a2 = bflo(d1), a3 = bfhi(d1);
        s1 += (a0 + a1) + (a2 + a3); s2 += (a0 * a0 + a1 * a1) + (a2 * a2 + a3 * a3);
    }
    s1 = sum_g(s1); s2 = sum_g(s2);
    if (g == 0) st64(c.rs, u32x2{asu(s1), asu(s2)}, (u32)((c.i * 16 + 2 * c.j + kh) * 64 + m * 16 + tok) * 8, WS_ST + xi * ST_BYTES);
    publish(c, rep * 6 + (ISB ? 5 : 2));
    if (!ISB && rep == 0) colsums_b(c);
    if (ISB) load_wimage_part<36, 90>(c, WS_WIMGA, 1, L_A1);
}

DI void a_tail(const Ctx& c0, const Params& p, int rep, ATail& st, u32 (&xres)[3][2], u32x4 (&w0)[WRING], u32x4 (&w1)[WRING]) {
    const Ctx c = fresh(c0);
    const int lane = c.lane, a = c.wave & 3, kh = c.wave >> 2;
    BAR();
    LAS char* theirs = c.L + L_RING + (1 - kh) * 2 * CHUNK_B + a * 3072 + lane * 16;
#pragma unroll
    for (int e = 0; e < 3; ++e) { const f32x4 v = lld<f32x4>(theirs + e * 1024); st.o[4 * e] += v[0]; st.o[4 * e + 1] += v[1]; st.o[4 * e + 2] += v[2]; st.o[4 * e + 3] += v[3]; }
    STAMP(8 + rep * 30); if (rep == 0) CSTAMP(62);
    if (rep == 1) { CSTAMPW(3, 4); CSTAMPW(11, 0); }
    if (attn_unsafe(st.o)) st.o = attn_slow(c, WS_KVA + rep * KVA_BYTES, 0, 63, false, st.qm, st.qr);
    store_o(c, WS_OF + (rep * RP * 2) * XF_BYTES, st.o, a, kh);
    if (rep == 1) { CSTAMPW(4, 4); CSTAMPW(12, 0); }
    publish(c, rep * 6 + 1);
    load_wring(c, w0, w1);
    STAMP(9 + rep * 30);
    out_proj<false>(c, p, rep, xres, [&](const Ctx& cc) {
        if (rep == 0) {
            const int m = cc.wave & 3, k2 = cc.wave >> 2, tok = cc.lane & 15, g = cc.lane >> 4;
            const char* rr = (const char*)p.x + (size_t)(cc.i * 64 + m * 16 + tok) * (DM * 2) + (cc.j * 72 + 4 * g) * 2;
            const u32x2 r0 = *(const u32x2*)(rr + (2 * k2) * 32), r1 = *(const u32x2*)(rr + (2 * k2 + 1) * 32), r2 = *(const u32x2*)(rr + (g < 2 ? 128 : 0));
            xres[0][0] = r0[0]; xres[0][1] = r0[1]; xres[1][0] = r1[0]; xres[1][1] = r1[1]; xres[2][0] = r2[0]; xres[2][1] = r2[1];
        }
    });
}

struct T_ { static constexpr bool value = true; };
struct F_ { static constexpr bool value = false; };
DI void b_proj(const Ctx& c0, const Params& p, int rep, u32x4 (&w0)[WRING], u32x4 (&w1)[WRING], u32x4& qm, u32x2& qr) {
    const Ctx c = fresh(c0);
    const int lane = c.lane, tok = lane & 15, g = lane >> 4, a = c.wave >> 1, odd = c.wave & 1;
    wait_group(c, rep * 6 + 2);
    STAMP(15 + rep * 30);
    const int xi = rep == 0 ? 0 : 2 * RP;
    u32x4 stv[4];
    if (c.wave == 5 || c.wave == 7) stats_issue32(c, WS_ST + xi * ST_BYTES, stv);
    { const u32 so = WS_XF + xi * XF_BYTES + (u32)(c.i * 72) * 1024;
#pragma unroll
      for (int n = 0; n < 9; ++n) glds_ws(c.srd, lane * 16, so + (c.wave + 8 * n) * 1024, c.Lb + L_XB + (c.wave + 8 * n) * 1024); }
    asm volatile("s_waitcnt vmcnt(5)" ::: "memory"); BAR();
    STAMP(16 + rep * 30);
    f32x4 a0[4] = {}, a1[4] = {};
    const u32 wso = WS_WF + (u32)((c.j * 8 + c.wave) * 2) * 18 * 1024;
    LAS char* xb = c.L + L_XB + lane * 16;
    const bool t0 = !odd, t1 = !odd || a == 0, has1 = !odd || a < 2;
    auto body = [&](auto T0, auto T1, auto H1) {
        u32x4 x[2][4];
#pragma unroll
        for (int m = 0; m < 4; ++m) { x[0][m] = lld<u32x4>(xb + m * 1024); asm volatile("" : "+v"(a0[m]), "+v"(a1[m])); }
        if (!H1.value) stats_finish32(c, stv);
#pragma unroll
        for (int s = 0; s < 18; ++s) {
            if (s == 7) {
                if (H1.value) asm volatile("s_waitcnt vmcnt(14)" ::: "memory"); else asm volatile("s_waitcnt vmcnt(7)" ::: "memory");
                static_assert(18 - WRING >= 7, "steps 0..6 each refill the ring");
                BAR();
            }
            if (s + 1 < 18) {
#pragma unroll
                for (int m = 0; m < 4; ++m) x[(s + 1) & 1][m] = lld<u32x4>(xb + ((s + 1) * 4 + m) * 1024);
            }
#pragma unroll
            for (int m = 0; m < 4; ++m) {
                if (T0.value) MF16(a0[m], w0[s % WRING], x[s & 1][m]); else MF16(a0[m], x[s & 1][m], w0[s % WRING]);
                if (H1.value) { if (T1.value) MF16(a1[m], w1[s % WRING], x[s & 1][m]); else MF16(a1[m], x[s & 1][m], w1[s % WRING]); }
            }
            if (s + WRING < 18) { w0[s % WRING] = ld128(c.rs, lane * 16, wso + (WRING + s) * 1024); if (H1.value) w1[s % WRING] = ld128(c.rs, lane * 16, wso + (18 + WRING + s) * 1024); }
        }
        asm volatile("s_nop 7" : "+v"(a0[0]), "+v"(a0[1]), "+v"(a0[2]), "+v"(a0[3]), "+v"(a1[0]), "+v"(a1[1]), "+v"(a1[2]), "+v"(a1[3]));
    };
    if (!odd) body(T_{}, T_{}, T_{}); else if (a == 0) body(F_{}, T_{}, T_{}); else if (a == 1) body(F_{}, F_{}, T_{}); else body(F_{}, F_{}, F_{});
    (void)t0; (void)t1; (void)has1;
    STAMP(17 + rep * 30);
    const u32 kvb = WS_KVB + rep * RP * KVB_BYTES;
    const u32 hb = kvb + (u32)((4 * c.j + a) * 64 + 2 * c.i) * TILE_B;
    auto fin_t = [&](const f32x4 (&acc)[4], int slot, float sc, u32 (&d)[4][2]) {
        const f32x4 cs = lld<f32x4>(c.L + L_CSB + (c.wave * 2 + slot) * 64 + g * 16);
#pragma unroll
        for (int m = 0; m < 4; ++m) {
            const float mean = lld<float>(c.L + L_MEAN + (m * 16 + tok) * 4), rs = lld<float>(c.L + L_MEAN + 256 + (m * 16 + tok) * 4) * sc;
            d[m][0] = pk((acc[m][0] - mean * cs[0]) * rs, (acc[m][1] - mean * cs[1]) * rs);
            d[m][1] = pk((acc[m][2] - mean * cs[2]) * rs, (acc[m][3] - mean * cs[3]) * rs);
        }
    };
    auto fin_p = [&](const f32x4 (&acc)[4], int slot, u32 (&d)[4][2]) {
        const float cs = lld<float>(c.L + L_CSB + (c.wave * 2 + slot) * 64 + tok * 4);
#pragma unroll
        for (int m = 0; m < 4; ++m) {
            const f32x4 mean = lld<f32x4>(c.L + L_MEAN + (m * 16 + 4 * g) * 4), rs = lld<f32x4>(c.L + L_MEAN + 256 + (m * 16 + 4 * g) * 4);
            d[m][0] = pk((acc[m][0] - mean[0] * cs) * rs[0], (acc[m][1] - mean[1] * cs) * rs[1]);
            d[m][1] = pk((acc[m][2] - mean[2] * cs) * rs[2], (acc[m][3] - mean[3] * cs) * rs[3]);
        }
    };
    auto frag16 = [&](const u32 (&d)[4][2], int t) {
        auto x = __builtin_amdgcn_permlane16_swap(d[2 * t][0], d[2 * t + 1][0], false, false);
        auto y = __builtin_amdgcn_permlane16_swap(d[2 * t][1], d[2 * t + 1][1], false, false);
        return u32x4{(u32)x[0], (u32)y[0], (u32)x[1], (u32)y[1]};
    };
    auto frag32 = [&](const u32 (&d)[4][2], int t) {
        auto x = __builtin_amdgcn_permlane32_swap(d[2 * t][0], d[2 * t + 1][0], false, false);
        auto y = __builtin_amdgcn_permlane32_swap(d[2 * t][1], d[2 * t + 1][1], false, false);
        return u32x4{(u32)x[0], (u32)y[0], (u32)x[1], (u32)y[1]};
    };
    u32 d[4][2];
    LAS char* lh = c.L + L_KVB + a * 25600 + 8 * TILE_B; LAS char* l0 = c.L + L_KVB + 8 * TILE_B;
    if (!odd) {
        fin_t(a1, 1, 1.f, d);
        { const u32x4 f0 = frag16(d, 0), f1 = frag16(d, 1);
          st128(c.rs, f0, lane * 16, hb); st128(c.rs, f1, lane * 16, hb + TILE_B);
          lst(lh + lane * 16, f0); lst(lh + TILE_B + lane * 16, f1); }
        fin_t(a0, 0, QSCALE, d);
        qm = frag16(d, 0);
        lst(c.L + L_QT1 + a * 1024 + lane * 16, frag16(d, 1));
    } else {
        fin_p(a0, 0, d);
        { const u32x4 f0 = frag32(d, 0), f1 = frag32(d, 1);
          st128(c.rs, f0, KT_V + g * KT_VBLK + tok * 16, hb); st128(c.rs, f1, KT_V + g * KT_VBLK + tok * 16, hb + TILE_B);
          lst(lh + KT_V + g * KT_VBLK + tok * 16, f0); lst(lh + TILE_B + KT_V + g * KT_VBLK + tok * 16, f1); }
        if (a == 0) {
            fin_t(a1, 1, g < 2 ? QSCALE : 1.f, d);
#pragma unroll
            for (int m = 0; m < 4; ++m) {
                if (g < 2) { lst(c.L + L_QREM + (2 * g) * 256 + (m * 16 + tok) * 4, d[m][0]); lst(c.L + L_QREM + (2 * g + 1) * 256 + (m * 16 + tok) * 4, d[m][1]); }
                else {
                    const u32 hb0 = kvb + (u32)(4 * c.j * 64 + 2 * c.i + (m >> 1)) * TILE_B;
                    st64(c.rs, u32x2{d[m][0], 0x00003f80u}, (u32)(2 * (g - 2)) * 64 * TILE_B + KT_KREM + ((m & 1) * 16 + tok) * 8, hb0);
                    st64(c.rs, u32x2{d[m][1], 0x00003f80u}, (u32)(2 * (g - 2) + 1) * 64 * TILE_B + KT_KREM + ((m & 1) * 16 + tok) * 8, hb0);
                    LAS char* lk = l0 + (2 * (g - 2)) * 25600 + (m >> 1) * TILE_B + KT_KREM + ((m & 1) * 16 + tok) * 8;
                    lst(lk, u32x2{d[m][0], 0x00003f80u}); lst(lk + 25600, u32x2{d[m][1], 0x00003f80u});
                }
            }
        } else if (a == 1) {
            fin_p(a1, 1, d);
            const u32 hb0 = kvb + (u32)(4 * c.j * 64 + 2 * c.i) * TILE_B;
            const u32 vo = (u32)(tok >> 1) * 64 * TILE_B + KT_V + g * KT_VBLK + (16 + (tok & 1)) * 16;
            const u32x4 f0 = frag32(d, 0), f1 = frag32(d, 1);
            if (tok < 8) {
                st128(c.rs, f0, vo, hb0); st128(c.rs, f1, vo, hb0 + TILE_B);
                LAS char* lv = l0 + (tok >> 1) * 25600 + KT_V + g * KT_VBLK + (16 + (tok & 1)) * 16;
                lst(lv, f0); lst(lv + TILE_B, f1);
            }
        } else if (a == 2) {
            const u32 hb0 = kvb + (u32)(4 * c.j * 64 + 2 * c.i) * TILE_B;
            if (lane < 32) {
                const u32x4 on = ones4();
                st128(c.rs, on, (u32)(lane >> 3) * 64 * TILE_B + ((lane >> 2) & 1) * TILE_B + KT_V + (lane & 3) * KT_VBLK + 18 * 16, hb0);
                lst(l0 + (lane >> 3) * 25600 + ((lane >> 2) & 1) * TILE_B + KT_V + (lane & 3) * KT_VBLK + 18 * 16, on);
            }
        }
    }
    publish(c, rep * 6 + 3);
    load_wimage_part<44, 90>(c, WS_WIMG + 8 * WIMG_BYTES, 1);
    if (odd) qm = lld<u32x4>(c.L + L_QT1 + a * 1024 + lane * 16);
    const u32 rem = lld<u32>(c.L + L_QREM + a * 256 + (32 * odd + (lane & 31)) * 4);
    qr = u32x2{lane < 32 ? rem : 0u, 0u};
}

DI void b_attn(const Ctx& c0, const Params& p, int rep, u32x4 qm, u32x2 qr) {
    const Ctx c = fresh(c0);
    const int lane = c.lane, r = lane & 31, h = lane >> 5, a = c.wave >> 1, t = c.wave & 1;
    const int qt = 2 * c.i + t;
    const f32x16 cd = mask_c(lane, true);
    if (c.wave == 1) {
        const f32x16 cm = mask_c(lane, false);
#pragma unroll
        for (int e = 0; e < 4; ++e) lst(c.L + L_MSK + e * 1024 + lane * 16, f32x4{cm[4 * e], cm[4 * e + 1], cm[4 * e + 2], cm[4 * e + 3]});
    }
    LAS char* tb = c.L + L_KVB + a * 25600 + (8 + t) * TILE_B;
    LAS char* kb = tb + lane * 16; LAS char* rb = tb + KT_KREM + r * 8; LAS char* vb = tb + KT_V + h * KT_VBLK + vrow_of(r) * 16;
    const u32x4 k0m = lld<u32x4>(kb); const u32x2 k0r = lld<u32x2>(rb);
    u32x4 km, v0 = lld<u32x4>(vb), v1 = lld<u32x4>(vb + 2 * KT_VBLK); u32x2 kr;
    f32x16 sA, sB, o;
    Pipe pp;
    { u32 z = 0u; asm volatile("" : "+v"(z)); pp.e[0] = pp.e[1] = pp.e[2] = pp.e[3] = asf(z); pp.p1 = u32x4{z, z, z, z}; }
    f32x16 cdp = cd;
    asm volatile("s_nop 1" : "+v"(pp.p1), "+v"(pp.e[0]), "+v"(pp.e[1]), "+v"(pp.e[2]), "+v"(pp.e[3]), "+v"(qm), "+v"(qr), "+v"(cdp));
    QK1(o, pp.p1, pp.p1);
    QK1C(sA, k0m, qm, cdp);
    asm volatile("v_mfma_f32_32x32x8_bf16 %0, %1, %2, %3" : "=&v"(sB) : "v"(k0r), "v"(qr), "v"(sA));
    asm volatile("s_nop 7\n\ts_nop 3" : "+v"(sB), "+v"(sA));
    { const float m0 = rowmax(sB); if (h == 0) qr[1] = pk(-m0, 0.f); }
    asm volatile("s_nop 1" : "+v"(qr));
    QK2(sA, k0r, qr);
    asm volatile("s_nop 7\n\ts_nop 3" : "+v"(sA), "+v"(o), "+v"(v0), "+v"(v1));
    if (c.wave == 0) poll(c, rep * 6 + 3, (lane < 5 && c.i - lane >= 0) ? (c.i - lane) * 8 + c.j : -1);
    BAR();
    STAMP(20 + rep * 30);
    const u32 hb = WS_KVB + rep * RP * KVB_BYTES + (u32)((4 * c.j + a) * 64) * TILE_B;
    { const u32 dst = c.Lb + L_KVB + a * 25600;
#pragma unroll
      for (int n = 0; n < 10; ++n) {
          const int pc = 19 - t - 2 * n;
          u32 vo = lane * 16;
          if (c.i < 4) { const int b = pc * 1024 + lane * 16, tau = b / 2560, kt = 2 * c.i - 8 + tau; vo = (u32)((kt < 0 ? 0 : kt) * 2560 + (b - tau * 2560)); glds_ws(c.srd, vo, hb, dst + pc * 1024); }
          else glds_ws(c.srd, vo, hb + (u32)(2 * c.i - 8) * 2560 + pc * 1024, dst + pc * 1024);
      } }
    asm volatile("s_waitcnt vmcnt(5)" ::: "memory");
    BAR();
    STAMP(21 + rep * 30);
    km = lld<u32x4>(kb - TILE_B); kr = lld<u32x2>(rb - TILE_B);
    auto first = [](f32x16& s, u32x4 k, u32x4 q) { QK1(s, k, q); };
    auto firstm = [&](f32x16& s, u32x4 k, u32x4 q) {
        f32x16 co;
#pragma unroll
        for (int e = 0; e < 4; ++e) { const f32x4 v = lld<f32x4>(c.L + L_MSK + e * 1024 + lane * 16); co[4 * e] = v[0]; co[4 * e + 1] = v[1]; co[4 * e + 2] = v[2]; co[4 * e + 3] = v[3]; }
        QK1C(s, k, q, co); };
    const u32 qoff = h == 0 ? pk(NEG_BIG, 0.f) : 0u;
    kb -= 8 * TILE_B; rb -= 8 * TILE_B; vb -= 8 * TILE_B;
#define B_TILE(U, SC, SN, FIRST) do { u32x2 qs = {qr[0], (U) + 1 <= qt ? qr[1] : qoff}; asm volatile("" : "+v"(qs)); \
        attn_step<((U) < 8)>(SC, SN, o, pp, km, kr, v0, v1, qm, qs, FIRST, \
            [&] { if ((U) + 2 <= 8) km = lld<u32x4>(kb + (8 - ((U) + 2)) * TILE_B); }, [&] { if ((U) + 2 <= 8) kr = lld<u32x2>(rb + (8 - ((U) + 2)) * TILE_B); }, \
            [&] { v1 = lld<u32x4>(vb + (8 - (U)) * TILE_B + 2 * KT_VBLK); }, [&] { if ((U) + 1 <= 8) v0 = lld<u32x4>(vb + (8 - ((U) + 1)) * TILE_B); }); } while (0)
    B_TILE(0, sA, sB, first); B_TILE(1, sB, sA, first);
    DRAIN();
    B_TILE(2, sA, sB, first);
    BAR();
    B_TILE(3, sB, sA, first); B_TILE(4, sA, sB, first); B_TILE(5, sB, sA, first); B_TILE(6, sA, sB, first); B_TILE(7, sB, sA, firstm); B_TILE(8, sA, sB, first);
#undef B_TILE
    attn_finish(o, pp, v1);
    STAMP(22 + rep * 30);
    if (attn_unsafe(o)) o = attn_slow(c, hb, qt - 8 < 0 ? 0 : qt - 8, qt, true, qm, qr);
    store_o(c, WS_OF + (rep * RP * 2 + 1) * XF_BYTES, o, a, t);
    publish(c, rep * 6 + 4);
    load_wimage_part<0, 44>(c, WS_WIMG + 8 * WIMG_BYTES, 1);
    if (rep == 0) load_wimage_part<0, 36>(c, WS_WIMGA, 1, L_A1);
}

__global__ void __launch_bounds__(512, 2) fused_block_kernel(Params p) {
    __shared__ __attribute__((aligned(16))) char lds_[LDS_BYTES];
    Ctx c;
    c.L = (LAS char*)lds_; c.Lb = (u32)(size_t)c.L;
    c.ws = p.ws;
    c.dgp = (u64*)(p.ws + WS_STAMP + (u32)blockIdx.x * 64);
    c.tid = threadIdx.x; c.lane = c.tid & 63; c.wave = __builtin_amdgcn_readfirstlane(c.tid >> 6);
    { const int wg = blockIdx.x, slot = wg >> 3; c.i = (wg & 7) * 4 + (slot >> 3); c.j = slot & 7; }
    c.rs = __builtin_amdgcn_make_buffer_rsrc((void*)p.ws, 0, WS_USED, 0x00020000);
    { const u64 b = (u64)p.ws; c.srd = u32x4{(u32)b, (u32)(b >> 32) & 0xffffu, WS_USED, 0x00020000u}; }
    STAMP(0);
    u32 warm;
    { u64 pc; u32 len;
      asm volatile("s_getpc_b64 %0\nfbk_code_first:\n\ts_mov_b32 %1, fbk_code_last-fbk_code_first" : "=s"(pc), "=s"(len));
      u32 off = ((u32)(blockIdx.x >> 3) * 8 + c.wave) * 256 + c.lane * 4;
      off = off < len ? off : 0u;
      warm = *(const GAS u32*)((pc & ~3ull) + off); }
    c.elo = 0; c.ehi = 0;
    u32 xres[3][2];
    STAMP(1);
    a_proj<0>(c, p);
    asm volatile("" :: "v"(warm));
    STAMP(5);
#pragma unroll 1
    for (int rep = 0; rep < 2; ++rep) {
        u32x4 w0[WRING], w1[WRING]; ATail st;
        a_attn(c, p, rep, st);
        if (rep == 0) {
            if (blockIdx.x == 0 && c.tid == 0) __hip_atomic_store((GAS u64*)(p.ws + WS_EPOCH), ((u64)c.ehi << 32) | c.elo, __ATOMIC_RELAXED, __HIP_MEMORY_SCOPE_AGENT);
        }
        a_tail(c, p, rep, st, xres, w0, w1);
        STAMP(14 + rep * 30);
        u32x4 qm; u32x2 qr;
        b_proj(c, p, rep, w0, w1, qm, qr);
        STAMP(19 + rep * 30);
        b_attn(c, p, rep, qm, qr);
        STAMP(24 + rep * 30);
        out_proj<true>(c, p, rep, xres, [](const Ctx&) {});
        STAMP(29 + rep * 30);
        if (rep == 1) break;
        STAMP(31);
        a_proj<1>(c, p);
        STAMP(35);
    }
    {
        DRAIN(); BAR();
        const u64 ta = __hip_atomic_load((GAS u64*)&c.dgp[0], __ATOMIC_RELAXED, __HIP_MEMORY_SCOPE_AGENT), tb = __hip_atomic_load((GAS u64*)&c.dgp[1], __ATOMIC_RELAXED, __HIP_MEMORY_SCOPE_AGENT);
        u64 span = tb - ta; if (span > 3000) span = 3000;
        const u64 t0 = __builtin_amdgcn_s_memrealtime();
        while (__builtin_amdgcn_s_memrealtime() - t0 < span * 0) { __builtin_amdgcn_s_sleep(1); }
    }
    asm volatile("fbk_code_last:");
}

static size_t ws_offset(size_t ws_size) { return (ws_size - WS_USED) & ~(size_t)0x1fffff; }

extern "C" void kernel_launch(void* const* d_in, const int* in_sizes, int n_in, void* d_out, int out_size, void* d_ws, size_t ws_size, hipStream_t stream) {
    Params p;
    p.x = (const u16*)d_in[0]; p.wq = (const u16*)d_in[1]; p.wk = (const u16*)d_in[2]; p.wv = (const u16*)d_in[3]; p.woA = (const u16*)d_in[4];
    p.wqkv = (const u16*)d_in[6]; p.woB = (const u16*)d_in[7]; p.out = (u16*)d_out;
    p.ws = (char*)d_ws + ws_offset(ws_size);
    fused_block_kernel<<<256, 512, 0, stream>>>(p);
}
```

```cpp
#include <hip/hip_runtime.h>
#include <hip/hip_bf16.h>

typedef unsigned short u16;
typedef unsigned u32;
typedef unsigned long long u64;
typedef __bf16 bf16x8 __attribute__((ext_vector_type(8)));
typedef __bf16 bf16x2 __attribute__((ext_vector_type(2)));
typedef short s16x4 __attribute__((ext_vector_type(4)));
typedef float f32x2 __attribute__((ext_vector_type(2)));
typedef float f32x4 __attribute__((ext_vector_type(4)));
typedef float f32x16 __attribute__((ext_vector_type(16)));
typedef u32 u32x2 __attribute__((ext_vector_type(2)));
typedef u32 u32x4 __attribute__((ext_vector_type(4)));
#define LAS __attribute__((address_space(3)))
#define GAS __attribute__((address_space(1)))
#define DI __device__ __forceinline__

constexpr int SEQ = 2048, DM = 576;
constexpr u32 TILE_B = 2560;
constexpr u32 KT_V = 1024, KT_VBLK = 304, KT_KREM = 2240;
constexpr u32 KA_KREM = 1024, KA_V0 = 1280, KA_V8 = 1792, KA_VX = 2304;
constexpr u32 XF_BYTES = 32u * 18 * 4 * 1024;
constexpr u32 WS_EPOCH = 0;
constexpr u32 WS_FLAG = 4096;
constexpr u32 WS_XF = WS_FLAG + 12u * 256 * 128 + 4096;
constexpr u32 WS_OF = WS_XF + 2 * XF_BYTES;
constexpr u32 ST_BYTES = 2048u * 16 * 8;
constexpr u32 WS_ST = WS_OF + 2 * XF_BYTES;
constexpr u32 KVA_BYTES = 64 * TILE_B;
constexpr u32 WS_KVA = WS_ST + 2 * ST_BYTES;
constexpr u32 KVB_BYTES = 32 * 64 * TILE_B;
constexpr u32 WS_KVB = WS_KVA + 2 * KVA_BYTES;
constexpr u32 WF_BYTES = 8u * 16 * 18 * 1024;
constexpr u32 WS_WF = WS_KVB + KVB_BYTES;
constexpr u32 WS_CSP = WS_WF + WF_BYTES;
constexpr u32 WIMG_BYTES = 90 * 1024;
constexpr u32 WS_WIMG = WS_CSP + 8u * 16 * 18 * 64;
constexpr u32 WS_WIMGA = WS_WIMG + 16 * WIMG_BYTES;
constexpr u32 WS_USED = WS_WIMGA + 8 * WIMG_BYTES;
constexpr int L_XCH = 0;
constexpr int L_RING = 0;
constexpr int CHUNK_B = 8 * TILE_B;
constexpr int L_STG = 40960;
constexpr int STG_P = 168;
constexpr int L_XIN = 0;
constexpr int L_XIN2 = 153600;
constexpr int L_FMT = 24576;
constexpr int L_KVB = 0;
constexpr int L_MSK = 102400;
constexpr int L_R1 = 61440;
constexpr int WP = 160;
constexpr int L_XB = 76800;
constexpr int L_A1 = 24576;
constexpr int L_STATP = 153600;
constexpr int L_MEAN = L_STATP + 1024;
constexpr int L_CSA = L_MEAN + 512;
constexpr int L_CSB = L_CSA + 1024;
constexpr int L_QREM = L_CSB + 1024;
constexpr int L_QT1 = L_QREM + 1024;
constexpr int LDS_BYTES = 163840;
static_assert(L_QT1 + 4096 <= LDS_BYTES, "lds");

constexpr int WRING = 4;
constexpr float QSCALE = 0.23570226039551584f * 1.4426950408889634f;
constexpr float NEG_BIG = -1e30f;
constexpr u32 SPIN_CAP = 1u << 20;
constexpr int AUX_SC1 = 16;

struct Params { const u16* x; const u16* wq; const u16* wk; const u16* wv; const u16* woA; const u16* wqkv; const u16* woB; u16* out; char* ws; };

DI u32 pk(float a, float b) { f32x2 v = {a, b}; return __builtin_bit_cast(u32, __builtin_convertvector(v, bf16x2)); }
DI float asf(u32 d) { return __builtin_bit_cast(float, d); }
DI u32 asu(float f) { return __builtin_bit_cast(u32, f); }
DI float bflo(u32 d) { return __builtin_bit_cast(float, d << 16); }
DI float bfhi(u32 d) { return __builtin_bit_cast(float, d & 0xffff0000u); }
DI bf16x8 asb(u32x4 v) { return __builtin_bit_cast(bf16x8, v); }
DI f32x16 mfma32(u32x4 a, u32x4 b, f32x16 c) { return __builtin_amdgcn_mfma_f32_32x32x16_bf16(asb(a), asb(b), c, 0, 0, 0); }
DI void MF16(f32x4& acc, u32x4 a, u32x4 b) { asm volatile("v_mfma_f32_16x16x32_bf16 %0, %1, %2, %0" : "+v"(acc) : "v"(a), "v"(b)); }
template <class T> DI T lld(LAS char* p) { return *(LAS const T*)p; }
template <class T> DI void lst(LAS char* p, T v) { *(LAS T*)p = v; }
DI u32x2 trr(LAS char* p) { return __builtin_bit_cast(u32x2, __builtin_amdgcn_ds_read_tr16_b64_v4i16((LAS s16x4*)p)); }
#define BAR() asm volatile("s_waitcnt lgkmcnt(0)\n\ts_barrier" ::: "memory")
#define DRAIN() asm volatile("s_waitcnt vmcnt(0)" ::: "memory")
typedef __amdgpu_buffer_rsrc_t rsrc_t;
DI u32x4 ld128(rsrc_t rs, u32 voff, u32 soff) { return __builtin_amdgcn_raw_buffer_load_b128(rs, voff, soff, AUX_SC1); }
DI u32x2 ld64(rsrc_t rs, u32 voff, u32 soff) { return __builtin_amdgcn_raw_buffer_load_b64(rs, voff, soff, AUX_SC1); }
DI u32 ld32(rsrc_t rs, u32 voff, u32 soff) { return __builtin_amdgcn_raw_buffer_load_b32(rs, voff, soff, AUX_SC1); }
DI void st128(rsrc_t rs, u32x4 v, u32 voff, u32 soff) { __builtin_amdgcn_raw_buffer_store_b128(v, rs, voff + soff, 0, AUX_SC1); }
DI void st64(rsrc_t rs, u32x2 v, u32 voff, u32 soff) { __builtin_amdgcn_raw_buffer_store_b64(v, rs, voff, soff, AUX_SC1); }
DI void st32(rsrc_t rs, u32 v, u32 voff, u32 soff) { __builtin_amdgcn_raw_buffer_store_b32(v, rs, voff, soff, AUX_SC1); }
DI void glds_ws(u32x4 srd, u32 voff, u32 soff, u32 ldsdst) {
    soff = __builtin_amdgcn_readfirstlane(soff); ldsdst = __builtin_amdgcn_readfirstlane(ldsdst);
    asm volatile("s_mov_b32 m0, %0\n\ts_nop 3\n\tbuffer_load_dwordx4 %1, %2, %3 offen sc1 lds" :: "s"(ldsdst), "v"(voff), "s"(srd), "s"(soff) : "memory");
}
DI void glds_in(const void* g, u32 ldsdst) {
    ldsdst = __builtin_amdgcn_readfirstlane(ldsdst);
    asm volatile("s_mov_b32 m0, %0\n\ts_nop 0\n\tglobal_load_lds_dwordx4 %1, off" :: "s"(ldsdst), "v"(g) : "memory");
}
DI void flag_load(u64& fl, u64 nomatch, u32 voff, u32x4 srd, u32 soff) {
    soff = __builtin_amdgcn_readfirstlane(soff);
    fl = nomatch;
    asm volatile("s_nop 4\n\tbuffer_load_dwordx2 %0, %1, %2, %3 offen sc1" : "+v"(fl) : "v"(voff), "s"(srd), "s"(soff) : "memory");
}
DI float sum_g(float v) {
    auto a = __builtin_amdgcn_permlane32_swap(__builtin_bit_cast(u32, v), __builtin_bit_cast(u32, v), false, false);
    v = __builtin_bit_cast(float, (u32)a[0]) + __builtin_bit_cast(float, (u32)a[1]);
    auto b = __builtin_amdgcn_permlane16_swap(__builtin_bit_cast(u32, v), __builtin_bit_cast(u32, v), false, false);
    return __builtin_bit_cast(float, (u32)b[0]) + __builtin_bit_cast(float, (u32)b[1]);
}
DI float max_h(float v) {
    auto a = __builtin_amdgcn_permlane32_swap(__builtin_bit_cast(u32, v), __builtin_bit_cast(u32, v), false, false);
    return fmaxf(__builtin_bit_cast(float, (u32)a[0]), __builtin_bit_cast(float, (u32)a[1]));
}
DI u32x4 ones4() { u32 o = 0x3f803f80u; asm volatile("" : "+v"(o)); return u32x4{o, o, o, o}; }
DI int crow(int reg, int h) { return (reg & 3) + 8 * (reg >> 2) + 4 * h; }

struct Ctx {
    LAS char* L; u32 Lb;
    char* ws; rsrc_t rs; u32x4 srd;
    int tid, lane, wave, i, j;
    u32 elo, ehi;
};

DI Ctx fresh(const Ctx& c0) {
    Ctx c = c0;
    asm volatile("" : "+v"(c.lane)); asm volatile("" : "+v"(c.tid));
    asm volatile("" : "+s"(c.wave)); asm volatile("" : "+s"(c.i)); asm volatile("" : "+s"(c.j));
    return c;
}
DI GAS u64* flag_ptr(const Ctx& c, int phase, int id) { return (GAS u64*)(c.ws + WS_FLAG + (u32)(phase * 256 + id) * 128); }
DI void signal(const Ctx& c, int phase) {
    if (c.tid == 0) __hip_atomic_store(flag_ptr(c, phase, c.i * 8 + c.j), ((u64)c.ehi << 32) | c.elo, __ATOMIC_RELAXED, __HIP_MEMORY_SCOPE_AGENT);
}
DI void publish(const Ctx& c, int phase) {
    DRAIN();
    BAR();
    signal(c, phase);
}
DI void poll(const Ctx& c, int phase, int id) {
    const u64 want = ((u64)c.ehi << 32) | c.elo;
    GAS u64* f = flag_ptr(c, phase, id < 0 ? 0 : id);
    for (u32 spins = 0; spins < SPIN_CAP; ++spins) {
        const u64 v = __hip_atomic_load(f, __ATOMIC_RELAXED, __HIP_MEMORY_SCOPE_AGENT);
        if (__all(id < 0 || v == want)) break;
        __builtin_amdgcn_s_sleep(1);
    }
    __builtin_amdgcn_fence(__ATOMIC_ACQUIRE, "wavefront");
}
DI void wait_group(const Ctx& c, int phase) {
    if (c.wave == 0) poll(c, phase, c.lane < 8 ? c.i * 8 + c.lane : -1);
    BAR();
}

DI void load_wimage_a(const Ctx& c, const Params& p) {
    const int jj = c.j, w = c.wave;
    const u16 *pk_ = p.wk, *pv_ = p.wv;
    asm volatile("" : "+s"(pk_), "+s"(pv_));
    const char* xsrc = (const char*)((jj >= 3 && jj < 6) ? pv_ : pk_); const int sub = jj < 6 ? jj % 3 : 0;
    xsrc += sub == 0 ? 0 : (sub == 1 ? 16 : 20);
    const char* msrc = (const char*)p.wq + jj * 144;
    const char* a[5]; u32 st[5];
#pragma unroll
    for (int k = 0; k < 5; ++k) {
        const u32 id = 64 * k + c.lane, row = (id * 6554u) >> 16, ch = id - row * 10;
        const bool ex = ch == 9;
        st[k] = ex ? 32 * 36 : 32 * 1152;
        a[k] = (ex ? xsrc : msrc) + (ex ? row * 36 : row * 1152 + ch * 16);
    }
#pragma unroll
    for (int n = 0; n < 3; ++n) {
        const int G = n < 2 ? (w & 3) + 8 * (w >> 2) + 4 * n : 16 + w;
        if (n < 2 || w < 2) {
#pragma unroll
            for (int k = 0; k < 5; ++k) glds_in(a[k] + (u32)G * st[k], c.Lb + L_R1 + (G * 5 + k) * 1024);
        }
    }
}
template <int P0, int P1>
DI void load_wimage_part(const Ctx& c, u32 base, int w0, int ldsbase = L_R1) {
    const u32 so = base + (u32)c.j * WIMG_BYTES; const int nw = 8 - w0;
    if (c.wave >= w0) {
#pragma unroll
        for (int n = 0; n < (P1 - P0 + 6) / 7; ++n) {
            const int pc = P0 + (c.wave - w0) + nw * n;
            if (pc < P1) glds_ws(c.srd, c.lane * 16, so + pc * 1024, c.Lb + ldsbase + pc * 1024);
        }
    }
}
DI void load_wimage_o4(const Ctx& c, int which, int part) {
    const u32 so = WS_WIMG + (u32)(which * 8 + c.j) * WIMG_BYTES + (u32)part * 30 * 1024; const u32 dst = c.Lb + L_R1 + part * 30 * 1024;
    if (c.wave < 4) {
#pragma unroll
        for (int n = 0; n < 8; ++n)
            if (n < 7 || c.wave < 2) glds_ws(c.srd, c.lane * 16, so + (c.wave + 4 * n) * 1024, dst + (c.wave + 4 * n) * 1024);
    }
}
struct Fmt { u32x4 v[2]; u32 g[8]; u32 dst[2], cdst[2]; int kind[2]; };
DI void fmt_issue(const Ctx& c, const Params& p, Fmt& m) {
    const u16 *pw = p.wqkv, *pa = p.woA, *pb = p.woB;
    asm volatile("" : "+s"(pw), "+s"(pa), "+s"(pb));
    const int lane = c.lane, rho = lane >> 1, g = rho >> 3, e = rho & 7;
#pragma unroll
    for (int r = 0; r < 2; ++r) {
        const int fr = blockIdx.x * 8 + c.wave + 2048 * r;
        int col, A, P, Q, pitch; const GAS char* src;
        if (fr < 2016) {
            const int jj = fr / 252, rem = fr - jj * 252, u = rem / 18, s = rem - u * 18;
            const int wv = u < 8 ? u : (u - 8 < 5 ? u - 8 : 6), slot = u < 8 ? 0 : 1, a = wv >> 1;
            const bool isrem = (wv & 1) && slot == 1;
            const u32 fo = (u32)(((jj * 8 + wv) * 2 + slot) * 18 + s);
            m.dst[r] = WS_WF + fo * 1024; m.cdst[r] = WS_CSP + fo * 64; m.kind[r] = isrem ? 3 : 1;
            P = s < 16 ? 16 : 4; Q = 2; pitch = 1728 * 2; src = (const GAS char*)pw;
            if (!isrem) {
                col = ((wv & 1) ? 1152 : (slot ? 576 : 0)) + 18 * (4 * jj + a) + 8 * (lane & 1);
                A = s < 16 ? 72 * (s >> 1) + 32 * (s & 1) + 4 * g : 72 * (4 * (s - 16) + g) + 64;
            } else {
                const int n = lane & 15, g2 = lane >> 4;
                col = (a == 0 ? (n < 8 ? 0 : 576) : 1152) + 18 * (4 * jj + ((n & 7) >> 1)) + 16 + (n & 1);
                A = s < 16 ? 72 * (s >> 1) + 32 * (s & 1) + 4 * g2 : 72 * (4 * (s - 16) + g2) + 64;
#pragma unroll
                for (int q = 0; q < 8; ++q) m.g[q] = *(const GAS u16*)(src + (A + P * (q >> 2) + (q & 3)) * pitch + col * 2);
            }
        } else {
            const bool on = fr < 3456;
            const int q = on ? fr - 2016 : 0, im = q / 720, rem = q - im * 720, jj = rem / 90, st = rem - jj * 90, s = st / 5, t = st - s * 5;
            m.dst[r] = WS_WIMG + (u32)(im * 8 + jj) * WIMG_BYTES + (u32)st * 1024; m.cdst[r] = 0; m.kind[r] = on ? 2 : 0;
            col = 72 * jj + 16 * t + (t == 4 ? 0 : 8 * (lane & 1));
            if (s < 16) { A = 36 * s + 18 * (g >> 1) + 4 * (g & 1); P = 8; Q = 2; } else { A = 72 * (4 * (s - 16) + g) + 16; P = 36; Q = 18; }
            src = (const GAS char*)(im ? pb : pa); pitch = 576 * 2;
        }
        if (r == 1 || m.kind[r] != 3) m.v[r] = *(const GAS u32x4*)(src + (A + P * (e >> 2) + Q * ((e >> 1) & 1) + (e & 1)) * pitch + col * 2);
    }
}
DI void fmt_finish(const Ctx& c, Fmt& m) {
    asm volatile("" : "+v"(m.v[0]), "+v"(m.v[1]), "+v"(m.g[0]), "+v"(m.g[1]), "+v"(m.g[2]), "+v"(m.g[3]), "+v"(m.g[4]), "+v"(m.g[5]), "+v"(m.g[6]), "+v"(m.g[7]));
    const int lane = c.lane, i16 = lane & 15, g = lane >> 4, q = i16 >> 2, p = i16 & 3;
#pragma unroll
    for (int r = 0; r < 2; ++r) {
        if (m.kind[r] != 0) {
            u32x4 f;
            if (m.kind[r] == 3) { const u32* w = m.g; f = u32x4{w[0] | (w[1] << 16), w[2] | (w[3] << 16), w[4] | (w[5] << 16), w[6] | (w[7] << 16)}; }
            else {
                LAS char* t = c.L + L_FMT + c.wave * 2048 + r * 1024;
                lst(t + lane * 16, m.v[r]);
                asm volatile("s_waitcnt lgkmcnt(0)" ::: "memory");
                const u32x2 lo = trr(t + (8 * g + q) * 32 + 8 * p), hi = trr(t + (8 * g + 4 + q) * 32 + 8 * p);
                f = u32x4{lo[0], lo[1], hi[0], hi[1]};
            }
            st128(c.rs, f, lane * 16, m.dst[r]);
            if (m.kind[r] != 2) {
                float cs = ((bflo(f[0]) + bfhi(f[0])) + (bflo(f[1]) + bfhi(f[1]))) + ((bflo(f[2]) + bfhi(f[2])) + (bflo(f[3]) + bfhi(f[3])));
                cs = sum_g(cs);
                if (lane < 16) st32(c.rs, asu(cs), lane * 4, m.cdst[r]);
            }
        }
    }
}

template <bool PROJ, bool NAT = false, class HK, class PRE>
DI void g1_mma(const Ctx& c, const u32x4 (&xf)[9], f32x4 (&acc)[5], HK hook, PRE pre) {
    const int lane = c.lane, kh = c.wave >> 2, q = (lane & 15) >> 2, p = lane & 3, g = lane >> 4;
    LAS char *bN, *bR, *bNo, *bNt, *bRo, *bRt;
    if (PROJ) {
        if (NAT) { bN = c.L + L_R1 + (kh * 256 + 4 * g + q) * WP + 8 * p; bR = c.L + L_R1 + (512 + 32 * kh + 8 * g + q) * WP + 8 * p; }
        else { bN = c.L + L_A1 + (kh * 288 + 4 * g + q) * WP + 8 * p; bR = c.L + L_A1 + (kh * 288 + 72 * g + q) * WP + 8 * p; }
        bNo = bN + 64 * kh; bNt = bN + 64 - 64 * kh; bRo = bR + 64 * kh; bRt = bR + 64 - 64 * kh;
    } else {
        bN = c.L + L_R1 + kh * 8 * 5120 + lane * 16; bR = c.L + L_R1 + (16 + kh) * 5120 + lane * 16;
        bNo = bN + 2048 * kh; bNt = bN + 2048 - 2048 * kh; bRo = bR + 2048 * kh; bRt = bR + 2048 - 2048 * kh;
    }
    u32x4 w[2][5];
    auto rd = [&](int sl, u32x4 (&d)[5]) {
#pragma unroll
        for (int u = 0; u < 5; ++u) {
            if (PROJ) {
                const int r0 = NAT ? (sl < 8 ? 32 * sl : 0) : (sl < 8 ? 72 * (sl >> 1) + 32 * (sl & 1) : 64), hs = sl < 8 ? 16 : 4;
                LAS char* bb = u < 2 ? (sl < 8 ? bNo : bRo) + 32 * u : (u == 2 ? (sl < 8 ? bN : bR) + 128 : (sl < 8 ? bNt : bRt) + 32 * (u - 3));
                const u32x2 lo = trr(bb + r0 * WP), hi = trr(bb + (r0 + hs) * WP);
                d[u] = u32x4{lo[0], lo[1], hi[0], hi[1]};
            } else {
                LAS char* bb = u < 2 ? (sl < 8 ? bNo : bRo) + 1024 * u : (u == 2 ? (sl < 8 ? bN : bR) + 4096 : (sl < 8 ? bNt : bRt) + 1024 * (u - 3));
                d[u] = lld<u32x4>(bb + (sl < 8 ? sl * 5120 : 0));
            }
        }
    };
    rd(0, w[0]);
#pragma unroll
    for (int u = 0; u < 5; ++u) asm volatile("" : "+v"(acc[u]));
#pragma unroll
    for (int sl = 0; sl < 9; ++sl) {
        pre(sl);
        if (sl + 1 < 9) rd(sl + 1, w[(sl + 1) & 1]);
#pragma unroll
        for (int u = 0; u < 5; ++u) MF16(acc[u], w[sl & 1][u], xf[sl]);
        hook(sl, w[sl & 1]);
    }
    asm volatile("s_nop 7" : "+v"(acc[0]), "+v"(acc[1]), "+v"(acc[2]), "+v"(acc[3]), "+v"(acc[4]));
}
DI void g1_exchange(const Ctx& c, f32x4 (&acc)[5]) {
    LAS char* xw = c.L + L_XCH + c.wave * 3072 + c.lane * 16;
    lst(xw, acc[3]); lst(xw + 1024, acc[4]); lst(xw + 2048, acc[2]);
    BAR();
    LAS char* xr = c.L + L_XCH + (c.wave ^ 4) * 3072 + c.lane * 16;
    acc[0] += lld<f32x4>(xr); acc[1] += lld<f32x4>(xr + 1024); acc[2] += lld<f32x4>(xr + 2048);
}
DI void load_xfrags(const Ctx& c, u32 buf, u32x4 (&xf)[9]) {
    const int m = c.wave & 3, kh = c.wave >> 2;
    const u32 so = buf + (u32)(c.i * 18 * 4 + m) * 1024;
#pragma unroll
    for (int sl = 0; sl < 9; ++sl) { const int s = sl < 8 ? 8 * kh + sl : 16 + kh; xf[sl] = ld128(c.rs, c.lane * 16, so + (u32)s * 4096); }
}
DI u32x4 stats_issue(const Ctx& c, u32 stbuf) {
    const u32 vo = (u32)((c.i * 16 + 2 * (c.lane >> 3)) * 64 + c.wave * 8 + (c.lane & 7)) * 8;
    const u32x2 a = ld64(c.rs, vo, stbuf), b = ld64(c.rs, vo + 512, stbuf);
    return u32x4{a[0], a[1], b[0], b[1]};
}
DI void stats_finish(const Ctx& c, u32x4 v) {
    const int tok = c.wave * 8 + (c.lane & 7);
    float s = asf(v[0]) + asf(v[2]), s2 = asf(v[1]) + asf(v[3]);
    s += __shfl_xor(s, 8); s2 += __shfl_xor(s2, 8);
    s = sum_g(s); s2 = sum_g(s2);
    if (c.lane < 8) {
        lst(c.L + L_STATP + tok * 8, f32x2{s, s2}); lst(c.L + L_STATP + 512 + tok * 8, f32x2{0.f, 0.f});
        const float mean = s * (1.f / DM), var = s2 * (1.f / DM) - mean * mean;
        lst(c.L + L_MEAN + tok * 4, mean); lst(c.L + L_MEAN + 256 + tok * 4, __builtin_amdgcn_rsqf(fmaxf(var, 0.f) + 1e-5f));
    }
}
DI void stats_issue32(const Ctx& c, u32 stbuf, u32x4 (&v)[4]) {
    const u32 vo = (u32)((c.i * 16 + (c.lane >> 4)) * 64 + (c.wave == 7 ? 32 : 0) + 2 * (c.lane & 15)) * 8;
#pragma unroll
    for (int n = 0; n < 4; ++n) v[n] = ld128(c.rs, vo + n * 4 * 512, stbuf);
}
DI void stats_finish32(const Ctx& c, const u32x4 (&v)[4]) {
    float s[4];
#pragma unroll
    for (int e = 0; e < 4; ++e) s[e] = sum_g((asf(v[0][e]) + asf(v[1][e])) + (asf(v[2][e]) + asf(v[3][e])));
    if (c.lane < 16) {
        const int tok = (c.wave == 7 ? 32 : 0) + 2 * c.lane;
        const float m0 = s[0] * (1.f / DM), m1 = s[2] * (1.f / DM);
        lst(c.L + L_MEAN + tok * 4, f32x2{m0, m1});
        lst(c.L + L_MEAN + 256 + tok * 4, f32x2{__builtin_amdgcn_rsqf(fmaxf(s[1] * (1.f / DM) - m0 * m0, 0.f) + 1e-5f), __builtin_amdgcn_rsqf(fmaxf(s[3] * (1.f / DM) - m1 * m1, 0.f) + 1e-5f)});
    }
}

struct KV { u32x4 km; u32x2 kr; u32x4 v0, v1; };
DI int vrow_of(int r) { return r < 18 ? r : 18; }
DI u32 ka_vrow(int blk, int row) { return row < 8 ? KA_V0 + blk * 128 + row * 16 : (row < 16 ? KA_V8 + blk * 128 + (row - 8) * 16 : KA_VX + blk * 64 + (row - 16) * 16); }
DI KV load_tile_ws(const Ctx& c, u32 tile, bool shared) {
    const int r = c.lane & 31, h = c.lane >> 5;
    KV t;
    if (shared) {
        t.km = ld128(c.rs, c.lane * 16, tile);
        t.kr = ld64(c.rs, KA_KREM + r * 8, tile);
        t.v0 = ld128(c.rs, ka_vrow(h, vrow_of(r)), tile);
        t.v1 = ld128(c.rs, ka_vrow(2 + h, vrow_of(r)), tile);
        return t;
    }
    t.km = ld128(c.rs, c.lane * 16, tile);
    t.kr = ld64(c.rs, KT_KREM + r * 8, tile);
    t.v0 = ld128(c.rs, KT_V + h * KT_VBLK + vrow_of(r) * 16, tile);
    t.v1 = ld128(c.rs, KT_V + (2 + h) * KT_VBLK + vrow_of(r) * 16, tile);
    return t;
}
DI f32x16 mfma32k8(u32x2 a, u32x2 b, f32x16 c) { return __builtin_amdgcn_mfma_f32_32x32x8bf16_1k(__builtin_bit_cast(s16x4, a), __builtin_bit_cast(s16x4, b), c, 0, 0, 0); }
DI f32x16 qk(const KV& t, u32x4 qm, u32x2 qr, f32x16 c0) {
    return mfma32k8(t.kr, qr, mfma32(t.km, qm, c0));
}
DI void QK1(f32x16& s, u32x4 km, u32x4 qm) { asm volatile("v_mfma_f32_32x32x16_bf16 %0, %1, %2, 0" : "=&v"(s) : "v"(km), "v"(qm)); }
DI void QK1C(f32x16& s, u32x4 km, u32x4 qm, const f32x16& c0) { asm volatile("v_mfma_f32_32x32x16_bf16 %0, %1, %2, %3" : "=&v"(s) : "v"(km), "v"(qm), "v"(c0)); }
DI void QK2(f32x16& s, u32x2 kr, u32x2 qr) { asm volatile("v_mfma_f32_32x32x8_bf16 %0, %1, %2, %0" : "+v"(s) : "v"(kr), "v"(qr)); }
DI void PV(f32x16& o, u32x4 v, u32x4 p) { asm volatile("v_mfma_f32_32x32x16_bf16 %0, %1, %2, %0" : "+v"(o) : "v"(v), "v"(p)); }
DI float EX(float x) { float r; asm volatile("v_exp_f32 %0, %1" : "=v"(r) : "v"(x)); return r; }
DI u32 CV(float a, float b) { u32 r; asm volatile("v_cvt_pk_bf16_f32 %0, %1, %2" : "=v"(r) : "v"(a), "v"(b)); return r; }
struct Pipe { float e[4]; u32x4 p1; };
template <bool NEXT = true, class F0, class F1, class F2, class F3, class F4>
DI void attn_step(const f32x16& sc, f32x16& sn, f32x16& o, Pipe& pp, const u32x4& km, const u32x2& kr, const u32x4& v0, const u32x4& v1, u32x4 qm, u32x2 qr, F0 first, F1 LK, F2 LR, F3 LV1, F4 LV0) {
    u32x4 p0; float a0, a1, a2, a3, b0, b1, b2, b3;
    if (NEXT) first(sn, km, qm);
    LK();
    pp.p1[2] = CV(pp.e[0], pp.e[1]); pp.p1[3] = CV(pp.e[2], pp.e[3]);
    a0 = EX(sc[0]); a1 = EX(sc[1]); a2 = EX(sc[2]); a3 = EX(sc[3]);
    if (NEXT) QK2(sn, kr, qr); else asm volatile("s_nop 0");
    LR();
    p0[0] = CV(a0, a1); p0[1] = CV(a2, a3);
    b0 = EX(sc[4]); b1 = EX(sc[5]); b2 = EX(sc[6]); b3 = EX(sc[7]);
    PV(o, v1, pp.p1); LV1();
    p0[2] = CV(b0, b1); p0[3] = CV(b2, b3);
    a0 = EX(sc[8]); a1 = EX(sc[9]); a2 = EX(sc[10]); a3 = EX(sc[11]);
    PV(o, v0, p0); LV0();
    pp.p1[0] = CV(a0, a1); pp.p1[1] = CV(a2, a3);
    pp.e[0] = EX(sc[12]); pp.e[1] = EX(sc[13]); pp.e[2] = EX(sc[14]); pp.e[3] = EX(sc[15]);
}
template <class F1, class F2, class F3, class F4>
DI void attn_step2(const f32x16& scx, f32x16& snx, f32x16& ox, Pipe& px, const f32x16& scy, f32x16& sny, f32x16& oy, Pipe& py,
                   const u32x4& km, const u32x2& kr, const u32x4& v0, const u32x4& v1, u32x4 qmx, u32x2 qrx, u32x4 qmy, u32x2 qry, F1 LK, F2 LR, F3 LV1, F4 LV0) {
    u32x4 p0x, p0y; float a0, a1, a2, a3, b0, b1, b2, b3, c0, c1, c2, c3, d0, d1, d2, d3;
#define GRP(MF, PD0, PD1, E0, E1, E2, E3, D0, D1, D2, D3, SRC, I) MF; PD0 = CV(E0, E1); PD1 = CV(E2, E3); D0 = EX(SRC[I]); D1 = EX(SRC[I + 1]); D2 = EX(SRC[I + 2]); D3 = EX(SRC[I + 3])
    GRP(QK1(snx, km, qmx), px.p1[2], px.p1[3], px.e[0], px.e[1], px.e[2], px.e[3], a0, a1, a2, a3, scx, 0);
    GRP(QK1(sny, km, qmy); LK(), py.p1[2], py.p1[3], py.e[0], py.e[1], py.e[2], py.e[3], b0, b1, b2, b3, scy, 0);
    GRP(QK2(snx, kr, qrx), p0x[0], p0x[1], a0, a1, a2, a3, c0, c1, c2, c3, scx, 4);
    GRP(QK2(sny, kr, qry); LR(), p0y[0], p0y[1], b0, b1, b2, b3, d0, d1, d2, d3, scy, 4);
    GRP(PV(ox, v1, px.p1), p0x[2], p0x[3], c0, c1, c2, c3, a0, a1, a2, a3, scx, 8);
    GRP(PV(oy, v1, py.p1); LV1(), p0y[2], p0y[3], d0, d1, d2, d3, b0, b1, b2, b3, scy, 8);
    GRP(PV(ox, v0, p0x), px.p1[0], px.p1[1], a0, a1, a2, a3, px.e[0], px.e[1], px.e[2], px.e[3], scx, 12);
    GRP(PV(oy, v0, p0y); LV0(), py.p1[0], py.p1[1], b0, b1, b2, b3, py.e[0], py.e[1], py.e[2], py.e[3], scy, 12);
#undef GRP
}
DI void attn_finish(f32x16& o, Pipe& pp, u32x4 v1) {
    asm volatile("s_nop 0");
    pp.p1[2] = CV(pp.e[0], pp.e[1]); pp.p1[3] = CV(pp.e[2], pp.e[3]);
    asm volatile("s_nop 1");
    PV(o, v1, pp.p1);
    asm volatile("s_nop 7\n\ts_nop 3" : "+v"(o));
}
DI void attn_finish2(f32x16& ox, Pipe& px, f32x16& oy, Pipe& py, u32x4 v1) {
    asm volatile("s_nop 0");
    px.p1[2] = CV(px.e[0], px.e[1]); px.p1[3] = CV(px.e[2], px.e[3]);
    py.p1[2] = CV(py.e[0], py.e[1]); py.p1[3] = CV(py.e[2], py.e[3]);
    PV(ox, v1, px.p1);
    asm volatile("s_nop 0");
    PV(oy, v1, py.p1);
    asm volatile("s_nop 7\n\ts_nop 3" : "+v"(ox), "+v"(oy));
}
DI void pexp(const f32x16& s, u32x4& p0, u32x4& p1) {
    float e[16];
#pragma unroll
    for (int r = 0; r < 16; ++r) e[r] = __builtin_amdgcn_exp2f(s[r]);
    p0 = u32x4{pk(e[0], e[1]), pk(e[2], e[3]), pk(e[4], e[5]), pk(e[6], e[7])};
    p1 = u32x4{pk(e[8], e[9]), pk(e[10], e[11]), pk(e[12], e[13]), pk(e[14], e[15])};
}
DI float rowmax(const f32x16& s) {
    float a = fmaxf(fmaxf(s[0], s[1]), s[2]), b = fmaxf(fmaxf(s[3], s[4]), s[5]);
#pragma unroll
    for (int r = 6; r < 16; r += 2) { if (r & 2) a = fmaxf(fmaxf(a, s[r]), s[r + 1]); else b = fmaxf(fmaxf(b, s[r]), s[r + 1]); }
    return max_h(fmaxf(a, b));
}
DI f32x16 mask_c(int lane, bool diag) {
    const int r = lane & 31, h = lane >> 5; f32x16 m;
#pragma unroll
    for (int e = 0; e < 16; ++e) { const bool ok = diag ? (crow(e, h) <= r) : (crow(e, h) > r); m[e] = ok ? 0.f : NEG_BIG; }
    return m;
}
DI f32x16 attn_slow(const Ctx& c, u32 tile0, int n0, int n1, bool masked, u32x4 qm, u32x2 qr) {
    const int h = c.lane >> 5;
    qr[1] = 0u;
    f32x16 o = {}; float m = NEG_BIG;
    for (int n = n0; n <= n1; ++n) {
        const KV t = load_tile_ws(c, tile0 + (u32)n * TILE_B, !masked);
        f32x16 z = {};
        f32x16 s = qk(t, qm, qr, z);
        if (masked && (n == n1 || n == n1 - 8)) {
            const int r = c.lane & 31;
#pragma unroll
            for (int e = 0; e < 16; ++e) { const bool ok = (n == n1) ? (crow(e, h) <= r) : (crow(e, h) > r); s[e] = ok ? s[e] : NEG_BIG; }
        }
        const float mn = fmaxf(m, rowmax(s)), al = __builtin_amdgcn_exp2f(m - mn);
        m = mn;
#pragma unroll
        for (int e = 0; e < 16; ++e) { o[e] *= al; s[e] = s[e] <= NEG_BIG ? NEG_BIG : s[e] - mn; }
        u32x4 p0, p1; pexp(s, p0, p1);
        o = mfma32(t.v0, p0, o); o = mfma32(t.v1, p1, o);
    }
    return o;
}
DI bool attn_unsafe(const f32x16& o) {
    float z = 0.f;
#pragma unroll
    for (int e = 0; e < 10; ++e) z = __builtin_fmaf(o[e], 0.f, z);
    const float l = o[10];
    return __any(!(l > 0x1p-40f && l < 0x1p30f && z == 0.f));
}
DI void store_o(const Ctx& c, u32 obuf, const f32x16& o, int a, int t) {
    const int r = c.lane & 31, h = c.lane >> 5;
    const float inv = __builtin_amdgcn_rcpf(o[10]);
    const u32x4 mainp = {pk(o[0] * inv, o[1] * inv), pk(o[2] * inv, o[3] * inv), pk(o[4] * inv, o[5] * inv), pk(o[6] * inv, o[7] * inv)};
    const int m = 2 * t + (r >> 4), tok = r & 15;
    const u32 so = obuf + (u32)(c.i * 18 * 4) * 1024;
    st128(c.rs, mainp, (u32)((2 * c.j + (a >> 1)) * 4 + m) * 1024 + ((2 * (a & 1) + h) * 16 + tok) * 16, so);
    if (h == 0) st32(c.rs, pk(o[8] * inv, o[9] * inv), (u32)((16 + (c.j >> 2)) * 4 + m) * 1024 + ((c.j & 3) * 16 + tok) * 16 + 4 * a, so);
}

template <int rep>
DI void a_proj(Ctx& c0, const Params& p) {
    Ctx c = fresh(c0);
    const int lane = c.lane, m = c.wave & 3, kh = c.wave >> 2, tok = lane & 15, g = lane >> 4;
    u32x4 xf[9]; f32x4 acc[5] = {};
    f32x4 s1 = {}, s2 = {}, ca = {}, cb = {};
    Fmt fm; u64 e0 = 0; u32x4 stv;
    if (rep == 0) {
        e0 = __hip_atomic_load((GAS u64*)(p.ws + WS_EPOCH), __ATOMIC_RELAXED, __HIP_MEMORY_SCOPE_AGENT);
        const char* xt = (const char*)p.x + (size_t)(c.i * 64) * (DM * 2);
#pragma unroll
        for (int n = 0; n < 8; ++n) {
            const int row = c.wave * 8 + n;
            glds_in(xt + row * (DM * 2) + (lane ^ (row & 15)) * 16, c.Lb + (row < 60 ? L_XIN + row * 1024 : L_XIN2 + (row - 60) * 1024));
        }
        xf[8] = *(const u32x4*)(xt + (m * 16 + tok) * (DM * 2) + (512 + 32 * kh + 8 * g) * 2);
        load_wimage_a(c, p);
        fmt_issue(c, p, fm);
        if (c.wave < 2) { if (fm.kind[0] == 3) asm volatile("s_waitcnt vmcnt(19)" ::: "memory"); else asm volatile("s_waitcnt vmcnt(12)" ::: "memory"); }
        else { if (fm.kind[0] == 3) asm volatile("s_waitcnt vmcnt(14)" ::: "memory"); else asm volatile("s_waitcnt vmcnt(7)" ::: "memory"); }
        BAR();
        asm volatile("" : "+v"(xf[8]), "+v"(e0));
        { const int row = m * 16 + tok, t2 = tok ^ (g >> 1);
          LAS char* xb = c.L + (row < 60 ? L_XIN + row * 1024 : L_XIN2 + (row - 60) * 1024) + kh * 512 + 8 * (g & 1);
#pragma unroll
          for (int sl = 0; sl < 8; ++sl) {
              const int K = 4 * (sl & 3);
              const u32x2 lo = lld<u32x2>(xb + (sl >> 2) * 256 + ((K ^ t2) * 16)), hi = lld<u32x2>(xb + (sl >> 2) * 256 + (((K + 2) ^ t2) * 16));
              xf[sl] = u32x4{lo[0], lo[1], hi[0], hi[1]};
          } }
        asm volatile("s_nop 1" : "+v"(xf[0]), "+v"(xf[1]), "+v"(xf[2]), "+v"(xf[3]), "+v"(xf[4]), "+v"(xf[5]), "+v"(xf[6]), "+v"(xf[7]), "+v"(xf[8]));
    } else {
        wait_group(c, 5);
        load_xfrags(c, WS_XF + 1 * XF_BYTES, xf);
        stv = stats_issue(c, WS_ST + 1 * ST_BYTES);
        asm volatile("s_waitcnt vmcnt(11)" ::: "memory"); BAR();
    }
    if (rep != 0) { g1_mma<true>(c, xf, acc, [](int, const u32x4 (&)[5]) {}, [](int) {}); stats_finish(c, stv); }
    else {
        u32x4 ones = ones4();
        const int tlc = m < 2 ? 2 * kh + m : 2 * (1 - kh) + (m - 2), q = (lane & 15) >> 2, pp = lane & 3;
        LAS char* bN = c.L + L_R1 + (kh * 256 + 4 * g + q) * WP + 8 * pp + 32 * tlc;
        LAS char* bR = c.L + L_R1 + (512 + 32 * kh + 8 * g + q) * WP + 8 * pp + 32 * tlc;
        asm volatile("" : "+v"(s1), "+v"(s2), "+v"(ca), "+v"(cb), "+v"(ones));
        u32x4 wx[2];
        auto rdx = [&](int sl) {
            const int r0 = sl < 8 ? 32 * sl : 0, hs = sl < 8 ? 16 : 4; LAS char* b = sl < 8 ? bN : bR;
            const u32x2 lo = trr(b + r0 * WP), hi = trr(b + (r0 + hs) * WP);
            wx[sl & 1] = u32x4{lo[0], lo[1], hi[0], hi[1]}; };
        rdx(0);
        g1_mma<true, true>(c, xf, acc, [&](int sl, const u32x4 (&w)[5]) {
            if (sl + 1 < 9) rdx(sl + 1);
            MF16(s1, ones, xf[sl]); MF16(s2, xf[sl], xf[sl]); MF16(cb, w[2], ones); MF16(ca, wx[sl & 1], ones);
        }, [&](int sl) {
            if (sl == 3) {
                if (fm.kind[0] == 3) asm volatile("s_waitcnt vmcnt(9)" ::: "memory"); else asm volatile("s_waitcnt vmcnt(2)" ::: "memory");
                BAR();
            }
        });
        if (c.tid < 180) st128(c.rs, lld<u32x4>(c.L + L_R1 + c.i * 2880 + c.tid * 16), c.tid * 16, WS_WIMGA + (u32)c.j * WIMG_BYTES + (u32)c.i * 2880);
        asm volatile("s_nop 7" : "+v"(s1), "+v"(s2), "+v"(ca), "+v"(cb));
        const int rg = tok & 3;
        float d = rg == 0 ? s2[0] : (rg == 1 ? s2[1] : (rg == 2 ? s2[2] : s2[3]));
        d = sum_g(g == (tok >> 2) ? d : 0.f);
        if (g == 0) lst(c.L + L_STATP + kh * 512 + (m * 16 + tok) * 8, f32x2{s1[0], d});
        const int tl = m < 2 ? 2 * kh + m : 2 * (1 - kh) + (m - 2);
        if (tok == 0) { lst(c.L + L_CSA + (kh * 5 + tl) * 64 + g * 16, ca); if (m == 0) lst(c.L + L_CSA + (kh * 5 + 4) * 64 + g * 16, cb); }
    }
    g1_exchange(c, acc);
    const f32x2 sa = lld<f32x2>(c.L + L_STATP + (m * 16 + tok) * 8), sb = lld<f32x2>(c.L + L_STATP + 512 + (m * 16 + tok) * 8);
    const float mean = (sa[0] + sb[0]) * (1.f / DM), var = (sa[1] + sb[1]) * (1.f / DM) - mean * mean;
    const float rstd = __builtin_amdgcn_rsqf(fmaxf(var, 0.f) + 1e-5f);
    LAS char* stg = c.L + L_STG + (m * 16 + tok) * STG_P + g * 8;
    auto fin = [&](int u, int t, float sc) {
        const f32x4 cs = lld<f32x4>(c.L + L_CSA + t * 64 + g * 16) + lld<f32x4>(c.L + L_CSA + (5 + t) * 64 + g * 16);
        f32x4 v;
#pragma unroll
        for (int e = 0; e < 4; ++e) v[e] = (acc[u][e] - mean * cs[e]) * sc;
        return u32x2{pk(v[0], v[1]), pk(v[2], v[3])};
    };
    if (kh == 1) {
        const u32x2 d4 = fin(2, 4, g < 2 ? rstd * QSCALE : rstd);
        lst(stg + 128, d4);
        const u32 kva = WS_KVA + rep * KVA_BYTES + (u32)(2 * c.i + (m >> 1)) * TILE_B;
        const int j = c.j, key = (m & 1) * 16 + tok;
        if (j < 2) { if (g >= 2) st64(c.rs, d4, j * 512 + key * 16 + (g - 2) * 8, kva); }
        else if (j == 2) { if (g == 3) st64(c.rs, u32x2{d4[1], 0x00003f80u}, KA_KREM + key * 8, kva); }
        else if (j < 6) {
            asm volatile("s_waitcnt lgkmcnt(0)" ::: "memory");
            const int q = (lane & 15) >> 2, pp = lane & 3;
            LAS char* s = c.L + L_STG + (16 * m + q + 4 * (pp >> 1)) * STG_P + 144 + 8 * (pp & 1);
            const u32x2 lo = trr(s), hi = trr(s + 8 * STG_P);
            const int dl = lane & 7, blk = (m & 1) * 2 + ((lane >> 3) & 1);
            if (j < 5) { if (lane < 16) st128(c.rs, u32x4{lo[0], lo[1], hi[0], hi[1]}, (j == 3 ? KA_V0 : KA_V8) + blk * 128 + dl * 16, kva); }
            else {
                const u32x4 on = ones4();
                const u32x4 v = dl >= 6 ? u32x4{lo[0], lo[1], hi[0], hi[1]} : on;
                if (lane < 16 && (dl >= 6 || dl < 2)) st128(c.rs, v, KA_VX + blk * 64 + (dl >= 6 ? dl - 6 : 2 + dl) * 16, kva);
            }
        }
    }
    lst(stg + (2 * kh) * 32, fin(0, 2 * kh, rstd * QSCALE));
    lst(stg + (2 * kh + 1) * 32, fin(1, 2 * kh + 1, rstd * QSCALE));
    if (rep == 0) {
        fmt_finish(c, fm);
        const u64 e = e0 + 1; c0.elo = c.elo = __builtin_amdgcn_readfirstlane((u32)e); c0.ehi = c.ehi = __builtin_amdgcn_readfirstlane((u32)(e >> 32));
    }
    publish(c, rep * 6);
}

DI void load_wring(const Ctx& c, u32x4 (&w0)[WRING], u32x4 (&w1)[WRING]) {
    const u32 wso = WS_WF + (u32)((c.j * 8 + c.wave) * 2) * 18 * 1024;
#pragma unroll
    for (int s = 0; s < WRING; ++s) { w0[s] = ld128(c.rs, c.lane * 16, wso + s * 1024); w1[s] = ld128(c.rs, c.lane * 16, wso + (18 + s) * 1024); }
}
DI void colsums_b(const Ctx& c) {
    const int tok = c.lane & 15, g = c.lane >> 4;
    if (c.wave >= 1) {
        const int extra = c.wave == 1 ? 0 : c.wave;
        u32 v[4][5];
#pragma unroll
        for (int q = 0; q < 4; ++q) {
            const int unit = (q < 2 ? c.wave : extra) * 2 + (q & 1);
#pragma unroll
            for (int u = 0; u < 5; ++u) { const int s = g + 4 * u; v[q][u] = (q < 2 || c.wave == 1) ? ld32(c.rs, (s < 18 ? s : 0) * 64 + tok * 4, WS_CSP + (u32)((c.j * 16 + unit) * 18) * 64) : 0u; }
        }
#pragma unroll
        for (int q = 0; q < 4; ++q) {
            const int unit = (q < 2 ? c.wave : extra) * 2 + (q & 1);
            float cs = (asf(v[q][0]) + asf(v[q][1])) + (asf(v[q][2]) + asf(v[q][3])) + (g < 2 ? asf(v[q][4]) : 0.f);
            cs = sum_g(cs);
            if (g == 0 && (q < 2 || c.wave == 1)) lst(c.L + L_CSB + unit * 64 + tok * 4, cs);
        }
    }
}
struct ATail { f32x16 o; u32x4 qm; u32x2 qr; };
DI void a_attn(const Ctx& c0, const Params& p, int rep, ATail& st) {
    const Ctx c = fresh(c0);
    const int lane = c.lane, r = lane & 31, h = lane >> 5, a = c.wave & 3, kh = c.wave >> 2;
    const int ph = rep * 6;
    const u32 kva = WS_KVA + rep * KVA_BYTES;
    u32x4 qmx, qmy; u32x2 qrx, qry;
    { LAS char* s = c.L + L_STG + (32 * kh + r) * STG_P + 36 * a;
      qmx = u32x4{lld<u32>(s + 16 * h), lld<u32>(s + 16 * h + 4), lld<u32>(s + 16 * h + 8), lld<u32>(s + 16 * h + 12)};
      const u32 rx = lld<u32>(s + 32);
      s += (32 - 64 * kh) * STG_P;
      qmy = u32x4{lld<u32>(s + 16 * h), lld<u32>(s + 16 * h + 4), lld<u32>(s + 16 * h + 8), lld<u32>(s + 16 * h + 12)};
      const u32 ry = lld<u32>(s + 32);
      qrx = u32x2{h == 0 ? rx : 0u, 0u}; qry = u32x2{h == 0 ? ry : 0u, 0u}; }
    const int ch0 = c.i >> 2;
    auto chunk_ids = [&](int n) { const int ch = (ch0 + n) & 7; return ch * 32 + (lane & 31); };
    u32 pvo[5];
#pragma unroll
    for (int k = 0; k < 5; ++k) {
        const u32 S = (u32)((c.wave & 3) + 4 * k) * 64 + lane, tile = (S * 52429u) >> 23, ps = S - tile * 160;
        const u32 v = ps - 64, blk = (v * 3450u) >> 16, row = v - blk * 19;
        const u32 src = ps < 64 ? ps * 16 : (ps < 140 ? ka_vrow(blk, row) : (ps < 156 ? KA_KREM + (ps - 140) * 16 : ps * 16));
        pvo[k] = tile * TILE_B + src;
    }
    auto issue_piece = [&](int n, int k) {
        const int ch = (ch0 + n) & 7; const u32 so = kva + (u32)ch * CHUNK_B; const u32 dst = c.Lb + L_RING + (n % 3) * CHUNK_B;
        glds_ws(c.srd, pvo[k], so, dst + ((c.wave & 3) + 4 * k) * 1024);
    };
    auto issue_chunk8 = [&](int n) {
        if (c.wave < 4) { issue_piece(n, 0); issue_piece(n, 2); issue_piece(n, 4); } else { issue_piece(n, 1); issue_piece(n, 3); }
    };
    if (c.wave == 0) poll(c, ph, lane < 32 ? chunk_ids(0) : chunk_ids(1));
    BAR();
    issue_chunk8(0);
    if (c.wave < 4) { issue_piece(1, 0); issue_piece(1, 1); issue_piece(1, 2); issue_piece(1, 3); issue_piece(1, 4); asm volatile("s_waitcnt vmcnt(5)" ::: "memory"); }
    else DRAIN();
    BAR();
    const int vr = vrow_of(r);
    LAS char* kb = c.L + L_RING + lane * 16;
    LAS char* rb = c.L + L_RING + KT_KREM + r * 8;
    LAS char* vb = c.L + L_RING + KT_V + h * KT_VBLK + vr * 16;
    f32x16 sAx, sBx, sAy, sBy, ox, oy;
    const u32x4 k0m = lld<u32x4>(kb); const u32x2 k0r = lld<u32x2>(rb);
    kb += kh * 4 * TILE_B; rb += kh * 4 * TILE_B; vb += kh * 4 * TILE_B;
    const u32x4 kom = lld<u32x4>(kb); const u32x2 kor = lld<u32x2>(rb);
    u32x4 km = lld<u32x4>(kb + TILE_B), v0 = lld<u32x4>(vb), v1 = lld<u32x4>(vb + 2 * KT_VBLK); u32x2 kr = lld<u32x2>(rb + TILE_B);
    Pipe px, py;
    { u32 z = 0u; asm volatile("" : "+v"(z));
      px.e[0] = px.e[1] = px.e[2] = px.e[3] = asf(z); px.p1 = u32x4{z, z, z, z}; py = px; }
    asm volatile("" : "+v"(px.p1), "+v"(px.e[0]), "+v"(px.e[1]), "+v"(px.e[2]), "+v"(px.e[3]), "+v"(qmx), "+v"(qrx));
    asm volatile("s_nop 1" : "+v"(py.p1), "+v"(py.e[0]), "+v"(py.e[1]), "+v"(py.e[2]), "+v"(py.e[3]), "+v"(qmy), "+v"(qry));
    QK1(ox, px.p1, px.p1); QK1(oy, px.p1, px.p1);
    QK1(sBx, k0m, qmx); QK1(sBy, k0m, qmy); QK2(sBx, k0r, qrx); QK2(sBy, k0r, qry);
    QK1(sAx, kom, qmx); QK1(sAy, kom, qmy);
    asm volatile("s_nop 7\n\ts_nop 1" : "+v"(sBx), "+v"(sBy));
    { const float mx = rowmax(sBx), my = rowmax(sBy);
      if (h == 0) { qrx[1] = pk(-mx, 0.f); qry[1] = pk(-my, 0.f); } }
    asm volatile("s_nop 1" : "+v"(qrx), "+v"(qry));
    QK2(sAx, kor, qrx); QK2(sAy, kor, qry);
    asm volatile("s_nop 7\n\ts_nop 3" : "+v"(sAx), "+v"(sAy));
    int slot = 0;
    const u64 want = ((u64)c.ehi << 32) | c.elo;
    const u32 fvo = (lane & 31) * 128;
    u64 fl = want;
    if (c.wave == 0) flag_load(fl, ~want, fvo, c.srd, WS_FLAG + (u32)(ph * 256 + ((ch0 + 2) & 7) * 32) * 128);
#pragma unroll 1
    for (int n = 0; n < 8; ++n) {
        const int nslot = slot == 2 ? 0 : slot + 1;
        LAS char* kc = kb + slot * CHUNK_B; LAS char* rc = rb + slot * CHUNK_B; LAS char* vc = vb + slot * CHUNK_B;
        LAS char* kn = kb + nslot * CHUNK_B; LAS char* rn = rb + nslot * CHUNK_B; LAS char* vn = vb + nslot * CHUNK_B;
#define A_TILE(U, SCX, SNX, SCY, SNY) asm volatile("" :: "v"(km), "v"(kr), "v"(v1));        \
        attn_step2(SCX, SNX, ox, px, SCY, SNY, oy, py, km, kr, v0, v1, qmx, qrx, qmy, qry, \
            [&] { km = lld<u32x4>((U) < 2 ? kc + ((U) + 2) * TILE_B : kn + ((U) - 2) * TILE_B); }, \
            [&] { kr = lld<u32x2>((U) < 2 ? rc + ((U) + 2) * TILE_B : rn + ((U) - 2) * TILE_B); }, \
            [&] { v1 = lld<u32x4>(vc + (U) * TILE_B + 2 * KT_VBLK); }, \
            [&] { v0 = lld<u32x4>((U) < 3 ? vc + ((U) + 1) * TILE_B : vn); })
        A_TILE(0, sAx, sBx, sAy, sBy); A_TILE(1, sBx, sAx, sBy, sAy);
        if (__builtin_expect(c.wave < 4, 0)) {
            asm volatile("s_waitcnt vmcnt(0)" : "+v"(fl) :: "memory");
            if (__builtin_expect(!__all(fl == want), 0)) poll(c, ph, chunk_ids(n + 2));
        }
        BAR();
        if (__builtin_expect(c.wave < 4, 0)) {
            fl = want;
            if (c.wave == 0 && n + 3 < 8) flag_load(fl, ~want, fvo, c.srd, WS_FLAG + (u32)(ph * 256 + ((ch0 + n + 3) & 7) * 32) * 128);
            if (n + 2 < 8) { issue_piece(n + 2, 0); issue_piece(n + 2, 1); issue_piece(n + 2, 2); issue_piece(n + 2, 3); issue_piece(n + 2, 4); }
            if (n >= 5) load_wimage_o4(c, 0, n - 5);
            if (rep == 0 && n == 0) {
                u32 z0 = 0u; asm volatile("" : "+v"(z0));
                const u32x4 z = {z0, z0, z0, z0};
#pragma unroll
                for (int q = 0; q < 5; ++q)
                    st128(c.rs, z, lane * 16 + q * 1024, WS_KVB + (u32)((4 * c.j + c.wave) * 64 + 2 * c.i) * TILE_B);
                if (c.wave < 2) st128(c.rs, z, (u32)((16 + (c.j >> 2)) * 4 + (lane >> 4)) * 1024 + ((c.j & 3) * 16 + (lane & 15)) * 16, WS_OF + c.wave * XF_BYTES + (u32)(c.i * 72) * 1024);
            }
        }
        A_TILE(2, sAx, sBx, sAy, sBy); A_TILE(3, sBx, sAx, sBy, sAy);
#undef A_TILE
        slot = nslot;
    }
    attn_finish2(ox, px, oy, py, v1);
    { LAS char* mine = c.L + L_RING + kh * 2 * CHUNK_B + a * 3072 + lane * 16;
#pragma unroll
      for (int e = 0; e < 3; ++e) lst(mine + e * 1024, f32x4{oy[4 * e], oy[4 * e + 1], oy[4 * e + 2], oy[4 * e + 3]}); }
    st.o = ox; st.qm = qmx; st.qr = qrx;
}

template <bool ISB, class HK>
DI void out_proj(const Ctx& c0, const Params& p, int rep, u32 (&xres)[3][2], HK behind_x) {
    const Ctx c = fresh(c0);
    const int lane = c.lane, m = c.wave & 3, kh = c.wave >> 2, tok = lane & 15, g = lane >> 4;
    wait_group(c, rep * 6 + (ISB ? 4 : 1));
    u32x4 xf[9]; f32x4 acc[5] = {};
    load_xfrags(c, WS_OF + (ISB ? 1 : 0) * XF_BYTES, xf);
    __builtin_amdgcn_sched_barrier(0);
    behind_x(c);
    if (ISB) {
        asm volatile("s_waitcnt vmcnt(9)" ::: "memory"); BAR();
    }
    g1_mma<false>(c, xf, acc, [](int, const u32x4 (&)[5]) {}, [](int) {});
    g1_exchange(c, acc);
#pragma unroll
    for (int u = 0; u < 3; ++u) {
        const float v0 = bflo(xres[u][0]) + acc[u][0], v1 = bfhi(xres[u][0]) + acc[u][1], v2 = bflo(xres[u][1]) + acc[u][2], v3 = bfhi(xres[u][1]) + acc[u][3];
        xres[u][0] = pk(v0, v1); xres[u][1] = pk(v2, v3);
    }
    if (ISB && rep == 1) {
        char* o = (char*)p.out + (size_t)(c.i * 64 + m * 16 + tok) * (DM * 2) + (c.j * 72 + 4 * g) * 2;
        *(u32x2*)(o + (2 * kh) * 32) = u32x2{xres[0][0], xres[0][1]};
        *(u32x2*)(o + (2 * kh + 1) * 32) = u32x2{xres[1][0], xres[1][1]};
        if (kh == 1 && g < 2) *(u32x2*)(o + 128) = u32x2{xres[2][0], xres[2][1]};
        return;
    }
    const int xi = ISB ? 1 : 0;
    const u32 so = WS_XF + xi * XF_BYTES + (u32)(c.i * 18 * 4 + m) * 1024;
    st128(c.rs, u32x4{xres[0][0], xres[0][1], xres[1][0], xres[1][1]}, lane * 16, so + (u32)(2 * c.j + kh) * 4096);
    if (kh == 1 && g < 2) st64(c.rs, u32x2{xres[2][0], xres[2][1]}, ((c.j & 3) * 16 + tok) * 16 + 8 * g, so + (u32)(16 + (c.j >> 2)) * 4096);
    __builtin_amdgcn_sched_barrier(0);
    float s1 = 0.f, s2 = 0.f;
#pragma unroll
    for (int u = 0; u < 3; ++u) {
        u32 d0 = xres[u][0], d1 = xres[u][1];
        asm volatile("" : "+v"(d0), "+v"(d1));
        if (u == 2 && !(kh == 1 && g < 2)) { d0 = 0u; d1 = 0u; }
        const float a0 = bflo(d0), a1 = bfhi(d0), a2 = bflo(d1), a3 = bfhi(d1);
        s1 += (a0 + a1) + (a2 + a3); s2 += (a0 * a0 + a1 * a1) + (a2 * a2 + a3 * a3);
    }
    s1 = sum_g(s1); s2 = sum_g(s2);
    if (g == 0) st64(c.rs, u32x2{asu(s1), asu(s2)}, (u32)((c.i * 16 + 2 * c.j + kh) * 64 + m * 16 + tok) * 8, WS_ST + xi * ST_BYTES);
    publish(c, rep * 6 + (ISB ? 5 : 2));
    if (!ISB && rep == 0) colsums_b(c);
    if (ISB) load_wimage_part<36, 90>(c, WS_WIMGA, 1, L_A1);
}

DI void a_tail(const Ctx& c0, const Params& p, int rep, ATail& st, u32 (&xres)[3][2], u32x4 (&w0)[WRING], u32x4 (&w1)[WRING]) {
    const Ctx c = fresh(c0);
    const int lane = c.lane, a = c.wave & 3, kh = c.wave >> 2;
    BAR();
    LAS char* theirs = c.L + L_RING + (1 - kh) * 2 * CHUNK_B + a * 3072 + lane * 16;
#pragma unroll
    for (int e = 0; e < 3; ++e) { const f32x4 v = lld<f32x4>(theirs + e * 1024); st.o[4 * e] += v[0]; st.o[4 * e + 1] += v[1]; st.o[4 * e + 2] += v[2]; st.o[4 * e + 3] += v[3]; }
    if (attn_unsafe(st.o)) st.o = attn_slow(c, WS_KVA + rep * KVA_BYTES, 0, 63, false, st.qm, st.qr);
    store_o(c, WS_OF, st.o, a, kh);
    publish(c, rep * 6 + 1);
    load_wring(c, w0, w1);
    out_proj<false>(c, p, rep, xres, [&](const Ctx& cc) {
        if (rep == 0) {
            const int m = cc.wave & 3, k2 = cc.wave >> 2, tok = cc.lane & 15, g = cc.lane >> 4;
            const char* rr = (const char*)p.x + (size_t)(cc.i * 64 + m * 16 + tok) * (DM * 2) + (cc.j * 72 + 4 * g) * 2;
            const u32x2 r0 = *(const u32x2*)(rr + (2 * k2) * 32), r1 = *(const u32x2*)(rr + (2 * k2 + 1) * 32), r2 = *(const u32x2*)(rr + (g < 2 ? 128 : 0));
            xres[0][0] = r0[0]; xres[0][1] = r0[1]; xres[1][0] = r1[0]; xres[1][1] = r1[1]; xres[2][0] = r2[0]; xres[2][1] = r2[1];
        }
    });
}

struct T_ { static constexpr bool value = true; };
struct F_ { static constexpr bool value = false; };
DI void b_proj(const Ctx& c0, const Params& p, int rep, u32x4 (&w0)[WRING], u32x4 (&w1)[WRING], u32x4& qm, u32x2& qr) {
    const Ctx c = fresh(c0);
    const int lane = c.lane, tok = lane & 15, g = lane >> 4, a = c.wave >> 1, odd = c.wave & 1;
    wait_group(c, rep * 6 + 2);
    u32x4 stv[4];
    if (c.wave == 5 || c.wave == 7) stats_issue32(c, WS_ST, stv);
    { const u32 so = WS_XF + (u32)(c.i * 72) * 1024;
#pragma unroll
      for (int n = 0; n < 9; ++n) glds_ws(c.srd, lane * 16, so + (c.wave + 8 * n) * 1024, c.Lb + L_XB + (c.wave + 8 * n) * 1024); }
    asm volatile("s_waitcnt vmcnt(5)" ::: "memory"); BAR();
    f32x4 a0[4] = {}, a1[4] = {};
    const u32 wso = WS_WF + (u32)((c.j * 8 + c.wave) * 2) * 18 * 1024;
    LAS char* xb = c.L + L_XB + lane * 16;
    auto body = [&](auto T0, auto T1, auto H1) {
        u32x4 x[2][4];
#pragma unroll
        for (int m = 0; m < 4; ++m) { x[0][m] = lld<u32x4>(xb + m * 1024); asm volatile("" : "+v"(a0[m]), "+v"(a1[m])); }
        if (!H1.value) stats_finish32(c, stv);
#pragma unroll
        for (int s = 0; s < 18; ++s) {
            if (s == 7) {
                if (H1.value) asm volatile("s_waitcnt vmcnt(14)" ::: "memory"); else asm volatile("s_waitcnt vmcnt(7)" ::: "memory");
                static_assert(18 - WRING >= 7, "steps 0..6 each refill the ring");
                BAR();
            }
            if (s + 1 < 18) {
#pragma unroll
                for (int m = 0; m < 4; ++m) x[(s + 1) & 1][m] = lld<u32x4>(xb + ((s + 1) * 4 + m) * 1024);
            }
#pragma unroll
            for (int m = 0; m < 4; ++m) {
                if (T0.value) MF16(a0[m], w0[s % WRING], x[s & 1][m]); else MF16(a0[m], x[s & 1][m], w0[s % WRING]);
                if (H1.value) { if (T1.value) MF16(a1[m], w1[s % WRING], x[s & 1][m]); else MF16(a1[m], x[s & 1][m], w1[s % WRING]); }
            }
            if (s + WRING < 18) { w0[s % WRING] = ld128(c.rs, lane * 16, wso + (WRING + s) * 1024); if (H1.value) w1[s % WRING] = ld128(c.rs, lane * 16, wso + (18 + WRING + s) * 1024); }
        }
        asm volatile("s_nop 7" : "+v"(a0[0]), "+v"(a0[1]), "+v"(a0[2]), "+v"(a0[3]), "+v"(a1[0]), "+v"(a1[1]), "+v"(a1[2]), "+v"(a1[3]));
    };
    if (!odd) body(T_{}, T_{}, T_{}); else if (a == 0) body(F_{}, T_{}, T_{}); else if (a == 1) body(F_{}, F_{}, T_{}); else body(F_{}, F_{}, F_{});
    const u32 kvb = WS_KVB;
    const u32 hb = kvb + (u32)((4 * c.j + a) * 64 + 2 * c.i) * TILE_B;
    auto fin_t = [&](const f32x4 (&acc)[4], int slot, float sc, u32 (&d)[4][2]) {
        const f32x4 cs = lld<f32x4>(c.L + L_CSB + (c.wave * 2 + slot) * 64 + g * 16);
#pragma unroll
        for (int m = 0; m < 4; ++m) {
            const float mean = lld<float>(c.L + L_MEAN + (m * 16 + tok) * 4), rs = lld<float>(c.L + L_MEAN + 256 + (m * 16 + tok) * 4) * sc;
            d[m][0] = pk((acc[m][0] - mean * cs[0]) * rs, (acc[m][1] - mean * cs[1]) * rs);
            d[m][1] = pk((acc[m][2] - mean * cs[2]) * rs, (acc[m][3] - mean * cs[3]) * rs);
        }
    };
    auto fin_p = [&](const f32x4 (&acc)[4], int slot, u32 (&d)[4][2]) {
        const float cs = lld<float>(c.L + L_CSB + (c.wave * 2 + slot) * 64 + tok * 4);
#pragma unroll
        for (int m = 0; m < 4; ++m) {
            const f32x4 mean = lld<f32x4>(c.L + L_MEAN + (m * 16 + 4 * g) * 4), rs = lld<f32x4>(c.L + L_MEAN + 256 + (m * 16 + 4 * g) * 4);
            d[m][0] = pk((acc[m][0] - mean[0] * cs) * rs[0], (acc[m][1] - mean[1] * cs) * rs[1]);
            d[m][1] = pk((acc[m][2] - mean[2] * cs) * rs[2], (acc[m][3] - mean[3] * cs) * rs[3]);
        }
    };
    auto frag16 = [&](const u32 (&d)[4][2], int t) {
        auto x = __builtin_amdgcn_permlane16_swap(d[2 * t][0], d[2 * t + 1][0], false, false);
        auto y = __builtin_amdgcn_permlane16_swap(d[2 * t][1], d[2 * t + 1][1], false, false);
        return u32x4{(u32)x[0], (u32)y[0], (u32)x[1], (u32)y[1]};
    };
    auto frag32 = [&](const u32 (&d)[4][2], int t) {
        auto x = __builtin_amdgcn_permlane32_swap(d[2 * t][0], d[2 * t + 1][0], false, false);
        auto y = __builtin_amdgcn_permlane32_swap(d[2 * t][1], d[2 * t + 1][1], false, false);
        return u32x4{(u32)x[0], (u32)y[0], (u32)x[1], (u32)y[1]};
    };
    u32 d[4][2];
    LAS char* lh = c.L + L_KVB + a * 25600 + 8 * TILE_B; LAS char* l0 = c.L + L_KVB + 8 * TILE_B;
    if (!odd) {
        fin_t(a1, 1, 1.f, d);
        { const u32x4 f0 = frag16(d, 0), f1 = frag16(d, 1);
          st128(c.rs, f0, lane * 16, hb); st128(c.rs, f1, lane * 16, hb + TILE_B);
          lst(lh + lane * 16, f0); lst(lh + TILE_B + lane * 16, f1); }
        fin_t(a0, 0, QSCALE, d);
        qm = frag16(d, 0);
        lst(c.L + L_QT1 + a * 1024 + lane * 16, frag16(d, 1));
    } else {
        fin_p(a0, 0, d);
        { const u32x4 f0 = frag32(d, 0), f1 = frag32(d, 1);
          st128(c.rs, f0, KT_V + g * KT_VBLK + tok * 16, hb); st128(c.rs, f1, KT_V + g * KT_VBLK + tok * 16, hb + TILE_B);
          lst(lh + KT_V + g * KT_VBLK + tok * 16, f0); lst(lh + TILE_B + KT_V + g * KT_VBLK + tok * 16, f1); }
        if (a == 0) {
            fin_t(a1, 1, g < 2 ? QSCALE : 1.f, d);
#pragma unroll
            for (int m = 0; m < 4; ++m) {
                if (g < 2) { lst(c.L + L_QREM + (2 * g) * 256 + (m * 16 + tok) * 4, d[m][0]); lst(c.L + L_QREM + (2 * g + 1) * 256 + (m * 16 + tok) * 4, d[m][1]); }
                else {
                    const u32 hb0 = kvb + (u32)(4 * c.j * 64 + 2 * c.i + (m >> 1)) * TILE_B;
                    st64(c.rs, u32x2{d[m][0], 0x00003f80u}, (u32)(2 * (g - 2)) * 64 * TILE_B + KT_KREM + ((m & 1) * 16 + tok) * 8, hb0);
                    st64(c.rs, u32x2{d[m][1], 0x00003f80u}, (u32)(2 * (g - 2) + 1) * 64 * TILE_B + KT_KREM + ((m & 1) * 16 + tok) * 8, hb0);
                    LAS char* lk = l0 + (2 * (g - 2)) * 25600 + (m >> 1) * TILE_B + KT_KREM + ((m & 1) * 16 + tok) * 8;
                    lst(lk, u32x2{d[m][0], 0x00003f80u}); lst(lk + 25600, u32x2{d[m][1], 0x00003f80u});
                }
            }
        } else if (a == 1) {
            fin_p(a1, 1, d);
            const u32 hb0 = kvb + (u32)(4 * c.j * 64 + 2 * c.i) * TILE_B;
            const u32 vo = (u32)(tok >> 1) * 64 * TILE_B + KT_V + g * KT_VBLK + (16 + (tok & 1)) * 16;
            const u32x4 f0 = frag32(d, 0), f1 = frag32(d, 1);
            if (tok < 8) {
                st128(c.rs, f0, vo, hb0); st128(c.rs, f1, vo, hb0 + TILE_B);
                LAS char* lv = l0 + (tok >> 1) * 25600 + KT_V + g * KT_VBLK + (16 + (tok & 1)) * 16;
                lst(lv, f0); lst(lv + TILE_B, f1);
            }
        } else if (a == 2) {
            const u32 hb0 = kvb + (u32)(4 * c.j * 64 + 2 * c.i) * TILE_B;
            if (lane < 32) {
                const u32x4 on = ones4();
                st128(c.rs, on, (u32)(lane >> 3) * 64 * TILE_B + ((lane >> 2) & 1) * TILE_B + KT_V + (lane & 3) * KT_VBLK + 18 * 16, hb0);
                lst(l0 + (lane >> 3) * 25600 + ((lane >> 2) & 1) * TILE_B + KT_V + (lane & 3) * KT_VBLK + 18 * 16, on);
            }
        }
    }
    publish(c, rep * 6 + 3);
    load_wimage_part<44, 90>(c, WS_WIMG + 8 * WIMG_BYTES, 1);
    if (odd) qm = lld<u32x4>(c.L + L_QT1 + a * 1024 + lane * 16);
    const u32 rem = lld<u32>(c.L + L_QREM + a * 256 + (32 * odd + (lane & 31)) * 4);
    qr = u32x2{lane < 32 ? rem : 0u, 0u};
}

DI void b_attn(const Ctx& c0, const Params& p, int rep, u32x4 qm, u32x2 qr) {
    const Ctx c = fresh(c0);
    const int lane = c.lane, r = lane & 31, h = lane >> 5, a = c.wave >> 1, t = c.wave & 1;
    const int qt = 2 * c.i + t;
    const f32x16 cd = mask_c(lane, true);
    if (c.wave == 1) {
        const f32x16 cm = mask_c(lane, false);
#pragma unroll
        for (int e = 0; e < 4; ++e) lst(c.L + L_MSK + e * 1024 + lane * 16, f32x4{cm[4 * e], cm[4 * e + 1], cm[4 * e + 2], cm[4 * e + 3]});
    }
    LAS char* tb = c.L + L_KVB + a * 25600 + (8 + t) * TILE_B;
    LAS char* kb = tb + lane * 16; LAS char* rb = tb + KT_KREM + r * 8; LAS char* vb = tb + KT_V + h * KT_VBLK + vrow_of(r) * 16;
    const u32x4 k0m = lld<u32x4>(kb); const u32x2 k0r = lld<u32x2>(rb);
    u32x4 km, v0 = lld<u32x4>(vb), v1 = lld<u32x4>(vb + 2 * KT_VBLK); u32x2 kr;
    f32x16 sA, sB, o;
    Pipe pp;
    { u32 z = 0u; asm volatile("" : "+v"(z)); pp.e[0] = pp.e[1] = pp.e[2] = pp.e[3] = asf(z); pp.p1 = u32x4{z, z, z, z}; }
    f32x16 cdp = cd;
    asm volatile("s_nop 1" : "+v"(pp.p1), "+v"(pp.e[0]), "+v"(pp.e[1]), "+v"(pp.e[2]), "+v"(pp.e[3]), "+v"(qm), "+v"(qr), "+v"(cdp));
    QK1(o, pp.p1, pp.p1);
    QK1C(sA, k0m, qm, cdp);
    asm volatile("v_mfma_f32_32x32x8_bf16 %0, %1, %2, %3" : "=&v"(sB) : "v"(k0r), "v"(qr), "v"(sA));
    asm volatile("s_nop 7\n\ts_nop 3" : "+v"(sB), "+v"(sA));
    { const float m0 = rowmax(sB); if (h == 0) qr[1] = pk(-m0, 0.f); }
    asm volatile("s_nop 1" : "+v"(qr));
    QK2(sA, k0r, qr);
    asm volatile("s_nop 7\n\ts_nop 3" : "+v"(sA), "+v"(o), "+v"(v0), "+v"(v1));
    if (c.wave == 0) poll(c, rep * 6 + 3, (lane < 5 && c.i - lane >= 0) ? (c.i - lane) * 8 + c.j : -1);
    BAR();
    const u32 hb = WS_KVB + (u32)((4 * c.j + a) * 64) * TILE_B;
    { const u32 dst = c.Lb + L_KVB + a * 25600;
#pragma unroll
      for (int n = 0; n < 10; ++n) {
          const int pc = 19 - t - 2 * n;
          u32 vo = lane * 16;
          if (c.i < 4) { const int b = pc * 1024 + lane * 16, tau = b / 2560, kt = 2 * c.i - 8 + tau; vo = (u32)((kt < 0 ? 0 : kt) * 2560 + (b - tau * 2560)); glds_ws(c.srd, vo, hb, dst + pc * 1024); }
          else glds_ws(c.srd, vo, hb + (u32)(2 * c.i - 8) * 2560 + pc * 1024, dst + pc * 1024);
      } }
    asm volatile("s_waitcnt vmcnt(5)" ::: "memory");
    BAR();
    km = lld<u32x4>(kb - TILE_B); kr = lld<u32x2>(rb - TILE_B);
    auto first = [](f32x16& s, u32x4 k, u32x4 q) { QK1(s, k, q); };
    auto firstm = [&](f32x16& s, u32x4 k, u32x4 q) {
        f32x16 co;
#pragma unroll
        for (int e = 0; e < 4; ++e) { const f32x4 v = lld<f32x4>(c.L + L_MSK + e * 1024 + lane * 16); co[4 * e] = v[0]; co[4 * e + 1] = v[1]; co[4 * e + 2] = v[2]; co[4 * e + 3] = v[3]; }
        QK1C(s, k, q, co); };
    const u32 qoff = h == 0 ? pk(NEG_BIG, 0.f) : 0u;
    kb -= 8 * TILE_B; rb -= 8 * TILE_B; vb -= 8 * TILE_B;
#define B_TILE(U, SC, SN, FIRST) do { u32x2 qs = {qr[0], (U) + 1 <= qt ? qr[1] : qoff}; asm volatile("" : "+v"(qs)); \
        attn_step<((U) < 8)>(SC, SN, o, pp, km, kr, v0, v1, qm, qs, FIRST, \
            [&] { if ((U) + 2 <= 8) km = lld<u32x4>(kb + (8 - ((U) + 2)) * TILE_B); }, [&] { if ((U) + 2 <= 8) kr = lld<u32x2>(rb + (8 - ((U) + 2)) * TILE_B); }, \
            [&] { v1 = lld<u32x4>(vb + (8 - (U)) * TILE_B + 2 * KT_VBLK); }, [&] { if ((U) + 1 <= 8) v0 = lld<u32x4>(vb + (8 - ((U) + 1)) * TILE_B); }); } while (0)
    B_TILE(0, sA, sB, first); B_TILE(1, sB, sA, first);
    DRAIN();
    B_TILE(2, sA, sB, first);
    BAR();
    B_TILE(3, sB, sA, first); B_TILE(4, sA, sB, first); B_TILE(5, sB, sA, first); B_TILE(6, sA, sB, first); B_TILE(7, sB, sA, firstm); B_TILE(8, sA, sB, first);
#undef B_TILE
    attn_finish(o, pp, v1);
    if (attn_unsafe(o)) o = attn_slow(c, hb, qt - 8 < 0 ? 0 : qt - 8, qt, true, qm, qr);
    store_o(c, WS_OF + XF_BYTES, o, a, t);
    publish(c, rep * 6 + 4);
    load_wimage_part<0, 44>(c, WS_WIMG + 8 * WIMG_BYTES, 1);
    if (rep == 0) load_wimage_part<0, 36>(c, WS_WIMGA, 1, L_A1);
}

__global__ void __launch_bounds__(512, 2) fused_block_kernel(Params p) {
    __shared__ __attribute__((aligned(16))) char lds_[LDS_BYTES];
    Ctx c;
    c.L = (LAS char*)lds_; c.Lb = (u32)(size_t)c.L;
    c.ws = p.ws;
    c.tid = threadIdx.x; c.lane = c.tid & 63; c.wave = __builtin_amdgcn_readfirstlane(c.tid >> 6);
    { const int wg = blockIdx.x, slot = wg >> 3; c.i = (wg & 7) * 4 + (slot >> 3); c.j = slot & 7; }
    c.rs = __builtin_amdgcn_make_buffer_rsrc((void*)p.ws, 0, WS_USED, 0x00020000);
    { const u64 b = (u64)p.ws; c.srd = u32x4{(u32)b, (u32)(b >> 32) & 0xffffu, WS_USED, 0x00020000u}; }
    u32 warm;
    { u64 pc; u32 len;
      asm volatile("s_getpc_b64 %0\nfbk_code_first:\n\ts_mov_b32 %1, fbk_code_last-fbk_code_first" : "=s"(pc), "=s"(len));
      u32 off = ((u32)(blockIdx.x >> 3) * 8 + c.wave) * 256 + c.lane * 4;
      off = off < len ? off : 0u;
      warm = *(const GAS u32*)((pc & ~3ull) + off); }
    c.elo = 0; c.ehi = 0;
    u32 xres[3][2];
    a_proj<0>(c, p);
    asm volatile("" :: "v"(warm));
#pragma unroll 1
    for (int rep = 0; rep < 2; ++rep) {
        u32x4 w0[WRING], w1[WRING]; ATail st;
        a_attn(c, p, rep, st);
        if (rep == 0) {
            if (blockIdx.x == 0 && c.tid == 0) __hip_atomic_store((GAS u64*)(p.ws + WS_EPOCH), ((u64)c.ehi << 32) | c.elo, __ATOMIC_RELAXED, __HIP_MEMORY_SCOPE_AGENT);
        }
        a_tail(c, p, rep, st, xres, w0, w1);
        u32x4 qm; u32x2 qr;
        b_proj(c, p, rep, w0, w1, qm, qr);
        b_attn(c, p, rep, qm, qr);
        out_proj<true>(c, p, rep, xres, [](const Ctx&) {});
        if (rep == 1) break;
        a_proj<1>(c, p);
    }
    asm volatile("fbk_code_last:");
}

static size_t ws_offset(size_t ws_size) { return (ws_size - WS_USED) & ~(size_t)0x1fffff; }

extern "C" void kernel_launch(void* const* d_in, const int* in_sizes, int n_in, void* d_out, int out_size, void* d_ws, size_t ws_size, hipStream_t stream) {
    Params p;
    p.x = (const u16*)d_in[0]; p.wq = (const u16*)d_in[1]; p.wk = (const u16*)d_in[2]; p.wv = (const u16*)d_in[3]; p.woA = (const u16*)d_in[4];
    p.wqkv = (const u16*)d_in[6]; p.woB = (const u16*)d_in[7]; p.out = (u16*)d_out;
    p.ws = (char*)d_ws + ws_offset(ws_size);
    fused_block_kernel<<<256, 512, 0, stream>>>(p);
}
```
